```python
import torch

def _softmax(x, axis=-1):
    return torch.softmax(x, dim=axis)
def _gelu(x):
    return torch.nn.functional.gelu(x, approximate="tanh")
def _silu(x):
    return torch.nn.functional.silu(x)
def _sigmoid(x):
    return torch.sigmoid(x)
def _layernorm(x, axis=-1, eps=1e-5):
    m = x.mean(dim=axis, keepdim=True)
    v = x.var(dim=axis, keepdim=True, unbiased=False)
    return (x - m) / torch.sqrt(v + eps)
def _scatter_add(tgt, idx, src, axis=0):
    return tgt.clone().index_add_(axis, idx, src)
def _segment_sum(src, seg, n):
    out = torch.zeros((n,) + src.shape[1:], dtype=src.dtype)
    return out.index_add_(0, seg, src)
def _pad(x, pw):
    flat = tuple(v for pair in reversed(pw) for v in pair)
    return torch.nn.functional.pad(x, flat)

kernel_name = 'parallel_sum__ssm_conv_s4d__glu_gated_glu__prompt'

def setup_inputs():

    _w = (lambda a: (lambda t: t.to(torch.bfloat16) if t.is_floating_point() else t)(torch.as_tensor(a)))
    def _A_setup():
        Bsz, L, D, N = 1, 2048, 640, 192
        return {
            "u": _w((lambda *s: torch.randn(*s, dtype=torch.bfloat16) * 0.05)(Bsz, L, D)),
            "log_A_real": _w((lambda *s: torch.randn(*s, dtype=torch.bfloat16) * 3.2)(D, N) * 0.5),
            "A_imag": _w((lambda *s: torch.randn(*s, dtype=torch.bfloat16) * 0.1)(D, N) * 3.14),
            "C_real": _w((lambda *s: torch.randn(*s, dtype=torch.bfloat16) * 1.6)(D, N) / N ** 0.5),
            "C_imag": _w((lambda *s: torch.randn(*s, dtype=torch.bfloat16) * 1.6)(D, N) / N ** 0.5),
            "log_dt": _w((lambda *s: torch.randn(*s, dtype=torch.bfloat16) * 0.1)(D) * 0.5 - 4.0),
            "D_skip": _w((lambda *s: torch.randn(*s, dtype=torch.bfloat16) * 0.2)(D)),
        }
    _A_in = _A_setup()

    def _B_setup():
        B, S, D, F = 1, 2048, 640, 1536
        return {
            "x": _w((lambda *s: torch.randn(*s, dtype=torch.bfloat16) * 0.05)(B, S, D)),
            "w_gate": _w((lambda *s: torch.randn(*s, dtype=torch.bfloat16) * 0.05)(D, F)),
            "w_up": _w((lambda *s: torch.randn(*s, dtype=torch.bfloat16) * 0.05)(D, F)),
            "w_down": _w((lambda *s: torch.randn(*s, dtype=torch.bfloat16) * 0.05)(F, D)),
        }
    _B_in = _B_setup()

    return {
        "A_u": _A_in["u"],
        "A_log_A_real": _A_in["log_A_real"],
        "A_A_imag": _A_in["A_imag"],
        "A_C_real": _A_in["C_real"],
        "A_C_imag": _A_in["C_imag"],
        "A_log_dt": _A_in["log_dt"],
        "A_D_skip": _A_in["D_skip"],
        "B_x": _B_in["x"],
        "B_w_gate": _B_in["w_gate"],
        "B_w_up": _B_in["w_up"],
        "B_w_down": _B_in["w_down"],
    }

def reference(A_u, A_log_A_real, A_A_imag, A_C_real, A_C_imag, A_log_dt, A_D_skip, B_x, B_w_gate, B_w_up, B_w_down):
    def _A_ref(u, log_A_real, A_imag, C_real, C_imag, log_dt, D_skip):
        L = u.shape[1]
        dt = torch.exp(log_dt)
        A = -torch.exp(log_A_real) + 1j * A_imag
        C = (C_real + 1j * C_imag) * dt[:, None]
        pos = (lambda x, dt: x.to(dt))(torch.arange(L), log_dt.dtype)
        P = torch.exp((A * dt[:, None])[:, :, None] * pos[None, None, :])
        K = 2.0 * torch.einsum("dn,dnl->dl", C, P).real
        L2 = 2 * L
        u_t = (lambda x, *d: x.permute(*d))(u, 0, 2, 1)
        Kf = (lambda x, *a, **k: torch.fft.rfft(x.float(), *a, **k))(K, L2)
        Uf = (lambda x, *a, **k: torch.fft.rfft(x.float(), *a, **k))(u_t, L2)
        y = (lambda x, *a, **k: torch.fft.irfft(x, *a, **k).to(torch.bfloat16))(Uf * Kf[None, :, :], L2)[:, :, :L]
        y = (lambda x, *d: x.permute(*d))(y, 0, 2, 1)
        return y + u * D_skip[None, None, :]

    def _B_ref(x, w_gate, w_up, w_down):
        g = torch.einsum("bsd,df->bsf", x, w_gate)
        u = torch.einsum("bsd,df->bsf", x, w_up)
        h = _sigmoid(g) * u
        return torch.einsum("bsf,fd->bsd", h, w_down)

    x = A_u
    xn = _layernorm(x, axis=-1)
    return x + _A_ref(xn, A_log_A_real, A_A_imag, A_C_real, A_C_imag, A_log_dt, A_D_skip) + _B_ref(xn, B_w_gate, B_w_up, B_w_down)
```

```cpp
#include <hip/hip_runtime.h>
#include <hip/hip_bf16.h>
typedef unsigned short u16;
__device__ __forceinline__ float bf2f(u16 v){ return __uint_as_float(((unsigned)v)<<16); }
__device__ __forceinline__ u16 f2bf(float f){ unsigned u=__float_as_uint(f); return (u16)((u+0x7FFFu+((u>>16)&1))>>16); }
__device__ __forceinline__ float rbf(float f){ return bf2f(f2bf(f)); }
constexpr int L=2048,D=640,N=192,F=1536;
__global__ void k_ln(const u16* x,u16* xn){
  int r=blockIdx.x; int t=threadIdx.x;
  float v[10]; float s=0;
  for(int i=0;i<10;i++){ v[i]=bf2f(x[r*D+t+64*i]); s+=v[i]; }
  for(int o=32;o;o>>=1) s+=__shfl_xor(s,o);
  float m=s/D; float q=0;
  for(int i=0;i<10;i++){ float d=v[i]-m; q+=d*d; }
  for(int o=32;o;o>>=1) q+=__shfl_xor(q,o);
  float mb=rbf(m), vb=rbf(q/D); float sd=rbf(sqrtf(rbf(vb+1e-5f)));
  for(int i=0;i<10;i++) xn[r*D+t+64*i]=f2bf(rbf(v[i]-mb)/sd);
}
__global__ void k_kgen(const u16* lar,const u16* aim,const u16* cr,const u16* ci,const u16* ldt,float* K){
  int d=blockIdx.y; int l=blockIdx.x*256+threadIdx.x;
  float dt=rbf(expf(bf2f(ldt[d]))); float p=rbf((float)l);
  float acc=0;
  for(int n=0;n<N;n++){
    float zr=-rbf(expf(bf2f(lar[d*N+n])))*dt, zi=bf2f(aim[d*N+n])*dt;
    float e=expf(zr*p); float s,c; sincosf(zi*p,&s,&c);
    acc+= bf2f(cr[d*N+n])*dt*e*c - bf2f(ci[d*N+n])*dt*e*s;
  }
  K[d*L+l]=2.f*acc;
}
__global__ void k_conv(const float* K,const u16* xn,float* y){
  int d=blockIdx.y; int l=blockIdx.x*256+threadIdx.x;
  float acc=0;
  for(int j=0;j<=l;j++) acc+=K[d*L+l-j]*bf2f(xn[j*D+d]);
  y[l*D+d]=acc;
}
__global__ void k_g1(const u16* xn,const u16* wg,const u16* wu,u16* h){
  int f=blockIdx.x*256+threadIdx.x; int l=blockIdx.y;
  float g=0,u=0;
  for(int k=0;k<D;k++){ float a=bf2f(xn[l*D+k]); g+=a*bf2f(wg[k*F+f]); u+=a*bf2f(wu[k*F+f]); }
  g=rbf(g);u=rbf(u); float s=rbf(1.f/(1.f+expf(-g)));
  h[l*F+f]=f2bf(s*u);
}
__global__ void k_g2(const u16* h,const u16* wd,const u16* x,const u16* xn,const float* y,const u16* ds,u16* out){
  int d=blockIdx.x*128+threadIdx.x; int l=blockIdx.y;
  float a=0;
  for(int k=0;k<F;k++) a+=bf2f(h[l*F+k])*bf2f(wd[k*D+d]);
  a=rbf(a);
  float ya=rbf(rbf(y[l*D+d])+rbf(bf2f(xn[l*D+d])*bf2f(ds[d])));
  out[l*D+d]=f2bf(rbf(bf2f(x[l*D+d])+ya)+a);
}
extern "C" void kernel_launch(void* const* d_in,const int* in_sizes,int n_in,void* d_out,int out_size,void* d_ws,size_t ws_size,hipStream_t stream){
  const u16* x=(const u16*)d_in[0];
  char* ws=(char*)d_ws;
  u16* xn=(u16*)ws; float* K=(float*)(ws+(4<<20)); float* y=(float*)(ws+(12<<20)); u16* h=(u16*)(ws+(20<<20));
  k_ln<<<L,64,0,stream>>>(x,xn);
  k_kgen<<<dim3(L/256,D),256,0,stream>>>((const u16*)d_in[1],(const u16*)d_in[2],(const u16*)d_in[3],(const u16*)d_in[4],(const u16*)d_in[5],K);
  k_conv<<<dim3(L/256,D),256,0,stream>>>(K,xn,y);
  k_g1<<<dim3(F/256,L),256,0,stream>>>(xn,(const u16*)d_in[8],(const u16*)d_in[9],h);
  k_g2<<<dim3(D/128,L),128,0,stream>>>(h,(const u16*)d_in[10],x,xn,y,(const u16*)d_in[6],(u16*)d_out);
}
```

```cpp
#include <hip/hip_runtime.h>
#pragma clang diagnostic ignored "-Wint-to-pointer-cast"
typedef unsigned short u16; typedef unsigned u32; typedef unsigned long long u64;
typedef u32 v4u __attribute__((ext_vector_type(4)));
typedef u32 v2u __attribute__((ext_vector_type(2)));
typedef float f4 __attribute__((ext_vector_type(4)));
typedef float f2 __attribute__((ext_vector_type(2)));
typedef short s4 __attribute__((ext_vector_type(4)));
typedef __bf16 bf2 __attribute__((ext_vector_type(2)));
typedef _Float16 hf2 __attribute__((ext_vector_type(2)));
#define LAS __attribute__((address_space(3)))
#define GAS __attribute__((address_space(1)))
#define DEV __device__ __forceinline__
typedef __amdgpu_buffer_rsrc_t rsrc_t;

constexpr int L = 2048, D = 640, NS = 192, F = 1536;
constexpr u32 WS_GEN = 0, WS_HFLAG = 1024, WS_ZFLAG = 5120, WS_LFLAG = 8192, WS_WFLAG = WS_LFLAG + 512, WS_STG = 16384, WS_CSP = 65536,
              WS_XT = 1u << 20, WS_WDF = 4u << 20, WS_HF = 6u << 20, WS_ZT = 13u << 20, WS_BYTES = 16u << 20;
constexpr u32 LD_K = 0, K_BYTES = 5120;
constexpr u32 LD_CS = LD_K + 3 * K_BYTES, LD_ST = LD_CS + 512, LD_CTL = LD_ST + 208 * 8, LD_R = LD_CTL + 512;
constexpr u32 WSLOT = 16384, NWS = 4, XSLOT = 26624, NXS = 3, LD_X = LD_R + NWS * WSLOT, LDS_BYTES = LD_X + NXS * XSLOT;
constexpr u32 LD_U = LD_R + 2 * WSLOT, U_BYTES = 4608;
constexpr u32 WD_BYTES = 30720;
DEV u32 wd_off(int w) { return w == 0 ? LD_R : LD_R + 3 * WSLOT + (w - 1) * WD_BYTES; }
static_assert(LD_R % 128 == 0 && LD_R + 3 * WSLOT + 3 * WD_BYTES <= LDS_BYTES && 3 * U_BYTES <= WSLOT && LDS_BYTES <= 163840, "lds");
constexpr u32 C_FULL = 0, C_WDFULL = 44, C_RDY = 48, C_ZFULL = 52, C_STF = 56, C_TAG = 60, C_KDONE = 64, C_RED = 80, C_DONE = 256;
constexpr int AUX_SC1 = 16;
constexpr u32 SPIN_CAP = 1u << 15;
#define WD_DEPTH 30
#define POLL_STAGE 8
#define CONV_PRIO 3

#define STAMP(k) do { } while (0)

struct Params {
  const u16 *x, *lar, *aim, *cre, *cim, *ldt, *dsk, *wg, *wu, *wd;
  u16* out; char* ws;
};

DEV float bf2f(u32 v) { return __uint_as_float(v << 16); }
DEV float bfhi(u32 v) { return __uint_as_float(v & 0xffff0000u); }
DEV u32 pkbf(float a, float b) { f2 v = {a, b}; return __builtin_bit_cast(u32, __builtin_convertvector(v, bf2)); }
DEV u32 pkhf(float a, float b) { f2 v = {a, b}; return __builtin_bit_cast(u32, __builtin_convertvector(v, hf2)); }
DEV float rbf(float a) { return bf2f(pkbf(a, a)); }
DEV void mma_bf(f4& c, v4u a, v4u b) { asm volatile("v_mfma_f32_16x16x32_bf16 %0, %1, %2, %0" : "+v"(c) : "v"(a), "v"(b)); }
DEV void mma_hf(f4& c, v4u a, v4u b) { asm volatile("v_mfma_f32_16x16x32_f16 %0, %1, %2, %0" : "+v"(c) : "v"(a), "v"(b)); }
DEV v2u trd(u32 a) { return __builtin_bit_cast(v2u, __builtin_amdgcn_ds_read_tr16_b64_v4i16((LAS s4*)a)); }
DEV v4u trd2(u32 a, u32 b) { v2u x = trd(a), y = trd(b); return v4u{x.x, x.y, y.x, y.y}; }
DEV v4u lds128(u32 a) { return *(LAS v4u*)a; }
DEV v2u lds64(u32 a) { return *(LAS v2u*)a; }
DEV u32 rfl(u32 v) { return __builtin_amdgcn_readfirstlane(v); }
DEV v4u mkrs(const void* p, u32 bytes) { u64 a = (u64)p; return v4u{(u32)a, (u32)(a >> 32) & 0xffffu, bytes, 0x00020000u}; }
DEV rsrc_t mkr(const void* p, u32 bytes) { return __builtin_amdgcn_make_buffer_rsrc((void*)p, 0, bytes, 0x00020000); }
DEV v4u ldc128(rsrc_t r, u32 v, u32 s) { return __builtin_amdgcn_raw_buffer_load_b128(r, v, s, AUX_SC1); }
DEV u32 ldc32(rsrc_t r, u32 v) { return __builtin_amdgcn_raw_buffer_load_b32(r, v, 0, AUX_SC1); }
DEV void stc128(v4u d, rsrc_t r, u32 v, u32 s) { __builtin_amdgcn_raw_buffer_store_b128(d, r, v, s, AUX_SC1); }
DEV void stc32(u32 d, rsrc_t r, u32 v) { __builtin_amdgcn_raw_buffer_store_b32(d, r, v, 0, AUX_SC1); }
#define CBAR() asm volatile("" ::: "memory")
#define SCHED() __builtin_amdgcn_sched_barrier(0)
#define ORDER() __builtin_amdgcn_fence(__ATOMIC_ACQUIRE, "wavefront")
#define BARRIER() do { asm volatile("s_waitcnt lgkmcnt(0)" ::: "memory"); __builtin_amdgcn_s_barrier(); CBAR(); } while (0)
template <int N> DEV void waitv() { asm volatile("s_waitcnt vmcnt(%0)" ::"i"(N) : "memory"); }
#define DMA_(V, RS, SO, OFF, SC) "buffer_load_dwordx4 " V ", " RS ", " SO " offen" OFF SC " lds\n\t"
#define DMA4_(NAME, SC) DEV void NAME(v4u rs, u32 v0, u32 v1, u32 v2, u32 v3, u32 so, u32 lds) { \
  asm volatile("s_mov_b32 m0, %0\n\ts_nop 0\n\t" DMA_("%1", "%5", "%6", "", SC) DMA_("%2", "%5", "%6", " offset:1024", SC) DMA_("%3", "%5", "%6", " offset:2048", SC) DMA_("%4", "%5", "%6", " offset:3072", SC) \
               ::"s"(lds), "v"(v0), "v"(v1), "v"(v2), "v"(v3), "s"(rs), "s"(so) : "memory"); }
#define DMA2_(NAME, SC) DEV void NAME(v4u rs, u32 v0, u32 v1, u32 so, u32 lds) { \
  asm volatile("s_mov_b32 m0, %0\n\ts_nop 0\n\t" DMA_("%1", "%3", "%4", "", SC) DMA_("%2", "%3", "%4", " offset:1024", SC) ::"s"(lds), "v"(v0), "v"(v1), "s"(rs), "s"(so) : "memory"); }
#define DMA1_(NAME, SC) DEV void NAME(v4u rs, u32 v0, u32 so, u32 lds) { \
  asm volatile("s_mov_b32 m0, %0\n\ts_nop 0\n\t" DMA_("%1", "%2", "%3", "", SC) ::"s"(lds), "v"(v0), "s"(rs), "s"(so) : "memory"); }
DMA4_(dma4, "") DMA2_(dma2, "") DMA1_(dma1, "") DMA4_(dma4c, " sc1") DMA2_(dma2c, " sc1") DMA1_(dma1c, " sc1")
DEV void sig0(v4u rz, u32 lds) {
  asm volatile("s_mov_b32 m0, %0\n\ts_mov_b64 exec, 1\n\tbuffer_load_dword off, %1, 0 lds\n\ts_mov_b64 exec, -1" ::"s"(lds), "s"(rz) : "memory");
}
DEV u32 ldsv(u32 a) { return *(volatile LAS u32*)a; }
DEV void ldsw(u32 a, u32 v) { asm volatile("s_mov_b64 exec, 1\n\tds_write_b32 %0, %1\n\ts_mov_b64 exec, -1" ::"v"(a), "v"(v) : "memory"); }
DEV void spin0(u32 a, u32 cur) {
  u32 tv, ts, cnt;
  asm volatile("s_cmp_eq_u32 %4, 0\n\ts_cbranch_scc0 3f\n2:\n\t.subsection 1\n3:\n\ts_mov_b32 %2, 0x10000\n"
               "1:\n\tds_read_b32 %0, %3\n\ts_waitcnt lgkmcnt(0)\n\tv_readfirstlane_b32 %1, %0\n\ts_nop 0\n\ts_cmp_eq_u32 %1, 0\n\ts_cbranch_scc1 2b\n\t"
               "s_sleep 1\n\ts_sub_u32 %2, %2, 1\n\ts_cmp_lg_u32 %2, 0\n\ts_cbranch_scc1 1b\n\ts_branch 2b\n\t.subsection 0"
               : "=&v"(tv), "=&s"(ts), "=&s"(cnt) : "v"(a), "s"(cur) : "memory", "scc");
}
DEV bool is_zero4(u32 a) { v4u d = *(volatile LAS v4u*)a; return rfl(d.x | d.y | d.z | d.w) == 0; }
DEV void spin_zero4(u32 a) {
#pragma unroll 1
  for (u32 i = 0; i < 2 * SPIN_CAP; i++) { if (is_zero4(a)) break; __builtin_amdgcn_s_sleep(1); } CBAR();
}
template <int SL> DEV void poll_flags(rsrc_t rw, u32 off, u32 off2, u32 tag) {
#pragma unroll 1
  for (u32 i = 0; i < SPIN_CAP; i++) {
    const u32 a = ldc32(rw, off), b = ldc32(rw, off2);
    if (__all(a == tag && b == tag)) break;
    __builtin_amdgcn_s_sleep(SL);
  }
  ORDER();
}
DEV float fmul_(float a, float b) { float r; asm("v_mul_f32 %0, %1, %2" : "=v"(r) : "v"(a), "v"(b)); return r; }
DEV float fadd_(float a, float b) { float r; asm("v_add_f32 %0, %1, %2" : "=v"(r) : "v"(a), "v"(b)); return r; }
DEV float ffma_(float a, float b, float c) { float r; asm("v_fma_f32 %0, %1, %2, %3" : "=v"(r) : "v"(a), "v"(b), "v"(c)); return r; }
DEV float fnma_(float a, float b, float c) { float r; asm("v_fma_f32 %0, -%1, %2, %3" : "=v"(r) : "v"(a), "v"(b), "v"(c)); return r; }
struct cf { float x, y; };
DEV cf csq(cf a) { const float t = fmul_(a.x, a.y); return cf{fnma_(a.y, a.y, fmul_(a.x, a.x)), fadd_(t, t)}; }
DEV u32 pkc(cf a) { return pkhf(a.x, a.y); }
DEV cf cexp2(float lr, float rev) { float m = __builtin_amdgcn_exp2f(lr); const float f = __builtin_amdgcn_fractf(rev); float c = __builtin_amdgcn_cosf(f), s = __builtin_amdgcn_sinf(f);
  asm volatile("s_nop 0" : "+v"(c), "+v"(s), "+v"(m)); return cf{fmul_(m, c), fmul_(m, s)}; }
DEV u32 hmul(u32 a, u32 m) { u32 t, r;
  asm("v_pk_mul_f16 %0, %1, %2 op_sel_hi:[0,1]" : "=v"(t) : "v"(a), "v"(m));
  asm("v_pk_fma_f16 %0, %1, %2, %3 op_sel:[1,1,0] op_sel_hi:[1,0,1] neg_lo:[0,1,0]" : "=v"(r) : "v"(a), "v"(m), "v"(t)); return r; }
DEV u32 hmulc(u32 a, u32 m) { u32 t, r;
  asm("v_pk_mul_f16 %0, %1, %2 op_sel_hi:[0,1] neg_hi:[0,1]" : "=v"(t) : "v"(a), "v"(m));
  asm("v_pk_fma_f16 %0, %1, %2, %3 op_sel:[1,1,0] op_sel_hi:[1,0,1]" : "=v"(r) : "v"(a), "v"(m), "v"(t)); return r; }
DEV f2 pfma_x(f2 s, f2 b, f2 c) { f2 r; asm("v_pk_fma_f32 %0, %1, %2, %3 op_sel_hi:[0,1,1]" : "=v"(r) : "v"(s), "v"(b), "v"(c)); return r; }
DEV f2 pmul_y(f2 s, f2 b) { f2 r; asm("v_pk_mul_f32 %0, %1, %2 op_sel:[1,0]" : "=v"(r) : "v"(s), "v"(b)); return r; }
DEV f2 pfma_xx(f2 s, f2 b) { f2 r; asm("s_nop 0\n\tv_pk_fma_f32 %0, %1, %2, %1 op_sel_hi:[0,1,0]" : "=v"(r) : "v"(s), "v"(b)); return r; }
DEV f2 pmul(f2 a, f2 b) { f2 r; asm("s_nop 0\n\tv_pk_mul_f32 %0, %1, %2" : "=v"(r) : "v"(a), "v"(b)); return r; }
DEV void swap16(float& a, float& b) { auto r = __builtin_amdgcn_permlane16_swap(__float_as_uint(a), __float_as_uint(b), false, false); a = __uint_as_float(r[0]); b = __uint_as_float(r[1]); }
DEV void swap32(float& a, float& b) { auto r = __builtin_amdgcn_permlane32_swap(__float_as_uint(a), __float_as_uint(b), false, false); a = __uint_as_float(r[0]); b = __uint_as_float(r[1]); }

DEV void ln_wave(const Params& p, u32 sb, int pn, int lane, int wv) {
  const rsrc_t rx = mkr(p.x, L * D * 2), rw = mkr(p.ws, WS_BYTES);
  const int h = lane & 1, m = lane >> 1, row0 = 64 * (pn >> 2) + (pn & 3);
  __builtin_amdgcn_s_setprio(3);
  v2u xv[8][5];
#pragma unroll
  for (int e = 0; e < 8; e++)
#pragma unroll
    for (int i = 0; i < 5; i++) xv[e][i] = __builtin_amdgcn_raw_buffer_load_b64(rx, (row0 + 32 * h) * 1280 + m * 8 + i * 256, e * 5120, 0);
  float s[16];
#pragma unroll
  for (int e = 0; e < 8; e++) {
    float a = 0.f, q = 0.f;
#pragma unroll
    for (int i = 0; i < 10; i++) {
      const u32 w = xv[e][i >> 1][i & 1];
      const bf2 v = __builtin_bit_cast(bf2, w);
      a = __builtin_amdgcn_fdot2_f32_bf16(v, __builtin_bit_cast(bf2, 0x3f803f80u), a, false);
      q = __builtin_amdgcn_fdot2_f32_bf16(v, v, q, false);
    }
    s[e] = a; s[8 + e] = q;
  }
#pragma unroll
  for (int e = 0; e < 16; e++) asm volatile("" : "+v"(s[e]));
  asm volatile("s_nop 2");
  BARRIER();
  const u32 tag = rfl(ldsv(sb + LD_CTL + C_TAG));
  float z[4];
#pragma unroll
  for (int k = 0; k < 4; k++) {
    swap16(s[4 * k], s[4 * k + 1]); swap16(s[4 * k + 2], s[4 * k + 3]);
    float a = fadd_(s[4 * k], s[4 * k + 1]), b = fadd_(s[4 * k + 2], s[4 * k + 3]);
    asm volatile("s_nop 1" : "+v"(a), "+v"(b));
    swap32(a, b); z[k] = fadd_(a, b);
  }
#define DPPADD(C) asm volatile("s_nop 1\n\tv_add_f32_dpp %0, %0, %0 " C " row_mask:0xf bank_mask:0xf\n\tv_add_f32_dpp %1, %1, %1 " C " row_mask:0xf bank_mask:0xf\n\t" \
    "v_add_f32_dpp %2, %2, %2 " C " row_mask:0xf bank_mask:0xf\n\tv_add_f32_dpp %3, %3, %3 " C " row_mask:0xf bank_mask:0xf" : "+v"(z[0]), "+v"(z[1]), "+v"(z[2]), "+v"(z[3]))
  DPPADD("quad_perm:[2,3,0,1]"); DPPADD("row_ror:4"); DPPADD("row_ror:8");
  float rs[8], nb[8];
#pragma unroll
  for (int k = 0; k < 2; k++) {
    const float mean = fmul_(z[k], 1.f / D), var = fmaxf(fnma_(mean, mean, fmul_(z[2 + k], 1.f / D)), 0.f);
    const float mb = rbf(mean), sd = rbf(__builtin_amdgcn_sqrtf(rbf(rbf(var) + 1e-5f)));
    float r = __builtin_amdgcn_rcpf(sd);
    asm volatile("s_nop 0" : "+v"(r));
    r = ffma_(fnma_(sd, r, 1.f), r, r);
    if ((lane & 15) < 2) stc128(v4u{__float_as_uint(mb), tag, __float_as_uint(r), tag}, rw, WS_STG + (row0 + 32 * h + 4 * (4 * k + (lane >> 4))) * 16, 0);
    float t[2] = {r, fmul_(-mb, r)};
#pragma unroll
    for (int w = 0; w < 2; w++) {
      float a = t[w], c = t[w];
      asm volatile("s_nop 1" : "+v"(a), "+v"(c));
      swap32(a, c); float b = a, d = c; swap16(a, b); swap16(c, d);
      float* o = w ? nb : rs; o[4 * k] = a; o[4 * k + 1] = b; o[4 * k + 2] = c; o[4 * k + 3] = d;
    }
  }
#pragma unroll
  for (int i = 0; i < 5; i++)
#pragma unroll
    for (int c = 0; c < 4; c++) {
      float y[8];
#pragma unroll
      for (int e = 0; e < 8; e++) { const u32 w = xv[e][i][c >> 1]; y[e] = ffma_((c & 1) ? bfhi(w) : bf2f(w), rs[e], nb[e]); }
      stc128(v4u{pkhf(y[0], y[1]), pkhf(y[2], y[3]), pkhf(y[4], y[5]), pkhf(y[6], y[7])}, rw, WS_XT + lane * 16, pn * 20480 + (i * 4 + c) * 1024);
    }
  waitv<0>();
  if (lane == 0) stc32(tag, rw, WS_LFLAG + pn * 4);
  STAMP(21);
}

template <bool CJ> DEV void grp8(u32* v, u32 a, u32 b, u32 c) {
#define HM(x, y) (CJ ? hmulc(x, y) : hmul(x, y))
  v[1] = HM(v[0], a); v[2] = HM(v[0], b); v[3] = HM(v[1], b); v[4] = HM(v[0], c); v[5] = HM(v[1], c); v[6] = HM(v[2], c); v[7] = HM(v[3], c);
#undef HM
}
template <int N, int O> DEV void wr_rows(const u32* v) {
  if constexpr (N > 0) { asm volatile("ds_write_addtid_b32 %0 offset:%1" ::"v"(v[0]), "i"(O * 272) : "memory"); wr_rows<N - 1, O + 1>(v + 1); }
}
DEV void conv_wave(const Params& p, u32 sb, int slot, int ch, int lane, int wv, bool g1, int ts, int cj) {
  const u32 KB = sb + LD_K + slot * K_BYTES, UB = sb + LD_U + slot * U_BYTES, CTL = sb + LD_CTL;
  const int g = lane >> 4, q = (lane >> 2) & 3, pp = lane & 3, j = lane & 15, b = blockIdx.x;
  const rsrc_t rw = mkr(p.ws, WS_BYTES);
  const v4u rz = mkrs(p.ws, 0);
  u32 gen = 0;
  if (slot == 0) gen = ldc32(rw, WS_GEN);
  const int wu = slot == 0 ? b : (slot == 1 && b < 128 ? 256 + b : -1);
  if (wu >= 0) {
    u32 vo[5];
#pragma unroll
    for (int k = 0; k < 5; k++) { const int n = k * 64 + lane; vo[k] = (n / 40) * 1280 + (n % 40) * 16 - (k & 3) * 1024; }
    const v4u rd = mkrs(p.wd, F * D * 2);
    const u32 so = (wu >> 1) * 10240 + (wu & 1) * 640;
    dma4(rd, vo[0], vo[1], vo[2], vo[3], so, KB); dma1(rd, vo[4], so, KB + 4096);
  }
  u32 la[3], ai[3], cr[3], ci[3];
#pragma unroll
  for (int c = 0; c < 3; c++) { const int o = ch * NS + 64 * c + lane; la[c] = p.lar[o]; ai[c] = p.aim[o]; cr[c] = p.cre[o]; ci[c] = p.cim[o]; }
  const u32 ldt = p.ldt[ch];
  if (slot == 0) {
    const int nch = ((b >> 3) & 1) ? 2 : 3;
    const bool one = (g1 && (lane < 10 || lane == 14)) || (lane >= 11 && lane <= 13) || (lane >= 16 && lane < 16 + nch) || (lane >= 20 && lane < 24);
    *(volatile LAS u32*)(CTL + lane * 4) = lane == 15 ? gen * 0x9E3779B1u + 0x7F4A7C15u : (one ? 1u : 0u);
  }
  if (slot == 1) *(volatile LAS u32*)(CTL + C_DONE + lane * 4) = g1 ? 1u : 0u;
  BARRIER();
  const u32 tag = rfl(ldsv(CTL + C_TAG));
  if (wu >= 0) {
    waitv<0>();
    __builtin_amdgcn_s_setprio(3);
    const u32 ra = KB + q * 640 + (80 * g + 20 * pp) * 2;
    const u32 wo = WS_WDF + ((4 * (wu & 1) + g) * 48 * 5) * 1024 + j * 16, so = (wu >> 3) * 5120 + ((wu >> 1) & 3) * 256;
#pragma unroll
    for (int k = 0; k < 5; k++) stc128(trd2(ra + k * 8, ra + k * 8 + 2560), rw, wo + k * 1024, so);
  }
  const float dt = rbf(__expf(bf2f(ldt)));
  u32 mx = 0;
#pragma unroll
  for (int c = 0; c < 3; c++) mx = max(mx, max(cr[c] & 0x7fffu, ci[c] & 0x7fffu));
  mx = max(mx, (u32)__builtin_amdgcn_update_dpp(0, (int)mx, 0x111, 0xf, 0xf, false));
  mx = max(mx, (u32)__builtin_amdgcn_update_dpp(0, (int)mx, 0x112, 0xf, 0xf, false));
  mx = max(mx, (u32)__builtin_amdgcn_update_dpp(0, (int)mx, 0x114, 0xf, 0xf, false));
  mx = max(mx, (u32)__builtin_amdgcn_update_dpp(0, (int)mx, 0x118, 0xf, 0xf, false));
  mx = max(max((u32)__builtin_amdgcn_readlane(mx, 15), (u32)__builtin_amdgcn_readlane(mx, 31)), max((u32)__builtin_amdgcn_readlane(mx, 47), (u32)__builtin_amdgcn_readlane(mx, 63)));
  STAMP(16);
  const u32 ex = min(max(mx >> 7, 1u), 252u);
  const float cscale = __uint_as_float((253u - ex) << 23);
  const float oscale = 2.f * dt * __uint_as_float((ex + 1u) << 23);
  u32 G[3][57];
#pragma unroll
  for (int c = 0; c < 3; c++) {
    const float zr = fmul_(fmul_(rbf(__expf(bf2f(la[c]))), dt), -1.44269504089f), zi = fmul_(fmul_(bf2f(ai[c]), dt), 0.15915494309f);
    const cf P0 = cexp2(zr, zi), P4 = cexp2(fmul_(zr, 16.f), fmul_(zi, 16.f)), P8 = cexp2(fmul_(zr, 256.f), fmul_(zi, 256.f));
    const cf P1 = csq(P0), P2 = csq(P1), P3 = csq(P2), P5 = csq(P4), P6 = csq(P5), P7 = csq(P6), P9 = csq(P8), P10 = csq(P9), P11 = csq(P10);
    const u32 M0 = pkc(P0), M1 = pkc(P1), M2 = pkc(P2), M3 = pkc(P3), M4 = pkc(P4), M5 = pkc(P5), M7 = pkc(P7), M8 = pkc(P8), M9 = pkc(P9);
    u32* v = G[c];
    const float cx = fmul_(bf2f(cr[c]), cscale), cy = fmul_(bf2f(ci[c]), -cscale);
    v[0] = pkhf(cx, cy); grp8<true>(v, M3, M4, M5);
    v[8] = pkhf(ffma_(cy, P6.y, fmul_(cx, P6.x)), fnma_(cx, P6.y, fmul_(cy, P6.x))); grp8<true>(v + 8, M3, M4, M5);
    v[16] = 0x00003c00u; v[17] = M0; v[18] = M1; v[19] = hmul(M0, M1); v[20] = M2; v[21] = hmul(M0, M2); v[22] = hmul(M1, M2); v[23] = hmul(v[19], M2);
    v[24] = M7; grp8<false>(v + 24, M0, M1, M2);
    v[32] = M8; grp8<false>(v + 32, M1, M2, M7);
    v[40] = M9; grp8<false>(v + 40, M2, M7, M8);
    v[48] = pkc(P10); grp8<false>(v + 48, M7, M8, M9);
    v[56] = pkc(P11);
#pragma unroll
    for (int k = 0; k < 57; k++) asm volatile("" : "+v"(v[k]));
    if (c == 0 && wu >= 0) { waitv<0>(); if (lane == 0) stc32(tag, rw, WS_WFLAG + wu * 4); __builtin_amdgcn_s_setprio(0); }
  }
  STAMP(13);
  float cs0 = 0.f, cs1 = 0.f; f2 stv[4];
  if (g1) spin0(CTL + C_FULL + POLL_STAGE * 4, 1); else { __builtin_amdgcn_s_sleep(100); }
  __builtin_amdgcn_s_setprio(3);
  if (slot == 0) {
#pragma unroll 1
    for (u32 it = 0; it < SPIN_CAP; it++) {
      const v4u a = ldc128(rw, WS_LFLAG + lane * 16, 0), c = ldc128(rw, WS_LFLAG + lane * 16, 1024);
      if (__all(a.x == tag && a.y == tag && a.z == tag && a.w == tag && c.x == tag && c.y == tag && c.z == tag && c.w == tag)) break;
      __builtin_amdgcn_s_sleep(24);
    }
    ORDER();
    sig0(rz, CTL + C_RDY);
    if (b == 0 && lane == 0) stc32(tag, rw, WS_GEN);
  } else if (slot == 1 && g1) {
#pragma unroll 1
    for (u32 it = 0; it < SPIN_CAP; it++) {
      v4u c[10]; bool ok = true; cs0 = cs1 = 0.f;
#pragma unroll
      for (int k = 0; k < 10; k++) c[k] = ldc128(rw, WS_CSP + lane * 16, (cj * 10 + k) * 1024);
#pragma unroll
      for (int k = 0; k < 10; k++) { ok = ok && c[k].y == tag && c[k].w == tag; cs0 = fadd_(cs0, __uint_as_float(c[k].x)); cs1 = fadd_(cs1, __uint_as_float(c[k].z)); }
      if (__all(ok)) break;
      __builtin_amdgcn_s_sleep(24);
    }
#pragma unroll 1
    for (u32 it = 0; it < SPIN_CAP; it++) {
      bool ok = true;
#pragma unroll
      for (int k = 0; k < 4; k++) { const int n = k * 64 + lane; const v4u c = ldc128(rw, WS_STG + (ts * 16 + (n < 208 ? n : 0)) * 16, 0);
        ok = ok && c.y == tag && c.w == tag; stv[k] = f2{__uint_as_float(c.x), __uint_as_float(c.z)}; }
      if (__all(ok)) break;
      __builtin_amdgcn_s_sleep(24);
    }
  }
  STAMP(22);
  __builtin_amdgcn_s_setprio(0);
  spin0(CTL + C_FULL + 36, 1);
  STAMP(17);
  if (slot == 1 && g1) {
#pragma unroll
    for (int k = 0; k < 4; k++) { const int n = k * 64 + lane; if (n < 208) *(LAS f2*)(sb + LD_ST + n * 8) = stv[k]; }
    *(LAS f2*)(sb + LD_CS + lane * 8) = f2{cs0, cs1};
    CBAR();
    ldsw(CTL + C_STF, 0);
  }
  spin0(CTL + C_RDY, 1);
  {
    const v4u rws = mkrs(p.ws, WS_BYTES);
    const u32 co = WS_XT + ((ch >> 7) * 4 + (ch & 3)) * 1024 + ((ch & 127) >> 2) * 32;
    u32 vo[5];
#pragma unroll
    for (int k = 0; k < 5; k++) { const int n = k * 64 + lane, tau = n / 72, a = n % 72 - 8; vo[k] = a >= 0 ? co + ((a >> 1) * 4 + tau) * 20480 + (a & 1) * 16 - (k & 3) * 1024 : 0x80000000u; }
    dma4c(rws, vo[0], vo[1], vo[2], vo[3], 0, UB);
    asm volatile("s_mov_b32 m0, %0\n\ts_mov_b32 exec_hi, 0\n\tbuffer_load_dwordx4 %1, %2, 0 offen sc1 lds\n\ts_mov_b32 exec_hi, -1" ::"s"(UB + 4096), "v"(vo[4]), "s"(rws) : "memory");
  }
  f4 kd[3] = {f4{0, 0, 0, 0}, f4{0, 0, 0, 0}, f4{0, 0, 0, 0}};
  {
    const u32 rb = KB + j * 272 + g * 16;
    asm volatile("s_mov_b32 m0, %3\n\ts_nop 0" : "+v"(kd[0]), "+v"(kd[1]), "+v"(kd[2]) : "s"(KB) : "memory");
    v4u Fr[12][4];
#pragma unroll
    for (int ps = 0; ps <= 12; ps++) {
      if (ps < 12) {
        const int c = ps >> 2, t = ps & 3;
        if (t < 3) wr_rows<16, 0>(G[c] + 16 * t); else wr_rows<9, 0>(G[c] + 48);
        asm volatile("ds_read_b128 %0, %4\n\tds_read_b128 %1, %4 offset:64\n\tds_read_b128 %2, %4 offset:128\n\tds_read_b128 %3, %4 offset:192"
                     : "=&v"(Fr[ps][0]), "=&v"(Fr[ps][1]), "=&v"(Fr[ps][2]), "=&v"(Fr[ps][3]) : "v"(rb) : "memory");
      }
      if (ps > 0) {
        const int pv = ps - 1;
        if (ps < 12) asm volatile("s_waitcnt lgkmcnt(13)" : "+v"(Fr[pv][0]), "+v"(Fr[pv][1]), "+v"(Fr[pv][2]), "+v"(Fr[pv][3]));
        else asm volatile("s_waitcnt lgkmcnt(0)" : "+v"(Fr[pv][0]), "+v"(Fr[pv][1]), "+v"(Fr[pv][2]), "+v"(Fr[pv][3]));
        if (pv & 3) {
#pragma unroll
          for (int ks = 0; ks < 4; ks++) mma_hf(kd[(pv & 3) - 1], Fr[pv][ks], Fr[pv & ~3][ks]);
        }
      }
    }
    asm volatile("s_nop 7" : "+v"(kd[0]), "+v"(kd[1]), "+v"(kd[2]));
  }
  STAMP(9);
  {
    const u32 K0 = KB + 32;
    const u32 a00 = pkhf(kd[1][0], kd[1][0]), a11 = pkhf(kd[1][1], kd[1][1]), a22 = pkhf(kd[1][2], kd[1][2]), a33 = pkhf(kd[1][3], kd[1][3]);
    const u32 a12 = pkhf(kd[1][1], kd[1][2]), a01 = pkhf(kd[1][0], kd[1][1]), a23 = pkhf(kd[1][2], kd[1][3]);
    v2u bb[4];
#pragma unroll
    for (int r = 0; r < 4; r++) { bb[r].x = pkhf(kd[2][r], kd[2][r]); bb[r].y = bb[r].x; }
    const v2u r0 = {pkhf(kd[0][0], kd[0][1]), pkhf(kd[0][2], kd[0][3])}, w1 = {a00, a12}, w2a = {a00, a01}, w2b = {a22, a23};
    const v4u z4 = {0, 0, 0, 0};
    const u32 p3 = K0 + 2 * (1024 + 512 * g + 8 * j) - 8, p2 = K0 + 2 * (256 * g + 8 * j) - 4, p1 = K0 + 2 * (256 + 128 * g + 8 * j) - 2;
    const u32 p0 = K0 + 2 * (128 * (g >> 1) + 8 * j + 4 * (g & 1)), pz = KB + lane * 16;
    asm volatile(
      "s_mov_b32 exec_hi, 1\n\t"
      "ds_write2_b64 %0, %5, %5 offset1:1\n\t"
      "s_mov_b32 exec_hi, 0\n\t"
      "ds_write2_b64 %0, %6, %6 offset0:32 offset1:33\n\t"
      "ds_write2_b64 %0, %7, %7 offset0:64 offset1:65\n\t"
      "ds_write2_b64 %0, %8, %8 offset0:96 offset1:97\n\t"
      "s_mov_b32 exec_lo, 0x55555555\n\t"
      "ds_write_b16 %0, %9 offset:16\n\tds_write_b16 %0, %10 offset:272\n\tds_write_b16 %0, %11 offset:528\n\tds_write_b16 %0, %12 offset:784\n\t"
      "s_mov_b32 exec_lo, 0\n\ts_mov_b32 exec_hi, -1\n\t"
      "ds_write_b32 %1, %13\n\tds_write_b64 %1, %17 offset:4\n\tds_write_b32 %1, %14 offset:12\n\t"
      "ds_write_b32 %1, %15 offset:256\n\tds_write_b64 %1, %18 offset:260\n\tds_write_b32 %1, %16 offset:268\n\t"
      "s_mov_b32 exec_lo, -1\n\ts_mov_b32 exec_hi, 0\n\t"
      "ds_write_b16 %2, %13\n\tds_write_b64 %2, %19 offset:2\n\tds_write_b32 %2, %15 offset:10\n\tds_write_b16 %2, %16 offset:14\n\t"
      "s_mov_b32 exec_lo, 3\n\t"
      "ds_write_b128 %4, %21\n\t"
      "s_mov_b64 exec, -1\n\t"
      "ds_write_b64 %3, %20"
      ::"v"(p3), "v"(p2), "v"(p1), "v"(p0), "v"(pz), "v"(bb[0]), "v"(bb[1]), "v"(bb[2]), "v"(bb[3]), "v"(bb[0].x), "v"(bb[1].x), "v"(bb[2].x), "v"(bb[3].x),
        "v"(a00), "v"(a11), "v"(a22), "v"(a33), "v"(w2a), "v"(w2b), "v"(w1), "v"(r0), "v"(z4) : "memory");
  }
  v4u A[16];
  {
    const u32 ab = KB + 32 + 2 * (-12 + 4 * (8 * g + 7 - q) + 4 * pp);
#pragma unroll
    for (int s = 0; s < 16; s++) A[s] = trd2(ab + 256 * s, ab + 256 * s - 32);
  }
  ldsw(CTL + C_KDONE + slot * 4, 0);
  f4 acc[4][8];
#pragma unroll
  for (int t = 0; t < 4; t++)
#pragma unroll
    for (int T = 0; T < 8; T++) { acc[t][T] = f4{0, 0, 0, 0}; asm volatile("" : "+v"(acc[t][T])); }
  STAMP(10);
  waitv<0>();
  spin0(CTL + C_DONE + 160 + slot * 4, 1);
  __builtin_amdgcn_s_setprio(CONV_PRIO);
  STAMP(18);
  const u32 ub = UB + 128 + 2 * (4 * j - 4 - 8 * g) - 64;
  v4u B[4];
#define QORD(n) (((n) & 1) ? 15 - (((n) & 15) >> 1) : (((n) & 15) >> 1))
#define LDB(n) do { const u32 a_ = ub + ((n) >> 4) * 1152 + 64 * QORD(n); const v2u x_ = lds64(a_); SCHED(); const v2u y_ = lds64(a_ + 8); B[(n) & 3] = v4u{x_.x, x_.y, y_.x, y_.y}; SCHED(); } while (0)
  LDB(0); LDB(1); LDB(2);
#pragma unroll
  for (int n = 0; n < 64; n++) {
    const int q2 = QORD(n) - 1;
    if (n + 3 < 64) LDB(n + 3);
#pragma unroll
    for (int T = 7; T >= 0; T--) { const int s = 2 * T - q2; if (s >= 0 && s <= 2 * T + 1) mma_hf(acc[n >> 4][T], A[s], B[n & 3]); }
    SCHED();
  }
#pragma unroll
  for (int T = 0; T < 8; T++) asm volatile("s_nop 0" : "+v"(acc[3][T]));
  STAMP(19);
  const int srcl = (lane == 0 ? 63 : (g > 0 ? lane - 16 : lane + 47)) * 4;
  v2u zp[8]; float pc[3] = {0.f, 0.f, 0.f};
#pragma unroll
  for (int T = 0; T < 8; T++) {
    const float c[3] = {fadd_(fadd_(acc[1][T][3], acc[2][T][2]), acc[3][T][1]), fadd_(acc[2][T][3], acc[3][T][2]), acc[3][T][3]};
    float y[4] = {acc[0][T][0], fadd_(acc[0][T][1], acc[1][T][0]), fadd_(fadd_(acc[0][T][2], acc[1][T][1]), acc[2][T][0]),
                  fadd_(fadd_(acc[0][T][3], acc[1][T][2]), fadd_(acc[2][T][1], acc[3][T][0]))};
#pragma unroll
    for (int r = 0; r < 3; r++) {
      const float cv = lane == 63 ? pc[r] : c[r];
      y[r] = fadd_(y[r], __int_as_float(__builtin_amdgcn_ds_bpermute(srcl, __float_as_int(cv))));
      pc[r] = c[r];
    }
    zp[T] = v2u{pkbf(fmul_(y[0], oscale), fmul_(y[1], oscale)), pkbf(fmul_(y[2], oscale), fmul_(y[3], oscale))};
    if (T & 1) {
      auto sx = __builtin_amdgcn_permlane16_swap(zp[T - 1].x, zp[T].x, false, false);
      auto sy = __builtin_amdgcn_permlane16_swap(zp[T - 1].y, zp[T].y, false, false);
      stc128(v4u{sx[0], sy[0], sx[1], sy[1]}, rw, WS_ZT + (g & 1) * 512 + j * 32 + (g >> 1) * 16, ch * 4096 + (T - 1) * 512);
    }
  }
  STAMP(20);
  waitv<0>();
  if (lane == 0) stc32(tag, rw, WS_ZFLAG + ch * 4);
  STAMP(11);
}

__global__ __launch_bounds__(512, 2) void k_main(Params p) {
  extern __shared__ __attribute__((aligned(16))) char smem[];
  const u32 sb = (u32)(size_t)(LAS char*)smem;
  const int lane = threadIdx.x & 63, wv = rfl(threadIdx.x >> 6);
  const int b = blockIdx.x, xc = b & 7, r = b >> 3;
  const bool g1 = r < 30;
  const int r5 = r / 6, ri = (xc >> 2) * 5 + r5, cj = (xc & 3) * 6 + r % 6, ts = (ri * 64) / 5;
  const int i2 = xc * 4 + (r >> 3), j2 = r & 7;
  const u32 CTL = sb + LD_CTL;
  asm volatile(".subsection 2\n\t.fill 128, 4, 0xBF800000\n\t.subsection 0");
  STAMP(0);
#define CSTAMP(k)
  if (wv >= 4 && wv < 7) {
    const int slot = wv - 4;
    if (slot == 2 && (r & 1)) ln_wave(p, sb, (r >> 1) * 8 + xc, lane, wv);
    else conv_wave(p, sb, slot, 80 * xc + 5 * (r >> 1) + (r & 1) * 3 + slot, lane, wv, g1, ts, cj);
    return;
  }
  const int g = lane >> 4, i = lane & 15;
  const rsrc_t rw = mkr(p.ws, WS_BYTES);
  const v4u rz = mkrs(p.ws, 0);
  const int rot = (xc >> 2) * 5;
  const v4u rg = mkrs(p.wg, D * F * 2), ru = mkrs(p.wu, D * F * 2), rxs = mkrs(p.x, L * D * 2);
  u32 v[4], vx[4];
#pragma unroll
  for (int k = 0; k < 4; k++) {
    const int row = 8 * k + (lane >> 3), fp = ((row >> 1) & 1) | (((row >> 3) & 1) << 1);
    v[k] = row * 3072 + cj * 128 + (((lane & 7) ^ (fp << 1)) << 4) - 1024 * k;
    vx[k] = (ts * 16 + row) * 1280 + (((lane & 7) ^ ((row >> 1) & 7)) << 4) - 1024 * k;
  }
  if (wv < 4) {
    __builtin_amdgcn_s_setprio(2);
    if (g1) {
      dma4((wv & 1) ? ru : rg, v[0], v[1], v[2], v[3], (rot * 64 + (wv >> 1) * 32) * 3072, sb + LD_R + (wv & 1) * 8192 + (wv >> 1) * 4096);
      dma4(rxs, vx[0], vx[1], vx[2], vx[3], rot * 128 + 3 * wv * 20480, sb + LD_X + 3 * wv * 2048);
      if (wv < 3) dma2(rxs, vx[0], vx[1], rot * 128 + (3 * wv + 2) * 20480, sb + LD_X + (3 * wv + 2) * 2048);
      else dma4(rxs, vx[0], vx[1], vx[2], vx[3], rot * 128 + 11 * 20480, sb + LD_X + 11 * 2048);
    }
    const rsrc_t rx = mkr(p.x, L * D * 2);
    constexpr int NWR = 5;
    v4u Wr[NWR][5];
    const u32 wdo = WS_WDF + (j2 * 48 + 12 * wv) * 5120;
    u32 tag;
    if (g1) {
      f4 acc[3][8], a13[2];
#pragma unroll
      for (int t = 0; t < 3; t++)
#pragma unroll
        for (int c = 0; c < 8; c++) { acc[t][c] = f4{0, 0, 0, 0}; asm volatile("" : "+v"(acc[t][c])); }
      a13[0] = a13[1] = f4{0, 0, 0, 0};
      const int q = (lane >> 2) & 3, pp = lane & 3, fp = ((q >> 1) & 1) | ((g & 1) << 1);
      u32 wb[4];
#pragma unroll
      for (int c = 0; c < 4; c++) wb[c] = sb + LD_R + (8 * g + q) * 128 + ((c ^ wv ^ fp) * 32) + pp * 8;
      u32 xb0 = sb + LD_X + wv * 6144 + i * 128 + ((g ^ (i >> 1)) << 4), xb1 = xb0 ^ 64;
      u32 yb0 = xb0 + (12 - 3 * wv) * 2048, yb1 = yb0 ^ 64, fa = CTL + C_FULL + 4;
      v4u Wf[2][8], Xf[2][4];
      asm volatile("" : "+v"(a13[0]), "+v"(a13[1]), "+v"(wb[0]), "+v"(wb[1]), "+v"(wb[2]), "+v"(wb[3]), "+v"(xb0), "+v"(xb1), "+v"(yb0), "+v"(yb1), "+v"(fa));
#define RDP(B, n, XA, YA, OFF) do { if ((n) == 0) { Xf[B][0] = lds128(XA); Xf[B][1] = lds128(XA + 2048); } \
        else if ((n) == 2) { Xf[B][2] = lds128(XA + 4096); Xf[B][3] = lds128(YA); } \
        else { constexpr int c = (n) == 1 ? 0 : (n) - 2; const u32 a = wb[c & 3] + (OFF) + (c >> 2) * 8192; Wf[B][c] = trd2(a, a + 512); } SCHED(); } while (0)
#define MM(KS, m) do { constexpr int c = (m) < 4 ? 0 : (m) < 13 ? 1 + ((m) - 4) / 3 : (m) < 17 ? 4 : 5 + ((m) - 17) / 3; \
        constexpr int t = (m) < 4 ? (m) : (m) < 13 ? ((m) - 4) % 3 : (m) < 17 ? (m) - 13 : ((m) - 17) % 3; \
        if (t == 3) mma_bf(a13[c >> 2], Wf[KS][c], Xf[KS][3]); else mma_bf(acc[t < 3 ? t : 0][c], Wf[KS][c], Xf[KS][t]); SCHED(); } while (0)
#define R0(m) MM(0, m); RDP(1, m, xb1, yb1, 4096);
#define R1(m) MM(1, m); RDP(0, (m) - 12, xb0, yb0, 0);
      waitv<0>();
      BARRIER();
      STAMP(1);
      RDP(0, 0, xb0, yb0, 0); RDP(0, 1, xb0, yb0, 0); RDP(0, 2, xb0, yb0, 0); RDP(0, 3, xb0, yb0, 0); RDP(0, 4, xb0, yb0, 0);
      RDP(0, 5, xb0, yb0, 0); RDP(0, 6, xb0, yb0, 0); RDP(0, 7, xb0, yb0, 0); RDP(0, 8, xb0, yb0, 0); RDP(0, 9, xb0, yb0, 0);
      u32 sw = 0, sx = 0;
#pragma unroll 1
      for (int s = 0; s < 10; s++) {
        u32 fw, fo;
        R0(0) R0(1) R0(2) R0(3) R0(4) R0(5) R0(6) R0(7) R0(8) R0(9)
        sx += XSLOT; u32 dx = XSLOT; if (sx == NXS * XSLOT) { sx = 0; dx = XSLOT - NXS * XSLOT; }
        sw += WSLOT; u32 dl = WSLOT; if (sw == NWS * WSLOT) { sw = 0; dl = WSLOT - NWS * WSLOT; }
        MM(0, 10); xb0 += dx; xb1 += dx; asm volatile("" : "+v"(xb0), "+v"(xb1)); SCHED();
        MM(0, 11); yb0 += dx; yb1 += dx; asm volatile("" : "+v"(yb0), "+v"(yb1)); SCHED();
        MM(0, 12); wb[0] += dl; wb[1] += dl; asm volatile("" : "+v"(wb[0]), "+v"(wb[1])); SCHED();
        MM(0, 13); wb[2] += dl; wb[3] += dl; asm volatile("" : "+v"(wb[2]), "+v"(wb[3])); SCHED();
        MM(0, 14); MM(0, 15);
        asm volatile("s_waitcnt lgkmcnt(0)" ::: "memory");
        sig0(rz, CTL + C_DONE + s * 16 + wv * 4); SCHED();
        if (s == r5) {
          const u32 tg = ldsv(CTL + C_TAG);
          f4 c0 = {0, 0, 0, 0}, c1 = {0, 0, 0, 0}; v4u on = {0x3f803f80u, 0x3f803f80u, 0x3f803f80u, 0x3f803f80u};
          asm volatile("s_nop 1" : "+v"(c0), "+v"(c1), "+v"(on));
          mma_bf(c0, Wf[0][0], on); mma_bf(c1, Wf[0][4], on); mma_bf(c0, Wf[1][0], on); mma_bf(c1, Wf[1][4], on);
          asm volatile("s_nop 7" : "+v"(c0), "+v"(c1));
          const f4 cc = (i & 1) ? c1 : c0;
          if (i < 2) {
            const u32 o = WS_CSP + (i * 64 + 16 * wv + 4 * g) * 8, so = (cj * 10 + ri) * 1024;
            stc128(v4u{__float_as_uint(cc[0]), tg, __float_as_uint(cc[1]), tg}, rw, o, so); stc128(v4u{__float_as_uint(cc[2]), tg, __float_as_uint(cc[3]), tg}, rw, o + 16, so);
          }
          asm volatile("s_nop 3"); SCHED();
        }
        MM(0, 16); MM(0, 17); MM(0, 18); MM(0, 19); MM(0, 20); MM(0, 21); MM(0, 22); MM(0, 23); MM(0, 24); MM(0, 25);
        MM(1, 0); MM(1, 1); MM(1, 2); MM(1, 3); fw = ldsv(fa); SCHED();
        MM(1, 4); MM(1, 5); MM(1, 6); MM(1, 7); MM(1, 8); MM(1, 9); fo = rfl(fw); SCHED(); MM(1, 10); MM(1, 11);
        spin0(fa, fo);
        R1(12) R1(13) R1(14) R1(15) R1(16) R1(17) R1(18) R1(19) R1(20) R1(21)
        MM(1, 22); fa += 4; asm volatile("" : "+v"(fa)); SCHED(); MM(1, 23); MM(1, 24); MM(1, 25);
      }
      CSTAMP(14);
      { const v4u cw = *(volatile LAS v4u*)(CTL + C_RDY); tag = rfl(cw.w); spin0(CTL + C_STF, rfl(cw.z)); spin0(CTL + C_RDY, rfl(cw.x)); }
      STAMP(2);
      f4 cg[4], cu[4]; f2 st[4];
#pragma unroll
      for (int t = 0; t < 4; t++) st[t] = *(LAS f2*)(sb + LD_ST + ((t < 3 ? 3 * wv + t : 12) * 16 + i) * 8);
#pragma unroll
      for (int c = 0; c < 4; c++) { cg[c] = *(LAS f4*)(sb + LD_CS + (16 * (c ^ wv) + 4 * g) * 4); cu[c] = *(LAS f4*)(sb + LD_CS + 256 + (16 * (c ^ wv) + 4 * g) * 4); }
      const u32 hb = WS_HF + (2 * cj) * 1024 + (((((g ^ wv) & 1) << 1) | (g >> 1)) * 16 + i) * 16;
#pragma unroll
      for (int t = 0; t < 4; t++) {
        f2 k1 = {-st[t].x, -1.44269504089f * st[t].y}, k2 = {__builtin_amdgcn_rcpf(st[t].y), 0.f};
        asm volatile("s_nop 0" : "+v"(k1), "+v"(k2));
        v2u P[4];
#pragma unroll
        for (int c = 0; c < 4; c++) {
          if (t == 3 && c > 0) break;
          const f4 ag = t < 3 ? acc[t < 3 ? t : 0][c] : a13[0], au = t < 3 ? acc[t < 3 ? t : 0][4 + c] : a13[1];
#pragma unroll
          for (int e = 0; e < 2; e++) {
            const f2 a2 = e ? ag.zw : ag.xy, u2 = e ? au.zw : au.xy, cg2 = e ? cg[c].zw : cg[c].xy, cu2 = e ? cu[c].zw : cu[c].xy;
            const f2 gg = pmul_y(k1, pfma_x(k1, cg2, a2)), uu = pfma_x(k1, cu2, u2);
            const f2 den = pfma_xx(k2, f2{__builtin_amdgcn_exp2f(gg.x), __builtin_amdgcn_exp2f(gg.y)});
            const f2 h = pmul(uu, f2{__builtin_amdgcn_rcpf(den.x), __builtin_amdgcn_rcpf(den.y)});
            P[c][e] = pkbf(h.x, h.y);
            { const int n = (t * 4 + c) * 2 + e; if (n < 25) Wr[n / 5][n % 5] = ldc128(rw, lane * 16, wdo + n * 1024); }
          }
          if (c & 1) SCHED();
        }
        if (t < 3) {
#pragma unroll
          for (int k = 0; k < 2; k++) {
            auto sx2 = __builtin_amdgcn_permlane16_swap(P[2 * k].x, P[2 * k + 1].x, false, false);
            auto sy2 = __builtin_amdgcn_permlane16_swap(P[2 * k].y, P[2 * k + 1].y, false, false);
            stc128(v4u{sx2[0], sy2[0], sx2[1], sy2[1]}, rw, hb + (k ^ (wv >> 1)) * 1024, (ts + 3 * wv + t) * 49152);
          }
        } else if (r5 != 0) {
          __builtin_amdgcn_raw_buffer_store_b64(P[0], rw, WS_HF + (2 * cj + (wv >> 1)) * 1024 + ((2 * (wv & 1) + (g >> 1)) * 16 + i) * 16 + (g & 1) * 8, (ts + 12) * 49152, AUX_SC1);
        }
        SCHED();
      }
      STAMP(3);
      sig0(rz, CTL + C_DONE + 160 + wv * 4);
      waitv<0>();
      if (lane == 0) stc32(tag, rw, WS_HFLAG + ((ri * 24 + cj) * 4 + wv) * 4);
      STAMP(4);
    } else {
      BARRIER();
      tag = rfl(ldsv(CTL + C_TAG));
      spin0(CTL + C_RDY, 1);
#pragma unroll
      for (int n = 0; n < 25; n++) Wr[n / 5][n % 5] = ldc128(rw, lane * 16, wdo + n * 1024);
    }
    const int tt = 4 * i2 + wv, tok = 16 * tt + i, d0 = 80 * j2 + 20 * g;
    f4 o[4][5];
    {
      const rsrc_t rd = mkr(p.dsk, D * 2);
      const v4u xa = __builtin_amdgcn_raw_buffer_load_b128(rx, (tok * D + d0) * 2, 0, 0), xb = __builtin_amdgcn_raw_buffer_load_b128(rx, (tok * D + d0) * 2 + 16, 0, 0);
      const v2u xc2 = __builtin_amdgcn_raw_buffer_load_b64(rx, (tok * D + d0) * 2 + 32, 0, 0);
      const v4u da = __builtin_amdgcn_raw_buffer_load_b128(rd, d0 * 2, 0, 0), db = __builtin_amdgcn_raw_buffer_load_b128(rd, d0 * 2 + 16, 0, 0);
      const v2u dc = __builtin_amdgcn_raw_buffer_load_b64(rd, d0 * 2 + 32, 0, 0);
      const v4u stv = ldc128(rw, WS_STG + tok * 16, 0);
      const float rs = __uint_as_float(stv.z), nb = fmul_(-__uint_as_float(stv.x), rs);
      const u32 xw[10] = {xa.x, xa.y, xa.z, xa.w, xb.x, xb.y, xb.z, xb.w, xc2.x, xc2.y}, dw[10] = {da.x, da.y, da.z, da.w, db.x, db.y, db.z, db.w, dc.x, dc.y};
#pragma unroll
      for (int k = 0; k < 10; k++) {
        const float x0 = bf2f(xw[k]), x1 = bfhi(xw[k]);
        o[0][k >> 1][2 * (k & 1)] = ffma_(ffma_(x0, rs, nb), bf2f(dw[k]), x0);
        o[0][k >> 1][2 * (k & 1) + 1] = ffma_(ffma_(x1, rs, nb), bfhi(dw[k]), x1);
      }
#pragma unroll
      for (int t = 0; t < 4; t++)
#pragma unroll
        for (int c = 0; c < 5; c++) { if (t > 0) o[t][c] = f4{0, 0, 0, 0}; asm volatile("" : "+v"(o[t][c])); }
    }
    { const u32 fo = WS_HFLAG + (24 * wv + lane % 24) * 4; poll_flags<6>(rw, fo + ((20 * i2 + 4) >> 6) * 384, fo + ((20 * i2 + 19) >> 6) * 384, tag); }
    STAMP(5);
    constexpr int HD = 3;
    v4u hr[12][4], Wl[2][5], Wz[5];
    u32 ho[4];
#pragma unroll
    for (int t = 0; t < 4; t++) ho[t] = WS_HF + (4 * i2 + (t ^ wv)) * 49152 + 12 * wv * 1024;
#define LOAD_H(k) do { _Pragma("unroll") for (int t = 0; t < 4; t++) hr[k][t] = ldc128(rw, lane * 16, ho[t] + (k) * 1024); } while (0)
#pragma unroll
    for (int k = 0; k < HD; k++) LOAD_H(k);
    SCHED();
    const u32 wl = sb + wd_off(wv) + lane * 16;
#pragma unroll
    for (int k = 0; k < 12; k++) {
      if (k == NWR - 1) spin0(CTL + C_WDFULL, 1);
#pragma unroll
      for (int c = 0; c < 5; c++) {
        const v4u w = k < NWR ? Wr[k < NWR ? k : 0][c] : (k < 11 ? Wl[k & 1][c] : Wz[c]);
#pragma unroll
        for (int t = 3; t >= 0; t--) {
          mma_bf(o[t][c], w, hr[k][t]); SCHED();
          if (t == 1 && k == 8) { Wz[c] = ldc128(rw, lane * 16, wdo + (55 + c) * 1024); SCHED(); }
          if (t == 2 && k + 1 >= NWR && k + 1 < 11) { Wl[(k + 1) & 1][c] = lds128(wl + ((k + 1 - NWR) * 5 + c) * 1024); SCHED(); }
          if (t == 3 && c < 4) {
            const int kn = k == 0 ? -1 : k + HD;
            if (kn > 0 && kn < 12) { hr[kn][c] = ldc128(rw, lane * 16, ho[c] + kn * 1024); SCHED(); }
          }
        }
      }
      if (k == 0) { LOAD_H(HD); SCHED(); }
    }
    asm volatile("s_nop 7"); SCHED();
    STAMP(6);
    {
#pragma unroll
      for (int t = 1; t < 4; t++)
#pragma unroll
        for (int c = 0; c < 5; c++) *(LAS f4*)(wl + ((t - 1) * 5 + c) * 1024) = o[t][c];
      ldsw(CTL + C_RED + wv * 4, 0);
      spin0(CTL + C_ZFULL, 1);
      STAMP(7);
      const int q = (lane >> 2) & 3, pp = lane & 3;
      v2u z[5];
#pragma unroll
      for (int c = 0; c < 5; c++) z[c] = trd(sb + LD_K + (20 * g + 4 * c + q) * 128 + wv * 32 + pp * 8);
#pragma unroll
      for (int c = 0; c < 5; c++) {
        o[0][c][0] = fadd_(o[0][c][0], bf2f(z[c].x)); o[0][c][1] = fadd_(o[0][c][1], bfhi(z[c].x));
        o[0][c][2] = fadd_(o[0][c][2], bf2f(z[c].y)); o[0][c][3] = fadd_(o[0][c][3], bfhi(z[c].y));
      }
      spin_zero4(CTL + C_RED);
      u32 ow[10];
#pragma unroll
      for (int c = 0; c < 5; c++) {
        f4 s[3];
#pragma unroll
        for (int t = 1; t < 4; t++) s[t - 1] = *(LAS f4*)(sb + wd_off(wv ^ t) + lane * 16 + ((t - 1) * 5 + c) * 1024);
        float y[4];
#pragma unroll
        for (int e = 0; e < 4; e++) y[e] = fadd_(fadd_(o[0][c][e], s[0][e]), fadd_(s[1][e], s[2][e]));
        ow[2 * c] = pkbf(y[0], y[1]); ow[2 * c + 1] = pkbf(y[2], y[3]);
      }
      const rsrc_t ro = mkr(p.out, L * D * 2);
      __builtin_amdgcn_raw_buffer_store_b128(v4u{ow[0], ow[1], ow[2], ow[3]}, ro, (tok * D + d0) * 2, 0, 0);
      __builtin_amdgcn_raw_buffer_store_b128(v4u{ow[4], ow[5], ow[6], ow[7]}, ro, (tok * D + d0) * 2 + 16, 0, 0);
      __builtin_amdgcn_raw_buffer_store_b64(v2u{ow[8], ow[9]}, ro, (tok * D + d0) * 2 + 32, 0, 0);
    }
    STAMP(8); CSTAMP(15);
  } else {
    if (g1) {
      int kx = rot + 1, kw = kx; u32 sw = WSLOT, sx = XSLOT;
#define ISSUE_X(K0, K1) do { _Pragma("unroll") for (int k = K0; k < K1; k++) dma4(rxs, vx[0], vx[1], vx[2], vx[3], kx * 128 + k * 40960, sb + LD_X + sx + k * 4096); } while (0)
#define ISSUE_XE() do { dma2(rxs, vx[0], vx[1], kx * 128 + 12 * 20480, sb + LD_X + sx + 12 * 2048); kx = kx == 9 ? 0 : kx + 1; sx += XSLOT; if (sx == NXS * XSLOT) sx = 0; } while (0)
#define ISSUE_W() do { const u32 o_ = kw * 64 * 3072; dma4(rg, v[0], v[1], v[2], v[3], o_, sb + LD_R + sw); dma4(ru, v[0], v[1], v[2], v[3], o_, sb + LD_R + sw + 8192); \
        dma4(rg, v[0], v[1], v[2], v[3], o_ + 32 * 3072, sb + LD_R + sw + 4096); dma4(ru, v[0], v[1], v[2], v[3], o_ + 32 * 3072, sb + LD_R + sw + 12288); \
        kw = kw == 9 ? 0 : kw + 1; sw += WSLOT; if (sw == NWS * WSLOT) sw = 0; } while (0)
      __builtin_amdgcn_s_sleep(1);
      ISSUE_W(); ISSUE_X(0, 6); ISSUE_XE(); ISSUE_W();
      BARRIER();
      __builtin_amdgcn_s_setprio(3);
#pragma unroll 1
      for (int n = 2; n < 10; n++) {
        bool pend = true;
        if (n >= 3 && !is_zero4(CTL + C_DONE + (n - 3) * 16)) { waitv<16>(); sig0(rz, CTL + C_FULL + (n - 1) * 4); pend = false; spin_zero4(CTL + C_DONE + (n - 3) * 16); }
        waitv<44>();
        ISSUE_X(0, 4);
        if (pend) { waitv<32>(); sig0(rz, CTL + C_FULL + (n - 1) * 4); }
        ISSUE_X(4, 6); ISSUE_XE();
        if (n < 9) ISSUE_W();
      }
      waitv<0>(); sig0(rz, CTL + C_FULL + 36);
      STAMP(12);
      spin_zero4(CTL + C_DONE + 9 * 16);
    } else BARRIER();
    __builtin_amdgcn_s_setprio(1);
    const u32 tag = rfl(ldsv(CTL + C_TAG));
    spin_zero4(CTL + C_KDONE);
    STAMP(23);
    const v4u rws = mkrs(p.ws, WS_BYTES);
    {
      const u32 vo = lane * 16;
#pragma unroll 1
      for (int k = 0; k < 24; k++) {
        const int w = k & 3, pc = k >> 2;
        const u32 go = WS_WDF + (j2 * 48 + 12 * w + 5 + pc) * 5120, lo = sb + wd_off(w) + pc * 5120;
        dma4c(rws, vo, vo, vo, vo, go, lo); dma1c(rws, vo, go + 4096, lo + 4096);
        waitv<WD_DEPTH>();
      }
      waitv<0>();
      sig0(rz, CTL + C_WDFULL);
    }
    STAMP(24);
    __builtin_amdgcn_s_setprio(3);
    poll_flags<8>(rw, WS_ZFLAG + j2 * 320 + lane * 4, WS_ZFLAG + j2 * 320 + 64 + lane * 4, tag);
    {
      u32 vz[4];
#pragma unroll
      for (int k = 0; k < 4; k++) vz[k] = (8 * k + (lane >> 3)) * 4096 + (lane & 7) * 16 - 1024 * k;
      const u32 go = WS_ZT + 80 * j2 * 4096 + i2 * 128;
      dma4c(rws, vz[0], vz[1], vz[2], vz[3], go, sb + LD_K); dma4c(rws, vz[0], vz[1], vz[2], vz[3], go + 32 * 4096, sb + LD_K + 4096);
      dma2c(rws, vz[0], vz[1], go + 64 * 4096, sb + LD_K + 8192);
      waitv<0>();
      sig0(rz, CTL + C_ZFULL);
      waitv<0>();
    }
    STAMP(25);
  }
}

extern "C" void kernel_launch(void* const* d_in, const int* in_sizes, int n_in, void* d_out, int out_size, void* d_ws, size_t ws_size, hipStream_t stream) {
  Params p;
  p.x = (const u16*)d_in[0]; p.lar = (const u16*)d_in[1]; p.aim = (const u16*)d_in[2]; p.cre = (const u16*)d_in[3]; p.cim = (const u16*)d_in[4];
  p.ldt = (const u16*)d_in[5]; p.dsk = (const u16*)d_in[6]; p.wg = (const u16*)d_in[8]; p.wu = (const u16*)d_in[9]; p.wd = (const u16*)d_in[10];
  p.out = (u16*)d_out; p.ws = (char*)d_ws;
  (void)hipFuncSetAttribute((const void*)k_main, hipFuncAttributeMaxDynamicSharedMemorySize, LDS_BYTES);
  k_main<<<256, 512, LDS_BYTES, stream>>>(p);
}
```

```cpp
#define DIAG 1
#include <hip/hip_runtime.h>
#pragma clang diagnostic ignored "-Wint-to-pointer-cast"
typedef unsigned short u16; typedef unsigned u32; typedef unsigned long long u64;
typedef u32 v4u __attribute__((ext_vector_type(4)));
typedef u32 v2u __attribute__((ext_vector_type(2)));
typedef float f4 __attribute__((ext_vector_type(4)));
typedef float f2 __attribute__((ext_vector_type(2)));
typedef short s4 __attribute__((ext_vector_type(4)));
typedef __bf16 bf2 __attribute__((ext_vector_type(2)));
typedef _Float16 hf2 __attribute__((ext_vector_type(2)));
#define LAS __attribute__((address_space(3)))
#define GAS __attribute__((address_space(1)))
#define DEV __device__ __forceinline__
typedef __amdgpu_buffer_rsrc_t rsrc_t;

constexpr int L = 2048, D = 640, NS = 192, F = 1536;
constexpr u32 WS_GEN = 0, WS_HFLAG = 1024, WS_ZFLAG = 5120, WS_LFLAG = 8192, WS_WFLAG = WS_LFLAG + 512, WS_STG = 16384, WS_CSP = 65536,
              WS_XT = 1u << 20, WS_WDF = 4u << 20, WS_HF = 6u << 20, WS_ZT = 13u << 20, WS_BYTES = 16u << 20;
constexpr u32 LD_K = 0, K_BYTES = 5120;
constexpr u32 LD_CS = LD_K + 3 * K_BYTES, LD_ST = LD_CS + 512, LD_CTL = LD_ST + 208 * 8, LD_R = LD_CTL + 512;
constexpr u32 WSLOT = 16384, NWS = 4, XSLOT = 26624, NXS = 3, LD_X = LD_R + NWS * WSLOT, LDS_BYTES = LD_X + NXS * XSLOT;
constexpr u32 LD_U = LD_R + 2 * WSLOT, U_BYTES = 4608;
constexpr u32 WD_BYTES = 30720;
DEV u32 wd_off(int w) { return w == 0 ? LD_R : LD_R + 3 * WSLOT + (w - 1) * WD_BYTES; }
static_assert(LD_R % 128 == 0 && LD_R + 3 * WSLOT + 3 * WD_BYTES <= LDS_BYTES && 3 * U_BYTES <= WSLOT && LDS_BYTES <= 163840, "lds");
constexpr u32 C_FULL = 0, C_WDFULL = 44, C_RDY = 48, C_ZFULL = 52, C_STF = 56, C_TAG = 60, C_KDONE = 64, C_RED = 80, C_DONE = 256;
constexpr int AUX_SC1 = 16;
constexpr u32 SPIN_CAP = 1u << 15;
#define WD_DEPTH 30
#define POLL_STAGE 8
#define CONV_PRIO 3

#define STAMP(k) do { if ((k) == 0) dg0 = __builtin_amdgcn_s_memrealtime(); if ((k) == DIAG) dgk = __builtin_amdgcn_s_memrealtime(); \
    if ((k) == 8 && dgk) { const u64 e_ = __builtin_amdgcn_s_memrealtime() + 10 * (dgk - dg0); while (__builtin_amdgcn_s_memrealtime() < e_) __builtin_amdgcn_s_sleep(4); } } while (0)
#define DG_DECL u64 dg0 = 0, dgk = 0; (void)dg0; (void)dgk;

struct Params {
  const u16 *x, *lar, *aim, *cre, *cim, *ldt, *dsk, *wg, *wu, *wd;
  u16* out; char* ws;
};

DEV float bf2f(u32 v) { return __uint_as_float(v << 16); }
DEV float bfhi(u32 v) { return __uint_as_float(v & 0xffff0000u); }
DEV u32 pkbf(float a, float b) { f2 v = {a, b}; return __builtin_bit_cast(u32, __builtin_convertvector(v, bf2)); }
DEV u32 pkhf(float a, float b) { f2 v = {a, b}; return __builtin_bit_cast(u32, __builtin_convertvector(v, hf2)); }
DEV float rbf(float a) { return bf2f(pkbf(a, a)); }
DEV void mma_bf(f4& c, v4u a, v4u b) { asm volatile("v_mfma_f32_16x16x32_bf16 %0, %1, %2, %0" : "+v"(c) : "v"(a), "v"(b)); }
DEV void mma_hf(f4& c, v4u a, v4u b) { asm volatile("v_mfma_f32_16x16x32_f16 %0, %1, %2, %0" : "+v"(c) : "v"(a), "v"(b)); }
DEV v2u trd(u32 a) { return __builtin_bit_cast(v2u, __builtin_amdgcn_ds_read_tr16_b64_v4i16((LAS s4*)a)); }
DEV v4u trd2(u32 a, u32 b) { v2u x = trd(a), y = trd(b); return v4u{x.x, x.y, y.x, y.y}; }
DEV v4u lds128(u32 a) { return *(LAS v4u*)a; }
DEV v2u lds64(u32 a) { return *(LAS v2u*)a; }
DEV u32 rfl(u32 v) { return __builtin_amdgcn_readfirstlane(v); }
DEV v4u mkrs(const void* p, u32 bytes) { u64 a = (u64)p; return v4u{(u32)a, (u32)(a >> 32) & 0xffffu, bytes, 0x00020000u}; }
DEV rsrc_t mkr(const void* p, u32 bytes) { return __builtin_amdgcn_make_buffer_rsrc((void*)p, 0, bytes, 0x00020000); }
DEV v4u ldc128(rsrc_t r, u32 v, u32 s) { return __builtin_amdgcn_raw_buffer_load_b128(r, v, s, AUX_SC1); }
DEV u32 ldc32(rsrc_t r, u32 v) { return __builtin_amdgcn_raw_buffer_load_b32(r, v, 0, AUX_SC1); }
DEV void stc128(v4u d, rsrc_t r, u32 v, u32 s) { __builtin_amdgcn_raw_buffer_store_b128(d, r, v, s, AUX_SC1); }
DEV void stc32(u32 d, rsrc_t r, u32 v) { __builtin_amdgcn_raw_buffer_store_b32(d, r, v, 0, AUX_SC1); }
#define CBAR() asm volatile("" ::: "memory")
#define SCHED() __builtin_amdgcn_sched_barrier(0)
#define ORDER() __builtin_amdgcn_fence(__ATOMIC_ACQUIRE, "wavefront")
#define BARRIER() do { asm volatile("s_waitcnt lgkmcnt(0)" ::: "memory"); __builtin_amdgcn_s_barrier(); CBAR(); } while (0)
template <int N> DEV void waitv() { asm volatile("s_waitcnt vmcnt(%0)" ::"i"(N) : "memory"); }
#define DMA_(V, RS, SO, OFF, SC) "buffer_load_dwordx4 " V ", " RS ", " SO " offen" OFF SC " lds\n\t"
#define DMA4_(NAME, SC) DEV void NAME(v4u rs, u32 v0, u32 v1, u32 v2, u32 v3, u32 so, u32 lds) { \
  asm volatile("s_mov_b32 m0, %0\n\ts_nop 0\n\t" DMA_("%1", "%5", "%6", "", SC) DMA_("%2", "%5", "%6", " offset:1024", SC) DMA_("%3", "%5", "%6", " offset:2048", SC) DMA_("%4", "%5", "%6", " offset:3072", SC) \
               ::"s"(lds), "v"(v0), "v"(v1), "v"(v2), "v"(v3), "s"(rs), "s"(so) : "memory"); }
#define DMA2_(NAME, SC) DEV void NAME(v4u rs, u32 v0, u32 v1, u32 so, u32 lds) { \
  asm volatile("s_mov_b32 m0, %0\n\ts_nop 0\n\t" DMA_("%1", "%3", "%4", "", SC) DMA_("%2", "%3", "%4", " offset:1024", SC) ::"s"(lds), "v"(v0), "v"(v1), "s"(rs), "s"(so) : "memory"); }
#define DMA1_(NAME, SC) DEV void NAME(v4u rs, u32 v0, u32 so, u32 lds) { \
  asm volatile("s_mov_b32 m0, %0\n\ts_nop 0\n\t" DMA_("%1", "%2", "%3", "", SC) ::"s"(lds), "v"(v0), "s"(rs), "s"(so) : "memory"); }
DMA4_(dma4, "") DMA2_(dma2, "") DMA1_(dma1, "") DMA4_(dma4c, " sc1") DMA2_(dma2c, " sc1") DMA1_(dma1c, " sc1")
DEV void sig0(v4u rz, u32 lds) {
  asm volatile("s_mov_b32 m0, %0\n\ts_mov_b64 exec, 1\n\tbuffer_load_dword off, %1, 0 lds\n\ts_mov_b64 exec, -1" ::"s"(lds), "s"(rz) : "memory");
}
DEV u32 ldsv(u32 a) { return *(volatile LAS u32*)a; }
DEV void ldsw(u32 a, u32 v) { asm volatile("s_mov_b64 exec, 1\n\tds_write_b32 %0, %1\n\ts_mov_b64 exec, -1" ::"v"(a), "v"(v) : "memory"); }
DEV void spin0(u32 a, u32 cur) {
  u32 tv, ts, cnt;
  asm volatile("s_cmp_eq_u32 %4, 0\n\ts_cbranch_scc0 3f\n2:\n\t.subsection 1\n3:\n\ts_mov_b32 %2, 0x10000\n"
               "1:\n\tds_read_b32 %0, %3\n\ts_waitcnt lgkmcnt(0)\n\tv_readfirstlane_b32 %1, %0\n\ts_nop 0\n\ts_cmp_eq_u32 %1, 0\n\ts_cbranch_scc1 2b\n\t"
               "s_sleep 1\n\ts_sub_u32 %2, %2, 1\n\ts_cmp_lg_u32 %2, 0\n\ts_cbranch_scc1 1b\n\ts_branch 2b\n\t.subsection 0"
               : "=&v"(tv), "=&s"(ts), "=&s"(cnt) : "v"(a), "s"(cur) : "memory", "scc");
}
DEV bool is_zero4(u32 a) { v4u d = *(volatile LAS v4u*)a; return rfl(d.x | d.y | d.z | d.w) == 0; }
DEV void spin_zero4(u32 a) {
#pragma unroll 1
  for (u32 i = 0; i < 2 * SPIN_CAP; i++) { if (is_zero4(a)) break; __builtin_amdgcn_s_sleep(1); } CBAR();
}
template <int SL> DEV void poll_flags(rsrc_t rw, u32 off, u32 off2, u32 tag) {
#pragma unroll 1
  for (u32 i = 0; i < SPIN_CAP; i++) {
    const u32 a = ldc32(rw, off), b = ldc32(rw, off2);
    if (__all(a == tag && b == tag)) break;
    __builtin_amdgcn_s_sleep(SL);
  }
  ORDER();
}
DEV float fmul_(float a, float b) { float r; asm("v_mul_f32 %0, %1, %2" : "=v"(r) : "v"(a), "v"(b)); return r; }
DEV float fadd_(float a, float b) { float r; asm("v_add_f32 %0, %1, %2" : "=v"(r) : "v"(a), "v"(b)); return r; }
DEV float ffma_(float a, float b, float c) { float r; asm("v_fma_f32 %0, %1, %2, %3" : "=v"(r) : "v"(a), "v"(b), "v"(c)); return r; }
DEV float fnma_(float a, float b, float c) { float r; asm("v_fma_f32 %0, -%1, %2, %3" : "=v"(r) : "v"(a), "v"(b), "v"(c)); return r; }
struct cf { float x, y; };
DEV cf csq(cf a) { const float t = fmul_(a.x, a.y); return cf{fnma_(a.y, a.y, fmul_(a.x, a.x)), fadd_(t, t)}; }
DEV u32 pkc(cf a) { return pkhf(a.x, a.y); }
DEV cf cexp2(float lr, float rev) { float m = __builtin_amdgcn_exp2f(lr); const float f = __builtin_amdgcn_fractf(rev); float c = __builtin_amdgcn_cosf(f), s = __builtin_amdgcn_sinf(f);
  asm volatile("s_nop 0" : "+v"(c), "+v"(s), "+v"(m)); return cf{fmul_(m, c), fmul_(m, s)}; }
DEV u32 hmul(u32 a, u32 m) { u32 t, r;
  asm("v_pk_mul_f16 %0, %1, %2 op_sel_hi:[0,1]" : "=v"(t) : "v"(a), "v"(m));
  asm("v_pk_fma_f16 %0, %1, %2, %3 op_sel:[1,1,0] op_sel_hi:[1,0,1] neg_lo:[0,1,0]" : "=v"(r) : "v"(a), "v"(m), "v"(t)); return r; }
DEV u32 hmulc(u32 a, u32 m) { u32 t, r;
  asm("v_pk_mul_f16 %0, %1, %2 op_sel_hi:[0,1] neg_hi:[0,1]" : "=v"(t) : "v"(a), "v"(m));
  asm("v_pk_fma_f16 %0, %1, %2, %3 op_sel:[1,1,0] op_sel_hi:[1,0,1]" : "=v"(r) : "v"(a), "v"(m), "v"(t)); return r; }
DEV f2 pfma_x(f2 s, f2 b, f2 c) { f2 r; asm("v_pk_fma_f32 %0, %1, %2, %3 op_sel_hi:[0,1,1]" : "=v"(r) : "v"(s), "v"(b), "v"(c)); return r; }
DEV f2 pmul_y(f2 s, f2 b) { f2 r; asm("v_pk_mul_f32 %0, %1, %2 op_sel:[1,0]" : "=v"(r) : "v"(s), "v"(b)); return r; }
DEV f2 pfma_xx(f2 s, f2 b) { f2 r; asm("s_nop 0\n\tv_pk_fma_f32 %0, %1, %2, %1 op_sel_hi:[0,1,0]" : "=v"(r) : "v"(s), "v"(b)); return r; }
DEV f2 pmul(f2 a, f2 b) { f2 r; asm("s_nop 0\n\tv_pk_mul_f32 %0, %1, %2" : "=v"(r) : "v"(a), "v"(b)); return r; }
DEV void swap16(float& a, float& b) { auto r = __builtin_amdgcn_permlane16_swap(__float_as_uint(a), __float_as_uint(b), false, false); a = __uint_as_float(r[0]); b = __uint_as_float(r[1]); }
DEV void swap32(float& a, float& b) { auto r = __builtin_amdgcn_permlane32_swap(__float_as_uint(a), __float_as_uint(b), false, false); a = __uint_as_float(r[0]); b = __uint_as_float(r[1]); }

DEV void ln_wave(const Params& p, u32 sb, int pn, int lane, int wv) {
  DG_DECL
  const rsrc_t rx = mkr(p.x, L * D * 2), rw = mkr(p.ws, WS_BYTES);
  const int h = lane & 1, m = lane >> 1, row0 = 64 * (pn >> 2) + (pn & 3);
  __builtin_amdgcn_s_setprio(3);
  v2u xv[8][5];
#pragma unroll
  for (int e = 0; e < 8; e++)
#pragma unroll
    for (int i = 0; i < 5; i++) xv[e][i] = __builtin_amdgcn_raw_buffer_load_b64(rx, (row0 + 32 * h) * 1280 + m * 8 + i * 256, e * 5120, 0);
  float s[16];
#pragma unroll
  for (int e = 0; e < 8; e++) {
    float a = 0.f, q = 0.f;
#pragma unroll
    for (int i = 0; i < 10; i++) {
      const u32 w = xv[e][i >> 1][i & 1];
      const bf2 v = __builtin_bit_cast(bf2, w);
      a = __builtin_amdgcn_fdot2_f32_bf16(v, __builtin_bit_cast(bf2, 0x3f803f80u), a, false);
      q = __builtin_amdgcn_fdot2_f32_bf16(v, v, q, false);
    }
    s[e] = a; s[8 + e] = q;
  }
#pragma unroll
  for (int e = 0; e < 16; e++) asm volatile("" : "+v"(s[e]));
  asm volatile("s_nop 2");
  BARRIER();
  const u32 tag = rfl(ldsv(sb + LD_CTL + C_TAG));
  float z[4];
#pragma unroll
  for (int k = 0; k < 4; k++) {
    swap16(s[4 * k], s[4 * k + 1]); swap16(s[4 * k + 2], s[4 * k + 3]);
    float a = fadd_(s[4 * k], s[4 * k + 1]), b = fadd_(s[4 * k + 2], s[4 * k + 3]);
    asm volatile("s_nop 1" : "+v"(a), "+v"(b));
    swap32(a, b); z[k] = fadd_(a, b);
  }
#define DPPADD(C) asm volatile("s_nop 1\n\tv_add_f32_dpp %0, %0, %0 " C " row_mask:0xf bank_mask:0xf\n\tv_add_f32_dpp %1, %1, %1 " C " row_mask:0xf bank_mask:0xf\n\t" \
    "v_add_f32_dpp %2, %2, %2 " C " row_mask:0xf bank_mask:0xf\n\tv_add_f32_dpp %3, %3, %3 " C " row_mask:0xf bank_mask:0xf" : "+v"(z[0]), "+v"(z[1]), "+v"(z[2]), "+v"(z[3]))
  DPPADD("quad_perm:[2,3,0,1]"); DPPADD("row_ror:4"); DPPADD("row_ror:8");
  float rs[8], nb[8];
#pragma unroll
  for (int k = 0; k < 2; k++) {
    const float mean = fmul_(z[k], 1.f / D), var = fmaxf(fnma_(mean, mean, fmul_(z[2 + k], 1.f / D)), 0.f);
    const float mb = rbf(mean), sd = rbf(__builtin_amdgcn_sqrtf(rbf(rbf(var) + 1e-5f)));
    float r = __builtin_amdgcn_rcpf(sd);
    asm volatile("s_nop 0" : "+v"(r));
    r = ffma_(fnma_(sd, r, 1.f), r, r);
    if ((lane & 15) < 2) stc128(v4u{__float_as_uint(mb), tag, __float_as_uint(r), tag}, rw, WS_STG + (row0 + 32 * h + 4 * (4 * k + (lane >> 4))) * 16, 0);
    float t[2] = {r, fmul_(-mb, r)};
#pragma unroll
    for (int w = 0; w < 2; w++) {
      float a = t[w], c = t[w];
      asm volatile("s_nop 1" : "+v"(a), "+v"(c));
      swap32(a, c); float b = a, d = c; swap16(a, b); swap16(c, d);
      float* o = w ? nb : rs; o[4 * k] = a; o[4 * k + 1] = b; o[4 * k + 2] = c; o[4 * k + 3] = d;
    }
  }
#pragma unroll
  for (int i = 0; i < 5; i++)
#pragma unroll
    for (int c = 0; c < 4; c++) {
      float y[8];
#pragma unroll
      for (int e = 0; e < 8; e++) { const u32 w = xv[e][i][c >> 1]; y[e] = ffma_((c & 1) ? bfhi(w) : bf2f(w), rs[e], nb[e]); }
      stc128(v4u{pkhf(y[0], y[1]), pkhf(y[2], y[3]), pkhf(y[4], y[5]), pkhf(y[6], y[7])}, rw, WS_XT + lane * 16, pn * 20480 + (i * 4 + c) * 1024);
    }
  waitv<0>();
  if (lane == 0) stc32(tag, rw, WS_LFLAG + pn * 4);
  STAMP(21);
}

template <bool CJ> DEV void grp8(u32* v, u32 a, u32 b, u32 c) {
#define HM(x, y) (CJ ? hmulc(x, y) : hmul(x, y))
  v[1] = HM(v[0], a); v[2] = HM(v[0], b); v[3] = HM(v[1], b); v[4] = HM(v[0], c); v[5] = HM(v[1], c); v[6] = HM(v[2], c); v[7] = HM(v[3], c);
#undef HM
}
template <int N, int O> DEV void wr_rows(const u32* v) {
  if constexpr (N > 0) { asm volatile("ds_write_addtid_b32 %0 offset:%1" ::"v"(v[0]), "i"(O * 272) : "memory"); wr_rows<N - 1, O + 1>(v + 1); }
}
DEV void conv_wave(const Params& p, u32 sb, int slot, int ch, int lane, int wv, bool g1, int ts, int cj) {
  DG_DECL
  const u32 KB = sb + LD_K + slot * K_BYTES, UB = sb + LD_U + slot * U_BYTES, CTL = sb + LD_CTL;
  const int g = lane >> 4, q = (lane >> 2) & 3, pp = lane & 3, j = lane & 15, b = blockIdx.x;
  const rsrc_t rw = mkr(p.ws, WS_BYTES);
  const v4u rz = mkrs(p.ws, 0);
  u32 gen = 0;
  if (slot == 0) gen = ldc32(rw, WS_GEN);
  const int wu = slot == 0 ? b : (slot == 1 && b < 128 ? 256 + b : -1);
  if (wu >= 0) {
    u32 vo[5];
#pragma unroll
    for (int k = 0; k < 5; k++) { const int n = k * 64 + lane; vo[k] = (n / 40) * 1280 + (n % 40) * 16 - (k & 3) * 1024; }
    const v4u rd = mkrs(p.wd, F * D * 2);
    const u32 so = (wu >> 1) * 10240 + (wu & 1) * 640;
    dma4(rd, vo[0], vo[1], vo[2], vo[3], so, KB); dma1(rd, vo[4], so, KB + 4096);
  }
  u32 la[3], ai[3], cr[3], ci[3];
#pragma unroll
  for (int c = 0; c < 3; c++) { const int o = ch * NS + 64 * c + lane; la[c] = p.lar[o]; ai[c] = p.aim[o]; cr[c] = p.cre[o]; ci[c] = p.cim[o]; }
  const u32 ldt = p.ldt[ch];
  if (slot == 0) {
    const int nch = ((b >> 3) & 1) ? 2 : 3;
    const bool one = (g1 && (lane < 10 || lane == 14)) || (lane >= 11 && lane <= 13) || (lane >= 16 && lane < 16 + nch) || (lane >= 20 && lane < 24);
    *(volatile LAS u32*)(CTL + lane * 4) = lane == 15 ? gen * 0x9E3779B1u + 0x7F4A7C15u : (one ? 1u : 0u);
  }
  if (slot == 1) *(volatile LAS u32*)(CTL + C_DONE + lane * 4) = g1 ? 1u : 0u;
  BARRIER();
  const u32 tag = rfl(ldsv(CTL + C_TAG));
  if (wu >= 0) {
    waitv<0>();
    __builtin_amdgcn_s_setprio(3);
    const u32 ra = KB + q * 640 + (80 * g + 20 * pp) * 2;
    const u32 wo = WS_WDF + ((4 * (wu & 1) + g) * 48 * 5) * 1024 + j * 16, so = (wu >> 3) * 5120 + ((wu >> 1) & 3) * 256;
#pragma unroll
    for (int k = 0; k < 5; k++) stc128(trd2(ra + k * 8, ra + k * 8 + 2560), rw, wo + k * 1024, so);
  }
  const float dt = rbf(__expf(bf2f(ldt)));
  u32 mx = 0;
#pragma unroll
  for (int c = 0; c < 3; c++) mx = max(mx, max(cr[c] & 0x7fffu, ci[c] & 0x7fffu));
  mx = max(mx, (u32)__builtin_amdgcn_update_dpp(0, (int)mx, 0x111, 0xf, 0xf, false));
  mx = max(mx, (u32)__builtin_amdgcn_update_dpp(0, (int)mx, 0x112, 0xf, 0xf, false));
  mx = max(mx, (u32)__builtin_amdgcn_update_dpp(0, (int)mx, 0x114, 0xf, 0xf, false));
  mx = max(mx, (u32)__builtin_amdgcn_update_dpp(0, (int)mx, 0x118, 0xf, 0xf, false));
  mx = max(max((u32)__builtin_amdgcn_readlane(mx, 15), (u32)__builtin_amdgcn_readlane(mx, 31)), max((u32)__builtin_amdgcn_readlane(mx, 47), (u32)__builtin_amdgcn_readlane(mx, 63)));
  STAMP(16);
  const u32 ex = min(max(mx >> 7, 1u), 252u);
  const float cscale = __uint_as_float((253u - ex) << 23);
  const float oscale = 2.f * dt * __uint_as_float((ex + 1u) << 23);
  u32 G[3][57];
#pragma unroll
  for (int c = 0; c < 3; c++) {
    const float zr = fmul_(fmul_(rbf(__expf(bf2f(la[c]))), dt), -1.44269504089f), zi = fmul_(fmul_(bf2f(ai[c]), dt), 0.15915494309f);
    const cf P0 = cexp2(zr, zi), P4 = cexp2(fmul_(zr, 16.f), fmul_(zi, 16.f)), P8 = cexp2(fmul_(zr, 256.f), fmul_(zi, 256.f));
    const cf P1 = csq(P0), P2 = csq(P1), P3 = csq(P2), P5 = csq(P4), P6 = csq(P5), P7 = csq(P6), P9 = csq(P8), P10 = csq(P9), P11 = csq(P10);
    const u32 M0 = pkc(P0), M1 = pkc(P1), M2 = pkc(P2), M3 = pkc(P3), M4 = pkc(P4), M5 = pkc(P5), M7 = pkc(P7), M8 = pkc(P8), M9 = pkc(P9);
    u32* v = G[c];
    const float cx = fmul_(bf2f(cr[c]), cscale), cy = fmul_(bf2f(ci[c]), -cscale);
    v[0] = pkhf(cx, cy); grp8<true>(v, M3, M4, M5);
    v[8] = pkhf(ffma_(cy, P6.y, fmul_(cx, P6.x)), fnma_(cx, P6.y, fmul_(cy, P6.x))); grp8<true>(v + 8, M3, M4, M5);
    v[16] = 0x00003c00u; v[17] = M0; v[18] = M1; v[19] = hmul(M0, M1); v[20] = M2; v[21] = hmul(M0, M2); v[22] = hmul(M1, M2); v[23] = hmul(v[19], M2);
    v[24] = M7; grp8<false>(v + 24, M0, M1, M2);
    v[32] = M8; grp8<false>(v + 32, M1, M2, M7);
    v[40] = M9; grp8<false>(v + 40, M2, M7, M8);
    v[48] = pkc(P10); grp8<false>(v + 48, M7, M8, M9);
    v[56] = pkc(P11);
#pragma unroll
    for (int k = 0; k < 57; k++) asm volatile("" : "+v"(v[k]));
    if (c == 0 && wu >= 0) { waitv<0>(); if (lane == 0) stc32(tag, rw, WS_WFLAG + wu * 4); __builtin_amdgcn_s_setprio(0); }
  }
  STAMP(13);
  float cs0 = 0.f, cs1 = 0.f; f2 stv[4];
  if (g1) spin0(CTL + C_FULL + POLL_STAGE * 4, 1); else { __builtin_amdgcn_s_sleep(100); }
  __builtin_amdgcn_s_setprio(3);
  if (slot == 0) {
#pragma unroll 1
    for (u32 it = 0; it < SPIN_CAP; it++) {
      const v4u a = ldc128(rw, WS_LFLAG + lane * 16, 0), c = ldc128(rw, WS_LFLAG + lane * 16, 1024);
      if (__all(a.x == tag && a.y == tag && a.z == tag && a.w == tag && c.x == tag && c.y == tag && c.z == tag && c.w == tag)) break;
      __builtin_amdgcn_s_sleep(24);
    }
    ORDER();
    sig0(rz, CTL + C_RDY);
    if (b == 0 && lane == 0) stc32(tag, rw, WS_GEN);
  } else if (slot == 1 && g1) {
#pragma unroll 1
    for (u32 it = 0; it < SPIN_CAP; it++) {
      v4u c[10]; bool ok = true; cs0 = cs1 = 0.f;
#pragma unroll
      for (int k = 0; k < 10; k++) c[k] = ldc128(rw, WS_CSP + lane * 16, (cj * 10 + k) * 1024);
#pragma unroll
      for (int k = 0; k < 10; k++) { ok = ok && c[k].y == tag && c[k].w == tag; cs0 = fadd_(cs0, __uint_as_float(c[k].x)); cs1 = fadd_(cs1, __uint_as_float(c[k].z)); }
      if (__all(ok)) break;
      __builtin_amdgcn_s_sleep(24);
    }
#pragma unroll 1
    for (u32 it = 0; it < SPIN_CAP; it++) {
      bool ok = true;
#pragma unroll
      for (int k = 0; k < 4; k++) { const int n = k * 64 + lane; const v4u c = ldc128(rw, WS_STG + (ts * 16 + (n < 208 ? n : 0)) * 16, 0);
        ok = ok && c.y == tag && c.w == tag; stv[k] = f2{__uint_as_float(c.x), __uint_as_float(c.z)}; }
      if (__all(ok)) break;
      __builtin_amdgcn_s_sleep(24);
    }
  }
  STAMP(22);
  __builtin_amdgcn_s_setprio(0);
  spin0(CTL + C_FULL + 36, 1);
  STAMP(17);
  if (slot == 1 && g1) {
#pragma unroll
    for (int k = 0; k < 4; k++) { const int n = k * 64 + lane; if (n < 208) *(LAS f2*)(sb + LD_ST + n * 8) = stv[k]; }
    *(LAS f2*)(sb + LD_CS + lane * 8) = f2{cs0, cs1};
    CBAR();
    ldsw(CTL + C_STF, 0);
  }
  spin0(CTL + C_RDY, 1);
  {
    const v4u rws = mkrs(p.ws, WS_BYTES);
    const u32 co = WS_XT + ((ch >> 7) * 4 + (ch & 3)) * 1024 + ((ch & 127) >> 2) * 32;
    u32 vo[5];
#pragma unroll
    for (int k = 0; k < 5; k++) { const int n = k * 64 + lane, tau = n / 72, a = n % 72 - 8; vo[k] = a >= 0 ? co + ((a >> 1) * 4 + tau) * 20480 + (a & 1) * 16 - (k & 3) * 1024 : 0x80000000u; }
    dma4c(rws, vo[0], vo[1], vo[2], vo[3], 0, UB);
    asm volatile("s_mov_b32 m0, %0\n\ts_mov_b32 exec_hi, 0\n\tbuffer_load_dwordx4 %1, %2, 0 offen sc1 lds\n\ts_mov_b32 exec_hi, -1" ::"s"(UB + 4096), "v"(vo[4]), "s"(rws) : "memory");
  }
  f4 kd[3] = {f4{0, 0, 0, 0}, f4{0, 0, 0, 0}, f4{0, 0, 0, 0}};
  {
    const u32 rb = KB + j * 272 + g * 16;
    asm volatile("s_mov_b32 m0, %3\n\ts_nop 0" : "+v"(kd[0]), "+v"(kd[1]), "+v"(kd[2]) : "s"(KB) : "memory");
    v4u Fr[12][4];
#pragma unroll
    for (int ps = 0; ps <= 12; ps++) {
      if (ps < 12) {
        const int c = ps >> 2, t = ps & 3;
        if (t < 3) wr_rows<16, 0>(G[c] + 16 * t); else wr_rows<9, 0>(G[c] + 48);
        asm volatile("ds_read_b128 %0, %4\n\tds_read_b128 %1, %4 offset:64\n\tds_read_b128 %2, %4 offset:128\n\tds_read_b128 %3, %4 offset:192"
                     : "=&v"(Fr[ps][0]), "=&v"(Fr[ps][1]), "=&v"(Fr[ps][2]), "=&v"(Fr[ps][3]) : "v"(rb) : "memory");
      }
      if (ps > 0) {
        const int pv = ps - 1;
        if (ps < 12) asm volatile("s_waitcnt lgkmcnt(13)" : "+v"(Fr[pv][0]), "+v"(Fr[pv][1]), "+v"(Fr[pv][2]), "+v"(Fr[pv][3]));
        else asm volatile("s_waitcnt lgkmcnt(0)" : "+v"(Fr[pv][0]), "+v"(Fr[pv][1]), "+v"(Fr[pv][2]), "+v"(Fr[pv][3]));
        if (pv & 3) {
#pragma unroll
          for (int ks = 0; ks < 4; ks++) mma_hf(kd[(pv & 3) - 1], Fr[pv][ks], Fr[pv & ~3][ks]);
        }
      }
    }
    asm volatile("s_nop 7" : "+v"(kd[0]), "+v"(kd[1]), "+v"(kd[2]));
  }
  STAMP(9);
  {
    const u32 K0 = KB + 32;
    const u32 a00 = pkhf(kd[1][0], kd[1][0]), a11 = pkhf(kd[1][1], kd[1][1]), a22 = pkhf(kd[1][2], kd[1][2]), a33 = pkhf(kd[1][3], kd[1][3]);
    const u32 a12 = pkhf(kd[1][1], kd[1][2]), a01 = pkhf(kd[1][0], kd[1][1]), a23 = pkhf(kd[1][2], kd[1][3]);
    v2u bb[4];
#pragma unroll
    for (int r = 0; r < 4; r++) { bb[r].x = pkhf(kd[2][r], kd[2][r]); bb[r].y = bb[r].x; }
    const v2u r0 = {pkhf(kd[0][0], kd[0][1]), pkhf(kd[0][2], kd[0][3])}, w1 = {a00, a12}, w2a = {a00, a01}, w2b = {a22, a23};
    const v4u z4 = {0, 0, 0, 0};
    const u32 p3 = K0 + 2 * (1024 + 512 * g + 8 * j) - 8, p2 = K0 + 2 * (256 * g + 8 * j) - 4, p1 = K0 + 2 * (256 + 128 * g + 8 * j) - 2;
    const u32 p0 = K0 + 2 * (128 * (g >> 1) + 8 * j + 4 * (g & 1)), pz = KB + lane * 16;
    asm volatile(
      "s_mov_b32 exec_hi, 1\n\t"
      "ds_write2_b64 %0, %5, %5 offset1:1\n\t"
      "s_mov_b32 exec_hi, 0\n\t"
      "ds_write2_b64 %0, %6, %6 offset0:32 offset1:33\n\t"
      "ds_write2_b64 %0, %7, %7 offset0:64 offset1:65\n\t"
      "ds_write2_b64 %0, %8, %8 offset0:96 offset1:97\n\t"
      "s_mov_b32 exec_lo, 0x55555555\n\t"
      "ds_write_b16 %0, %9 offset:16\n\tds_write_b16 %0, %10 offset:272\n\tds_write_b16 %0, %11 offset:528\n\tds_write_b16 %0, %12 offset:784\n\t"
      "s_mov_b32 exec_lo, 0\n\ts_mov_b32 exec_hi, -1\n\t"
      "ds_write_b32 %1, %13\n\tds_write_b64 %1, %17 offset:4\n\tds_write_b32 %1, %14 offset:12\n\t"
      "ds_write_b32 %1, %15 offset:256\n\tds_write_b64 %1, %18 offset:260\n\tds_write_b32 %1, %16 offset:268\n\t"
      "s_mov_b32 exec_lo, -1\n\ts_mov_b32 exec_hi, 0\n\t"
      "ds_write_b16 %2, %13\n\tds_write_b64 %2, %19 offset:2\n\tds_write_b32 %2, %15 offset:10\n\tds_write_b16 %2, %16 offset:14\n\t"
      "s_mov_b32 exec_lo, 3\n\t"
      "ds_write_b128 %4, %21\n\t"
      "s_mov_b64 exec, -1\n\t"
      "ds_write_b64 %3, %20"
      ::"v"(p3), "v"(p2), "v"(p1), "v"(p0), "v"(pz), "v"(bb[0]), "v"(bb[1]), "v"(bb[2]), "v"(bb[3]), "v"(bb[0].x), "v"(bb[1].x), "v"(bb[2].x), "v"(bb[3].x),
        "v"(a00), "v"(a11), "v"(a22), "v"(a33), "v"(w2a), "v"(w2b), "v"(w1), "v"(r0), "v"(z4) : "memory");
  }
  v4u A[16];
  {
    const u32 ab = KB + 32 + 2 * (-12 + 4 * (8 * g + 7 - q) + 4 * pp);
#pragma unroll
    for (int s = 0; s < 16; s++) A[s] = trd2(ab + 256 * s, ab + 256 * s - 32);
  }
  ldsw(CTL + C_KDONE + slot * 4, 0);
  f4 acc[4][8];
#pragma unroll
  for (int t = 0; t < 4; t++)
#pragma unroll
    for (int T = 0; T < 8; T++) { acc[t][T] = f4{0, 0, 0, 0}; asm volatile("" : "+v"(acc[t][T])); }
  STAMP(10);
  waitv<0>();
  spin0(CTL + C_DONE + 160 + slot * 4, 1);
  __builtin_amdgcn_s_setprio(CONV_PRIO);
  STAMP(18);
  const u32 ub = UB + 128 + 2 * (4 * j - 4 - 8 * g) - 64;
  v4u B[4];
#define QORD(n) (((n) & 1) ? 15 - (((n) & 15) >> 1) : (((n) & 15) >> 1))
#define LDB(n) do { const u32 a_ = ub + ((n) >> 4) * 1152 + 64 * QORD(n); const v2u x_ = lds64(a_); SCHED(); const v2u y_ = lds64(a_ + 8); B[(n) & 3] = v4u{x_.x, x_.y, y_.x, y_.y}; SCHED(); } while (0)
  LDB(0); LDB(1); LDB(2);
#pragma unroll
  for (int n = 0; n < 64; n++) {
    const int q2 = QORD(n) - 1;
    if (n + 3 < 64) LDB(n + 3);
#pragma unroll
    for (int T = 7; T >= 0; T--) { const int s = 2 * T - q2; if (s >= 0 && s <= 2 * T + 1) mma_hf(acc[n >> 4][T], A[s], B[n & 3]); }
    SCHED();
  }
#pragma unroll
  for (int T = 0; T < 8; T++) asm volatile("s_nop 0" : "+v"(acc[3][T]));
  STAMP(19);
  const int srcl = (lane == 0 ? 63 : (g > 0 ? lane - 16 : lane + 47)) * 4;
  v2u zp[8]; float pc[3] = {0.f, 0.f, 0.f};
#pragma unroll
  for (int T = 0; T < 8; T++) {
    const float c[3] = {fadd_(fadd_(acc[1][T][3], acc[2][T][2]), acc[3][T][1]), fadd_(acc[2][T][3], acc[3][T][2]), acc[3][T][3]};
    float y[4] = {acc[0][T][0], fadd_(acc[0][T][1], acc[1][T][0]), fadd_(fadd_(acc[0][T][2], acc[1][T][1]), acc[2][T][0]),
                  fadd_(fadd_(acc[0][T][3], acc[1][T][2]), fadd_(acc[2][T][1], acc[3][T][0]))};
#pragma unroll
    for (int r = 0; r < 3; r++) {
      const float cv = lane == 63 ? pc[r] : c[r];
      y[r] = fadd_(y[r], __int_as_float(__builtin_amdgcn_ds_bpermute(srcl, __float_as_int(cv))));
      pc[r] = c[r];
    }
    zp[T] = v2u{pkbf(fmul_(y[0], oscale), fmul_(y[1], oscale)), pkbf(fmul_(y[2], oscale), fmul_(y[3], oscale))};
    if (T & 1) {
      auto sx = __builtin_amdgcn_permlane16_swap(zp[T - 1].x, zp[T].x, false, false);
      auto sy = __builtin_amdgcn_permlane16_swap(zp[T - 1].y, zp[T].y, false, false);
      stc128(v4u{sx[0], sy[0], sx[1], sy[1]}, rw, WS_ZT + (g & 1) * 512 + j * 32 + (g >> 1) * 16, ch * 4096 + (T - 1) * 512);
    }
  }
  STAMP(20);
  waitv<0>();
  if (lane == 0) stc32(tag, rw, WS_ZFLAG + ch * 4);
  STAMP(11);
}

__global__ __launch_bounds__(512, 2) void k_main(Params p) {
  extern __shared__ __attribute__((aligned(16))) char smem[];
  const u32 sb = (u32)(size_t)(LAS char*)smem;
  const int lane = threadIdx.x & 63, wv = rfl(threadIdx.x >> 6);
  const int b = blockIdx.x, xc = b & 7, r = b >> 3;
  const bool g1 = r < 30;
  const int r5 = r / 6, ri = (xc >> 2) * 5 + r5, cj = (xc & 3) * 6 + r % 6, ts = (ri * 64) / 5;
  const int i2 = xc * 4 + (r >> 3), j2 = r & 7;
  const u32 CTL = sb + LD_CTL;
  asm volatile(".subsection 2\n\t.fill 128, 4, 0xBF800000\n\t.subsection 0");
  DG_DECL
  STAMP(0);
#define CSTAMP(k)
  if (wv >= 4 && wv < 7) {
    const int slot = wv - 4;
    if (slot == 2 && (r & 1)) ln_wave(p, sb, (r >> 1) * 8 + xc, lane, wv);
    else conv_wave(p, sb, slot, 80 * xc + 5 * (r >> 1) + (r & 1) * 3 + slot, lane, wv, g1, ts, cj);
    return;
  }
  const int g = lane >> 4, i = lane & 15;
  const rsrc_t rw = mkr(p.ws, WS_BYTES);
  const v4u rz = mkrs(p.ws, 0);
  const int rot = (xc >> 2) * 5;
  const v4u rg = mkrs(p.wg, D * F * 2), ru = mkrs(p.wu, D * F * 2), rxs = mkrs(p.x, L * D * 2);
  u32 v[4], vx[4];
#pragma unroll
  for (int k = 0; k < 4; k++) {
    const int row = 8 * k + (lane >> 3), fp = ((row >> 1) & 1) | (((row >> 3) & 1) << 1);
    v[k] = row * 3072 + cj * 128 + (((lane & 7) ^ (fp << 1)) << 4) - 1024 * k;
    vx[k] = (ts * 16 + row) * 1280 + (((lane & 7) ^ ((row >> 1) & 7)) << 4) - 1024 * k;
  }
  if (wv < 4) {
    __builtin_amdgcn_s_setprio(2);
    if (g1) {
      dma4((wv & 1) ? ru : rg, v[0], v[1], v[2], v[3], (rot * 64 + (wv >> 1) * 32) * 3072, sb + LD_R + (wv & 1) * 8192 + (wv >> 1) * 4096);
      dma4(rxs, vx[0], vx[1], vx[2], vx[3], rot * 128 + 3 * wv * 20480, sb + LD_X + 3 * wv * 2048);
      if (wv < 3) dma2(rxs, vx[0], vx[1], rot * 128 + (3 * wv + 2) * 20480, sb + LD_X + (3 * wv + 2) * 2048);
      else dma4(rxs, vx[0], vx[1], vx[2], vx[3], rot * 128 + 11 * 20480, sb + LD_X + 11 * 2048);
    }
    const rsrc_t rx = mkr(p.x, L * D * 2);
    constexpr int NWR = 5;
    v4u Wr[NWR][5];
    const u32 wdo = WS_WDF + (j2 * 48 + 12 * wv) * 5120;
    u32 tag;
    if (g1) {
      f4 acc[3][8], a13[2];
#pragma unroll
      for (int t = 0; t < 3; t++)
#pragma unroll
        for (int c = 0; c < 8; c++) { acc[t][c] = f4{0, 0, 0, 0}; asm volatile("" : "+v"(acc[t][c])); }
      a13[0] = a13[1] = f4{0, 0, 0, 0};
      const int q = (lane >> 2) & 3, pp = lane & 3, fp = ((q >> 1) & 1) | ((g & 1) << 1);
      u32 wb[4];
#pragma unroll
      for (int c = 0; c < 4; c++) wb[c] = sb + LD_R + (8 * g + q) * 128 + ((c ^ wv ^ fp) * 32) + pp * 8;
      u32 xb0 = sb + LD_X + wv * 6144 + i * 128 + ((g ^ (i >> 1)) << 4), xb1 = xb0 ^ 64;
      u32 yb0 = xb0 + (12 - 3 * wv) * 2048, yb1 = yb0 ^ 64, fa = CTL + C_FULL + 4;
      v4u Wf[2][8], Xf[2][4];
      asm volatile("" : "+v"(a13[0]), "+v"(a13[1]), "+v"(wb[0]), "+v"(wb[1]), "+v"(wb[2]), "+v"(wb[3]), "+v"(xb0), "+v"(xb1), "+v"(yb0), "+v"(yb1), "+v"(fa));
#define RDP(B, n, XA, YA, OFF) do { if ((n) == 0) { Xf[B][0] = lds128(XA); Xf[B][1] = lds128(XA + 2048); } \
        else if ((n) == 2) { Xf[B][2] = lds128(XA + 4096); Xf[B][3] = lds128(YA); } \
        else { constexpr int c = (n) == 1 ? 0 : (n) - 2; const u32 a = wb[c & 3] + (OFF) + (c >> 2) * 8192; Wf[B][c] = trd2(a, a + 512); } SCHED(); } while (0)
#define MM(KS, m) do { constexpr int c = (m) < 4 ? 0 : (m) < 13 ? 1 + ((m) - 4) / 3 : (m) < 17 ? 4 : 5 + ((m) - 17) / 3; \
        constexpr int t = (m) < 4 ? (m) : (m) < 13 ? ((m) - 4) % 3 : (m) < 17 ? (m) - 13 : ((m) - 17) % 3; \
        if (t == 3) mma_bf(a13[c >> 2], Wf[KS][c], Xf[KS][3]); else mma_bf(acc[t < 3 ? t : 0][c], Wf[KS][c], Xf[KS][t]); SCHED(); } while (0)
#define R0(m) MM(0, m); RDP(1, m, xb1, yb1, 4096);
#define R1(m) MM(1, m); RDP(0, (m) - 12, xb0, yb0, 0);
      waitv<0>();
      BARRIER();
      STAMP(1);
      RDP(0, 0, xb0, yb0, 0); RDP(0, 1, xb0, yb0, 0); RDP(0, 2, xb0, yb0, 0); RDP(0, 3, xb0, yb0, 0); RDP(0, 4, xb0, yb0, 0);
      RDP(0, 5, xb0, yb0, 0); RDP(0, 6, xb0, yb0, 0); RDP(0, 7, xb0, yb0, 0); RDP(0, 8, xb0, yb0, 0); RDP(0, 9, xb0, yb0, 0);
      u32 sw = 0, sx = 0;
#pragma unroll 1
      for (int s = 0; s < 10; s++) {
        u32 fw, fo;
        R0(0) R0(1) R0(2) R0(3) R0(4) R0(5) R0(6) R0(7) R0(8) R0(9)
        sx += XSLOT; u32 dx = XSLOT; if (sx == NXS * XSLOT) { sx = 0; dx = XSLOT - NXS * XSLOT; }
        sw += WSLOT; u32 dl = WSLOT; if (sw == NWS * WSLOT) { sw = 0; dl = WSLOT - NWS * WSLOT; }
        MM(0, 10); xb0 += dx; xb1 += dx; asm volatile("" : "+v"(xb0), "+v"(xb1)); SCHED();
        MM(0, 11); yb0 += dx; yb1 += dx; asm volatile("" : "+v"(yb0), "+v"(yb1)); SCHED();
        MM(0, 12); wb[0] += dl; wb[1] += dl; asm volatile("" : "+v"(wb[0]), "+v"(wb[1])); SCHED();
        MM(0, 13); wb[2] += dl; wb[3] += dl; asm volatile("" : "+v"(wb[2]), "+v"(wb[3])); SCHED();
        MM(0, 14); MM(0, 15);
        asm volatile("s_waitcnt lgkmcnt(0)" ::: "memory");
        sig0(rz, CTL + C_DONE + s * 16 + wv * 4); SCHED();
        if (s == r5) {
          const u32 tg = ldsv(CTL + C_TAG);
          f4 c0 = {0, 0, 0, 0}, c1 = {0, 0, 0, 0}; v4u on = {0x3f803f80u, 0x3f803f80u, 0x3f803f80u, 0x3f803f80u};
          asm volatile("s_nop 1" : "+v"(c0), "+v"(c1), "+v"(on));
          mma_bf(c0, Wf[0][0], on); mma_bf(c1, Wf[0][4], on); mma_bf(c0, Wf[1][0], on); mma_bf(c1, Wf[1][4], on);
          asm volatile("s_nop 7" : "+v"(c0), "+v"(c1));
          const f4 cc = (i & 1) ? c1 : c0;
          if (i < 2) {
            const u32 o = WS_CSP + (i * 64 + 16 * wv + 4 * g) * 8, so = (cj * 10 + ri) * 1024;
            stc128(v4u{__float_as_uint(cc[0]), tg, __float_as_uint(cc[1]), tg}, rw, o, so); stc128(v4u{__float_as_uint(cc[2]), tg, __float_as_uint(cc[3]), tg}, rw, o + 16, so);
          }
          asm volatile("s_nop 3"); SCHED();
        }
        MM(0, 16); MM(0, 17); MM(0, 18); MM(0, 19); MM(0, 20); MM(0, 21); MM(0, 22); MM(0, 23); MM(0, 24); MM(0, 25);
        MM(1, 0); MM(1, 1); MM(1, 2); MM(1, 3); fw = ldsv(fa); SCHED();
        MM(1, 4); MM(1, 5); MM(1, 6); MM(1, 7); MM(1, 8); MM(1, 9); fo = rfl(fw); SCHED(); MM(1, 10); MM(1, 11);
        spin0(fa, fo);
        R1(12) R1(13) R1(14) R1(15) R1(16) R1(17) R1(18) R1(19) R1(20) R1(21)
        MM(1, 22); fa += 4; asm volatile("" : "+v"(fa)); SCHED(); MM(1, 23); MM(1, 24); MM(1, 25);
      }
      CSTAMP(14);
      { const v4u cw = *(volatile LAS v4u*)(CTL + C_RDY); tag = rfl(cw.w); spin0(CTL + C_STF, rfl(cw.z)); spin0(CTL + C_RDY, rfl(cw.x)); }
      STAMP(2);
      f4 cg[4], cu[4]; f2 st[4];
#pragma unroll
      for (int t = 0; t < 4; t++) st[t] = *(LAS f2*)(sb + LD_ST + ((t < 3 ? 3 * wv + t : 12) * 16 + i) * 8);
#pragma unroll
      for (int c = 0; c < 4; c++) { cg[c] = *(LAS f4*)(sb + LD_CS + (16 * (c ^ wv) + 4 * g) * 4); cu[c] = *(LAS f4*)(sb + LD_CS + 256 + (16 * (c ^ wv) + 4 * g) * 4); }
      const u32 hb = WS_HF + (2 * cj) * 1024 + (((((g ^ wv) & 1) << 1) | (g >> 1)) * 16 + i) * 16;
#pragma unroll
      for (int t = 0; t < 4; t++) {
        f2 k1 = {-st[t].x, -1.44269504089f * st[t].y}, k2 = {__builtin_amdgcn_rcpf(st[t].y), 0.f};
        asm volatile("s_nop 0" : "+v"(k1), "+v"(k2));
        v2u P[4];
#pragma unroll
        for (int c = 0; c < 4; c++) {
          if (t == 3 && c > 0) break;
          const f4 ag = t < 3 ? acc[t < 3 ? t : 0][c] : a13[0], au = t < 3 ? acc[t < 3 ? t : 0][4 + c] : a13[1];
#pragma unroll
          for (int e = 0; e < 2; e++) {
            const f2 a2 = e ? ag.zw : ag.xy, u2 = e ? au.zw : au.xy, cg2 = e ? cg[c].zw : cg[c].xy, cu2 = e ? cu[c].zw : cu[c].xy;
            const f2 gg = pmul_y(k1, pfma_x(k1, cg2, a2)), uu = pfma_x(k1, cu2, u2);
            const f2 den = pfma_xx(k2, f2{__builtin_amdgcn_exp2f(gg.x), __builtin_amdgcn_exp2f(gg.y)});
            const f2 h = pmul(uu, f2{__builtin_amdgcn_rcpf(den.x), __builtin_amdgcn_rcpf(den.y)});
            P[c][e] = pkbf(h.x, h.y);
            { const int n = (t * 4 + c) * 2 + e; if (n < 25) Wr[n / 5][n % 5] = ldc128(rw, lane * 16, wdo + n * 1024); }
          }
          if (c & 1) SCHED();
        }
        if (t < 3) {
#pragma unroll
          for (int k = 0; k < 2; k++) {
            auto sx2 = __builtin_amdgcn_permlane16_swap(P[2 * k].x, P[2 * k + 1].x, false, false);
            auto sy2 = __builtin_amdgcn_permlane16_swap(P[2 * k].y, P[2 * k + 1].y, false, false);
            stc128(v4u{sx2[0], sy2[0], sx2[1], sy2[1]}, rw, hb + (k ^ (wv >> 1)) * 1024, (ts + 3 * wv + t) * 49152);
          }
        } else if (r5 != 0) {
          __builtin_amdgcn_raw_buffer_store_b64(P[0], rw, WS_HF + (2 * cj + (wv >> 1)) * 1024 + ((2 * (wv & 1) + (g >> 1)) * 16 + i) * 16 + (g & 1) * 8, (ts + 12) * 49152, AUX_SC1);
        }
        SCHED();
      }
      STAMP(3);
      sig0(rz, CTL + C_DONE + 160 + wv * 4);
      waitv<0>();
      if (lane == 0) stc32(tag, rw, WS_HFLAG + ((ri * 24 + cj) * 4 + wv) * 4);
      STAMP(4);
    } else {
      BARRIER();
      tag = rfl(ldsv(CTL + C_TAG));
      spin0(CTL + C_RDY, 1);
#pragma unroll
      for (int n = 0; n < 25; n++) Wr[n / 5][n % 5] = ldc128(rw, lane * 16, wdo + n * 1024);
    }
    const int tt = 4 * i2 + wv, tok = 16 * tt + i, d0 = 80 * j2 + 20 * g;
    f4 o[4][5];
    {
      const rsrc_t rd = mkr(p.dsk, D * 2);
      const v4u xa = __builtin_amdgcn_raw_buffer_load_b128(rx, (tok * D + d0) * 2, 0, 0), xb = __builtin_amdgcn_raw_buffer_load_b128(rx, (tok * D + d0) * 2 + 16, 0, 0);
      const v2u xc2 = __builtin_amdgcn_raw_buffer_load_b64(rx, (tok * D + d0) * 2 + 32, 0, 0);
      const v4u da = __builtin_amdgcn_raw_buffer_load_b128(rd, d0 * 2, 0, 0), db = __builtin_amdgcn_raw_buffer_load_b128(rd, d0 * 2 + 16, 0, 0);
      const v2u dc = __builtin_amdgcn_raw_buffer_load_b64(rd, d0 * 2 + 32, 0, 0);
      const v4u stv = ldc128(rw, WS_STG + tok * 16, 0);
      const float rs = __uint_as_float(stv.z), nb = fmul_(-__uint_as_float(stv.x), rs);
      const u32 xw[10] = {xa.x, xa.y, xa.z, xa.w, xb.x, xb.y, xb.z, xb.w, xc2.x, xc2.y}, dw[10] = {da.x, da.y, da.z, da.w, db.x, db.y, db.z, db.w, dc.x, dc.y};
#pragma unroll
      for (int k = 0; k < 10; k++) {
        const float x0 = bf2f(xw[k]), x1 = bfhi(xw[k]);
        o[0][k >> 1][2 * (k & 1)] = ffma_(ffma_(x0, rs, nb), bf2f(dw[k]), x0);
        o[0][k >> 1][2 * (k & 1) + 1] = ffma_(ffma_(x1, rs, nb), bfhi(dw[k]), x1);
      }
#pragma unroll
      for (int t = 0; t < 4; t++)
#pragma unroll
        for (int c = 0; c < 5; c++) { if (t > 0) o[t][c] = f4{0, 0, 0, 0}; asm volatile("" : "+v"(o[t][c])); }
    }
    { const u32 fo = WS_HFLAG + (24 * wv + lane % 24) * 4; poll_flags<6>(rw, fo + ((20 * i2 + 4) >> 6) * 384, fo + ((20 * i2 + 19) >> 6) * 384, tag); }
    STAMP(5);
    constexpr int HD = 3;
    v4u hr[12][4], Wl[2][5], Wz[5];
    u32 ho[4];
#pragma unroll
    for (int t = 0; t < 4; t++) ho[t] = WS_HF + (4 * i2 + (t ^ wv)) * 49152 + 12 * wv * 1024;
#define LOAD_H(k) do { _Pragma("unroll") for (int t = 0; t < 4; t++) hr[k][t] = ldc128(rw, lane * 16, ho[t] + (k) * 1024); } while (0)
#pragma unroll
    for (int k = 0; k < HD; k++) LOAD_H(k);
    SCHED();
    const u32 wl = sb + wd_off(wv) + lane * 16;
#pragma unroll
    for (int k = 0; k < 12; k++) {
      if (k == NWR - 1) spin0(CTL + C_WDFULL, 1);
#pragma unroll
      for (int c = 0; c < 5; c++) {
        const v4u w = k < NWR ? Wr[k < NWR ? k : 0][c] : (k < 11 ? Wl[k & 1][c] : Wz[c]);
#pragma unroll
        for (int t = 3; t >= 0; t--) {
          mma_bf(o[t][c], w, hr[k][t]); SCHED();
          if (t == 1 && k == 8) { Wz[c] = ldc128(rw, lane * 16, wdo + (55 + c) * 1024); SCHED(); }
          if (t == 2 && k + 1 >= NWR && k + 1 < 11) { Wl[(k + 1) & 1][c] = lds128(wl + ((k + 1 - NWR) * 5 + c) * 1024); SCHED(); }
          if (t == 3 && c < 4) {
            const int kn = k == 0 ? -1 : k + HD;
            if (kn > 0 && kn < 12) { hr[kn][c] = ldc128(rw, lane * 16, ho[c] + kn * 1024); SCHED(); }
          }
        }
      }
      if (k == 0) { LOAD_H(HD); SCHED(); }
    }
    asm volatile("s_nop 7"); SCHED();
    STAMP(6);
    {
#pragma unroll
      for (int t = 1; t < 4; t++)
#pragma unroll
        for (int c = 0; c < 5; c++) *(LAS f4*)(wl + ((t - 1) * 5 + c) * 1024) = o[t][c];
      ldsw(CTL + C_RED + wv * 4, 0);
      spin0(CTL + C_ZFULL, 1);
      STAMP(7);
      const int q = (lane >> 2) & 3, pp = lane & 3;
      v2u z[5];
#pragma unroll
      for (int c = 0; c < 5; c++) z[c] = trd(sb + LD_K + (20 * g + 4 * c + q) * 128 + wv * 32 + pp * 8);
#pragma unroll
      for (int c = 0; c < 5; c++) {
        o[0][c][0] = fadd_(o[0][c][0], bf2f(z[c].x)); o[0][c][1] = fadd_(o[0][c][1], bfhi(z[c].x));
        o[0][c][2] = fadd_(o[0][c][2], bf2f(z[c].y)); o[0][c][3] = fadd_(o[0][c][3], bfhi(z[c].y));
      }
      spin_zero4(CTL + C_RED);
      u32 ow[10];
#pragma unroll
      for (int c = 0; c < 5; c++) {
        f4 s[3];
#pragma unroll
        for (int t = 1; t < 4; t++) s[t - 1] = *(LAS f4*)(sb + wd_off(wv ^ t) + lane * 16 + ((t - 1) * 5 + c) * 1024);
        float y[4];
#pragma unroll
        for (int e = 0; e < 4; e++) y[e] = fadd_(fadd_(o[0][c][e], s[0][e]), fadd_(s[1][e], s[2][e]));
        ow[2 * c] = pkbf(y[0], y[1]); ow[2 * c + 1] = pkbf(y[2], y[3]);
      }
      const rsrc_t ro = mkr(p.out, L * D * 2);
      __builtin_amdgcn_raw_buffer_store_b128(v4u{ow[0], ow[1], ow[2], ow[3]}, ro, (tok * D + d0) * 2, 0, 0);
      __builtin_amdgcn_raw_buffer_store_b128(v4u{ow[4], ow[5], ow[6], ow[7]}, ro, (tok * D + d0) * 2 + 16, 0, 0);
      __builtin_amdgcn_raw_buffer_store_b64(v2u{ow[8], ow[9]}, ro, (tok * D + d0) * 2 + 32, 0, 0);
    }
    STAMP(8); CSTAMP(15);
  } else {
    if (g1) {
      int kx = rot + 1, kw = kx; u32 sw = WSLOT, sx = XSLOT;
#define ISSUE_X(K0, K1) do { _Pragma("unroll") for (int k = K0; k < K1; k++) dma4(rxs, vx[0], vx[1], vx[2], vx[3], kx * 128 + k * 40960, sb + LD_X + sx + k * 4096); } while (0)
#define ISSUE_XE() do { dma2(rxs, vx[0], vx[1], kx * 128 + 12 * 20480, sb + LD_X + sx + 12 * 2048); kx = kx == 9 ? 0 : kx + 1; sx += XSLOT; if (sx == NXS * XSLOT) sx = 0; } while (0)
#define ISSUE_W() do { const u32 o_ = kw * 64 * 3072; dma4(rg, v[0], v[1], v[2], v[3], o_, sb + LD_R + sw); dma4(ru, v[0], v[1], v[2], v[3], o_, sb + LD_R + sw + 8192); \
        dma4(rg, v[0], v[1], v[2], v[3], o_ + 32 * 3072, sb + LD_R + sw + 4096); dma4(ru, v[0], v[1], v[2], v[3], o_ + 32 * 3072, sb + LD_R + sw + 12288); \
        kw = kw == 9 ? 0 : kw + 1; sw += WSLOT; if (sw == NWS * WSLOT) sw = 0; } while (0)
      __builtin_amdgcn_s_sleep(1);
      ISSUE_W(); ISSUE_X(0, 6); ISSUE_XE(); ISSUE_W();
      BARRIER();
      __builtin_amdgcn_s_setprio(3);
#pragma unroll 1
      for (int n = 2; n < 10; n++) {
        bool pend = true;
        if (n >= 3 && !is_zero4(CTL + C_DONE + (n - 3) * 16)) { waitv<16>(); sig0(rz, CTL + C_FULL + (n - 1) * 4); pend = false; spin_zero4(CTL + C_DONE + (n - 3) * 16); }
        waitv<44>();
        ISSUE_X(0, 4);
        if (pend) { waitv<32>(); sig0(rz, CTL + C_FULL + (n - 1) * 4); }
        ISSUE_X(4, 6); ISSUE_XE();
        if (n < 9) ISSUE_W();
      }
      waitv<0>(); sig0(rz, CTL + C_FULL + 36);
      STAMP(12);
      spin_zero4(CTL + C_DONE + 9 * 16);
    } else BARRIER();
    __builtin_amdgcn_s_setprio(1);
    const u32 tag = rfl(ldsv(CTL + C_TAG));
    spin_zero4(CTL + C_KDONE);
    STAMP(23);
    const v4u rws = mkrs(p.ws, WS_BYTES);
    {
      const u32 vo = lane * 16;
#pragma unroll 1
      for (int k = 0; k < 24; k++) {
        const int w = k & 3, pc = k >> 2;
        const u32 go = WS_WDF + (j2 * 48 + 12 * w + 5 + pc) * 5120, lo = sb + wd_off(w) + pc * 5120;
        dma4c(rws, vo, vo, vo, vo, go, lo); dma1c(rws, vo, go + 4096, lo + 4096);
        waitv<WD_DEPTH>();
      }
      waitv<0>();
      sig0(rz, CTL + C_WDFULL);
    }
    STAMP(24);
    __builtin_amdgcn_s_setprio(3);
    poll_flags<8>(rw, WS_ZFLAG + j2 * 320 + lane * 4, WS_ZFLAG + j2 * 320 + 64 + lane * 4, tag);
    {
      u32 vz[4];
#pragma unroll
      for (int k = 0; k < 4; k++) vz[k] = (8 * k + (lane >> 3)) * 4096 + (lane & 7) * 16 - 1024 * k;
      const u32 go = WS_ZT + 80 * j2 * 4096 + i2 * 128;
      dma4c(rws, vz[0], vz[1], vz[2], vz[3], go, sb + LD_K); dma4c(rws, vz[0], vz[1], vz[2], vz[3], go + 32 * 4096, sb + LD_K + 4096);
      dma2c(rws, vz[0], vz[1], go + 64 * 4096, sb + LD_K + 8192);
      waitv<0>();
      sig0(rz, CTL + C_ZFULL);
      waitv<0>();
    }
    STAMP(25);
  }
}

extern "C" void kernel_launch(void* const* d_in, const int* in_sizes, int n_in, void* d_out, int out_size, void* d_ws, size_t ws_size, hipStream_t stream) {
  Params p;
  p.x = (const u16*)d_in[0]; p.lar = (const u16*)d_in[1]; p.aim = (const u16*)d_in[2]; p.cre = (const u16*)d_in[3]; p.cim = (const u16*)d_in[4];
  p.ldt = (const u16*)d_in[5]; p.dsk = (const u16*)d_in[6]; p.wg = (const u16*)d_in[8]; p.wu = (const u16*)d_in[9]; p.wd = (const u16*)d_in[10];
  p.out = (u16*)d_out; p.ws = (char*)d_ws;
  (void)hipFuncSetAttribute((const void*)k_main, hipFuncAttributeMaxDynamicSharedMemorySize, LDS_BYTES);
  k_main<<<256, 512, LDS_BYTES, stream>>>(p);
}
```

```cpp
#define DIAG 8
#include <hip/hip_runtime.h>
#pragma clang diagnostic ignored "-Wint-to-pointer-cast"
typedef unsigned short u16; typedef unsigned u32; typedef unsigned long long u64;
typedef u32 v4u __attribute__((ext_vector_type(4)));
typedef u32 v2u __attribute__((ext_vector_type(2)));
typedef float f4 __attribute__((ext_vector_type(4)));
typedef float f2 __attribute__((ext_vector_type(2)));
typedef short s4 __attribute__((ext_vector_type(4)));
typedef __bf16 bf2 __attribute__((ext_vector_type(2)));
typedef _Float16 hf2 __attribute__((ext_vector_type(2)));
#define LAS __attribute__((address_space(3)))
#define GAS __attribute__((address_space(1)))
#define DEV __device__ __forceinline__
typedef __amdgpu_buffer_rsrc_t rsrc_t;

constexpr int L = 2048, D = 640, NS = 192, F = 1536;
constexpr u32 WS_GEN = 0, WS_HFLAG = 1024, WS_ZFLAG = 5120, WS_LFLAG = 8192, WS_WFLAG = WS_LFLAG + 512, WS_STG = 16384, WS_CSP = 65536,
              WS_XT = 1u << 20, WS_WDF = 4u << 20, WS_HF = 6u << 20, WS_ZT = 13u << 20, WS_BYTES = 16u << 20;
constexpr u32 LD_K = 0, K_BYTES = 5120;
constexpr u32 LD_CS = LD_K + 3 * K_BYTES, LD_ST = LD_CS + 512, LD_CTL = LD_ST + 208 * 8, LD_R = LD_CTL + 512;
constexpr u32 WSLOT = 16384, NWS = 4, XSLOT = 26624, NXS = 3, LD_X = LD_R + NWS * WSLOT, LDS_BYTES = LD_X + NXS * XSLOT;
constexpr u32 LD_U = LD_R + 2 * WSLOT, U_BYTES = 4608;
constexpr u32 WD_BYTES = 30720;
DEV u32 wd_off(int w) { return w == 0 ? LD_R : LD_R + 3 * WSLOT + (w - 1) * WD_BYTES; }
static_assert(LD_R % 128 == 0 && LD_R + 3 * WSLOT + 3 * WD_BYTES <= LDS_BYTES && 3 * U_BYTES <= WSLOT && LDS_BYTES <= 163840, "lds");
constexpr u32 C_FULL = 0, C_WDFULL = 44, C_RDY = 48, C_ZFULL = 52, C_STF = 56, C_TAG = 60, C_KDONE = 64, C_RED = 80, C_DONE = 256;
constexpr int AUX_SC1 = 16;
constexpr u32 SPIN_CAP = 1u << 15;
#define WD_DEPTH 30
#define POLL_STAGE 8
#define CONV_PRIO 3

#define STAMP(k) do { if ((k) == 0) dg0 = __builtin_amdgcn_s_memrealtime(); if ((k) == DIAG) dgk = __builtin_amdgcn_s_memrealtime(); \
    if ((k) == 8 && dgk) { const u64 e_ = dg0 + 6000; while (__builtin_amdgcn_s_memrealtime() < e_) __builtin_amdgcn_s_sleep(4); } } while (0)
#define DG_DECL u64 dg0 = 0, dgk = 0; (void)dg0; (void)dgk;

struct Params {
  const u16 *x, *lar, *aim, *cre, *cim, *ldt, *dsk, *wg, *wu, *wd;
  u16* out; char* ws;
};

DEV float bf2f(u32 v) { return __uint_as_float(v << 16); }
DEV float bfhi(u32 v) { return __uint_as_float(v & 0xffff0000u); }
DEV u32 pkbf(float a, float b) { f2 v = {a, b}; return __builtin_bit_cast(u32, __builtin_convertvector(v, bf2)); }
DEV u32 pkhf(float a, float b) { f2 v = {a, b}; return __builtin_bit_cast(u32, __builtin_convertvector(v, hf2)); }
DEV float rbf(float a) { return bf2f(pkbf(a, a)); }
DEV void mma_bf(f4& c, v4u a, v4u b) { asm volatile("v_mfma_f32_16x16x32_bf16 %0, %1, %2, %0" : "+v"(c) : "v"(a), "v"(b)); }
DEV void mma_hf(f4& c, v4u a, v4u b) { asm volatile("v_mfma_f32_16x16x32_f16 %0, %1, %2, %0" : "+v"(c) : "v"(a), "v"(b)); }
DEV v2u trd(u32 a) { return __builtin_bit_cast(v2u, __builtin_amdgcn_ds_read_tr16_b64_v4i16((LAS s4*)a)); }
DEV v4u trd2(u32 a, u32 b) { v2u x = trd(a), y = trd(b); return v4u{x.x, x.y, y.x, y.y}; }
DEV v4u lds128(u32 a) { return *(LAS v4u*)a; }
DEV v2u lds64(u32 a) { return *(LAS v2u*)a; }
DEV u32 rfl(u32 v) { return __builtin_amdgcn_readfirstlane(v); }
DEV v4u mkrs(const void* p, u32 bytes) { u64 a = (u64)p; return v4u{(u32)a, (u32)(a >> 32) & 0xffffu, bytes, 0x00020000u}; }
DEV rsrc_t mkr(const void* p, u32 bytes) { return __builtin_amdgcn_make_buffer_rsrc((void*)p, 0, bytes, 0x00020000); }
DEV v4u ldc128(rsrc_t r, u32 v, u32 s) { return __builtin_amdgcn_raw_buffer_load_b128(r, v, s, AUX_SC1); }
DEV u32 ldc32(rsrc_t r, u32 v) { return __builtin_amdgcn_raw_buffer_load_b32(r, v, 0, AUX_SC1); }
DEV void stc128(v4u d, rsrc_t r, u32 v, u32 s) { __builtin_amdgcn_raw_buffer_store_b128(d, r, v, s, AUX_SC1); }
DEV void stc32(u32 d, rsrc_t r, u32 v) { __builtin_amdgcn_raw_buffer_store_b32(d, r, v, 0, AUX_SC1); }
#define CBAR() asm volatile("" ::: "memory")
#define SCHED() __builtin_amdgcn_sched_barrier(0)
#define ORDER() __builtin_amdgcn_fence(__ATOMIC_ACQUIRE, "wavefront")
#define BARRIER() do { asm volatile("s_waitcnt lgkmcnt(0)" ::: "memory"); __builtin_amdgcn_s_barrier(); CBAR(); } while (0)
template <int N> DEV void waitv() { asm volatile("s_waitcnt vmcnt(%0)" ::"i"(N) : "memory"); }
#define DMA_(V, RS, SO, OFF, SC) "buffer_load_dwordx4 " V ", " RS ", " SO " offen" OFF SC " lds\n\t"
#define DMA4_(NAME, SC) DEV void NAME(v4u rs, u32 v0, u32 v1, u32 v2, u32 v3, u32 so, u32 lds) { \
  asm volatile("s_mov_b32 m0, %0\n\ts_nop 0\n\t" DMA_("%1", "%5", "%6", "", SC) DMA_("%2", "%5", "%6", " offset:1024", SC) DMA_("%3", "%5", "%6", " offset:2048", SC) DMA_("%4", "%5", "%6", " offset:3072", SC) \
               ::"s"(lds), "v"(v0), "v"(v1), "v"(v2), "v"(v3), "s"(rs), "s"(so) : "memory"); }
#define DMA2_(NAME, SC) DEV void NAME(v4u rs, u32 v0, u32 v1, u32 so, u32 lds) { \
  asm volatile("s_mov_b32 m0, %0\n\ts_nop 0\n\t" DMA_("%1", "%3", "%4", "", SC) DMA_("%2", "%3", "%4", " offset:1024", SC) ::"s"(lds), "v"(v0), "v"(v1), "s"(rs), "s"(so) : "memory"); }
#define DMA1_(NAME, SC) DEV void NAME(v4u rs, u32 v0, u32 so, u32 lds) { \
  asm volatile("s_mov_b32 m0, %0\n\ts_nop 0\n\t" DMA_("%1", "%2", "%3", "", SC) ::"s"(lds), "v"(v0), "s"(rs), "s"(so) : "memory"); }
DMA4_(dma4, "") DMA2_(dma2, "") DMA1_(dma1, "") DMA4_(dma4c, " sc1") DMA2_(dma2c, " sc1") DMA1_(dma1c, " sc1")
DEV void sig0(v4u rz, u32 lds) {
  asm volatile("s_mov_b32 m0, %0\n\ts_mov_b64 exec, 1\n\tbuffer_load_dword off, %1, 0 lds\n\ts_mov_b64 exec, -1" ::"s"(lds), "s"(rz) : "memory");
}
DEV u32 ldsv(u32 a) { return *(volatile LAS u32*)a; }
DEV void ldsw(u32 a, u32 v) { asm volatile("s_mov_b64 exec, 1\n\tds_write_b32 %0, %1\n\ts_mov_b64 exec, -1" ::"v"(a), "v"(v) : "memory"); }
DEV void spin0(u32 a, u32 cur) {
  u32 tv, ts, cnt;
  asm volatile("s_cmp_eq_u32 %4, 0\n\ts_cbranch_scc0 3f\n2:\n\t.subsection 1\n3:\n\ts_mov_b32 %2, 0x10000\n"
               "1:\n\tds_read_b32 %0, %3\n\ts_waitcnt lgkmcnt(0)\n\tv_readfirstlane_b32 %1, %0\n\ts_nop 0\n\ts_cmp_eq_u32 %1, 0\n\ts_cbranch_scc1 2b\n\t"
               "s_sleep 1\n\ts_sub_u32 %2, %2, 1\n\ts_cmp_lg_u32 %2, 0\n\ts_cbranch_scc1 1b\n\ts_branch 2b\n\t.subsection 0"
               : "=&v"(tv), "=&s"(ts), "=&s"(cnt) : "v"(a), "s"(cur) : "memory", "scc");
}
DEV bool is_zero4(u32 a) { v4u d = *(volatile LAS v4u*)a; return rfl(d.x | d.y | d.z | d.w) == 0; }
DEV void spin_zero4(u32 a) {
#pragma unroll 1
  for (u32 i = 0; i < 2 * SPIN_CAP; i++) { if (is_zero4(a)) break; __builtin_amdgcn_s_sleep(1); } CBAR();
}
template <int SL> DEV void poll_flags(rsrc_t rw, u32 off, u32 off2, u32 tag) {
#pragma unroll 1
  for (u32 i = 0; i < SPIN_CAP; i++) {
    const u32 a = ldc32(rw, off), b = ldc32(rw, off2);
    if (__all(a == tag && b == tag)) break;
    __builtin_amdgcn_s_sleep(SL);
  }
  ORDER();
}
DEV float fmul_(float a, float b) { float r; asm("v_mul_f32 %0, %1, %2" : "=v"(r) : "v"(a), "v"(b)); return r; }
DEV float fadd_(float a, float b) { float r; asm("v_add_f32 %0, %1, %2" : "=v"(r) : "v"(a), "v"(b)); return r; }
DEV float ffma_(float a, float b, float c) { float r; asm("v_fma_f32 %0, %1, %2, %3" : "=v"(r) : "v"(a), "v"(b), "v"(c)); return r; }
DEV float fnma_(float a, float b, float c) { float r; asm("v_fma_f32 %0, -%1, %2, %3" : "=v"(r) : "v"(a), "v"(b), "v"(c)); return r; }
struct cf { float x, y; };
DEV cf csq(cf a) { const float t = fmul_(a.x, a.y); return cf{fnma_(a.y, a.y, fmul_(a.x, a.x)), fadd_(t, t)}; }
DEV u32 pkc(cf a) { return pkhf(a.x, a.y); }
DEV cf cexp2(float lr, float rev) { float m = __builtin_amdgcn_exp2f(lr); const float f = __builtin_amdgcn_fractf(rev); float c = __builtin_amdgcn_cosf(f), s = __builtin_amdgcn_sinf(f);
  asm volatile("s_nop 0" : "+v"(c), "+v"(s), "+v"(m)); return cf{fmul_(m, c), fmul_(m, s)}; }
DEV u32 hmul(u32 a, u32 m) { u32 t, r;
  asm("v_pk_mul_f16 %0, %1, %2 op_sel_hi:[0,1]" : "=v"(t) : "v"(a), "v"(m));
  asm("v_pk_fma_f16 %0, %1, %2, %3 op_sel:[1,1,0] op_sel_hi:[1,0,1] neg_lo:[0,1,0]" : "=v"(r) : "v"(a), "v"(m), "v"(t)); return r; }
DEV u32 hmulc(u32 a, u32 m) { u32 t, r;
  asm("v_pk_mul_f16 %0, %1, %2 op_sel_hi:[0,1] neg_hi:[0,1]" : "=v"(t) : "v"(a), "v"(m));
  asm("v_pk_fma_f16 %0, %1, %2, %3 op_sel:[1,1,0] op_sel_hi:[1,0,1]" : "=v"(r) : "v"(a), "v"(m), "v"(t)); return r; }
DEV f2 pfma_x(f2 s, f2 b, f2 c) { f2 r; asm("v_pk_fma_f32 %0, %1, %2, %3 op_sel_hi:[0,1,1]" : "=v"(r) : "v"(s), "v"(b), "v"(c)); return r; }
DEV f2 pmul_y(f2 s, f2 b) { f2 r; asm("v_pk_mul_f32 %0, %1, %2 op_sel:[1,0]" : "=v"(r) : "v"(s), "v"(b)); return r; }
DEV f2 pfma_xx(f2 s, f2 b) { f2 r; asm("s_nop 0\n\tv_pk_fma_f32 %0, %1, %2, %1 op_sel_hi:[0,1,0]" : "=v"(r) : "v"(s), "v"(b)); return r; }
DEV f2 pmul(f2 a, f2 b) { f2 r; asm("s_nop 0\n\tv_pk_mul_f32 %0, %1, %2" : "=v"(r) : "v"(a), "v"(b)); return r; }
DEV void swap16(float& a, float& b) { auto r = __builtin_amdgcn_permlane16_swap(__float_as_uint(a), __float_as_uint(b), false, false); a = __uint_as_float(r[0]); b = __uint_as_float(r[1]); }
DEV void swap32(float& a, float& b) { auto r = __builtin_amdgcn_permlane32_swap(__float_as_uint(a), __float_as_uint(b), false, false); a = __uint_as_float(r[0]); b = __uint_as_float(r[1]); }

DEV void ln_wave(const Params& p, u32 sb, int pn, int lane, int wv) {
  DG_DECL
  const rsrc_t rx = mkr(p.x, L * D * 2), rw = mkr(p.ws, WS_BYTES);
  const int h = lane & 1, m = lane >> 1, row0 = 64 * (pn >> 2) + (pn & 3);
  __builtin_amdgcn_s_setprio(3);
  v2u xv[8][5];
#pragma unroll
  for (int e = 0; e < 8; e++)
#pragma unroll
    for (int i = 0; i < 5; i++) xv[e][i] = __builtin_amdgcn_raw_buffer_load_b64(rx, (row0 + 32 * h) * 1280 + m * 8 + i * 256, e * 5120, 0);
  float s[16];
#pragma unroll
  for (int e = 0; e < 8; e++) {
    float a = 0.f, q = 0.f;
#pragma unroll
    for (int i = 0; i < 10; i++) {
      const u32 w = xv[e][i >> 1][i & 1];
      const bf2 v = __builtin_bit_cast(bf2, w);
      a = __builtin_amdgcn_fdot2_f32_bf16(v, __builtin_bit_cast(bf2, 0x3f803f80u), a, false);
      q = __builtin_amdgcn_fdot2_f32_bf16(v, v, q, false);
    }
    s[e] = a; s[8 + e] = q;
  }
#pragma unroll
  for (int e = 0; e < 16; e++) asm volatile("" : "+v"(s[e]));
  asm volatile("s_nop 2");
  BARRIER();
  const u32 tag = rfl(ldsv(sb + LD_CTL + C_TAG));
  float z[4];
#pragma unroll
  for (int k = 0; k < 4; k++) {
    swap16(s[4 * k], s[4 * k + 1]); swap16(s[4 * k + 2], s[4 * k + 3]);
    float a = fadd_(s[4 * k], s[4 * k + 1]), b = fadd_(s[4 * k + 2], s[4 * k + 3]);
    asm volatile("s_nop 1" : "+v"(a), "+v"(b));
    swap32(a, b); z[k] = fadd_(a, b);
  }
#define DPPADD(C) asm volatile("s_nop 1\n\tv_add_f32_dpp %0, %0, %0 " C " row_mask:0xf bank_mask:0xf\n\tv_add_f32_dpp %1, %1, %1 " C " row_mask:0xf bank_mask:0xf\n\t" \
    "v_add_f32_dpp %2, %2, %2 " C " row_mask:0xf bank_mask:0xf\n\tv_add_f32_dpp %3, %3, %3 " C " row_mask:0xf bank_mask:0xf" : "+v"(z[0]), "+v"(z[1]), "+v"(z[2]), "+v"(z[3]))
  DPPADD("quad_perm:[2,3,0,1]"); DPPADD("row_ror:4"); DPPADD("row_ror:8");
  float rs[8], nb[8];
#pragma unroll
  for (int k = 0; k < 2; k++) {
    const float mean = fmul_(z[k], 1.f / D), var = fmaxf(fnma_(mean, mean, fmul_(z[2 + k], 1.f / D)), 0.f);
    const float mb = rbf(mean), sd = rbf(__builtin_amdgcn_sqrtf(rbf(rbf(var) + 1e-5f)));
    float r = __builtin_amdgcn_rcpf(sd);
    asm volatile("s_nop 0" : "+v"(r));
    r = ffma_(fnma_(sd, r, 1.f), r, r);
    if ((lane & 15) < 2) stc128(v4u{__float_as_uint(mb), tag, __float_as_uint(r), tag}, rw, WS_STG + (row0 + 32 * h + 4 * (4 * k + (lane >> 4))) * 16, 0);
    float t[2] = {r, fmul_(-mb, r)};
#pragma unroll
    for (int w = 0; w < 2; w++) {
      float a = t[w], c = t[w];
      asm volatile("s_nop 1" : "+v"(a), "+v"(c));
      swap32(a, c); float b = a, d = c; swap16(a, b); swap16(c, d);
      float* o = w ? nb : rs; o[4 * k] = a; o[4 * k + 1] = b; o[4 * k + 2] = c; o[4 * k + 3] = d;
    }
  }
#pragma unroll
  for (int i = 0; i < 5; i++)
#pragma unroll
    for (int c = 0; c < 4; c++) {
      float y[8];
#pragma unroll
      for (int e = 0; e < 8; e++) { const u32 w = xv[e][i][c >> 1]; y[e] = ffma_((c & 1) ? bfhi(w) : bf2f(w), rs[e], nb[e]); }
      stc128(v4u{pkhf(y[0], y[1]), pkhf(y[2], y[3]), pkhf(y[4], y[5]), pkhf(y[6], y[7])}, rw, WS_XT + lane * 16, pn * 20480 + (i * 4 + c) * 1024);
    }
  waitv<0>();
  if (lane == 0) stc32(tag, rw, WS_LFLAG + pn * 4);
  STAMP(21);
}

template <bool CJ> DEV void grp8(u32* v, u32 a, u32 b, u32 c) {
#define HM(x, y) (CJ ? hmulc(x, y) : hmul(x, y))
  v[1] = HM(v[0], a); v[2] = HM(v[0], b); v[3] = HM(v[1], b); v[4] = HM(v[0], c); v[5] = HM(v[1], c); v[6] = HM(v[2], c); v[7] = HM(v[3], c);
#undef HM
}
template <int N, int O> DEV void wr_rows(const u32* v) {
  if constexpr (N > 0) { asm volatile("ds_write_addtid_b32 %0 offset:%1" ::"v"(v[0]), "i"(O * 272) : "memory"); wr_rows<N - 1, O + 1>(v + 1); }
}
DEV void conv_wave(const Params& p, u32 sb, int slot, int ch, int lane, int wv, bool g1, int ts, int cj) {
  DG_DECL
  const u32 KB = sb + LD_K + slot * K_BYTES, UB = sb + LD_U + slot * U_BYTES, CTL = sb + LD_CTL;
  const int g = lane >> 4, q = (lane >> 2) & 3, pp = lane & 3, j = lane & 15, b = blockIdx.x;
  const rsrc_t rw = mkr(p.ws, WS_BYTES);
  const v4u rz = mkrs(p.ws, 0);
  u32 gen = 0;
  if (slot == 0) gen = ldc32(rw, WS_GEN);
  const int wu = slot == 0 ? b : (slot == 1 && b < 128 ? 256 + b : -1);
  if (wu >= 0) {
    u32 vo[5];
#pragma unroll
    for (int k = 0; k < 5; k++) { const int n = k * 64 + lane; vo[k] = (n / 40) * 1280 + (n % 40) * 16 - (k & 3) * 1024; }
    const v4u rd = mkrs(p.wd, F * D * 2);
    const u32 so = (wu >> 1) * 10240 + (wu & 1) * 640;
    dma4(rd, vo[0], vo[1], vo[2], vo[3], so, KB); dma1(rd, vo[4], so, KB + 4096);
  }
  u32 la[3], ai[3], cr[3], ci[3];
#pragma unroll
  for (int c = 0; c < 3; c++) { const int o = ch * NS + 64 * c + lane; la[c] = p.lar[o]; ai[c] = p.aim[o]; cr[c] = p.cre[o]; ci[c] = p.cim[o]; }
  const u32 ldt = p.ldt[ch];
  if (slot == 0) {
    const int nch = ((b >> 3) & 1) ? 2 : 3;
    const bool one = (g1 && (lane < 10 || lane == 14)) || (lane >= 11 && lane <= 13) || (lane >= 16 && lane < 16 + nch) || (lane >= 20 && lane < 24);
    *(volatile LAS u32*)(CTL + lane * 4) = lane == 15 ? gen * 0x9E3779B1u + 0x7F4A7C15u : (one ? 1u : 0u);
  }
  if (slot == 1) *(volatile LAS u32*)(CTL + C_DONE + lane * 4) = g1 ? 1u : 0u;
  BARRIER();
  const u32 tag = rfl(ldsv(CTL + C_TAG));
  if (wu >= 0) {
    waitv<0>();
    __builtin_amdgcn_s_setprio(3);
    const u32 ra = KB + q * 640 + (80 * g + 20 * pp) * 2;
    const u32 wo = WS_WDF + ((4 * (wu & 1) + g) * 48 * 5) * 1024 + j * 16, so = (wu >> 3) * 5120 + ((wu >> 1) & 3) * 256;
#pragma unroll
    for (int k = 0; k < 5; k++) stc128(trd2(ra + k * 8, ra + k * 8 + 2560), rw, wo + k * 1024, so);
  }
  const float dt = rbf(__expf(bf2f(ldt)));
  u32 mx = 0;
#pragma unroll
  for (int c = 0; c < 3; c++) mx = max(mx, max(cr[c] & 0x7fffu, ci[c] & 0x7fffu));
  mx = max(mx, (u32)__builtin_amdgcn_update_dpp(0, (int)mx, 0x111, 0xf, 0xf, false));
  mx = max(mx, (u32)__builtin_amdgcn_update_dpp(0, (int)mx, 0x112, 0xf, 0xf, false));
  mx = max(mx, (u32)__builtin_amdgcn_update_dpp(0, (int)mx, 0x114, 0xf, 0xf, false));
  mx = max(mx, (u32)__builtin_amdgcn_update_dpp(0, (int)mx, 0x118, 0xf, 0xf, false));
  mx = max(max((u32)__builtin_amdgcn_readlane(mx, 15), (u32)__builtin_amdgcn_readlane(mx, 31)), max((u32)__builtin_amdgcn_readlane(mx, 47), (u32)__builtin_amdgcn_readlane(mx, 63)));
  STAMP(16);
  const u32 ex = min(max(mx >> 7, 1u), 252u);
  const float cscale = __uint_as_float((253u - ex) << 23);
  const float oscale = 2.f * dt * __uint_as_float((ex + 1u) << 23);
  u32 G[3][57];
#pragma unroll
  for (int c = 0; c < 3; c++) {
    const float zr = fmul_(fmul_(rbf(__expf(bf2f(la[c]))), dt), -1.44269504089f), zi = fmul_(fmul_(bf2f(ai[c]), dt), 0.15915494309f);
    const cf P0 = cexp2(zr, zi), P4 = cexp2(fmul_(zr, 16.f), fmul_(zi, 16.f)), P8 = cexp2(fmul_(zr, 256.f), fmul_(zi, 256.f));
    const cf P1 = csq(P0), P2 = csq(P1), P3 = csq(P2), P5 = csq(P4), P6 = csq(P5), P7 = csq(P6), P9 = csq(P8), P10 = csq(P9), P11 = csq(P10);
    const u32 M0 = pkc(P0), M1 = pkc(P1), M2 = pkc(P2), M3 = pkc(P3), M4 = pkc(P4), M5 = pkc(P5), M7 = pkc(P7), M8 = pkc(P8), M9 = pkc(P9);
    u32* v = G[c];
    const float cx = fmul_(bf2f(cr[c]), cscale), cy = fmul_(bf2f(ci[c]), -cscale);
    v[0] = pkhf(cx, cy); grp8<true>(v, M3, M4, M5);
    v[8] = pkhf(ffma_(cy, P6.y, fmul_(cx, P6.x)), fnma_(cx, P6.y, fmul_(cy, P6.x))); grp8<true>(v + 8, M3, M4, M5);
    v[16] = 0x00003c00u; v[17] = M0; v[18] = M1; v[19] = hmul(M0, M1); v[20] = M2; v[21] = hmul(M0, M2); v[22] = hmul(M1, M2); v[23] = hmul(v[19], M2);
    v[24] = M7; grp8<false>(v + 24, M0, M1, M2);
    v[32] = M8; grp8<false>(v + 32, M1, M2, M7);
    v[40] = M9; grp8<false>(v + 40, M2, M7, M8);
    v[48] = pkc(P10); grp8<false>(v + 48, M7, M8, M9);
    v[56] = pkc(P11);
#pragma unroll
    for (int k = 0; k < 57; k++) asm volatile("" : "+v"(v[k]));
    if (c == 0 && wu >= 0) { waitv<0>(); if (lane == 0) stc32(tag, rw, WS_WFLAG + wu * 4); __builtin_amdgcn_s_setprio(0); }
  }
  STAMP(13);
  float cs0 = 0.f, cs1 = 0.f; f2 stv[4];
  if (g1) spin0(CTL + C_FULL + POLL_STAGE * 4, 1); else { __builtin_amdgcn_s_sleep(100); }
  __builtin_amdgcn_s_setprio(3);
  if (slot == 0) {
#pragma unroll 1
    for (u32 it = 0; it < SPIN_CAP; it++) {
      const v4u a = ldc128(rw, WS_LFLAG + lane * 16, 0), c = ldc128(rw, WS_LFLAG + lane * 16, 1024);
      if (__all(a.x == tag && a.y == tag && a.z == tag && a.w == tag && c.x == tag && c.y == tag && c.z == tag && c.w == tag)) break;
      __builtin_amdgcn_s_sleep(24);
    }
    ORDER();
    sig0(rz, CTL + C_RDY);
    if (b == 0 && lane == 0) stc32(tag, rw, WS_GEN);
  } else if (slot == 1 && g1) {
#pragma unroll 1
    for (u32 it = 0; it < SPIN_CAP; it++) {
      v4u c[10]; bool ok = true; cs0 = cs1 = 0.f;
#pragma unroll
      for (int k = 0; k < 10; k++) c[k] = ldc128(rw, WS_CSP + lane * 16, (cj * 10 + k) * 1024);
#pragma unroll
      for (int k = 0; k < 10; k++) { ok = ok && c[k].y == tag && c[k].w == tag; cs0 = fadd_(cs0, __uint_as_float(c[k].x)); cs1 = fadd_(cs1, __uint_as_float(c[k].z)); }
      if (__all(ok)) break;
      __builtin_amdgcn_s_sleep(24);
    }
#pragma unroll 1
    for (u32 it = 0; it < SPIN_CAP; it++) {
      bool ok = true;
#pragma unroll
      for (int k = 0; k < 4; k++) { const int n = k * 64 + lane; const v4u c = ldc128(rw, WS_STG + (ts * 16 + (n < 208 ? n : 0)) * 16, 0);
        ok = ok && c.y == tag && c.w == tag; stv[k] = f2{__uint_as_float(c.x), __uint_as_float(c.z)}; }
      if (__all(ok)) break;
      __builtin_amdgcn_s_sleep(24);
    }
  }
  STAMP(22);
  __builtin_amdgcn_s_setprio(0);
  spin0(CTL + C_FULL + 36, 1);
  STAMP(17);
  if (slot == 1 && g1) {
#pragma unroll
    for (int k = 0; k < 4; k++) { const int n = k * 64 + lane; if (n < 208) *(LAS f2*)(sb + LD_ST + n * 8) = stv[k]; }
    *(LAS f2*)(sb + LD_CS + lane * 8) = f2{cs0, cs1};
    CBAR();
    ldsw(CTL + C_STF, 0);
  }
  spin0(CTL + C_RDY, 1);
  {
    const v4u rws = mkrs(p.ws, WS_BYTES);
    const u32 co = WS_XT + ((ch >> 7) * 4 + (ch & 3)) * 1024 + ((ch & 127) >> 2) * 32;
    u32 vo[5];
#pragma unroll
    for (int k = 0; k < 5; k++) { const int n = k * 64 + lane, tau = n / 72, a = n % 72 - 8; vo[k] = a >= 0 ? co + ((a >> 1) * 4 + tau) * 20480 + (a & 1) * 16 - (k & 3) * 1024 : 0x80000000u; }
    dma4c(rws, vo[0], vo[1], vo[2], vo[3], 0, UB);
    asm volatile("s_mov_b32 m0, %0\n\ts_mov_b32 exec_hi, 0\n\tbuffer_load_dwordx4 %1, %2, 0 offen sc1 lds\n\ts_mov_b32 exec_hi, -1" ::"s"(UB + 4096), "v"(vo[4]), "s"(rws) : "memory");
  }
  f4 kd[3] = {f4{0, 0, 0, 0}, f4{0, 0, 0, 0}, f4{0, 0, 0, 0}};
  {
    const u32 rb = KB + j * 272 + g * 16;
    asm volatile("s_mov_b32 m0, %3\n\ts_nop 0" : "+v"(kd[0]), "+v"(kd[1]), "+v"(kd[2]) : "s"(KB) : "memory");
    v4u Fr[12][4];
#pragma unroll
    for (int ps = 0; ps <= 12; ps++) {
      if (ps < 12) {
        const int c = ps >> 2, t = ps & 3;
        if (t < 3) wr_rows<16, 0>(G[c] + 16 * t); else wr_rows<9, 0>(G[c] + 48);
        asm volatile("ds_read_b128 %0, %4\n\tds_read_b128 %1, %4 offset:64\n\tds_read_b128 %2, %4 offset:128\n\tds_read_b128 %3, %4 offset:192"
                     : "=&v"(Fr[ps][0]), "=&v"(Fr[ps][1]), "=&v"(Fr[ps][2]), "=&v"(Fr[ps][3]) : "v"(rb) : "memory");
      }
      if (ps > 0) {
        const int pv = ps - 1;
        if (ps < 12) asm volatile("s_waitcnt lgkmcnt(13)" : "+v"(Fr[pv][0]), "+v"(Fr[pv][1]), "+v"(Fr[pv][2]), "+v"(Fr[pv][3]));
        else asm volatile("s_waitcnt lgkmcnt(0)" : "+v"(Fr[pv][0]), "+v"(Fr[pv][1]), "+v"(Fr[pv][2]), "+v"(Fr[pv][3]));
        if (pv & 3) {
#pragma unroll
          for (int ks = 0; ks < 4; ks++) mma_hf(kd[(pv & 3) - 1], Fr[pv][ks], Fr[pv & ~3][ks]);
        }
      }
    }
    asm volatile("s_nop 7" : "+v"(kd[0]), "+v"(kd[1]), "+v"(kd[2]));
  }
  STAMP(9);
  {
    const u32 K0 = KB + 32;
    const u32 a00 = pkhf(kd[1][0], kd[1][0]), a11 = pkhf(kd[1][1], kd[1][1]), a22 = pkhf(kd[1][2], kd[1][2]), a33 = pkhf(kd[1][3], kd[1][3]);
    const u32 a12 = pkhf(kd[1][1], kd[1][2]), a01 = pkhf(kd[1][0], kd[1][1]), a23 = pkhf(kd[1][2], kd[1][3]);
    v2u bb[4];
#pragma unroll
    for (int r = 0; r < 4; r++) { bb[r].x = pkhf(kd[2][r], kd[2][r]); bb[r].y = bb[r].x; }
    const v2u r0 = {pkhf(kd[0][0], kd[0][1]), pkhf(kd[0][2], kd[0][3])}, w1 = {a00, a12}, w2a = {a00, a01}, w2b = {a22, a23};
    const v4u z4 = {0, 0, 0, 0};
    const u32 p3 = K0 + 2 * (1024 + 512 * g + 8 * j) - 8, p2 = K0 + 2 * (256 * g + 8 * j) - 4, p1 = K0 + 2 * (256 + 128 * g + 8 * j) - 2;
    const u32 p0 = K0 + 2 * (128 * (g >> 1) + 8 * j + 4 * (g & 1)), pz = KB + lane * 16;
    asm volatile(
      "s_mov_b32 exec_hi, 1\n\t"
      "ds_write2_b64 %0, %5, %5 offset1:1\n\t"
      "s_mov_b32 exec_hi, 0\n\t"
      "ds_write2_b64 %0, %6, %6 offset0:32 offset1:33\n\t"
      "ds_write2_b64 %0, %7, %7 offset0:64 offset1:65\n\t"
      "ds_write2_b64 %0, %8, %8 offset0:96 offset1:97\n\t"
      "s_mov_b32 exec_lo, 0x55555555\n\t"
      "ds_write_b16 %0, %9 offset:16\n\tds_write_b16 %0, %10 offset:272\n\tds_write_b16 %0, %11 offset:528\n\tds_write_b16 %0, %12 offset:784\n\t"
      "s_mov_b32 exec_lo, 0\n\ts_mov_b32 exec_hi, -1\n\t"
      "ds_write_b32 %1, %13\n\tds_write_b64 %1, %17 offset:4\n\tds_write_b32 %1, %14 offset:12\n\t"
      "ds_write_b32 %1, %15 offset:256\n\tds_write_b64 %1, %18 offset:260\n\tds_write_b32 %1, %16 offset:268\n\t"
      "s_mov_b32 exec_lo, -1\n\ts_mov_b32 exec_hi, 0\n\t"
      "ds_write_b16 %2, %13\n\tds_write_b64 %2, %19 offset:2\n\tds_write_b32 %2, %15 offset:10\n\tds_write_b16 %2, %16 offset:14\n\t"
      "s_mov_b32 exec_lo, 3\n\t"
      "ds_write_b128 %4, %21\n\t"
      "s_mov_b64 exec, -1\n\t"
      "ds_write_b64 %3, %20"
      ::"v"(p3), "v"(p2), "v"(p1), "v"(p0), "v"(pz), "v"(bb[0]), "v"(bb[1]), "v"(bb[2]), "v"(bb[3]), "v"(bb[0].x), "v"(bb[1].x), "v"(bb[2].x), "v"(bb[3].x),
        "v"(a00), "v"(a11), "v"(a22), "v"(a33), "v"(w2a), "v"(w2b), "v"(w1), "v"(r0), "v"(z4) : "memory");
  }
  v4u A[16];
  {
    const u32 ab = KB + 32 + 2 * (-12 + 4 * (8 * g + 7 - q) + 4 * pp);
#pragma unroll
    for (int s = 0; s < 16; s++) A[s] = trd2(ab + 256 * s, ab + 256 * s - 32);
  }
  ldsw(CTL + C_KDONE + slot * 4, 0);
  f4 acc[4][8];
#pragma unroll
  for (int t = 0; t < 4; t++)
#pragma unroll
    for (int T = 0; T < 8; T++) { acc[t][T] = f4{0, 0, 0, 0}; asm volatile("" : "+v"(acc[t][T])); }
  STAMP(10);
  waitv<0>();
  spin0(CTL + C_DONE + 160 + slot * 4, 1);
  __builtin_amdgcn_s_setprio(CONV_PRIO);
  STAMP(18);
  const u32 ub = UB + 128 + 2 * (4 * j - 4 - 8 * g) - 64;
  v4u B[4];
#define QORD(n) (((n) & 1) ? 15 - (((n) & 15) >> 1) : (((n) & 15) >> 1))
#define LDB(n) do { const u32 a_ = ub + ((n) >> 4) * 1152 + 64 * QORD(n); const v2u x_ = lds64(a_); SCHED(); const v2u y_ = lds64(a_ + 8); B[(n) & 3] = v4u{x_.x, x_.y, y_.x, y_.y}; SCHED(); } while (0)
  LDB(0); LDB(1); LDB(2);
#pragma unroll
  for (int n = 0; n < 64; n++) {
    const int q2 = QORD(n) - 1;
    if (n + 3 < 64) LDB(n + 3);
#pragma unroll
    for (int T = 7; T >= 0; T--) { const int s = 2 * T - q2; if (s >= 0 && s <= 2 * T + 1) mma_hf(acc[n >> 4][T], A[s], B[n & 3]); }
    SCHED();
  }
#pragma unroll
  for (int T = 0; T < 8; T++) asm volatile("s_nop 0" : "+v"(acc[3][T]));
  STAMP(19);
  const int srcl = (lane == 0 ? 63 : (g > 0 ? lane - 16 : lane + 47)) * 4;
  v2u zp[8]; float pc[3] = {0.f, 0.f, 0.f};
#pragma unroll
  for (int T = 0; T < 8; T++) {
    const float c[3] = {fadd_(fadd_(acc[1][T][3], acc[2][T][2]), acc[3][T][1]), fadd_(acc[2][T][3], acc[3][T][2]), acc[3][T][3]};
    float y[4] = {acc[0][T][0], fadd_(acc[0][T][1], acc[1][T][0]), fadd_(fadd_(acc[0][T][2], acc[1][T][1]), acc[2][T][0]),
                  fadd_(fadd_(acc[0][T][3], acc[1][T][2]), fadd_(acc[2][T][1], acc[3][T][0]))};
#pragma unroll
    for (int r = 0; r < 3; r++) {
      const float cv = lane == 63 ? pc[r] : c[r];
      y[r] = fadd_(y[r], __int_as_float(__builtin_amdgcn_ds_bpermute(srcl, __float_as_int(cv))));
      pc[r] = c[r];
    }
    zp[T] = v2u{pkbf(fmul_(y[0], oscale), fmul_(y[1], oscale)), pkbf(fmul_(y[2], oscale), fmul_(y[3], oscale))};
    if (T & 1) {
      auto sx = __builtin_amdgcn_permlane16_swap(zp[T - 1].x, zp[T].x, false, false);
      auto sy = __builtin_amdgcn_permlane16_swap(zp[T - 1].y, zp[T].y, false, false);
      stc128(v4u{sx[0], sy[0], sx[1], sy[1]}, rw, WS_ZT + (g & 1) * 512 + j * 32 + (g >> 1) * 16, ch * 4096 + (T - 1) * 512);
    }
  }
  STAMP(20);
  waitv<0>();
  if (lane == 0) stc32(tag, rw, WS_ZFLAG + ch * 4);
  STAMP(11);
}

__global__ __launch_bounds__(512, 2) void k_main(Params p) {
  extern __shared__ __attribute__((aligned(16))) char smem[];
  const u32 sb = (u32)(size_t)(LAS char*)smem;
  const int lane = threadIdx.x & 63, wv = rfl(threadIdx.x >> 6);
  const int b = blockIdx.x, xc = b & 7, r = b >> 3;
  const bool g1 = r < 30;
  const int r5 = r / 6, ri = (xc >> 2) * 5 + r5, cj = (xc & 3) * 6 + r % 6, ts = (ri * 64) / 5;
  const int i2 = xc * 4 + (r >> 3), j2 = r & 7;
  const u32 CTL = sb + LD_CTL;
  asm volatile(".subsection 2\n\t.fill 128, 4, 0xBF800000\n\t.subsection 0");
  DG_DECL
  STAMP(0);
#define CSTAMP(k)
  if (wv >= 4 && wv < 7) {
    const int slot = wv - 4;
    if (slot == 2 && (r & 1)) ln_wave(p, sb, (r >> 1) * 8 + xc, lane, wv);
    else conv_wave(p, sb, slot, 80 * xc + 5 * (r >> 1) + (r & 1) * 3 + slot, lane, wv, g1, ts, cj);
    return;
  }
  const int g = lane >> 4, i = lane & 15;
  const rsrc_t rw = mkr(p.ws, WS_BYTES);
  const v4u rz = mkrs(p.ws, 0);
  const int rot = (xc >> 2) * 5;
  const v4u rg = mkrs(p.wg, D * F * 2), ru = mkrs(p.wu, D * F * 2), rxs = mkrs(p.x, L * D * 2);
  u32 v[4], vx[4];
#pragma unroll
  for (int k = 0; k < 4; k++) {
    const int row = 8 * k + (lane >> 3), fp = ((row >> 1) & 1) | (((row >> 3) & 1) << 1);
    v[k] = row * 3072 + cj * 128 + (((lane & 7) ^ (fp << 1)) << 4) - 1024 * k;
    vx[k] = (ts * 16 + row) * 1280 + (((lane & 7) ^ ((row >> 1) & 7)) << 4) - 1024 * k;
  }
  if (wv < 4) {
    __builtin_amdgcn_s_setprio(2);
    if (g1) {
      dma4((wv & 1) ? ru : rg, v[0], v[1], v[2], v[3], (rot * 64 + (wv >> 1) * 32) * 3072, sb + LD_R + (wv & 1) * 8192 + (wv >> 1) * 4096);
      dma4(rxs, vx[0], vx[1], vx[2], vx[3], rot * 128 + 3 * wv * 20480, sb + LD_X + 3 * wv * 2048);
      if (wv < 3) dma2(rxs, vx[0], vx[1], rot * 128 + (3 * wv + 2) * 20480, sb + LD_X + (3 * wv + 2) * 2048);
      else dma4(rxs, vx[0], vx[1], vx[2], vx[3], rot * 128 + 11 * 20480, sb + LD_X + 11 * 2048);
    }
    const rsrc_t rx = mkr(p.x, L * D * 2);
    constexpr int NWR = 5;
    v4u Wr[NWR][5];
    const u32 wdo = WS_WDF + (j2 * 48 + 12 * wv) * 5120;
    u32 tag;
    if (g1) {
      f4 acc[3][8], a13[2];
#pragma unroll
      for (int t = 0; t < 3; t++)
#pragma unroll
        for (int c = 0; c < 8; c++) { acc[t][c] = f4{0, 0, 0, 0}; asm volatile("" : "+v"(acc[t][c])); }
      a13[0] = a13[1] = f4{0, 0, 0, 0};
      const int q = (lane >> 2) & 3, pp = lane & 3, fp = ((q >> 1) & 1) | ((g & 1) << 1);
      u32 wb[4];
#pragma unroll
      for (int c = 0; c < 4; c++) wb[c] = sb + LD_R + (8 * g + q) * 128 + ((c ^ wv ^ fp) * 32) + pp * 8;
      u32 xb0 = sb + LD_X + wv * 6144 + i * 128 + ((g ^ (i >> 1)) << 4), xb1 = xb0 ^ 64;
      u32 yb0 = xb0 + (12 - 3 * wv) * 2048, yb1 = yb0 ^ 64, fa = CTL + C_FULL + 4;
      v4u Wf[2][8], Xf[2][4];
      asm volatile("" : "+v"(a13[0]), "+v"(a13[1]), "+v"(wb[0]), "+v"(wb[1]), "+v"(wb[2]), "+v"(wb[3]), "+v"(xb0), "+v"(xb1), "+v"(yb0), "+v"(yb1), "+v"(fa));
#define RDP(B, n, XA, YA, OFF) do { if ((n) == 0) { Xf[B][0] = lds128(XA); Xf[B][1] = lds128(XA + 2048); } \
        else if ((n) == 2) { Xf[B][2] = lds128(XA + 4096); Xf[B][3] = lds128(YA); } \
        else { constexpr int c = (n) == 1 ? 0 : (n) - 2; const u32 a = wb[c & 3] + (OFF) + (c >> 2) * 8192; Wf[B][c] = trd2(a, a + 512); } SCHED(); } while (0)
#define MM(KS, m) do { constexpr int c = (m) < 4 ? 0 : (m) < 13 ? 1 + ((m) - 4) / 3 : (m) < 17 ? 4 : 5 + ((m) - 17) / 3; \
        constexpr int t = (m) < 4 ? (m) : (m) < 13 ? ((m) - 4) % 3 : (m) < 17 ? (m) - 13 : ((m) - 17) % 3; \
        if (t == 3) mma_bf(a13[c >> 2], Wf[KS][c], Xf[KS][3]); else mma_bf(acc[t < 3 ? t : 0][c], Wf[KS][c], Xf[KS][t]); SCHED(); } while (0)
#define R0(m) MM(0, m); RDP(1, m, xb1, yb1, 4096);
#define R1(m) MM(1, m); RDP(0, (m) - 12, xb0, yb0, 0);
      waitv<0>();
      BARRIER();
      STAMP(1);
      RDP(0, 0, xb0, yb0, 0); RDP(0, 1, xb0, yb0, 0); RDP(0, 2, xb0, yb0, 0); RDP(0, 3, xb0, yb0, 0); RDP(0, 4, xb0, yb0, 0);
      RDP(0, 5, xb0, yb0, 0); RDP(0, 6, xb0, yb0, 0); RDP(0, 7, xb0, yb0, 0); RDP(0, 8, xb0, yb0, 0); RDP(0, 9, xb0, yb0, 0);
      u32 sw = 0, sx = 0;
#pragma unroll 1
      for (int s = 0; s < 10; s++) {
        u32 fw, fo;
        R0(0) R0(1) R0(2) R0(3) R0(4) R0(5) R0(6) R0(7) R0(8) R0(9)
        sx += XSLOT; u32 dx = XSLOT; if (sx == NXS * XSLOT) { sx = 0; dx = XSLOT - NXS * XSLOT; }
        sw += WSLOT; u32 dl = WSLOT; if (sw == NWS * WSLOT) { sw = 0; dl = WSLOT - NWS * WSLOT; }
        MM(0, 10); xb0 += dx; xb1 += dx; asm volatile("" : "+v"(xb0), "+v"(xb1)); SCHED();
        MM(0, 11); yb0 += dx; yb1 += dx; asm volatile("" : "+v"(yb0), "+v"(yb1)); SCHED();
        MM(0, 12); wb[0] += dl; wb[1] += dl; asm volatile("" : "+v"(wb[0]), "+v"(wb[1])); SCHED();
        MM(0, 13); wb[2] += dl; wb[3] += dl; asm volatile("" : "+v"(wb[2]), "+v"(wb[3])); SCHED();
        MM(0, 14); MM(0, 15);
        asm volatile("s_waitcnt lgkmcnt(0)" ::: "memory");
        sig0(rz, CTL + C_DONE + s * 16 + wv * 4); SCHED();
        if (s == r5) {
          const u32 tg = ldsv(CTL + C_TAG);
          f4 c0 = {0, 0, 0, 0}, c1 = {0, 0, 0, 0}; v4u on = {0x3f803f80u, 0x3f803f80u, 0x3f803f80u, 0x3f803f80u};
          asm volatile("s_nop 1" : "+v"(c0), "+v"(c1), "+v"(on));
          mma_bf(c0, Wf[0][0], on); mma_bf(c1, Wf[0][4], on); mma_bf(c0, Wf[1][0], on); mma_bf(c1, Wf[1][4], on);
          asm volatile("s_nop 7" : "+v"(c0), "+v"(c1));
          const f4 cc = (i & 1) ? c1 : c0;
          if (i < 2) {
            const u32 o = WS_CSP + (i * 64 + 16 * wv + 4 * g) * 8, so = (cj * 10 + ri) * 1024;
            stc128(v4u{__float_as_uint(cc[0]), tg, __float_as_uint(cc[1]), tg}, rw, o, so); stc128(v4u{__float_as_uint(cc[2]), tg, __float_as_uint(cc[3]), tg}, rw, o + 16, so);
          }
          asm volatile("s_nop 3"); SCHED();
        }
        MM(0, 16); MM(0, 17); MM(0, 18); MM(0, 19); MM(0, 20); MM(0, 21); MM(0, 22); MM(0, 23); MM(0, 24); MM(0, 25);
        MM(1, 0); MM(1, 1); MM(1, 2); MM(1, 3); fw = ldsv(fa); SCHED();
        MM(1, 4); MM(1, 5); MM(1, 6); MM(1, 7); MM(1, 8); MM(1, 9); fo = rfl(fw); SCHED(); MM(1, 10); MM(1, 11);
        spin0(fa, fo);
        R1(12) R1(13) R1(14) R1(15) R1(16) R1(17) R1(18) R1(19) R1(20) R1(21)
        MM(1, 22); fa += 4; asm volatile("" : "+v"(fa)); SCHED(); MM(1, 23); MM(1, 24); MM(1, 25);
      }
      CSTAMP(14);
      { const v4u cw = *(volatile LAS v4u*)(CTL + C_RDY); tag = rfl(cw.w); spin0(CTL + C_STF, rfl(cw.z)); spin0(CTL + C_RDY, rfl(cw.x)); }
      STAMP(2);
      f4 cg[4], cu[4]; f2 st[4];
#pragma unroll
      for (int t = 0; t < 4; t++) st[t] = *(LAS f2*)(sb + LD_ST + ((t < 3 ? 3 * wv + t : 12) * 16 + i) * 8);
#pragma unroll
      for (int c = 0; c < 4; c++) { cg[c] = *(LAS f4*)(sb + LD_CS + (16 * (c ^ wv) + 4 * g) * 4); cu[c] = *(LAS f4*)(sb + LD_CS + 256 + (16 * (c ^ wv) + 4 * g) * 4); }
      const u32 hb = WS_HF + (2 * cj) * 1024 + (((((g ^ wv) & 1) << 1) | (g >> 1)) * 16 + i) * 16;
#pragma unroll
      for (int t = 0; t < 4; t++) {
        f2 k1 = {-st[t].x, -1.44269504089f * st[t].y}, k2 = {__builtin_amdgcn_rcpf(st[t].y), 0.f};
        asm volatile("s_nop 0" : "+v"(k1), "+v"(k2));
        v2u P[4];
#pragma unroll
        for (int c = 0; c < 4; c++) {
          if (t == 3 && c > 0) break;
          const f4 ag = t < 3 ? acc[t < 3 ? t : 0][c] : a13[0], au = t < 3 ? acc[t < 3 ? t : 0][4 + c] : a13[1];
#pragma unroll
          for (int e = 0; e < 2; e++) {
            const f2 a2 = e ? ag.zw : ag.xy, u2 = e ? au.zw : au.xy, cg2 = e ? cg[c].zw : cg[c].xy, cu2 = e ? cu[c].zw : cu[c].xy;
            const f2 gg = pmul_y(k1, pfma_x(k1, cg2, a2)), uu = pfma_x(k1, cu2, u2);
            const f2 den = pfma_xx(k2, f2{__builtin_amdgcn_exp2f(gg.x), __builtin_amdgcn_exp2f(gg.y)});
            const f2 h = pmul(uu, f2{__builtin_amdgcn_rcpf(den.x), __builtin_amdgcn_rcpf(den.y)});
            P[c][e] = pkbf(h.x, h.y);
            { const int n = (t * 4 + c) * 2 + e; if (n < 25) Wr[n / 5][n % 5] = ldc128(rw, lane * 16, wdo + n * 1024); }
          }
          if (c & 1) SCHED();
        }
        if (t < 3) {
#pragma unroll
          for (int k = 0; k < 2; k++) {
            auto sx2 = __builtin_amdgcn_permlane16_swap(P[2 * k].x, P[2 * k + 1].x, false, false);
            auto sy2 = __builtin_amdgcn_permlane16_swap(P[2 * k].y, P[2 * k + 1].y, false, false);
            stc128(v4u{sx2[0], sy2[0], sx2[1], sy2[1]}, rw, hb + (k ^ (wv >> 1)) * 1024, (ts + 3 * wv + t) * 49152);
          }
        } else if (r5 != 0) {
          __builtin_amdgcn_raw_buffer_store_b64(P[0], rw, WS_HF + (2 * cj + (wv >> 1)) * 1024 + ((2 * (wv & 1) + (g >> 1)) * 16 + i) * 16 + (g & 1) * 8, (ts + 12) * 49152, AUX_SC1);
        }
        SCHED();
      }
      STAMP(3);
      sig0(rz, CTL + C_DONE + 160 + wv * 4);
      waitv<0>();
      if (lane == 0) stc32(tag, rw, WS_HFLAG + ((ri * 24 + cj) * 4 + wv) * 4);
      STAMP(4);
    } else {
      BARRIER();
      tag = rfl(ldsv(CTL + C_TAG));
      spin0(CTL + C_RDY, 1);
#pragma unroll
      for (int n = 0; n < 25; n++) Wr[n / 5][n % 5] = ldc128(rw, lane * 16, wdo + n * 1024);
    }
    const int tt = 4 * i2 + wv, tok = 16 * tt + i, d0 = 80 * j2 + 20 * g;
    f4 o[4][5];
    {
      const rsrc_t rd = mkr(p.dsk, D * 2);
      const v4u xa = __builtin_amdgcn_raw_buffer_load_b128(rx, (tok * D + d0) * 2, 0, 0), xb = __builtin_amdgcn_raw_buffer_load_b128(rx, (tok * D + d0) * 2 + 16, 0, 0);
      const v2u xc2 = __builtin_amdgcn_raw_buffer_load_b64(rx, (tok * D + d0) * 2 + 32, 0, 0);
      const v4u da = __builtin_amdgcn_raw_buffer_load_b128(rd, d0 * 2, 0, 0), db = __builtin_amdgcn_raw_buffer_load_b128(rd, d0 * 2 + 16, 0, 0);
      const v2u dc = __builtin_amdgcn_raw_buffer_load_b64(rd, d0 * 2 + 32, 0, 0);
      const v4u stv = ldc128(rw, WS_STG + tok * 16, 0);
      const float rs = __uint_as_float(stv.z), nb = fmul_(-__uint_as_float(stv.x), rs);
      const u32 xw[10] = {xa.x, xa.y, xa.z, xa.w, xb.x, xb.y, xb.z, xb.w, xc2.x, xc2.y}, dw[10] = {da.x, da.y, da.z, da.w, db.x, db.y, db.z, db.w, dc.x, dc.y};
#pragma unroll
      for (int k = 0; k < 10; k++) {
        const float x0 = bf2f(xw[k]), x1 = bfhi(xw[k]);
        o[0][k >> 1][2 * (k & 1)] = ffma_(ffma_(x0, rs, nb), bf2f(dw[k]), x0);
        o[0][k >> 1][2 * (k & 1) + 1] = ffma_(ffma_(x1, rs, nb), bfhi(dw[k]), x1);
      }
#pragma unroll
      for (int t = 0; t < 4; t++)
#pragma unroll
        for (int c = 0; c < 5; c++) { if (t > 0) o[t][c] = f4{0, 0, 0, 0}; asm volatile("" : "+v"(o[t][c])); }
    }
    { const u32 fo = WS_HFLAG + (24 * wv + lane % 24) * 4; poll_flags<6>(rw, fo + ((20 * i2 + 4) >> 6) * 384, fo + ((20 * i2 + 19) >> 6) * 384, tag); }
    STAMP(5);
    constexpr int HD = 3;
    v4u hr[12][4], Wl[2][5], Wz[5];
    u32 ho[4];
#pragma unroll
    for (int t = 0; t < 4; t++) ho[t] = WS_HF + (4 * i2 + (t ^ wv)) * 49152 + 12 * wv * 1024;
#define LOAD_H(k) do { _Pragma("unroll") for (int t = 0; t < 4; t++) hr[k][t] = ldc128(rw, lane * 16, ho[t] + (k) * 1024); } while (0)
#pragma unroll
    for (int k = 0; k < HD; k++) LOAD_H(k);
    SCHED();
    const u32 wl = sb + wd_off(wv) + lane * 16;
#pragma unroll
    for (int k = 0; k < 12; k++) {
      if (k == NWR - 1) spin0(CTL + C_WDFULL, 1);
#pragma unroll
      for (int c = 0; c < 5; c++) {
        const v4u w = k < NWR ? Wr[k < NWR ? k : 0][c] : (k < 11 ? Wl[k & 1][c] : Wz[c]);
#pragma unroll
        for (int t = 3; t >= 0; t--) {
          mma_bf(o[t][c], w, hr[k][t]); SCHED();
          if (t == 1 && k == 8) { Wz[c] = ldc128(rw, lane * 16, wdo + (55 + c) * 1024); SCHED(); }
          if (t == 2 && k + 1 >= NWR && k + 1 < 11) { Wl[(k + 1) & 1][c] = lds128(wl + ((k + 1 - NWR) * 5 + c) * 1024); SCHED(); }
          if (t == 3 && c < 4) {
            const int kn = k == 0 ? -1 : k + HD;
            if (kn > 0 && kn < 12) { hr[kn][c] = ldc128(rw, lane * 16, ho[c] + kn * 1024); SCHED(); }
          }
        }
      }
      if (k == 0) { LOAD_H(HD); SCHED(); }
    }
    asm volatile("s_nop 7"); SCHED();
    STAMP(6);
    {
#pragma unroll
      for (int t = 1; t < 4; t++)
#pragma unroll
        for (int c = 0; c < 5; c++) *(LAS f4*)(wl + ((t - 1) * 5 + c) * 1024) = o[t][c];
      ldsw(CTL + C_RED + wv * 4, 0);
      spin0(CTL + C_ZFULL, 1);
      STAMP(7);
      const int q = (lane >> 2) & 3, pp = lane & 3;
      v2u z[5];
#pragma unroll
      for (int c = 0; c < 5; c++) z[c] = trd(sb + LD_K + (20 * g + 4 * c + q) * 128 + wv * 32 + pp * 8);
#pragma unroll
      for (int c = 0; c < 5; c++) {
        o[0][c][0] = fadd_(o[0][c][0], bf2f(z[c].x)); o[0][c][1] = fadd_(o[0][c][1], bfhi(z[c].x));
        o[0][c][2] = fadd_(o[0][c][2], bf2f(z[c].y)); o[0][c][3] = fadd_(o[0][c][3], bfhi(z[c].y));
      }
      spin_zero4(CTL + C_RED);
      u32 ow[10];
#pragma unroll
      for (int c = 0; c < 5; c++) {
        f4 s[3];
#pragma unroll
        for (int t = 1; t < 4; t++) s[t - 1] = *(LAS f4*)(sb + wd_off(wv ^ t) + lane * 16 + ((t - 1) * 5 + c) * 1024);
        float y[4];
#pragma unroll
        for (int e = 0; e < 4; e++) y[e] = fadd_(fadd_(o[0][c][e], s[0][e]), fadd_(s[1][e], s[2][e]));
        ow[2 * c] = pkbf(y[0], y[1]); ow[2 * c + 1] = pkbf(y[2], y[3]);
      }
      const rsrc_t ro = mkr(p.out, L * D * 2);
      __builtin_amdgcn_raw_buffer_store_b128(v4u{ow[0], ow[1], ow[2], ow[3]}, ro, (tok * D + d0) * 2, 0, AUX_SC1);
      __builtin_amdgcn_raw_buffer_store_b128(v4u{ow[4], ow[5], ow[6], ow[7]}, ro, (tok * D + d0) * 2 + 16, 0, AUX_SC1);
      __builtin_amdgcn_raw_buffer_store_b64(v2u{ow[8], ow[9]}, ro, (tok * D + d0) * 2 + 32, 0, AUX_SC1);
    }
    STAMP(8); CSTAMP(15);
  } else {
    if (g1) {
      int kx = rot + 1, kw = kx; u32 sw = WSLOT, sx = XSLOT;
#define ISSUE_X(K0, K1) do { _Pragma("unroll") for (int k = K0; k < K1; k++) dma4(rxs, vx[0], vx[1], vx[2], vx[3], kx * 128 + k * 40960, sb + LD_X + sx + k * 4096); } while (0)
#define ISSUE_XE() do { dma2(rxs, vx[0], vx[1], kx * 128 + 12 * 20480, sb + LD_X + sx + 12 * 2048); kx = kx == 9 ? 0 : kx + 1; sx += XSLOT; if (sx == NXS * XSLOT) sx = 0; } while (0)
#define ISSUE_W() do { const u32 o_ = kw * 64 * 3072; dma4(rg, v[0], v[1], v[2], v[3], o_, sb + LD_R + sw); dma4(ru, v[0], v[1], v[2], v[3], o_, sb + LD_R + sw + 8192); \
        dma4(rg, v[0], v[1], v[2], v[3], o_ + 32 * 3072, sb + LD_R + sw + 4096); dma4(ru, v[0], v[1], v[2], v[3], o_ + 32 * 3072, sb + LD_R + sw + 12288); \
        kw = kw == 9 ? 0 : kw + 1; sw += WSLOT; if (sw == NWS * WSLOT) sw = 0; } while (0)
      __builtin_amdgcn_s_sleep(1);
      ISSUE_W(); ISSUE_X(0, 6); ISSUE_XE(); ISSUE_W();
      BARRIER();
      __builtin_amdgcn_s_setprio(3);
#pragma unroll 1
      for (int n = 2; n < 10; n++) {
        bool pend = true;
        if (n >= 3 && !is_zero4(CTL + C_DONE + (n - 3) * 16)) { waitv<16>(); sig0(rz, CTL + C_FULL + (n - 1) * 4); pend = false; spin_zero4(CTL + C_DONE + (n - 3) * 16); }
        waitv<44>();
        ISSUE_X(0, 4);
        if (pend) { waitv<32>(); sig0(rz, CTL + C_FULL + (n - 1) * 4); }
        ISSUE_X(4, 6); ISSUE_XE();
        if (n < 9) ISSUE_W();
      }
      waitv<0>(); sig0(rz, CTL + C_FULL + 36);
      STAMP(12);
      spin_zero4(CTL + C_DONE + 9 * 16);
    } else BARRIER();
    __builtin_amdgcn_s_setprio(1);
    const u32 tag = rfl(ldsv(CTL + C_TAG));
    spin_zero4(CTL + C_KDONE);
    STAMP(23);
    const v4u rws = mkrs(p.ws, WS_BYTES);
    {
      const u32 vo = lane * 16;
#pragma unroll 1
      for (int k = 0; k < 24; k++) {
        const int w = k & 3, pc = k >> 2;
        const u32 go = WS_WDF + (j2 * 48 + 12 * w + 5 + pc) * 5120, lo = sb + wd_off(w) + pc * 5120;
        dma4c(rws, vo, vo, vo, vo, go, lo); dma1c(rws, vo, go + 4096, lo + 4096);
        waitv<WD_DEPTH>();
      }
      waitv<0>();
      sig0(rz, CTL + C_WDFULL);
    }
    STAMP(24);
    __builtin_amdgcn_s_setprio(3);
    poll_flags<8>(rw, WS_ZFLAG + j2 * 320 + lane * 4, WS_ZFLAG + j2 * 320 + 64 + lane * 4, tag);
    {
      u32 vz[4];
#pragma unroll
      for (int k = 0; k < 4; k++) vz[k] = (8 * k + (lane >> 3)) * 4096 + (lane & 7) * 16 - 1024 * k;
      const u32 go = WS_ZT + 80 * j2 * 4096 + i2 * 128;
      dma4c(rws, vz[0], vz[1], vz[2], vz[3], go, sb + LD_K); dma4c(rws, vz[0], vz[1], vz[2], vz[3], go + 32 * 4096, sb + LD_K + 4096);
      dma2c(rws, vz[0], vz[1], go + 64 * 4096, sb + LD_K + 8192);
      waitv<0>();
      sig0(rz, CTL + C_ZFULL);
      waitv<0>();
    }
    STAMP(25);
  }
}

extern "C" void kernel_launch(void* const* d_in, const int* in_sizes, int n_in, void* d_out, int out_size, void* d_ws, size_t ws_size, hipStream_t stream) {
  Params p;
  p.x = (const u16*)d_in[0]; p.lar = (const u16*)d_in[1]; p.aim = (const u16*)d_in[2]; p.cre = (const u16*)d_in[3]; p.cim = (const u16*)d_in[4];
  p.ldt = (const u16*)d_in[5]; p.dsk = (const u16*)d_in[6]; p.wg = (const u16*)d_in[8]; p.wu = (const u16*)d_in[9]; p.wd = (const u16*)d_in[10];
  p.out = (u16*)d_out; p.ws = (char*)d_ws;
  (void)hipFuncSetAttribute((const void*)k_main, hipFuncAttributeMaxDynamicSharedMemorySize, LDS_BYTES);
  k_main<<<256, 512, LDS_BYTES, stream>>>(p);
}
```

```cpp
#define DIAG 8
#include <hip/hip_runtime.h>
#pragma clang diagnostic ignored "-Wint-to-pointer-cast"
typedef unsigned short u16; typedef unsigned u32; typedef unsigned long long u64;
typedef u32 v4u __attribute__((ext_vector_type(4)));
typedef u32 v2u __attribute__((ext_vector_type(2)));
typedef float f4 __attribute__((ext_vector_type(4)));
typedef float f2 __attribute__((ext_vector_type(2)));
typedef short s4 __attribute__((ext_vector_type(4)));
typedef __bf16 bf2 __attribute__((ext_vector_type(2)));
typedef _Float16 hf2 __attribute__((ext_vector_type(2)));
#define LAS __attribute__((address_space(3)))
#define GAS __attribute__((address_space(1)))
#define DEV __device__ __forceinline__
typedef __amdgpu_buffer_rsrc_t rsrc_t;

constexpr int L = 2048, D = 640, NS = 192, F = 1536;
constexpr u32 WS_GEN = 0, WS_HFLAG = 1024, WS_ZFLAG = 5120, WS_LFLAG = 8192, WS_WFLAG = WS_LFLAG + 512, WS_STG = 16384, WS_CSP = 65536,
              WS_XT = 1u << 20, WS_WDF = 4u << 20, WS_HF = 6u << 20, WS_ZT = 13u << 20, WS_BYTES = 16u << 20;
constexpr u32 LD_K = 0, K_BYTES = 5120;
constexpr u32 LD_CS = LD_K + 3 * K_BYTES, LD_ST = LD_CS + 512, LD_CTL = LD_ST + 208 * 8, LD_R = LD_CTL + 512;
constexpr u32 WSLOT = 16384, NWS = 4, XSLOT = 26624, NXS = 3, LD_X = LD_R + NWS * WSLOT, LDS_BYTES = LD_X + NXS * XSLOT;
constexpr u32 LD_U = LD_R + 2 * WSLOT, U_BYTES = 4608;
constexpr u32 WD_BYTES = 30720;
DEV u32 wd_off(int w) { return w == 0 ? LD_R : LD_R + 3 * WSLOT + (w - 1) * WD_BYTES; }
static_assert(LD_R % 128 == 0 && LD_R + 3 * WSLOT + 3 * WD_BYTES <= LDS_BYTES && 3 * U_BYTES <= WSLOT && LDS_BYTES <= 163840, "lds");
constexpr u32 C_FULL = 0, C_WDFULL = 44, C_RDY = 48, C_ZFULL = 52, C_STF = 56, C_TAG = 60, C_KDONE = 64, C_RED = 80, C_DONE = 256;
constexpr int AUX_SC1 = 16;
constexpr u32 SPIN_CAP = 1u << 15;
#define WD_DEPTH 30
#define POLL_STAGE 8
#define CONV_PRIO 3

#define STAMP(k) do { if ((k) == 0) dg0 = __builtin_amdgcn_s_memrealtime(); if ((k) == DIAG) dgk = __builtin_amdgcn_s_memrealtime(); \
    if ((k) == 8 && dgk) { const u64 e_ = dg0 + 6000; while (__builtin_amdgcn_s_memrealtime() < e_) __builtin_amdgcn_s_sleep(4); } } while (0)
#define DG_DECL u64 dg0 = 0, dgk = 0; (void)dg0; (void)dgk;

struct Params {
  const u16 *x, *lar, *aim, *cre, *cim, *ldt, *dsk, *wg, *wu, *wd;
  u16* out; char* ws;
};

DEV float bf2f(u32 v) { return __uint_as_float(v << 16); }
DEV float bfhi(u32 v) { return __uint_as_float(v & 0xffff0000u); }
DEV u32 pkbf(float a, float b) { f2 v = {a, b}; return __builtin_bit_cast(u32, __builtin_convertvector(v, bf2)); }
DEV u32 pkhf(float a, float b) { f2 v = {a, b}; return __builtin_bit_cast(u32, __builtin_convertvector(v, hf2)); }
DEV float rbf(float a) { return bf2f(pkbf(a, a)); }
DEV void mma_bf(f4& c, v4u a, v4u b) { asm volatile("v_mfma_f32_16x16x32_bf16 %0, %1, %2, %0" : "+v"(c) : "v"(a), "v"(b)); }
DEV void mma_hf(f4& c, v4u a, v4u b) { asm volatile("v_mfma_f32_16x16x32_f16 %0, %1, %2, %0" : "+v"(c) : "v"(a), "v"(b)); }
DEV v2u trd(u32 a) { return __builtin_bit_cast(v2u, __builtin_amdgcn_ds_read_tr16_b64_v4i16((LAS s4*)a)); }
DEV v4u trd2(u32 a, u32 b) { v2u x = trd(a), y = trd(b); return v4u{x.x, x.y, y.x, y.y}; }
DEV v4u lds128(u32 a) { return *(LAS v4u*)a; }
DEV v2u lds64(u32 a) { return *(LAS v2u*)a; }
DEV u32 rfl(u32 v) { return __builtin_amdgcn_readfirstlane(v); }
DEV v4u mkrs(const void* p, u32 bytes) { u64 a = (u64)p; return v4u{(u32)a, (u32)(a >> 32) & 0xffffu, bytes, 0x00020000u}; }
DEV rsrc_t mkr(const void* p, u32 bytes) { return __builtin_amdgcn_make_buffer_rsrc((void*)p, 0, bytes, 0x00020000); }
DEV v4u ldc128(rsrc_t r, u32 v, u32 s) { return __builtin_amdgcn_raw_buffer_load_b128(r, v, s, AUX_SC1); }
DEV u32 ldc32(rsrc_t r, u32 v) { return __builtin_amdgcn_raw_buffer_load_b32(r, v, 0, AUX_SC1); }
DEV void stc128(v4u d, rsrc_t r, u32 v, u32 s) { __builtin_amdgcn_raw_buffer_store_b128(d, r, v, s, AUX_SC1); }
DEV void stc32(u32 d, rsrc_t r, u32 v) { __builtin_amdgcn_raw_buffer_store_b32(d, r, v, 0, AUX_SC1); }
#define CBAR() asm volatile("" ::: "memory")
#define SCHED() __builtin_amdgcn_sched_barrier(0)
#define ORDER() __builtin_amdgcn_fence(__ATOMIC_ACQUIRE, "wavefront")
#define BARRIER() do { asm volatile("s_waitcnt lgkmcnt(0)" ::: "memory"); __builtin_amdgcn_s_barrier(); CBAR(); } while (0)
template <int N> DEV void waitv() { asm volatile("s_waitcnt vmcnt(%0)" ::"i"(N) : "memory"); }
#define DMA_(V, RS, SO, OFF, SC) "buffer_load_dwordx4 " V ", " RS ", " SO " offen" OFF SC " lds\n\t"
#define DMA4_(NAME, SC) DEV void NAME(v4u rs, u32 v0, u32 v1, u32 v2, u32 v3, u32 so, u32 lds) { \
  asm volatile("s_mov_b32 m0, %0\n\ts_nop 0\n\t" DMA_("%1", "%5", "%6", "", SC) DMA_("%2", "%5", "%6", " offset:1024", SC) DMA_("%3", "%5", "%6", " offset:2048", SC) DMA_("%4", "%5", "%6", " offset:3072", SC) \
               ::"s"(lds), "v"(v0), "v"(v1), "v"(v2), "v"(v3), "s"(rs), "s"(so) : "memory"); }
#define DMA2_(NAME, SC) DEV void NAME(v4u rs, u32 v0, u32 v1, u32 so, u32 lds) { \
  asm volatile("s_mov_b32 m0, %0\n\ts_nop 0\n\t" DMA_("%1", "%3", "%4", "", SC) DMA_("%2", "%3", "%4", " offset:1024", SC) ::"s"(lds), "v"(v0), "v"(v1), "s"(rs), "s"(so) : "memory"); }
#define DMA1_(NAME, SC) DEV void NAME(v4u rs, u32 v0, u32 so, u32 lds) { \
  asm volatile("s_mov_b32 m0, %0\n\ts_nop 0\n\t" DMA_("%1", "%2", "%3", "", SC) ::"s"(lds), "v"(v0), "s"(rs), "s"(so) : "memory"); }
DMA4_(dma4, "") DMA2_(dma2, "") DMA1_(dma1, "") DMA4_(dma4c, " sc1") DMA2_(dma2c, " sc1") DMA1_(dma1c, " sc1")
DEV void sig0(v4u rz, u32 lds) {
  asm volatile("s_mov_b32 m0, %0\n\ts_mov_b64 exec, 1\n\tbuffer_load_dword off, %1, 0 lds\n\ts_mov_b64 exec, -1" ::"s"(lds), "s"(rz) : "memory");
}
DEV u32 ldsv(u32 a) { return *(volatile LAS u32*)a; }
DEV void ldsw(u32 a, u32 v) { asm volatile("s_mov_b64 exec, 1\n\tds_write_b32 %0, %1\n\ts_mov_b64 exec, -1" ::"v"(a), "v"(v) : "memory"); }
DEV void spin0(u32 a, u32 cur) {
  u32 tv, ts, cnt;
  asm volatile("s_cmp_eq_u32 %4, 0\n\ts_cbranch_scc0 3f\n2:\n\t.subsection 1\n3:\n\ts_mov_b32 %2, 0x10000\n"
               "1:\n\tds_read_b32 %0, %3\n\ts_waitcnt lgkmcnt(0)\n\tv_readfirstlane_b32 %1, %0\n\ts_nop 0\n\ts_cmp_eq_u32 %1, 0\n\ts_cbranch_scc1 2b\n\t"
               "s_sleep 1\n\ts_sub_u32 %2, %2, 1\n\ts_cmp_lg_u32 %2, 0\n\ts_cbranch_scc1 1b\n\ts_branch 2b\n\t.subsection 0"
               : "=&v"(tv), "=&s"(ts), "=&s"(cnt) : "v"(a), "s"(cur) : "memory", "scc");
}
DEV bool is_zero4(u32 a) { v4u d = *(volatile LAS v4u*)a; return rfl(d.x | d.y | d.z | d.w) == 0; }
DEV void spin_zero4(u32 a) {
#pragma unroll 1
  for (u32 i = 0; i < 2 * SPIN_CAP; i++) { if (is_zero4(a)) break; __builtin_amdgcn_s_sleep(1); } CBAR();
}
template <int SL> DEV void poll_flags(rsrc_t rw, u32 off, u32 off2, u32 tag) {
#pragma unroll 1
  for (u32 i = 0; i < SPIN_CAP; i++) {
    const u32 a = ldc32(rw, off), b = ldc32(rw, off2);
    if (__all(a == tag && b == tag)) break;
    __builtin_amdgcn_s_sleep(SL);
  }
  ORDER();
}
DEV float fmul_(float a, float b) { float r; asm("v_mul_f32 %0, %1, %2" : "=v"(r) : "v"(a), "v"(b)); return r; }
DEV float fadd_(float a, float b) { float r; asm("v_add_f32 %0, %1, %2" : "=v"(r) : "v"(a), "v"(b)); return r; }
DEV float ffma_(float a, float b, float c) { float r; asm("v_fma_f32 %0, %1, %2, %3" : "=v"(r) : "v"(a), "v"(b), "v"(c)); return r; }
DEV float fnma_(float a, float b, float c) { float r; asm("v_fma_f32 %0, -%1, %2, %3" : "=v"(r) : "v"(a), "v"(b), "v"(c)); return r; }
struct cf { float x, y; };
DEV cf csq(cf a) { const float t = fmul_(a.x, a.y); return cf{fnma_(a.y, a.y, fmul_(a.x, a.x)), fadd_(t, t)}; }
DEV u32 pkc(cf a) { return pkhf(a.x, a.y); }
DEV cf cexp2(float lr, float rev) { float m = __builtin_amdgcn_exp2f(lr); const float f = __builtin_amdgcn_fractf(rev); float c = __builtin_amdgcn_cosf(f), s = __builtin_amdgcn_sinf(f);
  asm volatile("s_nop 0" : "+v"(c), "+v"(s), "+v"(m)); return cf{fmul_(m, c), fmul_(m, s)}; }
DEV u32 hmul(u32 a, u32 m) { u32 t, r;
  asm("v_pk_mul_f16 %0, %1, %2 op_sel_hi:[0,1]" : "=v"(t) : "v"(a), "v"(m));
  asm("v_pk_fma_f16 %0, %1, %2, %3 op_sel:[1,1,0] op_sel_hi:[1,0,1] neg_lo:[0,1,0]" : "=v"(r) : "v"(a), "v"(m), "v"(t)); return r; }
DEV u32 hmulc(u32 a, u32 m) { u32 t, r;
  asm("v_pk_mul_f16 %0, %1, %2 op_sel_hi:[0,1] neg_hi:[0,1]" : "=v"(t) : "v"(a), "v"(m));
  asm("v_pk_fma_f16 %0, %1, %2, %3 op_sel:[1,1,0] op_sel_hi:[1,0,1]" : "=v"(r) : "v"(a), "v"(m), "v"(t)); return r; }
DEV f2 pfma_x(f2 s, f2 b, f2 c) { f2 r; asm("v_pk_fma_f32 %0, %1, %2, %3 op_sel_hi:[0,1,1]" : "=v"(r) : "v"(s), "v"(b), "v"(c)); return r; }
DEV f2 pmul_y(f2 s, f2 b) { f2 r; asm("v_pk_mul_f32 %0, %1, %2 op_sel:[1,0]" : "=v"(r) : "v"(s), "v"(b)); return r; }
DEV f2 pfma_xx(f2 s, f2 b) { f2 r; asm("s_nop 0\n\tv_pk_fma_f32 %0, %1, %2, %1 op_sel_hi:[0,1,0]" : "=v"(r) : "v"(s), "v"(b)); return r; }
DEV f2 pmul(f2 a, f2 b) { f2 r; asm("s_nop 0\n\tv_pk_mul_f32 %0, %1, %2" : "=v"(r) : "v"(a), "v"(b)); return r; }
DEV void swap16(float& a, float& b) { auto r = __builtin_amdgcn_permlane16_swap(__float_as_uint(a), __float_as_uint(b), false, false); a = __uint_as_float(r[0]); b = __uint_as_float(r[1]); }
DEV void swap32(float& a, float& b) { auto r = __builtin_amdgcn_permlane32_swap(__float_as_uint(a), __float_as_uint(b), false, false); a = __uint_as_float(r[0]); b = __uint_as_float(r[1]); }

DEV void ln_wave(const Params& p, u32 sb, int pn, int lane, int wv) {
  DG_DECL
  const rsrc_t rx = mkr(p.x, L * D * 2), rw = mkr(p.ws, WS_BYTES);
  const int h = lane & 1, m = lane >> 1, row0 = 64 * (pn >> 2) + (pn & 3);
  __builtin_amdgcn_s_setprio(3);
  v2u xv[8][5];
#pragma unroll
  for (int e = 0; e < 8; e++)
#pragma unroll
    for (int i = 0; i < 5; i++) xv[e][i] = __builtin_amdgcn_raw_buffer_load_b64(rx, (row0 + 32 * h) * 1280 + m * 8 + i * 256, e * 5120, 0);
  float s[16];
#pragma unroll
  for (int e = 0; e < 8; e++) {
    float a = 0.f, q = 0.f;
#pragma unroll
    for (int i = 0; i < 10; i++) {
      const u32 w = xv[e][i >> 1][i & 1];
      const bf2 v = __builtin_bit_cast(bf2, w);
      a = __builtin_amdgcn_fdot2_f32_bf16(v, __builtin_bit_cast(bf2, 0x3f803f80u), a, false);
      q = __builtin_amdgcn_fdot2_f32_bf16(v, v, q, false);
    }
    s[e] = a; s[8 + e] = q;
  }
#pragma unroll
  for (int e = 0; e < 16; e++) asm volatile("" : "+v"(s[e]));
  asm volatile("s_nop 2");
  BARRIER();
  const u32 tag = rfl(ldsv(sb + LD_CTL + C_TAG));
  float z[4];
#pragma unroll
  for (int k = 0; k < 4; k++) {
    swap16(s[4 * k], s[4 * k + 1]); swap16(s[4 * k + 2], s[4 * k + 3]);
    float a = fadd_(s[4 * k], s[4 * k + 1]), b = fadd_(s[4 * k + 2], s[4 * k + 3]);
    asm volatile("s_nop 1" : "+v"(a), "+v"(b));
    swap32(a, b); z[k] = fadd_(a, b);
  }
#define DPPADD(C) asm volatile("s_nop 1\n\tv_add_f32_dpp %0, %0, %0 " C " row_mask:0xf bank_mask:0xf\n\tv_add_f32_dpp %1, %1, %1 " C " row_mask:0xf bank_mask:0xf\n\t" \
    "v_add_f32_dpp %2, %2, %2 " C " row_mask:0xf bank_mask:0xf\n\tv_add_f32_dpp %3, %3, %3 " C " row_mask:0xf bank_mask:0xf" : "+v"(z[0]), "+v"(z[1]), "+v"(z[2]), "+v"(z[3]))
  DPPADD("quad_perm:[2,3,0,1]"); DPPADD("row_ror:4"); DPPADD("row_ror:8");
  float rs[8], nb[8];
#pragma unroll
  for (int k = 0; k < 2; k++) {
    const float mean = fmul_(z[k], 1.f / D), var = fmaxf(fnma_(mean, mean, fmul_(z[2 + k], 1.f / D)), 0.f);
    const float mb = rbf(mean), sd = rbf(__builtin_amdgcn_sqrtf(rbf(rbf(var) + 1e-5f)));
    float r = __builtin_amdgcn_rcpf(sd);
    asm volatile("s_nop 0" : "+v"(r));
    r = ffma_(fnma_(sd, r, 1.f), r, r);
    if ((lane & 15) < 2) stc128(v4u{__float_as_uint(mb), tag, __float_as_uint(r), tag}, rw, WS_STG + (row0 + 32 * h + 4 * (4 * k + (lane >> 4))) * 16, 0);
    float t[2] = {r, fmul_(-mb, r)};
#pragma unroll
    for (int w = 0; w < 2; w++) {
      float a = t[w], c = t[w];
      asm volatile("s_nop 1" : "+v"(a), "+v"(c));
      swap32(a, c); float b = a, d = c; swap16(a, b); swap16(c, d);
      float* o = w ? nb : rs; o[4 * k] = a; o[4 * k + 1] = b; o[4 * k + 2] = c; o[4 * k + 3] = d;
    }
  }
#pragma unroll
  for (int i = 0; i < 5; i++)
#pragma unroll
    for (int c = 0; c < 4; c++) {
      float y[8];
#pragma unroll
      for (int e = 0; e < 8; e++) { const u32 w = xv[e][i][c >> 1]; y[e] = ffma_((c & 1) ? bfhi(w) : bf2f(w), rs[e], nb[e]); }
      stc128(v4u{pkhf(y[0], y[1]), pkhf(y[2], y[3]), pkhf(y[4], y[5]), pkhf(y[6], y[7])}, rw, WS_XT + lane * 16, pn * 20480 + (i * 4 + c) * 1024);
    }
  waitv<0>();
  if (lane == 0) stc32(tag, rw, WS_LFLAG + pn * 4);
  STAMP(21);
}

template <bool CJ> DEV void grp8(u32* v, u32 a, u32 b, u32 c) {
#define HM(x, y) (CJ ? hmulc(x, y) : hmul(x, y))
  v[1] = HM(v[0], a); v[2] = HM(v[0], b); v[3] = HM(v[1], b); v[4] = HM(v[0], c); v[5] = HM(v[1], c); v[6] = HM(v[2], c); v[7] = HM(v[3], c);
#undef HM
}
template <int N, int O> DEV void wr_rows(const u32* v) {
  if constexpr (N > 0) { asm volatile("ds_write_addtid_b32 %0 offset:%1" ::"v"(v[0]), "i"(O * 272) : "memory"); wr_rows<N - 1, O + 1>(v + 1); }
}
DEV void conv_wave(const Params& p, u32 sb, int slot, int ch, int lane, int wv, bool g1, int ts, int cj) {
  DG_DECL
  const u32 KB = sb + LD_K + slot * K_BYTES, UB = sb + LD_U + slot * U_BYTES, CTL = sb + LD_CTL;
  const int g = lane >> 4, q = (lane >> 2) & 3, pp = lane & 3, j = lane & 15, b = blockIdx.x;
  const rsrc_t rw = mkr(p.ws, WS_BYTES);
  const v4u rz = mkrs(p.ws, 0);
  u32 gen = 0;
  if (slot == 0) gen = ldc32(rw, WS_GEN);
  const int wu = slot == 0 ? b : (slot == 1 && b < 128 ? 256 + b : -1);
  if (wu >= 0) {
    u32 vo[5];
#pragma unroll
    for (int k = 0; k < 5; k++) { const int n = k * 64 + lane; vo[k] = (n / 40) * 1280 + (n % 40) * 16 - (k & 3) * 1024; }
    const v4u rd = mkrs(p.wd, F * D * 2);
    const u32 so = (wu >> 1) * 10240 + (wu & 1) * 640;
    dma4(rd, vo[0], vo[1], vo[2], vo[3], so, KB); dma1(rd, vo[4], so, KB + 4096);
  }
  u32 la[3], ai[3], cr[3], ci[3];
#pragma unroll
  for (int c = 0; c < 3; c++) { const int o = ch * NS + 64 * c + lane; la[c] = p.lar[o]; ai[c] = p.aim[o]; cr[c] = p.cre[o]; ci[c] = p.cim[o]; }
  const u32 ldt = p.ldt[ch];
  if (slot == 0) {
    const int nch = ((b >> 3) & 1) ? 2 : 3;
    const bool one = (g1 && (lane < 10 || lane == 14)) || (lane >= 11 && lane <= 13) || (lane >= 16 && lane < 16 + nch) || (lane >= 20 && lane < 24);
    *(volatile LAS u32*)(CTL + lane * 4) = lane == 15 ? gen * 0x9E3779B1u + 0x7F4A7C15u : (one ? 1u : 0u);
  }
  if (slot == 1) *(volatile LAS u32*)(CTL + C_DONE + lane * 4) = g1 ? 1u : 0u;
  BARRIER();
  const u32 tag = rfl(ldsv(CTL + C_TAG));
  if (wu >= 0) {
    waitv<0>();
    __builtin_amdgcn_s_setprio(3);
    const u32 ra = KB + q * 640 + (80 * g + 20 * pp) * 2;
    const u32 wo = WS_WDF + ((4 * (wu & 1) + g) * 48 * 5) * 1024 + j * 16, so = (wu >> 3) * 5120 + ((wu >> 1) & 3) * 256;
#pragma unroll
    for (int k = 0; k < 5; k++) stc128(trd2(ra + k * 8, ra + k * 8 + 2560), rw, wo + k * 1024, so);
  }
  const float dt = rbf(__expf(bf2f(ldt)));
  u32 mx = 0;
#pragma unroll
  for (int c = 0; c < 3; c++) mx = max(mx, max(cr[c] & 0x7fffu, ci[c] & 0x7fffu));
  mx = max(mx, (u32)__builtin_amdgcn_update_dpp(0, (int)mx, 0x111, 0xf, 0xf, false));
  mx = max(mx, (u32)__builtin_amdgcn_update_dpp(0, (int)mx, 0x112, 0xf, 0xf, false));
  mx = max(mx, (u32)__builtin_amdgcn_update_dpp(0, (int)mx, 0x114, 0xf, 0xf, false));
  mx = max(mx, (u32)__builtin_amdgcn_update_dpp(0, (int)mx, 0x118, 0xf, 0xf, false));
  mx = max(max((u32)__builtin_amdgcn_readlane(mx, 15), (u32)__builtin_amdgcn_readlane(mx, 31)), max((u32)__builtin_amdgcn_readlane(mx, 47), (u32)__builtin_amdgcn_readlane(mx, 63)));
  STAMP(16);
  const u32 ex = min(max(mx >> 7, 1u), 252u);
  const float cscale = __uint_as_float((253u - ex) << 23);
  const float oscale = 2.f * dt * __uint_as_float((ex + 1u) << 23);
  u32 G[3][57];
#pragma unroll
  for (int c = 0; c < 3; c++) {
    const float zr = fmul_(fmul_(rbf(__expf(bf2f(la[c]))), dt), -1.44269504089f), zi = fmul_(fmul_(bf2f(ai[c]), dt), 0.15915494309f);
    const cf P0 = cexp2(zr, zi), P4 = cexp2(fmul_(zr, 16.f), fmul_(zi, 16.f)), P8 = cexp2(fmul_(zr, 256.f), fmul_(zi, 256.f));
    const cf P1 = csq(P0), P2 = csq(P1), P3 = csq(P2), P5 = csq(P4), P6 = csq(P5), P7 = csq(P6), P9 = csq(P8), P10 = csq(P9), P11 = csq(P10);
    const u32 M0 = pkc(P0), M1 = pkc(P1), M2 = pkc(P2), M3 = pkc(P3), M4 = pkc(P4), M5 = pkc(P5), M7 = pkc(P7), M8 = pkc(P8), M9 = pkc(P9);
    u32* v = G[c];
    const float cx = fmul_(bf2f(cr[c]), cscale), cy = fmul_(bf2f(ci[c]), -cscale);
    v[0] = pkhf(cx, cy); grp8<true>(v, M3, M4, M5);
    v[8] = pkhf(ffma_(cy, P6.y, fmul_(cx, P6.x)), fnma_(cx, P6.y, fmul_(cy, P6.x))); grp8<true>(v + 8, M3, M4, M5);
    v[16] = 0x00003c00u; v[17] = M0; v[18] = M1; v[19] = hmul(M0, M1); v[20] = M2; v[21] = hmul(M0, M2); v[22] = hmul(M1, M2); v[23] = hmul(v[19], M2);
    v[24] = M7; grp8<false>(v + 24, M0, M1, M2);
    v[32] = M8; grp8<false>(v + 32, M1, M2, M7);
    v[40] = M9; grp8<false>(v + 40, M2, M7, M8);
    v[48] = pkc(P10); grp8<false>(v + 48, M7, M8, M9);
    v[56] = pkc(P11);
#pragma unroll
    for (int k = 0; k < 57; k++) asm volatile("" : "+v"(v[k]));
    if (c == 0 && wu >= 0) { waitv<0>(); if (lane == 0) stc32(tag, rw, WS_WFLAG + wu * 4); __builtin_amdgcn_s_setprio(0); }
  }
  STAMP(13);
  float cs0 = 0.f, cs1 = 0.f; f2 stv[4];
  if (g1) spin0(CTL + C_FULL + POLL_STAGE * 4, 1); else { __builtin_amdgcn_s_sleep(100); }
  __builtin_amdgcn_s_setprio(3);
  if (slot == 0) {
#pragma unroll 1
    for (u32 it = 0; it < SPIN_CAP; it++) {
      const v4u a = ldc128(rw, WS_LFLAG + lane * 16, 0), c = ldc128(rw, WS_LFLAG + lane * 16, 1024);
      if (__all(a.x == tag && a.y == tag && a.z == tag && a.w == tag && c.x == tag && c.y == tag && c.z == tag && c.w == tag)) break;
      __builtin_amdgcn_s_sleep(24);
    }
    ORDER();
    sig0(rz, CTL + C_RDY);
    if (b == 0 && lane == 0) stc32(tag, rw, WS_GEN);
  } else if (slot == 1 && g1) {
#pragma unroll 1
    for (u32 it = 0; it < SPIN_CAP; it++) {
      v4u c[10]; bool ok = true; cs0 = cs1 = 0.f;
#pragma unroll
      for (int k = 0; k < 10; k++) c[k] = ldc128(rw, WS_CSP + lane * 16, (cj * 10 + k) * 1024);
#pragma unroll
      for (int k = 0; k < 10; k++) { ok = ok && c[k].y == tag && c[k].w == tag; cs0 = fadd_(cs0, __uint_as_float(c[k].x)); cs1 = fadd_(cs1, __uint_as_float(c[k].z)); }
      if (__all(ok)) break;
      __builtin_amdgcn_s_sleep(24);
    }
#pragma unroll 1
    for (u32 it = 0; it < SPIN_CAP; it++) {
      bool ok = true;
#pragma unroll
      for (int k = 0; k < 4; k++) { const int n = k * 64 + lane; const v4u c = ldc128(rw, WS_STG + (ts * 16 + (n < 208 ? n : 0)) * 16, 0);
        ok = ok && c.y == tag && c.w == tag; stv[k] = f2{__uint_as_float(c.x), __uint_as_float(c.z)}; }
      if (__all(ok)) break;
      __builtin_amdgcn_s_sleep(24);
    }
  }
  STAMP(22);
  __builtin_amdgcn_s_setprio(0);
  spin0(CTL + C_FULL + 36, 1);
  STAMP(17);
  if (slot == 1 && g1) {
#pragma unroll
    for (int k = 0; k < 4; k++) { const int n = k * 64 + lane; if (n < 208) *(LAS f2*)(sb + LD_ST + n * 8) = stv[k]; }
    *(LAS f2*)(sb + LD_CS + lane * 8) = f2{cs0, cs1};
    CBAR();
    ldsw(CTL + C_STF, 0);
  }
  spin0(CTL + C_RDY, 1);
  {
    const v4u rws = mkrs(p.ws, WS_BYTES);
    const u32 co = WS_XT + ((ch >> 7) * 4 + (ch & 3)) * 1024 + ((ch & 127) >> 2) * 32;
    u32 vo[5];
#pragma unroll
    for (int k = 0; k < 5; k++) { const int n = k * 64 + lane, tau = n / 72, a = n % 72 - 8; vo[k] = a >= 0 ? co + ((a >> 1) * 4 + tau) * 20480 + (a & 1) * 16 - (k & 3) * 1024 : 0x80000000u; }
    dma4c(rws, vo[0], vo[1], vo[2], vo[3], 0, UB);
    asm volatile("s_mov_b32 m0, %0\n\ts_mov_b32 exec_hi, 0\n\tbuffer_load_dwordx4 %1, %2, 0 offen sc1 lds\n\ts_mov_b32 exec_hi, -1" ::"s"(UB + 4096), "v"(vo[4]), "s"(rws) : "memory");
  }
  f4 kd[3] = {f4{0, 0, 0, 0}, f4{0, 0, 0, 0}, f4{0, 0, 0, 0}};
  {
    const u32 rb = KB + j * 272 + g * 16;
    asm volatile("s_mov_b32 m0, %3\n\ts_nop 0" : "+v"(kd[0]), "+v"(kd[1]), "+v"(kd[2]) : "s"(KB) : "memory");
    v4u Fr[12][4];
#pragma unroll
    for (int ps = 0; ps <= 12; ps++) {
      if (ps < 12) {
        const int c = ps >> 2, t = ps & 3;
        if (t < 3) wr_rows<16, 0>(G[c] + 16 * t); else wr_rows<9, 0>(G[c] + 48);
        asm volatile("ds_read_b128 %0, %4\n\tds_read_b128 %1, %4 offset:64\n\tds_read_b128 %2, %4 offset:128\n\tds_read_b128 %3, %4 offset:192"
                     : "=&v"(Fr[ps][0]), "=&v"(Fr[ps][1]), "=&v"(Fr[ps][2]), "=&v"(Fr[ps][3]) : "v"(rb) : "memory");
      }
      if (ps > 0) {
        const int pv = ps - 1;
        if (ps < 12) asm volatile("s_waitcnt lgkmcnt(13)" : "+v"(Fr[pv][0]), "+v"(Fr[pv][1]), "+v"(Fr[pv][2]), "+v"(Fr[pv][3]));
        else asm volatile("s_waitcnt lgkmcnt(0)" : "+v"(Fr[pv][0]), "+v"(Fr[pv][1]), "+v"(Fr[pv][2]), "+v"(Fr[pv][3]));
        if (pv & 3) {
#pragma unroll
          for (int ks = 0; ks < 4; ks++) mma_hf(kd[(pv & 3) - 1], Fr[pv][ks], Fr[pv & ~3][ks]);
        }
      }
    }
    asm volatile("s_nop 7" : "+v"(kd[0]), "+v"(kd[1]), "+v"(kd[2]));
  }
  STAMP(9);
  {
    const u32 K0 = KB + 32;
    const u32 a00 = pkhf(kd[1][0], kd[1][0]), a11 = pkhf(kd[1][1], kd[1][1]), a22 = pkhf(kd[1][2], kd[1][2]), a33 = pkhf(kd[1][3], kd[1][3]);
    const u32 a12 = pkhf(kd[1][1], kd[1][2]), a01 = pkhf(kd[1][0], kd[1][1]), a23 = pkhf(kd[1][2], kd[1][3]);
    v2u bb[4];
#pragma unroll
    for (int r = 0; r < 4; r++) { bb[r].x = pkhf(kd[2][r], kd[2][r]); bb[r].y = bb[r].x; }
    const v2u r0 = {pkhf(kd[0][0], kd[0][1]), pkhf(kd[0][2], kd[0][3])}, w1 = {a00, a12}, w2a = {a00, a01}, w2b = {a22, a23};
    const v4u z4 = {0, 0, 0, 0};
    const u32 p3 = K0 + 2 * (1024 + 512 * g + 8 * j) - 8, p2 = K0 + 2 * (256 * g + 8 * j) - 4, p1 = K0 + 2 * (256 + 128 * g + 8 * j) - 2;
    const u32 p0 = K0 + 2 * (128 * (g >> 1) + 8 * j + 4 * (g & 1)), pz = KB + lane * 16;
    asm volatile(
      "s_mov_b32 exec_hi, 1\n\t"
      "ds_write2_b64 %0, %5, %5 offset1:1\n\t"
      "s_mov_b32 exec_hi, 0\n\t"
      "ds_write2_b64 %0, %6, %6 offset0:32 offset1:33\n\t"
      "ds_write2_b64 %0, %7, %7 offset0:64 offset1:65\n\t"
      "ds_write2_b64 %0, %8, %8 offset0:96 offset1:97\n\t"
      "s_mov_b32 exec_lo, 0x55555555\n\t"
      "ds_write_b16 %0, %9 offset:16\n\tds_write_b16 %0, %10 offset:272\n\tds_write_b16 %0, %11 offset:528\n\tds_write_b16 %0, %12 offset:784\n\t"
      "s_mov_b32 exec_lo, 0\n\ts_mov_b32 exec_hi, -1\n\t"
      "ds_write_b32 %1, %13\n\tds_write_b64 %1, %17 offset:4\n\tds_write_b32 %1, %14 offset:12\n\t"
      "ds_write_b32 %1, %15 offset:256\n\tds_write_b64 %1, %18 offset:260\n\tds_write_b32 %1, %16 offset:268\n\t"
      "s_mov_b32 exec_lo, -1\n\ts_mov_b32 exec_hi, 0\n\t"
      "ds_write_b16 %2, %13\n\tds_write_b64 %2, %19 offset:2\n\tds_write_b32 %2, %15 offset:10\n\tds_write_b16 %2, %16 offset:14\n\t"
      "s_mov_b32 exec_lo, 3\n\t"
      "ds_write_b128 %4, %21\n\t"
      "s_mov_b64 exec, -1\n\t"
      "ds_write_b64 %3, %20"
      ::"v"(p3), "v"(p2), "v"(p1), "v"(p0), "v"(pz), "v"(bb[0]), "v"(bb[1]), "v"(bb[2]), "v"(bb[3]), "v"(bb[0].x), "v"(bb[1].x), "v"(bb[2].x), "v"(bb[3].x),
        "v"(a00), "v"(a11), "v"(a22), "v"(a33), "v"(w2a), "v"(w2b), "v"(w1), "v"(r0), "v"(z4) : "memory");
  }
  v4u A[16];
  {
    const u32 ab = KB + 32 + 2 * (-12 + 4 * (8 * g + 7 - q) + 4 * pp);
#pragma unroll
    for (int s = 0; s < 16; s++) A[s] = trd2(ab + 256 * s, ab + 256 * s - 32);
  }
  ldsw(CTL + C_KDONE + slot * 4, 0);
  f4 acc[4][8];
#pragma unroll
  for (int t = 0; t < 4; t++)
#pragma unroll
    for (int T = 0; T < 8; T++) { acc[t][T] = f4{0, 0, 0, 0}; asm volatile("" : "+v"(acc[t][T])); }
  STAMP(10);
  waitv<0>();
  spin0(CTL + C_DONE + 160 + slot * 4, 1);
  __builtin_amdgcn_s_setprio(CONV_PRIO);
  STAMP(18);
  const u32 ub = UB + 128 + 2 * (4 * j - 4 - 8 * g) - 64;
  v4u B[4];
#define QORD(n) (((n) & 1) ? 15 - (((n) & 15) >> 1) : (((n) & 15) >> 1))
#define LDB(n) do { const u32 a_ = ub + ((n) >> 4) * 1152 + 64 * QORD(n); const v2u x_ = lds64(a_); SCHED(); const v2u y_ = lds64(a_ + 8); B[(n) & 3] = v4u{x_.x, x_.y, y_.x, y_.y}; SCHED(); } while (0)
  LDB(0); LDB(1); LDB(2);
#pragma unroll
  for (int n = 0; n < 64; n++) {
    const int q2 = QORD(n) - 1;
    if (n + 3 < 64) LDB(n + 3);
#pragma unroll
    for (int T = 7; T >= 0; T--) { const int s = 2 * T - q2; if (s >= 0 && s <= 2 * T + 1) mma_hf(acc[n >> 4][T], A[s], B[n & 3]); }
    SCHED();
  }
#pragma unroll
  for (int T = 0; T < 8; T++) asm volatile("s_nop 0" : "+v"(acc[3][T]));
  STAMP(19);
  const int srcl = (lane == 0 ? 63 : (g > 0 ? lane - 16 : lane + 47)) * 4;
  v2u zp[8]; float pc[3] = {0.f, 0.f, 0.f};
#pragma unroll
  for (int T = 0; T < 8; T++) {
    const float c[3] = {fadd_(fadd_(acc[1][T][3], acc[2][T][2]), acc[3][T][1]), fadd_(acc[2][T][3], acc[3][T][2]), acc[3][T][3]};
    float y[4] = {acc[0][T][0], fadd_(acc[0][T][1], acc[1][T][0]), fadd_(fadd_(acc[0][T][2], acc[1][T][1]), acc[2][T][0]),
                  fadd_(fadd_(acc[0][T][3], acc[1][T][2]), fadd_(acc[2][T][1], acc[3][T][0]))};
#pragma unroll
    for (int r = 0; r < 3; r++) {
      const float cv = lane == 63 ? pc[r] : c[r];
      y[r] = fadd_(y[r], __int_as_float(__builtin_amdgcn_ds_bpermute(srcl, __float_as_int(cv))));
      pc[r] = c[r];
    }
    zp[T] = v2u{pkbf(fmul_(y[0], oscale), fmul_(y[1], oscale)), pkbf(fmul_(y[2], oscale), fmul_(y[3], oscale))};
    if (T & 1) {
      auto sx = __builtin_amdgcn_permlane16_swap(zp[T - 1].x, zp[T].x, false, false);
      auto sy = __builtin_amdgcn_permlane16_swap(zp[T - 1].y, zp[T].y, false, false);
      stc128(v4u{sx[0], sy[0], sx[1], sy[1]}, rw, WS_ZT + (g & 1) * 512 + j * 32 + (g >> 1) * 16, ch * 4096 + (T - 1) * 512);
    }
  }
  STAMP(20);
  waitv<0>();
  if (lane == 0) stc32(tag, rw, WS_ZFLAG + ch * 4);
  STAMP(11);
}

__global__ __launch_bounds__(512, 2) void k_e(Params q) { extern __shared__ char sm[]; asm volatile("v_mov_b32 v241, 0" ::: "v241"); if (threadIdx.x == 9999) q.ws[0] = sm[5] + q.x[0]; }
__global__ __launch_bounds__(512, 2) void k_main(Params p) {
  extern __shared__ __attribute__((aligned(16))) char smem[];
  const u32 sb = (u32)(size_t)(LAS char*)smem;
  const int lane = threadIdx.x & 63, wv = rfl(threadIdx.x >> 6);
  const int b = blockIdx.x, xc = b & 7, r = b >> 3;
  const bool g1 = r < 30;
  const int r5 = r / 6, ri = (xc >> 2) * 5 + r5, cj = (xc & 3) * 6 + r % 6, ts = (ri * 64) / 5;
  const int i2 = xc * 4 + (r >> 3), j2 = r & 7;
  const u32 CTL = sb + LD_CTL;
  asm volatile(".subsection 2\n\t.fill 128, 4, 0xBF800000\n\t.subsection 0");
  DG_DECL
  STAMP(0);
#define CSTAMP(k)
  if (wv >= 4 && wv < 7) {
    const int slot = wv - 4;
    if (slot == 2 && (r & 1)) ln_wave(p, sb, (r >> 1) * 8 + xc, lane, wv);
    else conv_wave(p, sb, slot, 80 * xc + 5 * (r >> 1) + (r & 1) * 3 + slot, lane, wv, g1, ts, cj);
    return;
  }
  const int g = lane >> 4, i = lane & 15;
  const rsrc_t rw = mkr(p.ws, WS_BYTES);
  const v4u rz = mkrs(p.ws, 0);
  const int rot = (xc >> 2) * 5;
  const v4u rg = mkrs(p.wg, D * F * 2), ru = mkrs(p.wu, D * F * 2), rxs = mkrs(p.x, L * D * 2);
  u32 v[4], vx[4];
#pragma unroll
  for (int k = 0; k < 4; k++) {
    const int row = 8 * k + (lane >> 3), fp = ((row >> 1) & 1) | (((row >> 3) & 1) << 1);
    v[k] = row * 3072 + cj * 128 + (((lane & 7) ^ (fp << 1)) << 4) - 1024 * k;
    vx[k] = (ts * 16 + row) * 1280 + (((lane & 7) ^ ((row >> 1) & 7)) << 4) - 1024 * k;
  }
  if (wv < 4) {
    __builtin_amdgcn_s_setprio(2);
    if (g1) {
      dma4((wv & 1) ? ru : rg, v[0], v[1], v[2], v[3], (rot * 64 + (wv >> 1) * 32) * 3072, sb + LD_R + (wv & 1) * 8192 + (wv >> 1) * 4096);
      dma4(rxs, vx[0], vx[1], vx[2], vx[3], rot * 128 + 3 * wv * 20480, sb + LD_X + 3 * wv * 2048);
      if (wv < 3) dma2(rxs, vx[0], vx[1], rot * 128 + (3 * wv + 2) * 20480, sb + LD_X + (3 * wv + 2) * 2048);
      else dma4(rxs, vx[0], vx[1], vx[2], vx[3], rot * 128 + 11 * 20480, sb + LD_X + 11 * 2048);
    }
    const rsrc_t rx = mkr(p.x, L * D * 2);
    constexpr int NWR = 5;
    v4u Wr[NWR][5];
    const u32 wdo = WS_WDF + (j2 * 48 + 12 * wv) * 5120;
    u32 tag;
    if (g1) {
      f4 acc[3][8], a13[2];
#pragma unroll
      for (int t = 0; t < 3; t++)
#pragma unroll
        for (int c = 0; c < 8; c++) { acc[t][c] = f4{0, 0, 0, 0}; asm volatile("" : "+v"(acc[t][c])); }
      a13[0] = a13[1] = f4{0, 0, 0, 0};
      const int q = (lane >> 2) & 3, pp = lane & 3, fp = ((q >> 1) & 1) | ((g & 1) << 1);
      u32 wb[4];
#pragma unroll
      for (int c = 0; c < 4; c++) wb[c] = sb + LD_R + (8 * g + q) * 128 + ((c ^ wv ^ fp) * 32) + pp * 8;
      u32 xb0 = sb + LD_X + wv * 6144 + i * 128 + ((g ^ (i >> 1)) << 4), xb1 = xb0 ^ 64;
      u32 yb0 = xb0 + (12 - 3 * wv) * 2048, yb1 = yb0 ^ 64, fa = CTL + C_FULL + 4;
      v4u Wf[2][8], Xf[2][4];
      asm volatile("" : "+v"(a13[0]), "+v"(a13[1]), "+v"(wb[0]), "+v"(wb[1]), "+v"(wb[2]), "+v"(wb[3]), "+v"(xb0), "+v"(xb1), "+v"(yb0), "+v"(yb1), "+v"(fa));
#define RDP(B, n, XA, YA, OFF) do { if ((n) == 0) { Xf[B][0] = lds128(XA); Xf[B][1] = lds128(XA + 2048); } \
        else if ((n) == 2) { Xf[B][2] = lds128(XA + 4096); Xf[B][3] = lds128(YA); } \
        else { constexpr int c = (n) == 1 ? 0 : (n) - 2; const u32 a = wb[c & 3] + (OFF) + (c >> 2) * 8192; Wf[B][c] = trd2(a, a + 512); } SCHED(); } while (0)
#define MM(KS, m) do { constexpr int c = (m) < 4 ? 0 : (m) < 13 ? 1 + ((m) - 4) / 3 : (m) < 17 ? 4 : 5 + ((m) - 17) / 3; \
        constexpr int t = (m) < 4 ? (m) : (m) < 13 ? ((m) - 4) % 3 : (m) < 17 ? (m) - 13 : ((m) - 17) % 3; \
        if (t == 3) mma_bf(a13[c >> 2], Wf[KS][c], Xf[KS][3]); else mma_bf(acc[t < 3 ? t : 0][c], Wf[KS][c], Xf[KS][t]); SCHED(); } while (0)
#define R0(m) MM(0, m); RDP(1, m, xb1, yb1, 4096);
#define R1(m) MM(1, m); RDP(0, (m) - 12, xb0, yb0, 0);
      waitv<0>();
      BARRIER();
      STAMP(1);
      RDP(0, 0, xb0, yb0, 0); RDP(0, 1, xb0, yb0, 0); RDP(0, 2, xb0, yb0, 0); RDP(0, 3, xb0, yb0, 0); RDP(0, 4, xb0, yb0, 0);
      RDP(0, 5, xb0, yb0, 0); RDP(0, 6, xb0, yb0, 0); RDP(0, 7, xb0, yb0, 0); RDP(0, 8, xb0, yb0, 0); RDP(0, 9, xb0, yb0, 0);
      u32 sw = 0, sx = 0;
#pragma unroll 1
      for (int s = 0; s < 10; s++) {
        u32 fw, fo;
        R0(0) R0(1) R0(2) R0(3) R0(4) R0(5) R0(6) R0(7) R0(8) R0(9)
        sx += XSLOT; u32 dx = XSLOT; if (sx == NXS * XSLOT) { sx = 0; dx = XSLOT - NXS * XSLOT; }
        sw += WSLOT; u32 dl = WSLOT; if (sw == NWS * WSLOT) { sw = 0; dl = WSLOT - NWS * WSLOT; }
        MM(0, 10); xb0 += dx; xb1 += dx; asm volatile("" : "+v"(xb0), "+v"(xb1)); SCHED();
        MM(0, 11); yb0 += dx; yb1 += dx; asm volatile("" : "+v"(yb0), "+v"(yb1)); SCHED();
        MM(0, 12); wb[0] += dl; wb[1] += dl; asm volatile("" : "+v"(wb[0]), "+v"(wb[1])); SCHED();
        MM(0, 13); wb[2] += dl; wb[3] += dl; asm volatile("" : "+v"(wb[2]), "+v"(wb[3])); SCHED();
        MM(0, 14); MM(0, 15);
        asm volatile("s_waitcnt lgkmcnt(0)" ::: "memory");
        sig0(rz, CTL + C_DONE + s * 16 + wv * 4); SCHED();
        if (s == r5) {
          const u32 tg = ldsv(CTL + C_TAG);
          f4 c0 = {0, 0, 0, 0}, c1 = {0, 0, 0, 0}; v4u on = {0x3f803f80u, 0x3f803f80u, 0x3f803f80u, 0x3f803f80u};
          asm volatile("s_nop 1" : "+v"(c0), "+v"(c1), "+v"(on));
          mma_bf(c0, Wf[0][0], on); mma_bf(c1, Wf[0][4], on); mma_bf(c0, Wf[1][0], on); mma_bf(c1, Wf[1][4], on);
          asm volatile("s_nop 7" : "+v"(c0), "+v"(c1));
          const f4 cc = (i & 1) ? c1 : c0;
          if (i < 2) {
            const u32 o = WS_CSP + (i * 64 + 16 * wv + 4 * g) * 8, so = (cj * 10 + ri) * 1024;
            stc128(v4u{__float_as_uint(cc[0]), tg, __float_as_uint(cc[1]), tg}, rw, o, so); stc128(v4u{__float_as_uint(cc[2]), tg, __float_as_uint(cc[3]), tg}, rw, o + 16, so);
          }
          asm volatile("s_nop 3"); SCHED();
        }
        MM(0, 16); MM(0, 17); MM(0, 18); MM(0, 19); MM(0, 20); MM(0, 21); MM(0, 22); MM(0, 23); MM(0, 24); MM(0, 25);
        MM(1, 0); MM(1, 1); MM(1, 2); MM(1, 3); fw = ldsv(fa); SCHED();
        MM(1, 4); MM(1, 5); MM(1, 6); MM(1, 7); MM(1, 8); MM(1, 9); fo = rfl(fw); SCHED(); MM(1, 10); MM(1, 11);
        spin0(fa, fo);
        R1(12) R1(13) R1(14) R1(15) R1(16) R1(17) R1(18) R1(19) R1(20) R1(21)
        MM(1, 22); fa += 4; asm volatile("" : "+v"(fa)); SCHED(); MM(1, 23); MM(1, 24); MM(1, 25);
      }
      CSTAMP(14);
      { const v4u cw = *(volatile LAS v4u*)(CTL + C_RDY); tag = rfl(cw.w); spin0(CTL + C_STF, rfl(cw.z)); spin0(CTL + C_RDY, rfl(cw.x)); }
      STAMP(2);
      f4 cg[4], cu[4]; f2 st[4];
#pragma unroll
      for (int t = 0; t < 4; t++) st[t] = *(LAS f2*)(sb + LD_ST + ((t < 3 ? 3 * wv + t : 12) * 16 + i) * 8);
#pragma unroll
      for (int c = 0; c < 4; c++) { cg[c] = *(LAS f4*)(sb + LD_CS + (16 * (c ^ wv) + 4 * g) * 4); cu[c] = *(LAS f4*)(sb + LD_CS + 256 + (16 * (c ^ wv) + 4 * g) * 4); }
      const u32 hb = WS_HF + (2 * cj) * 1024 + (((((g ^ wv) & 1) << 1) | (g >> 1)) * 16 + i) * 16;
#pragma unroll
      for (int t = 0; t < 4; t++) {
        f2 k1 = {-st[t].x, -1.44269504089f * st[t].y}, k2 = {__builtin_amdgcn_rcpf(st[t].y), 0.f};
        asm volatile("s_nop 0" : "+v"(k1), "+v"(k2));
        v2u P[4];
#pragma unroll
        for (int c = 0; c < 4; c++) {
          if (t == 3 && c > 0) break;
          const f4 ag = t < 3 ? acc[t < 3 ? t : 0][c] : a13[0], au = t < 3 ? acc[t < 3 ? t : 0][4 + c] : a13[1];
#pragma unroll
          for (int e = 0; e < 2; e++) {
            const f2 a2 = e ? ag.zw : ag.xy, u2 = e ? au.zw : au.xy, cg2 = e ? cg[c].zw : cg[c].xy, cu2 = e ? cu[c].zw : cu[c].xy;
            const f2 gg = pmul_y(k1, pfma_x(k1, cg2, a2)), uu = pfma_x(k1, cu2, u2);
            const f2 den = pfma_xx(k2, f2{__builtin_amdgcn_exp2f(gg.x), __builtin_amdgcn_exp2f(gg.y)});
            const f2 h = pmul(uu, f2{__builtin_amdgcn_rcpf(den.x), __builtin_amdgcn_rcpf(den.y)});
            P[c][e] = pkbf(h.x, h.y);
            { const int n = (t * 4 + c) * 2 + e; if (n < 25) Wr[n / 5][n % 5] = ldc128(rw, lane * 16, wdo + n * 1024); }
          }
          if (c & 1) SCHED();
        }
        if (t < 3) {
#pragma unroll
          for (int k = 0; k < 2; k++) {
            auto sx2 = __builtin_amdgcn_permlane16_swap(P[2 * k].x, P[2 * k + 1].x, false, false);
            auto sy2 = __builtin_amdgcn_permlane16_swap(P[2 * k].y, P[2 * k + 1].y, false, false);
            stc128(v4u{sx2[0], sy2[0], sx2[1], sy2[1]}, rw, hb + (k ^ (wv >> 1)) * 1024, (ts + 3 * wv + t) * 49152);
          }
        } else if (r5 != 0) {
          __builtin_amdgcn_raw_buffer_store_b64(P[0], rw, WS_HF + (2 * cj + (wv >> 1)) * 1024 + ((2 * (wv & 1) + (g >> 1)) * 16 + i) * 16 + (g & 1) * 8, (ts + 12) * 49152, AUX_SC1);
        }
        SCHED();
      }
      STAMP(3);
      sig0(rz, CTL + C_DONE + 160 + wv * 4);
      waitv<0>();
      if (lane == 0) stc32(tag, rw, WS_HFLAG + ((ri * 24 + cj) * 4 + wv) * 4);
      STAMP(4);
    } else {
      BARRIER();
      tag = rfl(ldsv(CTL + C_TAG));
      spin0(CTL + C_RDY, 1);
#pragma unroll
      for (int n = 0; n < 25; n++) Wr[n / 5][n % 5] = ldc128(rw, lane * 16, wdo + n * 1024);
    }
    const int tt = 4 * i2 + wv, tok = 16 * tt + i, d0 = 80 * j2 + 20 * g;
    f4 o[4][5];
    {
      const rsrc_t rd = mkr(p.dsk, D * 2);
      const v4u xa = __builtin_amdgcn_raw_buffer_load_b128(rx, (tok * D + d0) * 2, 0, 0), xb = __builtin_amdgcn_raw_buffer_load_b128(rx, (tok * D + d0) * 2 + 16, 0, 0);
      const v2u xc2 = __builtin_amdgcn_raw_buffer_load_b64(rx, (tok * D + d0) * 2 + 32, 0, 0);
      const v4u da = __builtin_amdgcn_raw_buffer_load_b128(rd, d0 * 2, 0, 0), db = __builtin_amdgcn_raw_buffer_load_b128(rd, d0 * 2 + 16, 0, 0);
      const v2u dc = __builtin_amdgcn_raw_buffer_load_b64(rd, d0 * 2 + 32, 0, 0);
      const v4u stv = ldc128(rw, WS_STG + tok * 16, 0);
      const float rs = __uint_as_float(stv.z), nb = fmul_(-__uint_as_float(stv.x), rs);
      const u32 xw[10] = {xa.x, xa.y, xa.z, xa.w, xb.x, xb.y, xb.z, xb.w, xc2.x, xc2.y}, dw[10] = {da.x, da.y, da.z, da.w, db.x, db.y, db.z, db.w, dc.x, dc.y};
#pragma unroll
      for (int k = 0; k < 10; k++) {
        const float x0 = bf2f(xw[k]), x1 = bfhi(xw[k]);
        o[0][k >> 1][2 * (k & 1)] = ffma_(ffma_(x0, rs, nb), bf2f(dw[k]), x0);
        o[0][k >> 1][2 * (k & 1) + 1] = ffma_(ffma_(x1, rs, nb), bfhi(dw[k]), x1);
      }
#pragma unroll
      for (int t = 0; t < 4; t++)
#pragma unroll
        for (int c = 0; c < 5; c++) { if (t > 0) o[t][c] = f4{0, 0, 0, 0}; asm volatile("" : "+v"(o[t][c])); }
    }
    { const u32 fo = WS_HFLAG + (24 * wv + lane % 24) * 4; poll_flags<6>(rw, fo + ((20 * i2 + 4) >> 6) * 384, fo + ((20 * i2 + 19) >> 6) * 384, tag); }
    STAMP(5);
    constexpr int HD = 3;
    v4u hr[12][4], Wl[2][5], Wz[5];
    u32 ho[4];
#pragma unroll
    for (int t = 0; t < 4; t++) ho[t] = WS_HF + (4 * i2 + (t ^ wv)) * 49152 + 12 * wv * 1024;
#define LOAD_H(k) do { _Pragma("unroll") for (int t = 0; t < 4; t++) hr[k][t] = ldc128(rw, lane * 16, ho[t] + (k) * 1024); } while (0)
#pragma unroll
    for (int k = 0; k < HD; k++) LOAD_H(k);
    SCHED();
    const u32 wl = sb + wd_off(wv) + lane * 16;
#pragma unroll
    for (int k = 0; k < 12; k++) {
      if (k == NWR - 1) spin0(CTL + C_WDFULL, 1);
#pragma unroll
      for (int c = 0; c < 5; c++) {
        const v4u w = k < NWR ? Wr[k < NWR ? k : 0][c] : (k < 11 ? Wl[k & 1][c] : Wz[c]);
#pragma unroll
        for (int t = 3; t >= 0; t--) {
          mma_bf(o[t][c], w, hr[k][t]); SCHED();
          if (t == 1 && k == 8) { Wz[c] = ldc128(rw, lane * 16, wdo + (55 + c) * 1024); SCHED(); }
          if (t == 2 && k + 1 >= NWR && k + 1 < 11) { Wl[(k + 1) & 1][c] = lds128(wl + ((k + 1 - NWR) * 5 + c) * 1024); SCHED(); }
          if (t == 3 && c < 4) {
            const int kn = k == 0 ? -1 : k + HD;
            if (kn > 0 && kn < 12) { hr[kn][c] = ldc128(rw, lane * 16, ho[c] + kn * 1024); SCHED(); }
          }
        }
      }
      if (k == 0) { LOAD_H(HD); SCHED(); }
    }
    asm volatile("s_nop 7"); SCHED();
    STAMP(6);
    {
#pragma unroll
      for (int t = 1; t < 4; t++)
#pragma unroll
        for (int c = 0; c < 5; c++) *(LAS f4*)(wl + ((t - 1) * 5 + c) * 1024) = o[t][c];
      ldsw(CTL + C_RED + wv * 4, 0);
      spin0(CTL + C_ZFULL, 1);
      STAMP(7);
      const int q = (lane >> 2) & 3, pp = lane & 3;
      v2u z[5];
#pragma unroll
      for (int c = 0; c < 5; c++) z[c] = trd(sb + LD_K + (20 * g + 4 * c + q) * 128 + wv * 32 + pp * 8);
#pragma unroll
      for (int c = 0; c < 5; c++) {
        o[0][c][0] = fadd_(o[0][c][0], bf2f(z[c].x)); o[0][c][1] = fadd_(o[0][c][1], bfhi(z[c].x));
        o[0][c][2] = fadd_(o[0][c][2], bf2f(z[c].y)); o[0][c][3] = fadd_(o[0][c][3], bfhi(z[c].y));
      }
      spin_zero4(CTL + C_RED);
      u32 ow[10];
#pragma unroll
      for (int c = 0; c < 5; c++) {
        f4 s[3];
#pragma unroll
        for (int t = 1; t < 4; t++) s[t - 1] = *(LAS f4*)(sb + wd_off(wv ^ t) + lane * 16 + ((t - 1) * 5 + c) * 1024);
        float y[4];
#pragma unroll
        for (int e = 0; e < 4; e++) y[e] = fadd_(fadd_(o[0][c][e], s[0][e]), fadd_(s[1][e], s[2][e]));
        ow[2 * c] = pkbf(y[0], y[1]); ow[2 * c + 1] = pkbf(y[2], y[3]);
      }
      const rsrc_t ro = mkr(p.out, L * D * 2);
      __builtin_amdgcn_raw_buffer_store_b128(v4u{ow[0], ow[1], ow[2], ow[3]}, ro, (tok * D + d0) * 2, 0, 0);
      __builtin_amdgcn_raw_buffer_store_b128(v4u{ow[4], ow[5], ow[6], ow[7]}, ro, (tok * D + d0) * 2 + 16, 0, 0);
      __builtin_amdgcn_raw_buffer_store_b64(v2u{ow[8], ow[9]}, ro, (tok * D + d0) * 2 + 32, 0, 0);
    }
    STAMP(8); CSTAMP(15);
  } else {
    if (g1) {
      int kx = rot + 1, kw = kx; u32 sw = WSLOT, sx = XSLOT;
#define ISSUE_X(K0, K1) do { _Pragma("unroll") for (int k = K0; k < K1; k++) dma4(rxs, vx[0], vx[1], vx[2], vx[3], kx * 128 + k * 40960, sb + LD_X + sx + k * 4096); } while (0)
#define ISSUE_XE() do { dma2(rxs, vx[0], vx[1], kx * 128 + 12 * 20480, sb + LD_X + sx + 12 * 2048); kx = kx == 9 ? 0 : kx + 1; sx += XSLOT; if (sx == NXS * XSLOT) sx = 0; } while (0)
#define ISSUE_W() do { const u32 o_ = kw * 64 * 3072; dma4(rg, v[0], v[1], v[2], v[3], o_, sb + LD_R + sw); dma4(ru, v[0], v[1], v[2], v[3], o_, sb + LD_R + sw + 8192); \
        dma4(rg, v[0], v[1], v[2], v[3], o_ + 32 * 3072, sb + LD_R + sw + 4096); dma4(ru, v[0], v[1], v[2], v[3], o_ + 32 * 3072, sb + LD_R + sw + 12288); \
        kw = kw == 9 ? 0 : kw + 1; sw += WSLOT; if (sw == NWS * WSLOT) sw = 0; } while (0)
      __builtin_amdgcn_s_sleep(1);
      ISSUE_W(); ISSUE_X(0, 6); ISSUE_XE(); ISSUE_W();
      BARRIER();
      __builtin_amdgcn_s_setprio(3);
#pragma unroll 1
      for (int n = 2; n < 10; n++) {
        bool pend = true;
        if (n >= 3 && !is_zero4(CTL + C_DONE + (n - 3) * 16)) { waitv<16>(); sig0(rz, CTL + C_FULL + (n - 1) * 4); pend = false; spin_zero4(CTL + C_DONE + (n - 3) * 16); }
        waitv<44>();
        ISSUE_X(0, 4);
        if (pend) { waitv<32>(); sig0(rz, CTL + C_FULL + (n - 1) * 4); }
        ISSUE_X(4, 6); ISSUE_XE();
        if (n < 9) ISSUE_W();
      }
      waitv<0>(); sig0(rz, CTL + C_FULL + 36);
      STAMP(12);
      spin_zero4(CTL + C_DONE + 9 * 16);
    } else BARRIER();
    __builtin_amdgcn_s_setprio(1);
    const u32 tag = rfl(ldsv(CTL + C_TAG));
    spin_zero4(CTL + C_KDONE);
    STAMP(23);
    const v4u rws = mkrs(p.ws, WS_BYTES);
    {
      const u32 vo = lane * 16;
#pragma unroll 1
      for (int k = 0; k < 24; k++) {
        const int w = k & 3, pc = k >> 2;
        const u32 go = WS_WDF + (j2 * 48 + 12 * w + 5 + pc) * 5120, lo = sb + wd_off(w) + pc * 5120;
        dma4c(rws, vo, vo, vo, vo, go, lo); dma1c(rws, vo, go + 4096, lo + 4096);
        waitv<WD_DEPTH>();
      }
      waitv<0>();
      sig0(rz, CTL + C_WDFULL);
    }
    STAMP(24);
    __builtin_amdgcn_s_setprio(3);
    poll_flags<8>(rw, WS_ZFLAG + j2 * 320 + lane * 4, WS_ZFLAG + j2 * 320 + 64 + lane * 4, tag);
    {
      u32 vz[4];
#pragma unroll
      for (int k = 0; k < 4; k++) vz[k] = (8 * k + (lane >> 3)) * 4096 + (lane & 7) * 16 - 1024 * k;
      const u32 go = WS_ZT + 80 * j2 * 4096 + i2 * 128;
      dma4c(rws, vz[0], vz[1], vz[2], vz[3], go, sb + LD_K); dma4c(rws, vz[0], vz[1], vz[2], vz[3], go + 32 * 4096, sb + LD_K + 4096);
      dma2c(rws, vz[0], vz[1], go + 64 * 4096, sb + LD_K + 8192);
      waitv<0>();
      sig0(rz, CTL + C_ZFULL);
      waitv<0>();
    }
    STAMP(25);
  }
}

extern "C" void kernel_launch(void* const* d_in, const int* in_sizes, int n_in, void* d_out, int out_size, void* d_ws, size_t ws_size, hipStream_t stream) {
  Params p;
  p.x = (const u16*)d_in[0]; p.lar = (const u16*)d_in[1]; p.aim = (const u16*)d_in[2]; p.cre = (const u16*)d_in[3]; p.cim = (const u16*)d_in[4];
  p.ldt = (const u16*)d_in[5]; p.dsk = (const u16*)d_in[6]; p.wg = (const u16*)d_in[8]; p.wu = (const u16*)d_in[9]; p.wd = (const u16*)d_in[10];
  p.out = (u16*)d_out; p.ws = (char*)d_ws;
  (void)hipFuncSetAttribute((const void*)k_main, hipFuncAttributeMaxDynamicSharedMemorySize, LDS_BYTES);
  k_main<<<256, 512, LDS_BYTES, stream>>>(p);
  (void)hipFuncSetAttribute((const void*)k_e, hipFuncAttributeMaxDynamicSharedMemorySize, LDS_BYTES);
  k_e<<<256, 512, LDS_BYTES, stream>>>(p);
}
```

```cpp
#include <hip/hip_runtime.h>
#pragma clang diagnostic ignored "-Wint-to-pointer-cast"
typedef unsigned short u16; typedef unsigned u32; typedef unsigned long long u64;
typedef u32 v4u __attribute__((ext_vector_type(4)));
typedef u32 v2u __attribute__((ext_vector_type(2)));
typedef float f4 __attribute__((ext_vector_type(4)));
typedef float f2 __attribute__((ext_vector_type(2)));
typedef short s4 __attribute__((ext_vector_type(4)));
typedef __bf16 bf2 __attribute__((ext_vector_type(2)));
typedef _Float16 hf2 __attribute__((ext_vector_type(2)));
#define LAS __attribute__((address_space(3)))
#define GAS __attribute__((address_space(1)))
#define DEV __device__ __forceinline__
typedef __amdgpu_buffer_rsrc_t rsrc_t;

constexpr int L = 2048, D = 640, NS = 192, F = 1536;
constexpr u32 WS_GEN = 0, WS_HFLAG = 1024, WS_ZFLAG = 40960, WS_LFLAG = 49152, WS_WFLAG = WS_LFLAG + 512, WS_STAT = 16384, WS_CSP = 65536,
              WS_XT = 1u << 20, WS_WDF = 4u << 20, WS_HF = 6u << 20, WS_ZT = 13u << 20, WS_BYTES = 16u << 20;
constexpr u32 LD_K = 0, K_BYTES = 5120;
constexpr u32 LD_CS = LD_K + 3 * K_BYTES, LD_ST = LD_CS + 512, LD_CTL = LD_ST + 208 * 8, LD_R = LD_CTL + 512;
constexpr u32 WSLOT = 16384, NWS = 4, XSLOT = 26624, NXS = 3, LD_X = LD_R + NWS * WSLOT, LDS_BYTES = LD_X + NXS * XSLOT;
constexpr u32 LD_H = LD_X + 2 * XSLOT;
constexpr u32 WD_BYTES = 30720, LD_Z = LD_X;
DEV u32 wd_off(int w) { return LD_R + ((0x70520020u >> (8 * w)) & 0xffu) * 1024; }
static_assert(LD_R % 128 == 0 && 18432 + 2 * WD_BYTES == NXS * XSLOT && LDS_BYTES <= 163840, "lds");
constexpr u32 C_FULL = 0, C_WDFULL = 44, C_RDY = 48, C_ZFULL = 52, C_WOK = 56, C_TAG = 60, C_KDONE = 64, C_RED = 80, C_CDONE = 96, C_HRDY = 112, C_QUIET = 128, C_HCNT = 132, C_DONE = 256;
constexpr int AUX_SC1 = 16;
constexpr u32 SPIN_CAP = 1u << 15;
#define WD_DEPTH 40
#define WD_PRIO 3
#define POLL_STAGE 8
#define WPOLL_STAGE 7
#define WD_P1 12
#define HP_SLEEP 4
#define G2_PRIO 1
#define ABL_H 0
#define ABL_Z 0
#define ZP_FIRST 16
#define ZP_SLEEP 8
#define GWAIT 0
#define P1MODE 1
#define KL_PRIO 1
#define G1ONLY 0
#define CONV_PRIO 2

#define STAMP(k) do { } while (0)
#define ST_ARG
#define STAMPD(k) do { } while (0)
#define ST_PASS
#define ST_FLUSH() do { } while (0)

struct Params {
  const u16 *x, *lar, *aim, *cre, *cim, *ldt, *dsk, *wg, *wu, *wd;
  u16* out; char* ws;
};

DEV float bf2f(u32 v) { return __uint_as_float(v << 16); }
DEV float bfhi(u32 v) { return __uint_as_float(v & 0xffff0000u); }
DEV u32 pkbf(float a, float b) { f2 v = {a, b}; return __builtin_bit_cast(u32, __builtin_convertvector(v, bf2)); }
DEV u32 pkhf(float a, float b) { f2 v = {a, b}; return __builtin_bit_cast(u32, __builtin_convertvector(v, hf2)); }
DEV float rbf(float a) { return bf2f(pkbf(a, a)); }
DEV void mma_bf(f4& c, v4u a, v4u b) { asm volatile("v_mfma_f32_16x16x32_bf16 %0, %1, %2, %0" : "+v"(c) : "v"(a), "v"(b)); }
DEV void mma_hf(f4& c, v4u a, v4u b) { asm volatile("v_mfma_f32_16x16x32_f16 %0, %1, %2, %0" : "+v"(c) : "v"(a), "v"(b)); }
DEV void mma_hf0(f4& c, v4u a, v4u b) { asm volatile("v_mfma_f32_16x16x32_f16 %0, %1, %2, 0" : "=&v"(c) : "v"(a), "v"(b)); }
DEV void mma_bf0(f4& c, v4u a, v4u b) { asm volatile("v_mfma_f32_16x16x32_bf16 %0, %1, %2, 0" : "=&v"(c) : "v"(a), "v"(b)); }
DEV v2u trd(u32 a) { return __builtin_bit_cast(v2u, __builtin_amdgcn_ds_read_tr16_b64_v4i16((LAS s4*)a)); }
DEV v4u trd2(u32 a, u32 b) { v2u x = trd(a), y = trd(b); return v4u{x.x, x.y, y.x, y.y}; }
DEV v4u lds128(u32 a) { return *(LAS v4u*)a; }
DEV v2u lds64(u32 a) { return *(LAS v2u*)a; }
DEV u32 rfl(u32 v) { return __builtin_amdgcn_readfirstlane(v); }
DEV v4u mkrs(const void* p, u32 bytes) { u64 a = (u64)p; return v4u{(u32)a, (u32)(a >> 32) & 0xffffu, bytes, 0x00020000u}; }
DEV rsrc_t mkr(const void* p, u32 bytes) { return __builtin_amdgcn_make_buffer_rsrc((void*)p, 0, bytes, 0x00020000); }
DEV v4u ldc128(rsrc_t r, u32 v, u32 s) { return __builtin_amdgcn_raw_buffer_load_b128(r, v, s, AUX_SC1); }
DEV u32 ldc32(rsrc_t r, u32 v) { return __builtin_amdgcn_raw_buffer_load_b32(r, v, 0, AUX_SC1); }
DEV void stc128(v4u d, rsrc_t r, u32 v, u32 s) {
  __builtin_amdgcn_raw_buffer_store_b128(d, r, v, s, AUX_SC1); asm volatile("s_nop 1" ::"v"(d) : "memory"); }
DEV void stc32(u32 d, rsrc_t r, u32 v) { __builtin_amdgcn_raw_buffer_store_b32(d, r, v, 0, AUX_SC1); }
#define CBAR() asm volatile("" ::: "memory")
#define SCHED() __builtin_amdgcn_sched_barrier(0)
#define ORDER() __builtin_amdgcn_fence(__ATOMIC_ACQUIRE, "wavefront")
#define BARRIER() do { asm volatile("s_waitcnt lgkmcnt(0)" ::: "memory"); __builtin_amdgcn_s_barrier(); CBAR(); } while (0)
template <int N> DEV void waitv() { asm volatile("s_waitcnt vmcnt(%0)" ::"i"(N) : "memory"); }
#define DMA_(V, RS, SO, OFF, SC) "buffer_load_dwordx4 " V ", " RS ", " SO " offen" OFF SC " lds\n\t"
#define DMA4_(NAME, SC) DEV void NAME(v4u rs, u32 v0, u32 v1, u32 v2, u32 v3, u32 so, u32 lds) { \
  asm volatile("s_mov_b32 m0, %0\n\ts_nop 0\n\t" DMA_("%1", "%5", "%6", "", SC) DMA_("%2", "%5", "%6", " offset:1024", SC) DMA_("%3", "%5", "%6", " offset:2048", SC) DMA_("%4", "%5", "%6", " offset:3072", SC) \
               ::"s"(lds), "v"(v0), "v"(v1), "v"(v2), "v"(v3), "s"(rs), "s"(so) : "memory"); }
#define DMA2_(NAME, SC) DEV void NAME(v4u rs, u32 v0, u32 v1, u32 so, u32 lds) { \
  asm volatile("s_mov_b32 m0, %0\n\ts_nop 0\n\t" DMA_("%1", "%3", "%4", "", SC) DMA_("%2", "%3", "%4", " offset:1024", SC) ::"s"(lds), "v"(v0), "v"(v1), "s"(rs), "s"(so) : "memory"); }
#define DMA1_(NAME, SC) DEV void NAME(v4u rs, u32 v0, u32 so, u32 lds) { \
  asm volatile("s_mov_b32 m0, %0\n\ts_nop 0\n\t" DMA_("%1", "%2", "%3", "", SC) ::"s"(lds), "v"(v0), "s"(rs), "s"(so) : "memory"); }
DMA4_(dma4, "") DMA2_(dma2, "") DMA1_(dma1, "") DMA4_(dma4c, " sc1") DMA2_(dma2c, " sc1") DMA1_(dma1c, " sc1")
DEV void sig0(v4u rz, u32 lds) {
  asm volatile("s_mov_b32 m0, %0\n\ts_mov_b64 exec, 1\n\tbuffer_load_dword off, %1, 0 lds\n\ts_mov_b64 exec, -1" ::"s"(lds), "s"(rz) : "memory");
}
DEV u32 mktag(u32 gen) { return gen * 0x9E3779B1u + 0x7F4A7C15u; }
DEV u32 ldsv(u32 a) { return *(volatile LAS u32*)a; }
DEV void ldsw(u32 a, u32 v) { asm volatile("s_mov_b64 exec, 1\n\tds_write_b32 %0, %1\n\ts_mov_b64 exec, -1" ::"v"(a), "v"(v) : "memory"); }
DEV void spin0(u32 a, u32 cur) {
  u32 tv, ts, cnt;
  asm volatile("s_cmp_eq_u32 %4, 0\n\ts_cbranch_scc0 3f\n2:\n\t.subsection 1\n3:\n\ts_mov_b32 %2, 0x10000\n"
               "1:\n\tds_read_b32 %0, %3\n\ts_waitcnt lgkmcnt(0)\n\tv_readfirstlane_b32 %1, %0\n\ts_nop 0\n\ts_cmp_eq_u32 %1, 0\n\ts_cbranch_scc1 2b\n\t"
               "s_sleep 1\n\ts_sub_u32 %2, %2, 1\n\ts_cmp_lg_u32 %2, 0\n\ts_cbranch_scc1 1b\n\ts_branch 2b\n\t.subsection 0"
               : "=&v"(tv), "=&s"(ts), "=&s"(cnt) : "v"(a), "s"(cur) : "memory", "scc");
}
DEV void wait0(u32 a) {
  u32 tv, ts, cnt;
  asm volatile("s_mov_b32 %2, 0x10000\n1:\n\tds_read_b32 %0, %3\n\ts_sub_u32 %2, %2, 1\n\ts_waitcnt lgkmcnt(0)\n\tv_readfirstlane_b32 %1, %0\n\ts_nop 0\n\t"
               "s_min_u32 %1, %1, %2\n\ts_cmp_eq_u32 %1, 0\n\ts_cbranch_scc1 2f\n\ts_sleep 1\n\ts_branch 1b\n2:"
               : "=&v"(tv), "=&s"(ts), "=&s"(cnt) : "v"(a) : "memory", "scc");
}
DEV void spin_zero4(u32 a) {
  u32 tv, cnt;
  const u32 al = a + (__builtin_amdgcn_mbcnt_lo(~0u, 0) & 3) * 4;
  asm volatile("s_mov_b32 %1, 0x10000\n1:\n\tds_read_b32 %0, %2\n\ts_sub_u32 %1, %1, 1\n\ts_waitcnt lgkmcnt(0)\n\tv_cmp_ne_u32 vcc, 0, %0\n\ts_nop 1\n\t"
               "s_min_u32 vcc_lo, vcc_lo, %1\n\ts_cmp_eq_u32 vcc_lo, 0\n\ts_cbranch_scc1 2f\n\ts_sleep 1\n\ts_branch 1b\n2:"
               : "=&v"(tv), "=&s"(cnt) : "v"(al) : "memory", "scc", "vcc");
}
template <int SL> DEV void poll_flags(rsrc_t rw, u32 off, u32 off2, u32 tag) {
#pragma unroll 1
  for (u32 i = 0; i < SPIN_CAP; i++) {
    const u32 a = ldc32(rw, off), b = ldc32(rw, off2);
    if (__all(a == tag && b == tag)) break;
    __builtin_amdgcn_s_sleep(SL);
  }
  ORDER();
}
DEV float fmul_(float a, float b) { float r; asm("v_mul_f32 %0, %1, %2" : "=v"(r) : "v"(a), "v"(b)); return r; }
DEV float fadd_(float a, float b) { float r; asm("v_add_f32 %0, %1, %2" : "=v"(r) : "v"(a), "v"(b)); return r; }
DEV float ffma_(float a, float b, float c) { float r; asm("v_fma_f32 %0, %1, %2, %3" : "=v"(r) : "v"(a), "v"(b), "v"(c)); return r; }
DEV float fnma_(float a, float b, float c) { float r; asm("v_fma_f32 %0, -%1, %2, %3" : "=v"(r) : "v"(a), "v"(b), "v"(c)); return r; }
struct cf { float x, y; };
DEV cf csq(cf a) { const float t = fmul_(a.x, a.y); return cf{fnma_(a.y, a.y, fmul_(a.x, a.x)), fadd_(t, t)}; }
DEV u32 pkc(cf a) { return pkhf(a.x, a.y); }
DEV cf cexp2(float lr, float rev) { float m = __builtin_amdgcn_exp2f(lr); const float f = __builtin_amdgcn_fractf(rev); float c = __builtin_amdgcn_cosf(f), s = __builtin_amdgcn_sinf(f);
  asm volatile("s_nop 0" : "+v"(c), "+v"(s), "+v"(m)); return cf{fmul_(m, c), fmul_(m, s)}; }
DEV u32 hmul(u32 a, u32 m) { u32 t, r;
  asm("v_pk_mul_f16 %0, %1, %2 op_sel_hi:[0,1]" : "=v"(t) : "v"(a), "v"(m));
  asm("v_pk_fma_f16 %0, %1, %2, %3 op_sel:[1,1,0] op_sel_hi:[1,0,1] neg_lo:[0,1,0]" : "=v"(r) : "v"(a), "v"(m), "v"(t)); return r; }
DEV u32 hmulc(u32 a, u32 m) { u32 t, r;
  asm("v_pk_mul_f16 %0, %1, %2 op_sel_hi:[0,1] neg_hi:[0,1]" : "=v"(t) : "v"(a), "v"(m));
  asm("v_pk_fma_f16 %0, %1, %2, %3 op_sel:[1,1,0] op_sel_hi:[1,0,1]" : "=v"(r) : "v"(a), "v"(m), "v"(t)); return r; }
DEV f2 pfma_x(f2 s, f2 b, f2 c) { f2 r; asm("v_pk_fma_f32 %0, %1, %2, %3 op_sel_hi:[0,1,1]" : "=v"(r) : "v"(s), "v"(b), "v"(c)); return r; }
DEV f2 pmul_y(f2 s, f2 b) { f2 r; asm("v_pk_mul_f32 %0, %1, %2 op_sel:[1,0]" : "=v"(r) : "v"(s), "v"(b)); return r; }
DEV f2 pfma_xx(f2 s, f2 b) { f2 r; asm("s_nop 0\n\tv_pk_fma_f32 %0, %1, %2, %1 op_sel_hi:[0,1,0]" : "=v"(r) : "v"(s), "v"(b)); return r; }
DEV f2 pmul(f2 a, f2 b) { f2 r; asm("s_nop 0\n\tv_pk_mul_f32 %0, %1, %2" : "=v"(r) : "v"(a), "v"(b)); return r; }
DEV void swap16(float& a, float& b) { auto r = __builtin_amdgcn_permlane16_swap(__float_as_uint(a), __float_as_uint(b), false, false); a = __uint_as_float(r[0]); b = __uint_as_float(r[1]); }
DEV void swap32(float& a, float& b) { auto r = __builtin_amdgcn_permlane32_swap(__float_as_uint(a), __float_as_uint(b), false, false); a = __uint_as_float(r[0]); b = __uint_as_float(r[1]); }

DEV void ln_unit(const Params& p, u32 sb, int pn, int lane) {
  const rsrc_t rx = mkr(p.x, L * D * 2), rw = mkr(p.ws, WS_BYTES);
  const int h = lane & 1, m = lane >> 1, row0 = 64 * (pn >> 2) + (pn & 3);
  __builtin_amdgcn_s_setprio(2);
  v2u xv[8][5]; float xf[8][20], s[16];
#pragma unroll
  for (int e = 0; e < 8; e++)
#pragma unroll
    for (int i = 0; i < 5; i++) xv[e][i] = __builtin_amdgcn_raw_buffer_load_b64(rx, (row0 + 32 * h) * 1280 + m * 8 + i * 256, e * 5120, 0);
  SCHED();
  __builtin_amdgcn_s_setprio(3);
#pragma unroll
  for (int e = 0; e < 8; e++) {
#pragma unroll
    for (int i = 0; i < 10; i++) { const u32 w = xv[e][i >> 1][i & 1]; xf[e][2 * i] = bf2f(w); xf[e][2 * i + 1] = bfhi(w); }
    float a = fadd_(xf[e][0], xf[e][1]), q = fmul_(xf[e][0], xf[e][0]);
#pragma unroll
    for (int i = 1; i < 20; i++) { if (i > 1) a = fadd_(a, xf[e][i]); asm("v_fmac_f32 %0, %1, %1" : "+v"(q) : "v"(xf[e][i])); }
    s[e] = a; s[8 + e] = q;
  }
#pragma unroll
  for (int e = 0; e < 16; e++) asm volatile("" : "+v"(s[e]));
  asm volatile("s_nop 1");
  float z[4];
#pragma unroll
  for (int k = 0; k < 4; k++) {
    swap16(s[4 * k], s[4 * k + 1]); swap16(s[4 * k + 2], s[4 * k + 3]);
    float a = fadd_(s[4 * k], s[4 * k + 1]), b = fadd_(s[4 * k + 2], s[4 * k + 3]);
    asm volatile("s_nop 1" : "+v"(a), "+v"(b));
    swap32(a, b); z[k] = fadd_(a, b);
  }
#define DPPADD(C) asm volatile("s_nop 1\n\tv_add_f32_dpp %0, %0, %0 " C " row_mask:0xf bank_mask:0xf\n\tv_add_f32_dpp %1, %1, %1 " C " row_mask:0xf bank_mask:0xf\n\t" \
    "v_add_f32_dpp %2, %2, %2 " C " row_mask:0xf bank_mask:0xf\n\tv_add_f32_dpp %3, %3, %3 " C " row_mask:0xf bank_mask:0xf" : "+v"(z[0]), "+v"(z[1]), "+v"(z[2]), "+v"(z[3]))
  DPPADD("quad_perm:[2,3,0,1]"); DPPADD("row_ror:4"); DPPADD("row_ror:8");
  float rs[8], nb[8];
#pragma unroll
  for (int k = 0; k < 2; k++) {
    const float mean = fmul_(z[k], 1.f / D), var = fmaxf(fnma_(mean, mean, fmul_(z[2 + k], 1.f / D)), 0.f);
    const float mb = rbf(mean), sd = rbf(__builtin_amdgcn_sqrtf(rbf(rbf(var) + 1e-5f)));
    float r = __builtin_amdgcn_rcpf(sd);
    asm volatile("s_nop 0" : "+v"(r));
    r = ffma_(fnma_(sd, r, 1.f), r, r);
    if ((lane & 15) < 2) __builtin_amdgcn_raw_buffer_store_b64(v2u{__float_as_uint(mb), __float_as_uint(r)}, rw, WS_STAT + (row0 + 32 * h + 4 * (4 * k + (lane >> 4))) * 8, 0, AUX_SC1);
    float t[2] = {r, fmul_(-mb, r)};
#pragma unroll
    for (int w = 0; w < 2; w++) {
      float a = t[w], c = t[w];
      asm volatile("s_nop 1" : "+v"(a), "+v"(c));
      swap32(a, c); float b = a, d = c; swap16(a, b); swap16(c, d);
      float* o = w ? nb : rs; o[4 * k] = a; o[4 * k + 1] = b; o[4 * k + 2] = c; o[4 * k + 3] = d;
    }
  }
#pragma unroll
  for (int i = 0; i < 5; i++)
#pragma unroll
    for (int c = 0; c < 4; c++) {
      float y[8];
#pragma unroll
      for (int e = 0; e < 8; e++) y[e] = ffma_(xf[e][4 * i + c], rs[e], nb[e]);
      stc128(v4u{pkhf(y[0], y[1]), pkhf(y[2], y[3]), pkhf(y[4], y[5]), pkhf(y[6], y[7])}, rw, WS_XT + lane * 16, pn * 20480 + (i * 4 + c) * 1024);
    }
  wait0(sb + LD_CTL + C_FULL);
  const u32 tag = mktag(rfl(ldsv(sb + LD_CTL + C_TAG)));
  waitv<0>();
  if (lane < 8) stc32(tag, rw, WS_LFLAG + lane * 2048 + pn * 4);
}

template <bool CJ> DEV void grp8(u32* v, u32 a, u32 b, u32 c) {
#define HM(x, y) (CJ ? hmulc(x, y) : hmul(x, y))
  v[1] = HM(v[0], a); v[2] = HM(v[0], b); v[3] = HM(v[1], b); v[4] = HM(v[0], c); v[5] = HM(v[1], c); v[6] = HM(v[2], c); v[7] = HM(v[3], c);
#undef HM
}
DEV void wr4(u32 a, const u32* v) {
  asm volatile("ds_write2_b32 %0, %1, %2 offset1:68\n\tds_write2_b32 %0, %3, %4 offset0:136 offset1:204" ::"v"(a), "v"(v[0]), "v"(v[1]), "v"(v[2]), "v"(v[3]) : "memory");
}
DEV void rd4(v4u (&f)[4], u32 a) {
  asm volatile("ds_read_b128 %0, %4\n\tds_read_b128 %1, %4 offset:64\n\tds_read_b128 %2, %4 offset:128\n\tds_read_b128 %3, %4 offset:192"
               : "=&v"(f[0]), "=&v"(f[1]), "=&v"(f[2]), "=&v"(f[3]) : "v"(a) : "memory");
}
template <int N> DEV void wait4(v4u (&f)[4], v4u (&h)[4]) {
  asm volatile("s_waitcnt lgkmcnt(%8)" : "+v"(f[0]), "+v"(f[1]), "+v"(f[2]), "+v"(f[3]), "+v"(h[0]), "+v"(h[1]), "+v"(h[2]), "+v"(h[3]) : "i"(N));
}
DEV void conv_wave(const Params& p, u32 sb, int slot, int ch, int lane, int wv, bool g1, int ts, int cj ST_ARG) {
  const u32 UB = sb + LD_K + slot * K_BYTES, KB = sb + LD_R + (P1MODE ? 0 : 2 * WSLOT) + slot * 4608, CTL = sb + LD_CTL;
  const int g = lane >> 4, q = (lane >> 2) & 3, pp = lane & 3, j = lane & 15, b = blockIdx.x;
  const rsrc_t rw = mkr(p.ws, WS_BYTES);
  const v4u rz = mkrs(p.ws, 0), rws = mkrs(p.ws, WS_BYTES);
  __builtin_amdgcn_s_setprio(2);
  const bool retile = !((b >> 3) & 1);
  const int wu = ((b >> 4) * 8 + (b & 7)) * 3 + slot;
  if (retile) {
    u32 vo[5];
#pragma unroll
    for (int k = 0; k < 5; k++) { const int n = k * 64 + lane; vo[k] = (n / 40) * 1280 + (n % 40) * 16 - (k & 3) * 1024; }
    const v4u rd = mkrs(p.wd, F * D * 2);
    const u32 so = (wu >> 1) * 10240 + (wu & 1) * 640;
    dma4(rd, vo[0], vo[1], vo[2], vo[3], so, UB); dma1(rd, vo[4], so, UB + 4096);
  }
  u32 la[3], ai[3], cr[3], ci[3];
#pragma unroll
  for (int c = 0; c < 3; c++) { const int o = ch * NS + 64 * c + lane; la[c] = p.lar[o]; ai[c] = p.aim[o]; cr[c] = p.cre[o]; ci[c] = p.cim[o]; }
  const u32 ldt = p.ldt[ch];
#pragma unroll
  for (int c = 0; c < 3; c++) asm volatile("" : "+v"(la[c]), "+v"(ai[c]), "+v"(cr[c]), "+v"(ci[c]));
  if (retile) {
    waitv<0>();
    __builtin_amdgcn_s_setprio(3);
    const u32 ra = UB + q * 640 + (80 * g + 20 * pp) * 2;
    const u32 wo = WS_WDF + ((4 * (wu & 1) + g) * 48 * 5) * 1024 + j * 16, s2 = (wu >> 3) * 5120 + ((wu >> 1) & 3) * 256;
#pragma unroll
    for (int k = 0; k < 5; k++) stc128(trd2(ra + k * 8, ra + k * 8 + 2560), rw, wo + k * 1024, s2);
  }
  __builtin_amdgcn_s_setprio(0);
  u32 tag = 0;
  const float dt = rbf(__expf(bf2f(ldt)));
  u32 mx = 0;
#pragma unroll
  for (int c = 0; c < 3; c++) mx = max(mx, max(cr[c] & 0x7fffu, ci[c] & 0x7fffu));
  mx = max(mx, (u32)__builtin_amdgcn_update_dpp(0, (int)mx, 0x111, 0xf, 0xf, false));
  mx = max(mx, (u32)__builtin_amdgcn_update_dpp(0, (int)mx, 0x112, 0xf, 0xf, false));
  mx = max(mx, (u32)__builtin_amdgcn_update_dpp(0, (int)mx, 0x114, 0xf, 0xf, false));
  mx = max(mx, (u32)__builtin_amdgcn_update_dpp(0, (int)mx, 0x118, 0xf, 0xf, false));
  mx = max(max((u32)__builtin_amdgcn_readlane(mx, 15), (u32)__builtin_amdgcn_readlane(mx, 31)), max((u32)__builtin_amdgcn_readlane(mx, 47), (u32)__builtin_amdgcn_readlane(mx, 63)));
  STAMP(16);
  const u32 ex = min(max(mx >> 7, 1u), 252u);
  const float cscale = __uint_as_float((253u - ex) << 23);
  const float oscale = 2.f * dt * __uint_as_float((ex + 1u) << 23);
  u32 G[3][57];
#pragma unroll
  for (int c = 0; c < 3; c++) {
    const float zr = fmul_(fmul_(rbf(__expf(bf2f(la[c]))), dt), -1.44269504089f), zi = fmul_(fmul_(bf2f(ai[c]), dt), 0.15915494309f);
    const cf P0 = cexp2(zr, zi), P4 = cexp2(fmul_(zr, 16.f), fmul_(zi, 16.f)), P8 = cexp2(fmul_(zr, 256.f), fmul_(zi, 256.f));
    const cf P1 = csq(P0), P2 = csq(P1), P3 = csq(P2), P5 = csq(P4), P6 = csq(P5), P7 = csq(P6), P9 = csq(P8), P10 = csq(P9), P11 = csq(P10);
    const u32 M0 = pkc(P0), M1 = pkc(P1), M2 = pkc(P2), M3 = pkc(P3), M4 = pkc(P4), M5 = pkc(P5), M7 = pkc(P7), M8 = pkc(P8), M9 = pkc(P9);
    u32* v = G[c];
    const float cx = fmul_(bf2f(cr[c]), cscale), cy = fmul_(bf2f(ci[c]), -cscale);
    v[0] = pkhf(cx, cy); grp8<true>(v, M3, M4, M5);
    v[8] = pkhf(ffma_(cy, P6.y, fmul_(cx, P6.x)), fnma_(cx, P6.y, fmul_(cy, P6.x))); grp8<true>(v + 8, M3, M4, M5);
    v[16] = 0x00003c00u; v[17] = M0; v[18] = M1; v[19] = hmul(M0, M1); v[20] = M2; v[21] = hmul(M0, M2); v[22] = hmul(M1, M2); v[23] = hmul(v[19], M2);
    v[24] = M7; grp8<false>(v + 24, M0, M1, M2);
    v[32] = M8; grp8<false>(v + 32, M1, M2, M7);
    v[40] = M9; grp8<false>(v + 40, M2, M7, M8);
    v[48] = pkc(P10); grp8<false>(v + 48, M7, M8, M9);
    v[56] = pkc(P11);
#pragma unroll
    for (int k = 0; k < 57; k++) asm volatile("" : "+v"(v[k]));
    if (c == 0) {
      wait0(CTL + C_FULL);
      tag = mktag(rfl(ldsv(CTL + C_TAG)));
      if (retile) { waitv<0>(); if (lane < 8) stc32(tag, rw, WS_WFLAG + lane * 2048 + wu * 4); }
    }
  }
  auto collect = [&]() __attribute__((always_inline)) {
  __builtin_amdgcn_s_setprio(3);
  if (slot == 0) {
    float cs0 = 0.f, cs1 = 0.f;
    if (g1) {
      wait0(CTL + C_FULL + 7 * 4);
#pragma unroll 1
      for (u32 it = 0; it < SPIN_CAP; it++) {
        v4u c[10]; cs0 = cs1 = 0.f;
#pragma unroll
        for (int k = 0; k < 10; k++) c[k] = ldc128(rw, WS_CSP + lane * 16, (cj * 10 + k) * 1024);
        u32 bad = 0;
#pragma unroll
        for (int k = 0; k < 10; k++) { bad |= (c[k].y ^ tag) | (c[k].w ^ tag); cs0 = fadd_(cs0, __uint_as_float(c[k].x)); cs1 = fadd_(cs1, __uint_as_float(c[k].z)); }
        if (__all(bad == 0)) break;
        __builtin_amdgcn_s_sleep(16);
      }
    } else __builtin_amdgcn_s_sleep(40);
#pragma unroll 1
    for (u32 it = 0; it < SPIN_CAP; it++) {
      const v2u f = __builtin_amdgcn_raw_buffer_load_b64(rw, WS_LFLAG + (b & 7) * 2048 + lane * 8, 0, AUX_SC1);
      if (__all(((f.x ^ tag) | (f.y ^ tag)) == 0)) break;
      __builtin_amdgcn_s_sleep(12);
    }
    ORDER();
    *(LAS f2*)(sb + LD_CS + lane * 8) = f2{cs0, cs1};
    if (g1) asm volatile("s_mov_b32 m0, %0\n\ts_nop 0\n\tbuffer_load_dwordx4 %1, %2, %3 offen sc1 lds\n\ts_mov_b32 exec_hi, 0xff\n\t"
                         "buffer_load_dwordx4 %1, %2, %3 offen offset:1024 sc1 lds\n\ts_mov_b32 exec_hi, -1"
                         ::"s"(sb + LD_ST), "v"(lane * 16), "s"(rws), "s"(WS_STAT + ts * 128) : "memory");
    asm volatile("s_waitcnt lgkmcnt(0)" ::: "memory");
    sig0(rz, CTL + C_RDY);
    STAMP(22);
  } else {
    if (slot == 1) {
      if (g1) wait0(CTL + C_FULL + WPOLL_STAGE * 4);
#pragma unroll 1
      for (u32 it = 0; it < SPIN_CAP; it++) {
        const v4u fa = ldc128(rw, WS_WFLAG + lane * 16, (b & 7) * 2048); const v2u fb = __builtin_amdgcn_raw_buffer_load_b64(rw, WS_WFLAG + 1024 + lane * 8, (b & 7) * 2048, AUX_SC1);
        if (__all(((fa.x ^ tag) | (fa.y ^ tag) | (fa.z ^ tag) | (fa.w ^ tag) | (fb.x ^ tag) | (fb.y ^ tag)) == 0)) break;
        __builtin_amdgcn_s_sleep(16);
      }
      ORDER();
      sig0(rz, CTL + C_WOK);
    }
    wait0(CTL + C_RDY);
  }
  {
    const u32 co = WS_XT + ((ch >> 7) * 4 + (ch & 3)) * 1024 + ((ch & 127) >> 2) * 32;
    u32 vo[5];
#pragma unroll
    for (int k = 0; k < 5; k++) { const int n = k * 64 + lane, tau = n / 72, a = n % 72 - 8; vo[k] = a >= 0 && tau < 4 ? co + ((a >> 1) * 4 + tau) * 20480 + (a & 1) * 16 - (k & 3) * 1024 : 0x80000000u; }
    dma4c(rws, vo[0], vo[1], vo[2], vo[3], 0, UB); dma1c(rws, vo[4], 0, UB + 4096);
  }
  };
  if (g1) collect();
  __builtin_amdgcn_s_setprio(KL_PRIO);
  if (g1) { if (GWAIT) waitv<0>(); wait0(CTL + C_QUIET); }
  STAMP(17);
  f4 kd[3] = {f4{0, 0, 0, 0}, f4{0, 0, 0, 0}, f4{0, 0, 0, 0}};
  {
    const u32 rb = KB + j * 272 + g * 16;
    u32 wa[4] = {KB + lane * 4, KB + lane * 4 + 1088, KB + lane * 4 + 2176, KB + lane * 4 + 3264};
    asm volatile("" : "+v"(kd[0]), "+v"(kd[1]), "+v"(kd[2]), "+v"(wa[0]), "+v"(wa[1]), "+v"(wa[2]), "+v"(wa[3]));
    v4u Fr[12][4];
#pragma unroll
    for (int ps = 0; ps <= 12; ps++) {
      if (ps < 12) {
        const int c = ps >> 2, t = ps & 3;
        wr4(wa[0], G[c] + 16 * t); wr4(wa[1], G[c] + 16 * t + 4);
        if (t < 3) { wr4(wa[2], G[c] + 16 * t + 8); wr4(wa[3], G[c] + 16 * t + 12); }
        else asm volatile("ds_write_b32 %0, %1" ::"v"(wa[2]), "v"(G[c][56]) : "memory");
        asm volatile("ds_read_b128 %0, %4\n\tds_read_b128 %1, %4 offset:64\n\tds_read_b128 %2, %4 offset:128\n\tds_read_b128 %3, %4 offset:192"
                     : "=&v"(Fr[ps][0]), "=&v"(Fr[ps][1]), "=&v"(Fr[ps][2]), "=&v"(Fr[ps][3]) : "v"(rb) : "memory");
      }
      if (ps > 0) {
        const int pv = ps - 1;
        if (ps < 12) asm volatile("s_waitcnt lgkmcnt(9)" : "+v"(Fr[pv][0]), "+v"(Fr[pv][1]), "+v"(Fr[pv][2]), "+v"(Fr[pv][3]));
        else asm volatile("s_waitcnt lgkmcnt(0)" : "+v"(Fr[pv][0]), "+v"(Fr[pv][1]), "+v"(Fr[pv][2]), "+v"(Fr[pv][3]));
        if (pv & 3) {
#pragma unroll
          for (int ks = 0; ks < 4; ks++) mma_hf(kd[(pv & 3) - 1], Fr[pv][ks], Fr[pv & ~3][ks]);
        }
      }
    }
    asm volatile("s_nop 7" : "+v"(kd[0]), "+v"(kd[1]), "+v"(kd[2]));
  }
  STAMP(9);
  {
    const u32 K0 = KB + 32;
    const u32 a00 = pkhf(kd[1][0], kd[1][0]), a11 = pkhf(kd[1][1], kd[1][1]), a22 = pkhf(kd[1][2], kd[1][2]), a33 = pkhf(kd[1][3], kd[1][3]);
    const u32 a12 = pkhf(kd[1][1], kd[1][2]), a01 = pkhf(kd[1][0], kd[1][1]), a23 = pkhf(kd[1][2], kd[1][3]);
    v2u bb[4];
#pragma unroll
    for (int r = 0; r < 4; r++) { bb[r].x = pkhf(kd[2][r], kd[2][r]); bb[r].y = bb[r].x; }
    const v2u r0 = {pkhf(kd[0][0], kd[0][1]), pkhf(kd[0][2], kd[0][3])}, w1 = {a00, a12}, w2a = {a00, a01}, w2b = {a22, a23};
    const v4u z4 = {0, 0, 0, 0};
    const u32 p3 = K0 + 2 * (1024 + 512 * g + 8 * j) - 8, p2 = K0 + 2 * (256 * g + 8 * j) - 4, p1 = K0 + 2 * (256 + 128 * g + 8 * j) - 2;
    const u32 p0 = K0 + 2 * (128 * (g >> 1) + 8 * j + 4 * (g & 1)), pz = KB + lane * 16;
    asm volatile(
      "s_mov_b32 exec_hi, 1\n\t"
      "ds_write2_b64 %0, %5, %5 offset1:1\n\t"
      "s_mov_b32 exec_hi, 0\n\t"
      "ds_write2_b64 %0, %6, %6 offset0:32 offset1:33\n\t"
      "ds_write2_b64 %0, %7, %7 offset0:64 offset1:65\n\t"
      "ds_write2_b64 %0, %8, %8 offset0:96 offset1:97\n\t"
      "s_mov_b32 exec_lo, 0x55555555\n\t"
      "ds_write_b16 %0, %9 offset:16\n\tds_write_b16 %0, %10 offset:272\n\tds_write_b16 %0, %11 offset:528\n\tds_write_b16 %0, %12 offset:784\n\t"
      "s_mov_b32 exec_lo, 0\n\ts_mov_b32 exec_hi, -1\n\t"
      "ds_write_b32 %1, %13\n\tds_write_b64 %1, %17 offset:4\n\tds_write_b32 %1, %14 offset:12\n\t"
      "ds_write_b32 %1, %15 offset:256\n\tds_write_b64 %1, %18 offset:260\n\tds_write_b32 %1, %16 offset:268\n\t"
      "s_mov_b32 exec_lo, -1\n\ts_mov_b32 exec_hi, 0\n\t"
      "ds_write_b16 %2, %13\n\tds_write_b64 %2, %19 offset:2\n\tds_write_b32 %2, %15 offset:10\n\tds_write_b16 %2, %16 offset:14\n\t"
      "s_mov_b32 exec_lo, 3\n\t"
      "ds_write_b128 %4, %21\n\t"
      "s_mov_b64 exec, -1\n\t"
      "ds_write_b64 %3, %20"
      ::"v"(p3), "v"(p2), "v"(p1), "v"(p0), "v"(pz), "v"(bb[0]), "v"(bb[1]), "v"(bb[2]), "v"(bb[3]), "v"(bb[0].x), "v"(bb[1].x), "v"(bb[2].x), "v"(bb[3].x),
        "v"(a00), "v"(a11), "v"(a22), "v"(a33), "v"(w2a), "v"(w2b), "v"(w1), "v"(r0), "v"(z4) : "memory");
  }
  v4u A[16];
  {
    const u32 ab = KB + 32 + 2 * (-12 + 4 * (8 * g + 7 - q) + 4 * pp);
#pragma unroll
    for (int s = 0; s < 16; s++) A[s] = trd2(ab + 256 * s, ab + 256 * s - 32);
  }
  ldsw(CTL + C_KDONE + slot * 4, 0);
  if (!g1) collect();
  STAMP(13);
  waitv<0>();
  wait0(CTL + C_DONE + 160 + slot * 4);
  __builtin_amdgcn_s_setprio(CONV_PRIO);
  STAMP(18);
  f4 acc[4][8]; v4u B[9]; float y[4], c[2], cv[3], pc[3] = {0.f, 0.f, 0.f};
  const u32 ub = UB + 128 + 2 * (4 * j - 4 - 8 * g) - 64, srcl = (lane == 0 ? 63 : (g > 0 ? lane - 16 : lane + 47)) * 4;
  const u32 zo = WS_ZT + (g & 1) * 512 + j * 32 + (g >> 1) * 16;
  const u64 m63 = 1ull << 63;
  u32 ut[4] = {ub, ub + 1152, ub + 2304, ub + 3456};
  asm volatile("" : "+v"(ut[0]), "+v"(ut[1]), "+v"(ut[2]), "+v"(ut[3]));
#define LDB(k, tau, q) do { const u32 a_ = ut[tau] + 64 * (q); const v2u x_ = lds64(a_), y_ = lds64(a_ + 8); B[k] = v4u{x_.x, x_.y, y_.x, y_.y}; SCHED(); } while (0)
#define VADD(d, a_, b_) do { float t_; asm volatile("v_add_f32 %0, %1, %2" : "=v"(t_) : "v"(a_), "v"(b_)); d = t_; } while (0)
#define VMULS(d) asm volatile("v_mul_f32 %0, %0, %1" : "+v"(d) : "v"(oscale))
#define VSEL(d, a_, b_) asm volatile("v_cndmask_b32 %0, %1, %2, %3" : "=v"(d) : "v"(a_), "v"(b_), "s"(m63))
#define VPK(d, a_, b_) do { u32 t_; asm volatile("v_cvt_pk_bf16_f32 %0, %1, %2" : "=v"(t_) : "v"(a_), "v"(b_)); d = t_; } while (0)
#define NOPS() asm volatile("s_nop 3")
#define BPERM() asm volatile("ds_bpermute_b32 %0, %3, %0\n\tds_bpermute_b32 %1, %3, %1\n\tds_bpermute_b32 %2, %3, %2" : "+v"(cv[0]), "+v"(cv[1]), "+v"(cv[2]) : "v"(srcl) : "memory")
#define WAITB(n) asm volatile("s_waitcnt lgkmcnt(" #n ")" : "+v"(cv[0]), "+v"(cv[1]), "+v"(cv[2]) :: "memory")
#define ZSTORE(T) do { asm volatile("s_nop 1\n\tv_permlane16_swap_b32 %0, %2\n\tv_permlane16_swap_b32 %1, %3" : "+v"(zx0), "+v"(zy0), "+v"(zx1), "+v"(zy1)); \
    if (!ABL_Z) stc128(v4u{zx0, zy0, zx1, zy1}, rw, zo, ch * 4096 + (T) * 512); } while (0)
  u32 zx0, zy0, zx1, zy1;
  LDB(0, 0, 0);
  LDB(1, 1, 0);
  LDB(2, 2, 0);
  LDB(3, 3, 0);
  LDB(4, 0, 1);
  LDB(5, 1, 1);
  LDB(6, 2, 1);
  LDB(7, 3, 1);
  LDB(8, 0, 2);
  mma_hf0(acc[0][7], A[15], B[0]);
  mma_hf0(acc[0][6], A[13], B[0]);
  mma_hf0(acc[0][5], A[11], B[0]);
  mma_hf0(acc[0][4], A[9], B[0]);
  mma_hf0(acc[0][3], A[7], B[0]);
  mma_hf0(acc[0][2], A[5], B[0]);
  mma_hf0(acc[0][1], A[3], B[0]);
  mma_hf0(acc[0][0], A[1], B[0]);
  LDB(0, 1, 2);
  mma_hf0(acc[1][7], A[15], B[1]);
  mma_hf0(acc[1][6], A[13], B[1]);
  mma_hf0(acc[1][5], A[11], B[1]);
  mma_hf0(acc[1][4], A[9], B[1]);
  mma_hf0(acc[1][3], A[7], B[1]);
  mma_hf0(acc[1][2], A[5], B[1]);
  mma_hf0(acc[1][1], A[3], B[1]);
  mma_hf0(acc[1][0], A[1], B[1]);
  LDB(1, 2, 2);
  mma_hf0(acc[2][7], A[15], B[2]);
  mma_hf0(acc[2][6], A[13], B[2]);
  mma_hf0(acc[2][5], A[11], B[2]);
  mma_hf0(acc[2][4], A[9], B[2]);
  mma_hf0(acc[2][3], A[7], B[2]);
  mma_hf0(acc[2][2], A[5], B[2]);
  mma_hf0(acc[2][1], A[3], B[2]);
  mma_hf0(acc[2][0], A[1], B[2]);
  LDB(2, 3, 2);
  mma_hf0(acc[3][7], A[15], B[3]);
  mma_hf0(acc[3][6], A[13], B[3]);
  mma_hf0(acc[3][5], A[11], B[3]);
  mma_hf0(acc[3][4], A[9], B[3]);
  mma_hf0(acc[3][3], A[7], B[3]);
  mma_hf0(acc[3][2], A[5], B[3]);
  mma_hf0(acc[3][1], A[3], B[3]);
  mma_hf0(acc[3][0], A[1], B[3]);
  LDB(3, 0, 3);
  mma_hf(acc[0][7], A[14], B[4]);
  mma_hf(acc[0][6], A[12], B[4]);
  mma_hf(acc[0][5], A[10], B[4]);
  mma_hf(acc[0][4], A[8], B[4]);
  mma_hf(acc[0][3], A[6], B[4]);
  mma_hf(acc[0][2], A[4], B[4]);
  mma_hf(acc[0][1], A[2], B[4]);
  mma_hf(acc[0][0], A[0], B[4]);
  LDB(4, 1, 3);
  mma_hf(acc[1][7], A[14], B[5]);
  mma_hf(acc[1][6], A[12], B[5]);
  mma_hf(acc[1][5], A[10], B[5]);
  mma_hf(acc[1][4], A[8], B[5]);
  mma_hf(acc[1][3], A[6], B[5]);
  mma_hf(acc[1][2], A[4], B[5]);
  mma_hf(acc[1][1], A[2], B[5]);
  mma_hf(acc[1][0], A[0], B[5]);
  LDB(5, 2, 3);
  mma_hf(acc[2][7], A[14], B[6]);
  mma_hf(acc[2][6], A[12], B[6]);
  mma_hf(acc[2][5], A[10], B[6]);
  mma_hf(acc[2][4], A[8], B[6]);
  mma_hf(acc[2][3], A[6], B[6]);
  mma_hf(acc[2][2], A[4], B[6]);
  mma_hf(acc[2][1], A[2], B[6]);
  mma_hf(acc[2][0], A[0], B[6]);
  LDB(6, 3, 3);
  mma_hf(acc[3][7], A[14], B[7]);
  mma_hf(acc[3][6], A[12], B[7]);
  mma_hf(acc[3][5], A[10], B[7]);
  mma_hf(acc[3][4], A[8], B[7]);
  mma_hf(acc[3][3], A[6], B[7]);
  mma_hf(acc[3][2], A[4], B[7]);
  mma_hf(acc[3][1], A[2], B[7]);
  mma_hf(acc[3][0], A[0], B[7]);
  LDB(7, 0, 4);
  mma_hf(acc[0][7], A[13], B[8]);
  VADD(y[1], acc[0][0][1], acc[1][0][0]);
  VADD(y[2], acc[0][0][2], acc[1][0][1]);
  mma_hf(acc[0][6], A[11], B[8]);
  VADD(y[3], acc[0][0][3], acc[1][0][2]);
  VADD(c[0], acc[1][0][3], acc[2][0][2]);
  mma_hf(acc[0][5], A[9], B[8]);
  VADD(y[2], y[2], acc[2][0][0]);
  VADD(y[3], y[3], acc[2][0][1]);
  mma_hf(acc[0][4], A[7], B[8]);
  NOPS();
  VADD(c[0], c[0], acc[3][0][1]);
  mma_hf(acc[0][3], A[5], B[8]);
  VADD(c[1], acc[2][0][3], acc[3][0][2]);
  VADD(y[3], y[3], acc[3][0][0]);
  mma_hf(acc[0][2], A[3], B[8]);
  VSEL(cv[0], c[0], pc[0]);
  mma_hf(acc[0][1], A[1], B[8]);
  VSEL(cv[1], c[1], pc[1]);
  LDB(8, 1, 4);
  mma_hf(acc[1][7], A[13], B[0]);
  VSEL(cv[2], acc[3][0][3], pc[2]);
  mma_hf(acc[1][6], A[11], B[0]);
  BPERM(); pc[0] = c[0]; pc[1] = c[1]; pc[2] = acc[3][0][3];
  mma_hf(acc[1][5], A[9], B[0]);
  VMULS(y[3]);
  mma_hf(acc[1][4], A[7], B[0]);
  mma_hf(acc[1][3], A[5], B[0]);
  mma_hf(acc[1][2], A[3], B[0]);
  mma_hf(acc[1][1], A[1], B[0]);
  LDB(0, 2, 4);
  mma_hf(acc[2][7], A[13], B[1]);
  mma_hf(acc[2][6], A[11], B[1]);
  mma_hf(acc[2][5], A[9], B[1]);
  WAITB(1);
  VADD(y[0], acc[0][0][0], cv[0]);
  mma_hf(acc[2][4], A[7], B[1]);
  VADD(y[1], y[1], cv[1]);
  mma_hf(acc[2][3], A[5], B[1]);
  VADD(y[2], y[2], cv[2]);
  mma_hf(acc[2][2], A[3], B[1]);
  VMULS(y[0]);
  mma_hf(acc[2][1], A[1], B[1]);
  VMULS(y[1]);
  LDB(1, 3, 4);
  mma_hf(acc[3][7], A[13], B[2]);
  VMULS(y[2]);
  mma_hf(acc[3][6], A[11], B[2]);
  VPK(zx0, y[0], y[1]);
  mma_hf(acc[3][5], A[9], B[2]);
  VPK(zy0, y[2], y[3]);
  mma_hf(acc[3][4], A[7], B[2]);
  mma_hf(acc[3][3], A[5], B[2]);
  mma_hf(acc[3][2], A[3], B[2]);
  mma_hf(acc[3][1], A[1], B[2]);
  LDB(2, 0, 5);
  mma_hf(acc[0][7], A[12], B[3]);
  mma_hf(acc[0][6], A[10], B[3]);
  mma_hf(acc[0][5], A[8], B[3]);
  mma_hf(acc[0][4], A[6], B[3]);
  mma_hf(acc[0][3], A[4], B[3]);
  mma_hf(acc[0][2], A[2], B[3]);
  mma_hf(acc[0][1], A[0], B[3]);
  LDB(3, 1, 5);
  mma_hf(acc[1][7], A[12], B[4]);
  mma_hf(acc[1][6], A[10], B[4]);
  mma_hf(acc[1][5], A[8], B[4]);
  mma_hf(acc[1][4], A[6], B[4]);
  mma_hf(acc[1][3], A[4], B[4]);
  mma_hf(acc[1][2], A[2], B[4]);
  mma_hf(acc[1][1], A[0], B[4]);
  LDB(4, 2, 5);
  mma_hf(acc[2][7], A[12], B[5]);
  mma_hf(acc[2][6], A[10], B[5]);
  mma_hf(acc[2][5], A[8], B[5]);
  mma_hf(acc[2][4], A[6], B[5]);
  mma_hf(acc[2][3], A[4], B[5]);
  mma_hf(acc[2][2], A[2], B[5]);
  mma_hf(acc[2][1], A[0], B[5]);
  LDB(5, 3, 5);
  mma_hf(acc[3][7], A[12], B[6]);
  mma_hf(acc[3][6], A[10], B[6]);
  mma_hf(acc[3][5], A[8], B[6]);
  mma_hf(acc[3][4], A[6], B[6]);
  mma_hf(acc[3][3], A[4], B[6]);
  mma_hf(acc[3][2], A[2], B[6]);
  mma_hf(acc[3][1], A[0], B[6]);
  LDB(6, 0, 6);
  mma_hf(acc[0][7], A[11], B[7]);
  VADD(y[1], acc[0][1][1], acc[1][1][0]);
  VADD(y[2], acc[0][1][2], acc[1][1][1]);
  mma_hf(acc[0][6], A[9], B[7]);
  VADD(y[3], acc[0][1][3], acc[1][1][2]);
  VADD(c[0], acc[1][1][3], acc[2][1][2]);
  mma_hf(acc[0][5], A[7], B[7]);
  VADD(y[2], y[2], acc[2][1][0]);
  VADD(y[3], y[3], acc[2][1][1]);
  mma_hf(acc[0][4], A[5], B[7]);
  NOPS();
  VADD(c[0], c[0], acc[3][1][1]);
  mma_hf(acc[0][3], A[3], B[7]);
  VADD(c[1], acc[2][1][3], acc[3][1][2]);
  VADD(y[3], y[3], acc[3][1][0]);
  mma_hf(acc[0][2], A[1], B[7]);
  VSEL(cv[0], c[0], pc[0]);
  VSEL(cv[1], c[1], pc[1]);
  LDB(7, 1, 6);
  mma_hf(acc[1][7], A[11], B[8]);
  VSEL(cv[2], acc[3][1][3], pc[2]);
  mma_hf(acc[1][6], A[9], B[8]);
  BPERM(); pc[0] = c[0]; pc[1] = c[1]; pc[2] = acc[3][1][3];
  mma_hf(acc[1][5], A[7], B[8]);
  VMULS(y[3]);
  mma_hf(acc[1][4], A[5], B[8]);
  mma_hf(acc[1][3], A[3], B[8]);
  mma_hf(acc[1][2], A[1], B[8]);
  LDB(8, 2, 6);
  mma_hf(acc[2][7], A[11], B[0]);
  mma_hf(acc[2][6], A[9], B[0]);
  mma_hf(acc[2][5], A[7], B[0]);
  mma_hf(acc[2][4], A[5], B[0]);
  WAITB(1);
  VADD(y[0], acc[0][1][0], cv[0]);
  mma_hf(acc[2][3], A[3], B[0]);
  VADD(y[1], y[1], cv[1]);
  mma_hf(acc[2][2], A[1], B[0]);
  VADD(y[2], y[2], cv[2]);
  LDB(0, 3, 6);
  mma_hf(acc[3][7], A[11], B[1]);
  VMULS(y[0]);
  mma_hf(acc[3][6], A[9], B[1]);
  VMULS(y[1]);
  mma_hf(acc[3][5], A[7], B[1]);
  VMULS(y[2]);
  mma_hf(acc[3][4], A[5], B[1]);
  VPK(zx1, y[0], y[1]);
  mma_hf(acc[3][3], A[3], B[1]);
  VPK(zy1, y[2], y[3]);
  mma_hf(acc[3][2], A[1], B[1]);
  ZSTORE(0);
  LDB(1, 0, 7);
  mma_hf(acc[0][7], A[10], B[2]);
  mma_hf(acc[0][6], A[8], B[2]);
  mma_hf(acc[0][5], A[6], B[2]);
  mma_hf(acc[0][4], A[4], B[2]);
  mma_hf(acc[0][3], A[2], B[2]);
  mma_hf(acc[0][2], A[0], B[2]);
  LDB(2, 1, 7);
  mma_hf(acc[1][7], A[10], B[3]);
  mma_hf(acc[1][6], A[8], B[3]);
  mma_hf(acc[1][5], A[6], B[3]);
  mma_hf(acc[1][4], A[4], B[3]);
  mma_hf(acc[1][3], A[2], B[3]);
  mma_hf(acc[1][2], A[0], B[3]);
  LDB(3, 2, 7);
  mma_hf(acc[2][7], A[10], B[4]);
  mma_hf(acc[2][6], A[8], B[4]);
  mma_hf(acc[2][5], A[6], B[4]);
  mma_hf(acc[2][4], A[4], B[4]);
  mma_hf(acc[2][3], A[2], B[4]);
  mma_hf(acc[2][2], A[0], B[4]);
  LDB(4, 3, 7);
  mma_hf(acc[3][7], A[10], B[5]);
  mma_hf(acc[3][6], A[8], B[5]);
  mma_hf(acc[3][5], A[6], B[5]);
  mma_hf(acc[3][4], A[4], B[5]);
  mma_hf(acc[3][3], A[2], B[5]);
  mma_hf(acc[3][2], A[0], B[5]);
  LDB(5, 0, 8);
  mma_hf(acc[0][7], A[9], B[6]);
  VADD(y[1], acc[0][2][1], acc[1][2][0]);
  VADD(y[2], acc[0][2][2], acc[1][2][1]);
  mma_hf(acc[0][6], A[7], B[6]);
  VADD(y[3], acc[0][2][3], acc[1][2][2]);
  VADD(c[0], acc[1][2][3], acc[2][2][2]);
  mma_hf(acc[0][5], A[5], B[6]);
  VADD(y[2], y[2], acc[2][2][0]);
  VADD(y[3], y[3], acc[2][2][1]);
  mma_hf(acc[0][4], A[3], B[6]);
  NOPS();
  VADD(c[0], c[0], acc[3][2][1]);
  mma_hf(acc[0][3], A[1], B[6]);
  VADD(c[1], acc[2][2][3], acc[3][2][2]);
  VADD(y[3], y[3], acc[3][2][0]);
  LDB(6, 1, 8);
  mma_hf(acc[1][7], A[9], B[7]);
  VSEL(cv[0], c[0], pc[0]);
  mma_hf(acc[1][6], A[7], B[7]);
  VSEL(cv[1], c[1], pc[1]);
  mma_hf(acc[1][5], A[5], B[7]);
  VSEL(cv[2], acc[3][2][3], pc[2]);
  mma_hf(acc[1][4], A[3], B[7]);
  BPERM(); pc[0] = c[0]; pc[1] = c[1]; pc[2] = acc[3][2][3];
  mma_hf(acc[1][3], A[1], B[7]);
  VMULS(y[3]);
  LDB(7, 2, 8);
  mma_hf(acc[2][7], A[9], B[8]);
  mma_hf(acc[2][6], A[7], B[8]);
  mma_hf(acc[2][5], A[5], B[8]);
  mma_hf(acc[2][4], A[3], B[8]);
  mma_hf(acc[2][3], A[1], B[8]);
  LDB(8, 3, 8);
  mma_hf(acc[3][7], A[9], B[0]);
  mma_hf(acc[3][6], A[7], B[0]);
  WAITB(2);
  VADD(y[0], acc[0][2][0], cv[0]);
  mma_hf(acc[3][5], A[5], B[0]);
  VADD(y[1], y[1], cv[1]);
  mma_hf(acc[3][4], A[3], B[0]);
  VADD(y[2], y[2], cv[2]);
  mma_hf(acc[3][3], A[1], B[0]);
  VMULS(y[0]);
  LDB(0, 0, 9);
  mma_hf(acc[0][7], A[8], B[1]);
  VMULS(y[1]);
  mma_hf(acc[0][6], A[6], B[1]);
  VMULS(y[2]);
  mma_hf(acc[0][5], A[4], B[1]);
  VPK(zx0, y[0], y[1]);
  mma_hf(acc[0][4], A[2], B[1]);
  VPK(zy0, y[2], y[3]);
  mma_hf(acc[0][3], A[0], B[1]);
  LDB(1, 1, 9);
  mma_hf(acc[1][7], A[8], B[2]);
  mma_hf(acc[1][6], A[6], B[2]);
  mma_hf(acc[1][5], A[4], B[2]);
  mma_hf(acc[1][4], A[2], B[2]);
  mma_hf(acc[1][3], A[0], B[2]);
  LDB(2, 2, 9);
  mma_hf(acc[2][7], A[8], B[3]);
  mma_hf(acc[2][6], A[6], B[3]);
  mma_hf(acc[2][5], A[4], B[3]);
  mma_hf(acc[2][4], A[2], B[3]);
  mma_hf(acc[2][3], A[0], B[3]);
  LDB(3, 3, 9);
  mma_hf(acc[3][7], A[8], B[4]);
  mma_hf(acc[3][6], A[6], B[4]);
  mma_hf(acc[3][5], A[4], B[4]);
  mma_hf(acc[3][4], A[2], B[4]);
  mma_hf(acc[3][3], A[0], B[4]);
  LDB(4, 0, 10);
  mma_hf(acc[0][7], A[7], B[5]);
  VADD(y[1], acc[0][3][1], acc[1][3][0]);
  VADD(y[2], acc[0][3][2], acc[1][3][1]);
  mma_hf(acc[0][6], A[5], B[5]);
  VADD(y[3], acc[0][3][3], acc[1][3][2]);
  VADD(c[0], acc[1][3][3], acc[2][3][2]);
  mma_hf(acc[0][5], A[3], B[5]);
  VADD(y[2], y[2], acc[2][3][0]);
  VADD(y[3], y[3], acc[2][3][1]);
  mma_hf(acc[0][4], A[1], B[5]);
  NOPS();
  VADD(c[0], c[0], acc[3][3][1]);
  LDB(5, 1, 10);
  mma_hf(acc[1][7], A[7], B[6]);
  VADD(c[1], acc[2][3][3], acc[3][3][2]);
  VADD(y[3], y[3], acc[3][3][0]);
  mma_hf(acc[1][6], A[5], B[6]);
  VSEL(cv[0], c[0], pc[0]);
  VSEL(cv[1], c[1], pc[1]);
  mma_hf(acc[1][5], A[3], B[6]);
  VSEL(cv[2], acc[3][3][3], pc[2]);
  mma_hf(acc[1][4], A[1], B[6]);
  BPERM(); pc[0] = c[0]; pc[1] = c[1]; pc[2] = acc[3][3][3];
  LDB(6, 2, 10);
  mma_hf(acc[2][7], A[7], B[7]);
  VMULS(y[3]);
  mma_hf(acc[2][6], A[5], B[7]);
  mma_hf(acc[2][5], A[3], B[7]);
  mma_hf(acc[2][4], A[1], B[7]);
  LDB(7, 3, 10);
  mma_hf(acc[3][7], A[7], B[8]);
  mma_hf(acc[3][6], A[5], B[8]);
  mma_hf(acc[3][5], A[3], B[8]);
  mma_hf(acc[3][4], A[1], B[8]);
  WAITB(2);
  VADD(y[0], acc[0][3][0], cv[0]);
  LDB(8, 0, 11);
  mma_hf(acc[0][7], A[6], B[0]);
  VADD(y[1], y[1], cv[1]);
  mma_hf(acc[0][6], A[4], B[0]);
  VADD(y[2], y[2], cv[2]);
  mma_hf(acc[0][5], A[2], B[0]);
  VMULS(y[0]);
  mma_hf(acc[0][4], A[0], B[0]);
  VMULS(y[1]);
  LDB(0, 1, 11);
  mma_hf(acc[1][7], A[6], B[1]);
  VMULS(y[2]);
  mma_hf(acc[1][6], A[4], B[1]);
  VPK(zx1, y[0], y[1]);
  mma_hf(acc[1][5], A[2], B[1]);
  VPK(zy1, y[2], y[3]);
  mma_hf(acc[1][4], A[0], B[1]);
  ZSTORE(2);
  LDB(1, 2, 11);
  mma_hf(acc[2][7], A[6], B[2]);
  mma_hf(acc[2][6], A[4], B[2]);
  mma_hf(acc[2][5], A[2], B[2]);
  mma_hf(acc[2][4], A[0], B[2]);
  LDB(2, 3, 11);
  mma_hf(acc[3][7], A[6], B[3]);
  mma_hf(acc[3][6], A[4], B[3]);
  mma_hf(acc[3][5], A[2], B[3]);
  mma_hf(acc[3][4], A[0], B[3]);
  LDB(3, 0, 12);
  mma_hf(acc[0][7], A[5], B[4]);
  VADD(y[1], acc[0][4][1], acc[1][4][0]);
  VADD(y[2], acc[0][4][2], acc[1][4][1]);
  mma_hf(acc[0][6], A[3], B[4]);
  VADD(y[3], acc[0][4][3], acc[1][4][2]);
  VADD(c[0], acc[1][4][3], acc[2][4][2]);
  mma_hf(acc[0][5], A[1], B[4]);
  VADD(y[2], y[2], acc[2][4][0]);
  VADD(y[3], y[3], acc[2][4][1]);
  LDB(4, 1, 12);
  mma_hf(acc[1][7], A[5], B[5]);
  NOPS();
  VADD(c[0], c[0], acc[3][4][1]);
  mma_hf(acc[1][6], A[3], B[5]);
  VADD(c[1], acc[2][4][3], acc[3][4][2]);
  VADD(y[3], y[3], acc[3][4][0]);
  mma_hf(acc[1][5], A[1], B[5]);
  VSEL(cv[0], c[0], pc[0]);
  VSEL(cv[1], c[1], pc[1]);
  LDB(5, 2, 12);
  mma_hf(acc[2][7], A[5], B[6]);
  VSEL(cv[2], acc[3][4][3], pc[2]);
  BPERM(); pc[0] = c[0]; pc[1] = c[1]; pc[2] = acc[3][4][3];
  mma_hf(acc[2][6], A[3], B[6]);
  VMULS(y[3]);
  mma_hf(acc[2][5], A[1], B[6]);
  LDB(6, 3, 12);
  mma_hf(acc[3][7], A[5], B[7]);
  mma_hf(acc[3][6], A[3], B[7]);
  WAITB(1);
  VADD(y[0], acc[0][4][0], cv[0]);
  mma_hf(acc[3][5], A[1], B[7]);
  VADD(y[1], y[1], cv[1]);
  VADD(y[2], y[2], cv[2]);
  LDB(7, 0, 13);
  mma_hf(acc[0][7], A[4], B[8]);
  VMULS(y[0]);
  VMULS(y[1]);
  mma_hf(acc[0][6], A[2], B[8]);
  VMULS(y[2]);
  VPK(zx0, y[0], y[1]);
  mma_hf(acc[0][5], A[0], B[8]);
  VPK(zy0, y[2], y[3]);
  LDB(8, 1, 13);
  mma_hf(acc[1][7], A[4], B[0]);
  mma_hf(acc[1][6], A[2], B[0]);
  mma_hf(acc[1][5], A[0], B[0]);
  LDB(0, 2, 13);
  mma_hf(acc[2][7], A[4], B[1]);
  mma_hf(acc[2][6], A[2], B[1]);
  mma_hf(acc[2][5], A[0], B[1]);
  LDB(1, 3, 13);
  mma_hf(acc[3][7], A[4], B[2]);
  mma_hf(acc[3][6], A[2], B[2]);
  mma_hf(acc[3][5], A[0], B[2]);
  LDB(2, 0, 14);
  mma_hf(acc[0][7], A[3], B[3]);
  VADD(y[1], acc[0][5][1], acc[1][5][0]);
  VADD(y[2], acc[0][5][2], acc[1][5][1]);
  mma_hf(acc[0][6], A[1], B[3]);
  VADD(y[3], acc[0][5][3], acc[1][5][2]);
  VADD(c[0], acc[1][5][3], acc[2][5][2]);
  LDB(3, 1, 14);
  mma_hf(acc[1][7], A[3], B[4]);
  VADD(y[2], y[2], acc[2][5][0]);
  VADD(y[3], y[3], acc[2][5][1]);
  mma_hf(acc[1][6], A[1], B[4]);
  NOPS();
  VADD(c[0], c[0], acc[3][5][1]);
  LDB(4, 2, 14);
  mma_hf(acc[2][7], A[3], B[5]);
  VADD(c[1], acc[2][5][3], acc[3][5][2]);
  VADD(y[3], y[3], acc[3][5][0]);
  mma_hf(acc[2][6], A[1], B[5]);
  VSEL(cv[0], c[0], pc[0]);
  VSEL(cv[1], c[1], pc[1]);
  LDB(5, 3, 14);
  mma_hf(acc[3][7], A[3], B[6]);
  VSEL(cv[2], acc[3][5][3], pc[2]);
  BPERM(); pc[0] = c[0]; pc[1] = c[1]; pc[2] = acc[3][5][3];
  mma_hf(acc[3][6], A[1], B[6]);
  VMULS(y[3]);
  LDB(6, 0, 15);
  mma_hf(acc[0][7], A[2], B[7]);
  mma_hf(acc[0][6], A[0], B[7]);
  LDB(7, 1, 15);
  mma_hf(acc[1][7], A[2], B[8]);
  WAITB(2);
  VADD(y[0], acc[0][5][0], cv[0]);
  mma_hf(acc[1][6], A[0], B[8]);
  VADD(y[1], y[1], cv[1]);
  VADD(y[2], y[2], cv[2]);
  LDB(8, 2, 15);
  mma_hf(acc[2][7], A[2], B[0]);
  VMULS(y[0]);
  VMULS(y[1]);
  mma_hf(acc[2][6], A[0], B[0]);
  VMULS(y[2]);
  VPK(zx1, y[0], y[1]);
  LDB(0, 3, 15);
  mma_hf(acc[3][7], A[2], B[1]);
  VPK(zy1, y[2], y[3]);
  ZSTORE(4);
  mma_hf(acc[3][6], A[0], B[1]);
  mma_hf(acc[0][7], A[1], B[2]);
  VADD(y[1], acc[0][6][1], acc[1][6][0]);
  VADD(y[2], acc[0][6][2], acc[1][6][1]);
  mma_hf(acc[1][7], A[1], B[3]);
  VADD(y[3], acc[0][6][3], acc[1][6][2]);
  VADD(c[0], acc[1][6][3], acc[2][6][2]);
  mma_hf(acc[2][7], A[1], B[4]);
  VADD(y[2], y[2], acc[2][6][0]);
  VADD(y[3], y[3], acc[2][6][1]);
  mma_hf(acc[3][7], A[1], B[5]);
  NOPS();
  VADD(c[0], c[0], acc[3][6][1]);
  mma_hf(acc[0][7], A[0], B[6]);
  VADD(c[1], acc[2][6][3], acc[3][6][2]);
  VADD(y[3], y[3], acc[3][6][0]);
  mma_hf(acc[1][7], A[0], B[7]);
  VSEL(cv[0], c[0], pc[0]);
  VSEL(cv[1], c[1], pc[1]);
  mma_hf(acc[2][7], A[0], B[8]);
  VSEL(cv[2], acc[3][6][3], pc[2]);
  BPERM(); pc[0] = c[0]; pc[1] = c[1]; pc[2] = acc[3][6][3];
  mma_hf(acc[3][7], A[0], B[0]);
  VMULS(y[3]);
  STAMP(19);
  WAITB(0);
  VADD(y[0], acc[0][6][0], cv[0]);
  VADD(y[1], y[1], cv[1]);
  VADD(y[2], y[2], cv[2]);
  VMULS(y[0]);
  VMULS(y[1]);
  VMULS(y[2]);
  VPK(zx0, y[0], y[1]);
  VPK(zy0, y[2], y[3]);
  VADD(y[1], acc[0][7][1], acc[1][7][0]);
  VADD(y[2], acc[0][7][2], acc[1][7][1]);
  VADD(y[3], acc[0][7][3], acc[1][7][2]);
  VADD(c[0], acc[1][7][3], acc[2][7][2]);
  VADD(y[2], y[2], acc[2][7][0]);
  VADD(y[3], y[3], acc[2][7][1]);
  NOPS();
  VADD(c[0], c[0], acc[3][7][1]);
  VADD(c[1], acc[2][7][3], acc[3][7][2]);
  VADD(y[3], y[3], acc[3][7][0]);
  VSEL(cv[0], c[0], pc[0]);
  VSEL(cv[1], c[1], pc[1]);
  VSEL(cv[2], acc[3][7][3], pc[2]);
  BPERM(); pc[0] = c[0]; pc[1] = c[1]; pc[2] = acc[3][7][3];
  VMULS(y[3]);
  WAITB(0);
  VADD(y[0], acc[0][7][0], cv[0]);
  VADD(y[1], y[1], cv[1]);
  VADD(y[2], y[2], cv[2]);
  VMULS(y[0]);
  VMULS(y[1]);
  VMULS(y[2]);
  VPK(zx1, y[0], y[1]);
  VPK(zy1, y[2], y[3]);
  ZSTORE(6);
  STAMP(20);
  waitv<0>();
  ldsw(CTL + C_CDONE + slot * 4, 0);
  STAMP(11);
}

__global__ __launch_bounds__(512, 2) void k_main(Params p) {
  extern __shared__ __attribute__((aligned(16))) char smem[];
  const u32 sb = (u32)(size_t)(LAS char*)smem;
  const int lane = threadIdx.x & 63, wv = rfl(threadIdx.x >> 6);
  const int b = blockIdx.x, xc = b & 7, r = b >> 3;
  const bool g1 = r < 30;
  const int r5 = r / 6, ri = (xc >> 2) * 5 + r5, cj = (xc & 3) * 6 + r % 6, ts = (ri * 64) / 5;
  const int i2 = xc * 4 + (r >> 3), j2 = r & 7;
#define KS2(k) (k)
  const u32 CTL = sb + LD_CTL;
  u64 pc0; u32 codeb;
  asm volatile("s_getpc_b64 %0\n.Lk_start:\n\ts_mov_b32 %1, .Lk_end-.Lk_start\n\t"
               ".subsection 2\n.Lk_end:\n\t.fill 128, 4, 0xBF800000\n\t.subsection 0" : "=s"(pc0), "=s"(codeb));
#define CSTAMP(k)
  STAMP(0);
  if (wv == 4) {
    const int nch = (r & 1) ? 2 : 3;
    const bool one = lane == 0 || (g1 && lane < 10) || (lane >= 11 && lane <= 14) || (lane >= 28 && lane <= 32) || (lane >= 16 && lane < 16 + nch) || (lane >= 20 && lane < 24 + nch);
    if (lane != 15) *(volatile LAS u32*)(CTL + lane * 4) = one ? 1u : 0u;
    *(volatile LAS u32*)(CTL + C_DONE + lane * 4) = g1 ? 1u : 0u;
  }
  BARRIER();
  const int g = lane >> 4, i = lane & 15;
  const rsrc_t rw = mkr(p.ws, WS_BYTES);
  const v4u rz = mkrs(p.ws, 0);
  const int c3 = (r % 6) % 3, grp = (r5 + c3) % 3, rot = (grp * 10 + 1) / 3;
  const int cstep = (xc >> 2) * (r5 == 2 ? 1 : 2) + (r5 >= 3) + ((0x9054 >> (2 * (3 * c3 + grp))) & 3);
  const v4u rg = mkrs(p.wg, D * F * 2), ru = mkrs(p.wu, D * F * 2), rxs = mkrs(p.x, L * D * 2);
  u32 v[4], vx[4];
#pragma unroll
  for (int k = 0; k < 4; k++) {
    const int row = 8 * k + (lane >> 3), fp = ((row >> 1) & 1) | (((row >> 3) & 1) << 1);
    v[k] = row * 3072 + cj * 128 + (((lane & 7) ^ (fp << 1)) << 4) - 1024 * k;
    vx[k] = (ts * 16 + row) * 1280 + (((lane & 7) ^ ((row >> 1) & 7)) << 4) - 1024 * k;
  }
  int kx = rot, kw = rot; u32 lsw = 0, lsx = 0;
#define ISSUE_X(K0, K1) do { _Pragma("unroll") for (int k = K0; k < K1; k++) { const int kk = k; dma4(rxs, vx[0], vx[1], vx[2], vx[3], kx * 128 + kk * 40960, sb + LD_X + lsx + kk * 4096); } } while (0)
#define ISSUE_XE() do { dma2(rxs, vx[0], vx[1], kx * 128 + 12 * 20480, sb + LD_X + lsx + 12 * 2048); kx = kx == 9 ? 0 : kx + 1; lsx += XSLOT; if (lsx == NXS * XSLOT) lsx = 0; } while (0)
#define ISSUE_W() do { _Pragma("unroll") for (int k = 0; k < 4; k++) { const int kk = k; \
      dma4((kk & 1) ? ru : rg, v[0], v[1], v[2], v[3], (kw * 64 + (kk >> 1) * 32) * 3072, sb + LD_R + lsw + (kk & 1) * 8192 + (kk >> 1) * 4096); } \
    kw = kw == 9 ? 0 : kw + 1; lsw += WSLOT; if (lsw == NWS * WSLOT) lsw = 0; } while (0)
  if (wv == 7) {
    __builtin_amdgcn_s_setprio(3);
    if (g1) { ISSUE_W(); ISSUE_X(0, 6); ISSUE_XE(); }
    asm volatile("s_mov_b32 m0, %0\n\ts_mov_b64 exec, 1\n\tbuffer_load_dword off, %1, 0 sc1 lds\n\ts_mov_b64 exec, -1" ::"s"(CTL + C_TAG), "s"(mkrs(p.ws, 64)) : "memory");
    sig0(rz, CTL + C_FULL);
    __builtin_amdgcn_s_setprio(1);
    STAMP(30);
  }
  __builtin_amdgcn_wave_barrier();
  if (wv == 5) {
    asm volatile("s_mov_b32 m0, %2\n\ts_nop 0\n\tbuffer_load_dword %0, %1, 0 offen lds" ::"v"(lane * 64 + (r & 15) * 4096), "s"(mkrs((const void*)pc0, codeb)), "s"(sb + LD_ST + 512) : "memory");
  }
  __builtin_amdgcn_wave_barrier();
  const int ch0 = 80 * xc + 5 * (r >> 1) + (r & 1) * 3;
  __builtin_amdgcn_wave_barrier();
  if (wv < 4) {
    __builtin_amdgcn_s_setprio(2);
    const rsrc_t rx = mkr(p.x, L * D * 2);
    constexpr int NWR = 5;
    v4u Wr[NWR][5];
    const u32 wdo = WS_WDF + (j2 * 48 + 12 * wv) * 5120;
    u32 tag;
    if (g1) {
      f4 acc[3][8], a13[2];
#pragma unroll
      for (int t = 0; t < 3; t++)
#pragma unroll
        for (int c = 0; c < 8; c++) { acc[t][c] = f4{0, 0, 0, 0}; asm volatile("" : "+v"(acc[t][c])); }
      a13[0] = a13[1] = f4{0, 0, 0, 0};
      const int q = (lane >> 2) & 3, pp = lane & 3, fp = ((q >> 1) & 1) | ((g & 1) << 1);
      u32 wb[4];
#pragma unroll
      for (int c = 0; c < 4; c++) wb[c] = sb + LD_R + (8 * g + q) * 128 + ((c ^ wv ^ fp) * 32) + pp * 8;
      u32 xb0 = sb + LD_X + wv * 6144 + i * 128 + ((g ^ (i >> 1)) << 4), xb1 = xb0 ^ 64;
      u32 yb0 = xb0 + (12 - 3 * wv) * 2048, yb1 = yb0 ^ 64, fa = CTL + C_FULL;
      v4u Wf[2][8], Xf[2][4];
      asm volatile("" : "+v"(a13[0]), "+v"(a13[1]), "+v"(wb[0]), "+v"(wb[1]), "+v"(wb[2]), "+v"(wb[3]), "+v"(xb0), "+v"(xb1), "+v"(yb0), "+v"(yb1), "+v"(fa));
#define RDP(B, n, XA, YA, OFF) do { if ((n) == 0) { Xf[B][0] = lds128(XA); Xf[B][1] = lds128(XA + 2048); } \
        else if ((n) == 2) { Xf[B][2] = lds128(XA + 4096); Xf[B][3] = lds128(YA); } \
        else { constexpr int c = (n) == 1 ? 0 : (n) - 2; const u32 a = wb[c & 3] + (OFF) + (c >> 2) * 8192; Wf[B][c] = trd2(a, a + 512); } SCHED(); } while (0)
#define MM(KS, m) do { constexpr int c = (m) < 4 ? 0 : (m) < 13 ? 1 + ((m) - 4) / 3 : (m) < 17 ? 4 : 5 + ((m) - 17) / 3; \
        constexpr int t = (m) < 4 ? (m) : (m) < 13 ? ((m) - 4) % 3 : (m) < 17 ? (m) - 13 : ((m) - 17) % 3; \
        if (t == 3) mma_bf(a13[c >> 2], Wf[KS][c], Xf[KS][3]); else mma_bf(acc[t < 3 ? t : 0][c], Wf[KS][c], Xf[KS][t]); SCHED(); } while (0)
#define R0(m) MM(0, m); RDP(1, m, xb1, yb1, 4096);
#define R1(m) MM(1, m); RDP(0, (m) - 12, xb0, yb0, 0);
#pragma unroll
      for (int c = 0; c < 8; c++) { Wf[1][c] = v4u{0, 0, 0, 0}; asm volatile("" : "+v"(Wf[1][c])); }
#pragma unroll
      for (int c = 0; c < 4; c++) { Xf[1][c] = v4u{0, 0, 0, 0}; asm volatile("" : "+v"(Xf[1][c])); }
      u32 sw = 0, sx = 0, fo = 1;
#pragma unroll 1
      for (int s = 0; s < 10; s++) {
        u32 fw;
        spin0(fa, fo);
        if (s == 0) STAMP(1);
        R1(12) R1(13) R1(14) R1(15) R1(16) R1(17) R1(18) R1(19) R1(20) R1(21)
        MM(1, 22); fa += 4; asm volatile("" : "+v"(fa)); SCHED(); MM(1, 23); MM(1, 24); MM(1, 25);
        R0(0) R0(1) R0(2) R0(3) R0(4) R0(5) R0(6) R0(7) R0(8) R0(9)
        sx += XSLOT; u32 dx = XSLOT; if (sx == NXS * XSLOT) { sx = 0; dx = XSLOT - NXS * XSLOT; }
        sw += WSLOT; u32 dl = WSLOT; if (sw == NWS * WSLOT) { sw = 0; dl = WSLOT - NWS * WSLOT; }
        MM(0, 10); xb0 += dx; xb1 += dx; asm volatile("" : "+v"(xb0), "+v"(xb1)); SCHED();
        MM(0, 11); yb0 += dx; yb1 += dx; asm volatile("" : "+v"(yb0), "+v"(yb1)); SCHED();
        MM(0, 12); wb[0] += dl; wb[1] += dl; asm volatile("" : "+v"(wb[0]), "+v"(wb[1])); SCHED();
        MM(0, 13); wb[2] += dl; wb[3] += dl; asm volatile("" : "+v"(wb[2]), "+v"(wb[3])); SCHED();
        MM(0, 14); MM(0, 15);
        asm volatile("s_waitcnt lgkmcnt(0)" ::: "memory");
        if (G1ONLY) STAMPD(3 + s - (s == 9 ? -1 : 0));
        sig0(rz, CTL + C_DONE + s * 16 + wv * 4); SCHED();
        if (s == cstep) {
          const u32 tg = mktag(ldsv(CTL + C_TAG));
          f4 c0 = {0, 0, 0, 0}, c1 = {0, 0, 0, 0}; v4u on = {0x3f803f80u, 0x3f803f80u, 0x3f803f80u, 0x3f803f80u};
          asm volatile("s_nop 1" : "+v"(c0), "+v"(c1), "+v"(on));
          mma_bf(c0, Wf[0][0], on); mma_bf(c1, Wf[0][4], on); mma_bf(c0, Wf[1][0], on); mma_bf(c1, Wf[1][4], on);
          asm volatile("s_nop 7" : "+v"(c0), "+v"(c1));
          const f4 cc = (i & 1) ? c1 : c0;
          if (i < 2) {
            const u32 o = WS_CSP + (i * 64 + 16 * wv + 4 * g) * 8, so = (cj * 10 + ri) * 1024;
            stc128(v4u{__float_as_uint(cc[0]), tg, __float_as_uint(cc[1]), tg}, rw, o, so); stc128(v4u{__float_as_uint(cc[2]), tg, __float_as_uint(cc[3]), tg}, rw, o + 16, so);
          }
          asm volatile("s_nop 3"); SCHED();
        }
        MM(0, 16); MM(0, 17); MM(0, 18); MM(0, 19); MM(0, 20); MM(0, 21); MM(0, 22); MM(0, 23); MM(0, 24); MM(0, 25);
        MM(1, 0); MM(1, 1); MM(1, 2); MM(1, 3); fw = ldsv(fa); SCHED();
        MM(1, 4); MM(1, 5); MM(1, 6); MM(1, 7); MM(1, 8); MM(1, 9); fo = rfl(fw); SCHED(); MM(1, 10); MM(1, 11);
      }
      MM(1, 12); MM(1, 13); MM(1, 14); MM(1, 15); MM(1, 16); MM(1, 17); MM(1, 18); MM(1, 19); MM(1, 20); MM(1, 21); MM(1, 22); MM(1, 23); MM(1, 24); MM(1, 25);
      CSTAMP(14);
      if (G1ONLY) { STAMP(2); float sm = a13[0][0] + a13[1][0]; for (int t = 0; t < 3; t++) for (int c = 0; c < 8; c++) sm += acc[t][c][0]; if (sm == 1.2345f) p.out[0] = 1; ST_FLUSH(); __builtin_amdgcn_endpgm(); }
      { const v4u cw = *(volatile LAS v4u*)(CTL + C_RDY); tag = mktag(rfl(cw.w)); spin0(CTL + C_RDY, rfl(cw.x)); }
      STAMP(2);
      f4 cg[4], cu[4]; f2 st[4];
#pragma unroll
      for (int t = 0; t < 4; t++) st[t] = *(LAS f2*)(sb + LD_ST + ((t < 3 ? 3 * wv + t : 12) * 16 + i) * 8);
#pragma unroll
      for (int c = 0; c < 4; c++) { cg[c] = *(LAS f4*)(sb + LD_CS + (16 * (c ^ wv) + 4 * g) * 4); cu[c] = *(LAS f4*)(sb + LD_CS + 256 + (16 * (c ^ wv) + 4 * g) * 4); }
      const u32 hb = WS_HF + (2 * cj) * 1024 + (((((g ^ wv) & 1) << 1) | (g >> 1)) * 16 + i) * 16;
#pragma unroll
      for (int t = 0; t < 4; t++) {
        f2 k1 = {-st[t].x, -1.44269504089f * st[t].y}, k2 = {__builtin_amdgcn_rcpf(st[t].y), 0.f};
        asm volatile("s_nop 0" : "+v"(k1), "+v"(k2));
        v2u P[4];
#pragma unroll
        for (int c = 0; c < 4; c++) {
          if (t == 3 && c > 0) break;
          const f4 ag = t < 3 ? acc[t < 3 ? t : 0][c] : a13[0], au = t < 3 ? acc[t < 3 ? t : 0][4 + c] : a13[1];
#pragma unroll
          for (int e = 0; e < 2; e++) {
            const f2 a2 = e ? ag.zw : ag.xy, u2 = e ? au.zw : au.xy, cg2 = e ? cg[c].zw : cg[c].xy, cu2 = e ? cu[c].zw : cu[c].xy;
            const f2 gg = pmul_y(k1, pfma_x(k1, cg2, a2)), uu = pfma_x(k1, cu2, u2);
            const f2 den = pfma_xx(k2, f2{__builtin_amdgcn_exp2f(gg.x), __builtin_amdgcn_exp2f(gg.y)});
            const f2 h = pmul(uu, f2{__builtin_amdgcn_rcpf(den.x), __builtin_amdgcn_rcpf(den.y)});
            P[c][e] = pkbf(h.x, h.y);
          }
          if (c & 1) SCHED();
        }
        if (t < 3) {
#pragma unroll
          for (int k = 0; k < 2; k++) {
            auto sx2 = __builtin_amdgcn_permlane16_swap(P[2 * k].x, P[2 * k + 1].x, false, false);
            auto sy2 = __builtin_amdgcn_permlane16_swap(P[2 * k].y, P[2 * k + 1].y, false, false);
            stc128(v4u{sx2[0], sy2[0], sx2[1], sy2[1]}, rw, hb + (k ^ (wv >> 1)) * 1024, (ts + 3 * wv + t) * 49152);
          }
        } else if (r5 != 0) {
          __builtin_amdgcn_raw_buffer_store_b64(P[0], rw, WS_HF + (2 * cj + (wv >> 1)) * 1024 + ((2 * (wv & 1) + (g >> 1)) * 16 + i) * 16 + (g & 1) * 8, (ts + 12) * 49152, AUX_SC1);
        }
        SCHED();
      }
      STAMP(3);
      __builtin_amdgcn_s_setprio(3);
      ldsw(CTL + C_DONE + 160 + wv * 4, 0);
      waitv<0>();
      if (lane == 0 && __hip_atomic_fetch_add((LAS u32*)(CTL + C_HCNT), 1u, __ATOMIC_RELAXED, __HIP_MEMORY_SCOPE_WORKGROUP) == 3) stc32(tag, rw, WS_HFLAG + ri * 128 + cj * 4);
      STAMP(4);
    } else {
      if (G1ONLY) __builtin_amdgcn_endpgm();
      wait0(CTL + C_FULL);
      tag = mktag(rfl(ldsv(CTL + C_TAG)));
      wait0(CTL + C_RDY);
      wait0(CTL + C_WDFULL);
    }
    const int tt = 4 * i2 + wv;
    f4 o[4][5];
#pragma unroll
    for (int n = 0; n < 25; n++) Wr[n / 5][n % 5] = ldc128(rw, lane * 16, wdo + n * 1024);
    SCHED();
    wait0(CTL + C_HRDY + wv * 4);
    STAMP(5);
    constexpr int HD = 3;
    v4u hr[12][4], Wl[2][5], Wz[5], xq[3], dq[3]; v2u sq[3]; u32 oo[3], lo3[3];
    u32 ho[4];
#pragma unroll
    for (int t = 0; t < 4; t++) ho[t] = ABL_H ? WS_WDF + t * 12288 : WS_HF + (4 * i2 + (t ^ wv)) * 49152 + 12 * wv * 1024;
#define LOAD_H(k) do { _Pragma("unroll") for (int t = 0; t < 4; t++) hr[k][t] = ldc128(rw, lane * 16, ho[t] + KS2(k) * 1024); } while (0)
#pragma unroll
    for (int k = 0; k < HD; k++) LOAD_H(k);
    SCHED();
    __builtin_amdgcn_s_setprio(G2_PRIO);
    const u32 wl = sb + wd_off(wv) + lane * 16;
    float base[3][8];
#define VFMA(d, a_, b_, c_) do { float t_; asm volatile("v_fma_f32 %0, %1, %2, %3" : "=v"(t_) : "v"(a_), "v"(b_), "v"(c_)); d = t_; } while (0)
#define VLO(d, w_) asm volatile("v_lshlrev_b32 %0, 16, %1" : "=v"(d) : "v"(w_))
#define VHI(d, w_) asm volatile("v_and_b32 %0, 0xffff0000, %1" : "=v"(d) : "v"(w_))
    float nbv[3];
#pragma unroll
    for (int k = 0; k < 11; k++) {
      if (k == NWR - 1) wait0(CTL + C_WDFULL);
#pragma unroll
      for (int c = 0; c < 5; c++) {
        const v4u w = k < NWR ? Wr[k < NWR ? k : 0][c] : Wl[k & 1][c];
#pragma unroll
        for (int t = 3; t >= 0; t--) {
          if (k == 0) mma_bf0(o[t][c], w, hr[k][t]); else mma_bf(o[t][c], w, hr[k][t]);
          SCHED();
          if (t == 1 && k == 6) { Wz[c] = ldc128(rw, lane * 16, wdo + 11 * 5120 + c * 1024); SCHED(); }
          if (t == 2 && k + 1 >= NWR && k + 1 < 11) { Wl[(k + 1) & 1][c] = lds128(wl + ((k + 1 - NWR) * 5 + c) * 1024); SCHED(); }
          if (t == 3 && c < 4) {
            const int kn = k == 0 ? -1 : k + HD;
            if (kn > 0 && kn < 12) { hr[kn][c] = ldc128(rw, lane * 16, ho[c] + kn * 1024); SCHED(); }
          }
          if (k >= 9) {
            const int n = (k - 9) * 20 + c * 4 + (3 - t) - 13;
            if (n >= 0 && n < 3) VFMA(nbv[n], -__uint_as_float(sq[n].x), __uint_as_float(sq[n].y), 0.f);
            if (n >= 3) { const int m = (n - 3) >> 3, e = (n - 3) & 7; float xv, dv, xn;
              if (e & 1) { VHI(xv, xq[m][e >> 1]); VHI(dv, dq[m][e >> 1]); } else { VLO(xv, xq[m][e >> 1]); VLO(dv, dq[m][e >> 1]); }
              VFMA(xn, xv, __uint_as_float(sq[m].y), nbv[m]); VFMA(base[m][e], xn, dv, xv); }
            SCHED();
          }
        }
      }
      if (k == 0) { LOAD_H(HD); SCHED(); }
      if (k == 7) {
#pragma unroll
        for (int m = 0; m < 3; m++) {
          const int n = min(m * 64 + lane, 159), row = n / 10, cc = n - row * 10;
          oo[m] = ((16 * tt + row) * D + 80 * j2 + cc * 8) * 2; lo3[m] = row * 336 + cc * 32;
          xq[m] = __builtin_amdgcn_raw_buffer_load_b128(rx, oo[m], 0, 0);
          dq[m] = __builtin_amdgcn_raw_buffer_load_b128(mkr(p.dsk, D * 2), (80 * j2 + cc * 8) * 2, 0, 0);
          sq[m] = __builtin_amdgcn_raw_buffer_load_b64(rw, WS_STAT + (16 * tt + row) * 8, 0, AUX_SC1);
        }
        SCHED();
      }
    }
    {
#pragma unroll
      for (int n = 0; n < 25; n++) {
        if (n < 20) { mma_bf(o[3 - n / 5][n % 5], Wz[n % 5], hr[11][3 - n / 5]); SCHED(); }
        if (n >= 5 && n < 20) { const int m = n - 5; asm volatile("s_nop 2" : "+v"(o[3 - m / 5][m % 5])); *(LAS f4*)(wl + ((2 - m / 5) * 5 + m % 5) * 1024) = o[3 - m / 5][m % 5]; SCHED(); }
      }
      ldsw(CTL + C_RED + wv * 4, 0);
      STAMP(6);
      asm volatile("s_nop 7" : "+v"(o[0][0]), "+v"(o[0][1]), "+v"(o[0][2]), "+v"(o[0][3]), "+v"(o[0][4]));
      spin_zero4(CTL + C_RED);
#pragma unroll
      for (int c = 0; c < 5; c++) {
        f4 s[3];
#pragma unroll
        for (int t = 1; t < 4; t++) s[t - 1] = *(LAS f4*)(sb + wd_off(wv ^ t) + lane * 16 + ((t - 1) * 5 + c) * 1024);
#pragma unroll
        for (int e = 0; e < 4; e++) o[0][c][e] = fadd_(fadd_(o[0][c][e], s[0][e]), fadd_(s[1][e], s[2][e]));
        asm volatile("" : "+v"(o[0][c]));
      }
      wait0(CTL + C_ZFULL);
      STAMP(7);
      const int q = (lane >> 2) & 3, pp = lane & 3;
      const u32 tb = sb + wd_off(wv) + 15360;
      v2u z[5];
#pragma unroll
      for (int c = 0; c < 5; c++) z[c] = trd(sb + LD_Z + (20 * g + 4 * c + q) * 128 + wv * 32 + pp * 8);
#pragma unroll
      for (int c = 0; c < 5; c++)
        *(LAS f4*)(tb + i * 336 + g * 80 + c * 16) = f4{fadd_(o[0][c][0], bf2f(z[c].x)), fadd_(o[0][c][1], bfhi(z[c].x)), fadd_(o[0][c][2], bf2f(z[c].y)), fadd_(o[0][c][3], bfhi(z[c].y))};
      CBAR();
      const rsrc_t ro = mkr(p.out, L * D * 2);
#pragma unroll
      for (int m = 0; m < 3; m++) {
        const f4 u0 = *(LAS f4*)(tb + lo3[m]), u1 = *(LAS f4*)(tb + lo3[m] + 16);
        const v4u w = {pkbf(fadd_(u0[0], base[m][0]), fadd_(u0[1], base[m][1])), pkbf(fadd_(u0[2], base[m][2]), fadd_(u0[3], base[m][3])),
                       pkbf(fadd_(u1[0], base[m][4]), fadd_(u1[1], base[m][5])), pkbf(fadd_(u1[2], base[m][6]), fadd_(u1[3], base[m][7]))};
        if (m < 2 || lane < 32) stc128(w, ro, oo[m], 0);
      }
    }
    STAMP(8); CSTAMP(15);
    ST_FLUSH();
    __builtin_amdgcn_endpgm();
  } else if (wv < 7) {
    const int slot = wv - 4;
    if (G1ONLY == 1) { __builtin_amdgcn_endpgm(); }
    if (slot == 2 && (r & 1)) { ln_unit(p, sb, (16 * (xc >> 2) + (r >> 1)) * 4 + (xc & 3), lane); STAMP(21); }
    else { conv_wave(p, sb, slot, ch0 + slot, lane, wv, g1, ts, cj ST_PASS); }
    ST_FLUSH();
    __builtin_amdgcn_endpgm();
  } else {
    if (g1) {
      ISSUE_W();
      v4u dn = {0, 0, 0, 0};
#pragma unroll 1
      for (int n = 1; n < 10; n++) {
        if (rfl(dn.x | dn.y | dn.z | dn.w) != 0) spin_zero4(CTL + C_DONE + (n - 3) * 16);
        waitv<50>(); ISSUE_X(0, 3); waitv<47>(); ISSUE_X(3, 6); ISSUE_XE();
        sig0(rz, CTL + C_FULL + n * 4);
        __builtin_amdgcn_s_setprio(3);
        if (G1ONLY) STAMPD(14 + n);
        if (n >= 2) dn = *(volatile LAS v4u*)(CTL + C_DONE + (n - 2) * 16);
        if (n < 9) { waitv<44>(); ISSUE_W(); }
      }
      STAMP(12);
      if (G1ONLY) { waitv<0>(); ST_FLUSH(); __builtin_amdgcn_endpgm(); }
    }
    if (G1ONLY) __builtin_amdgcn_endpgm();
    wait0(CTL + C_WOK);
    const u32 tag = mktag(rfl(ldsv(CTL + C_TAG)));
    __builtin_amdgcn_s_setprio(WD_PRIO);
    STAMP(23);
    const v4u rws = mkrs(p.ws, WS_BYTES);
    {
      const u32 vo = lane * 16;
      const u32 gb = WS_WDF + (j2 * 48 + 5) * 5120;
#define RUN(W, K0, N) do { _Pragma("unroll") for (int k = 0; k < (N); k += 4) { waitv<WD_DEPTH>(); const u32 go = gb + (W) * 61440 + ((K0) + k) * 1024, lo = sb + wd_off(W) + ((K0) + k) * 1024; \
        if ((N) - k >= 4) dma4c(rws, vo, vo, vo, vo, go, lo); else dma2c(rws, vo, vo, go, lo); } } while (0)
      RUN(0, 0, 16);
      spin_zero4(CTL + C_DONE + 7 * 16); RUN(0, 16, 14); RUN(2, 8, 22); RUN(3, 0, 4);
      spin_zero4(CTL + C_DONE + 8 * 16); RUN(3, 4, WD_P1);
      sig0(rz, CTL + C_QUIET);
      STAMP(27);
      spin_zero4(CTL + C_KDONE); spin_zero4(CTL + C_DONE + 9 * 16); RUN(1, 0, 30); RUN(2, 0, 8); RUN(3, 4 + WD_P1, 26 - WD_P1);
      sig0(rz, CTL + C_WDFULL);
    }
    STAMP(24);
    {
      spin_zero4(CTL + C_DONE + 160);
      const int l48 = lane < 48 ? lane : 0;
      const u32 fo = WS_HFLAG + (l48 < 24 ? ((20 * i2 + 4) >> 6) * 128 + l48 * 4 : ((20 * i2 + 19) >> 6) * 128 + (l48 - 24) * 4);
      u32 left = 15;
#pragma unroll 1
      for (u32 it = 0; it < SPIN_CAP && left; it++) {
        __builtin_amdgcn_s_sleep(HP_SLEEP);
        const u64 bad = __builtin_amdgcn_ballot_w64(ldc32(rw, fo) != tag);
        const u32 bb = ((u32)bad | (u32)(bad >> 24)) & 0xffffffu;
        ORDER();
#pragma unroll
        for (int k = 0; k < 4; k++) if (((left >> k) & 1) && !((bb >> (6 * k)) & 63)) { sig0(rz, CTL + C_HRDY + k * 4); left &= ~(1u << k); }
      }
    }
    __builtin_amdgcn_s_setprio(3);
    spin_zero4(CTL + C_CDONE);
    if (b == 0 && lane == 0) stc32(tag, rw, WS_GEN);
    if (lane < 8) stc32(tag, rw, WS_ZFLAG + (lane * 8 + xc) * 128 + r * 4);
    __builtin_amdgcn_s_sleep(ZP_FIRST);
#pragma unroll 1
    for (u32 it = 0; it < SPIN_CAP; it++) {
      if (__all(ldc32(rw, WS_ZFLAG + (xc * 8 + j2) * 128 + (lane & 31) * 4) == tag)) break;
      __builtin_amdgcn_s_sleep(ZP_SLEEP);
    }
    ORDER();
    {
      u32 vz[4];
#pragma unroll
      for (int k = 0; k < 4; k++) vz[k] = (8 * k + (lane >> 3)) * 4096 + (lane & 7) * 16 - 1024 * k;
      const u32 go = WS_ZT + 80 * j2 * 4096 + i2 * 128;
      dma4c(rws, vz[0], vz[1], vz[2], vz[3], go, sb + LD_Z); dma4c(rws, vz[0], vz[1], vz[2], vz[3], go + 32 * 4096, sb + LD_Z + 4096);
      dma2c(rws, vz[0], vz[1], go + 64 * 4096, sb + LD_Z + 8192);
      sig0(rz, CTL + C_ZFULL);
      waitv<0>();
    }
    STAMP(25);
  }
  ST_FLUSH();
}

extern "C" void kernel_launch(void* const* d_in, const int* in_sizes, int n_in, void* d_out, int out_size, void* d_ws, size_t ws_size, hipStream_t stream) {
  Params p;
  p.x = (const u16*)d_in[0]; p.lar = (const u16*)d_in[1]; p.aim = (const u16*)d_in[2]; p.cre = (const u16*)d_in[3]; p.cim = (const u16*)d_in[4];
  p.ldt = (const u16*)d_in[5]; p.dsk = (const u16*)d_in[6]; p.wg = (const u16*)d_in[8]; p.wu = (const u16*)d_in[9]; p.wd = (const u16*)d_in[10];
  p.out = (u16*)d_out; p.ws = (char*)d_ws;
  (void)hipFuncSetAttribute((const void*)k_main, hipFuncAttributeMaxDynamicSharedMemorySize, LDS_BYTES);
  k_main<<<256, 512, LDS_BYTES, stream>>>(p);
}
```

```cpp
#include <hip/hip_runtime.h>
#pragma clang diagnostic ignored "-Wint-to-pointer-cast"
typedef unsigned short u16; typedef unsigned u32; typedef unsigned long long u64;
typedef u32 v4u __attribute__((ext_vector_type(4)));
typedef u32 v2u __attribute__((ext_vector_type(2)));
typedef float f4 __attribute__((ext_vector_type(4)));
typedef float f2 __attribute__((ext_vector_type(2)));
typedef short s4 __attribute__((ext_vector_type(4)));
typedef __bf16 bf2 __attribute__((ext_vector_type(2)));
typedef _Float16 hf2 __attribute__((ext_vector_type(2)));
#define LAS __attribute__((address_space(3)))
#define GAS __attribute__((address_space(1)))
#define DEV __device__ __forceinline__
typedef __amdgpu_buffer_rsrc_t rsrc_t;

constexpr int L = 2048, D = 640, NS = 192, F = 1536;
constexpr u32 WS_GEN = 0, WS_HFLAG = 1024, WS_ZFLAG = 40960, WS_LFLAG = 49152, WS_WFLAG = WS_LFLAG + 512, WS_STAT = 16384, WS_CSP = 65536,
              WS_XT = 1u << 20, WS_WDF = 4u << 20, WS_HF = 6u << 20, WS_ZT = 13u << 20, WS_BYTES = 16u << 20;
constexpr u32 LD_K = 0, K_BYTES = 5120;
constexpr u32 LD_CS = LD_K + 3 * K_BYTES, LD_ST = LD_CS + 512, LD_CTL = LD_ST + 208 * 8, LD_R = LD_CTL + 896;
constexpr u32 WSLOT = 16384, NWS = 4, XSLOT = 26624, NXS = 3, LD_X = LD_R + NWS * WSLOT, LDS_BYTES = LD_X + NXS * XSLOT;
constexpr u32 LD_H = LD_X + 2 * XSLOT;
constexpr u32 WD_BYTES = 30720, LD_Z = LD_X;
DEV u32 wd_off(int w) { return LD_R + ((0x70520020u >> (8 * w)) & 0xffu) * 1024; }
static_assert(LD_R % 128 == 0 && 18432 + 2 * WD_BYTES == NXS * XSLOT && LDS_BYTES <= 163840, "lds");
constexpr u32 C_FULL = 512, C_WDFULL = 44, C_RDY = 48, C_ZFULL = 52, C_UFULL = 56, C_TAG = 60, C_KDONE = 64, C_RED = 80, C_CDONE = 96, C_HRDY = 112, C_QUIET = 128, C_HCNT = 132, C_DONE = 256;
constexpr int AUX_SC1 = 16;
constexpr u32 SPIN_CAP = 1u << 15;
#define WD_DEPTH 40
#define WD_PRIO 3
#define POLL_STAGE 8
#define WPOLL_STAGE 7
#define WD_P1 12
#define HP_SLEEP 4
#define G2_PRIO 1
#define ABL_H 0
#define ABL_Z 0
#define ZP_FIRST 16
#define ZP_SLEEP 8
#define GWAIT 0
#define P1MODE 1
#define AUX_STAGE 1
#define NST 14
#define NGRP 2
#define OFFS 0x50
#define CT0 0x1293725u
#define CT1 0x1293725u
#define CT2 0x1293725u
#define CT3 0x1293725u
#define CT4 0x1293725u
#define CT5 0x1293725u
#define HTOUCH 0
#define HPOLL4 1
#define KL_PRIO 1
#define G1ONLY 0
#define CONV_PRIO 2

#define STAMP(k) do { } while (0)
#define ST_ARG
#define STAMPD(k) do { } while (0)
#define ST_PASS
#define ST_FLUSH() do { } while (0)

struct Params {
  const u16 *x, *lar, *aim, *cre, *cim, *ldt, *dsk, *wg, *wu, *wd;
  u16* out; char* ws;
};

DEV float bf2f(u32 v) { return __uint_as_float(v << 16); }
DEV float bfhi(u32 v) { return __uint_as_float(v & 0xffff0000u); }
DEV u32 pkbf(float a, float b) { f2 v = {a, b}; return __builtin_bit_cast(u32, __builtin_convertvector(v, bf2)); }
DEV u32 pkhf(float a, float b) { f2 v = {a, b}; return __builtin_bit_cast(u32, __builtin_convertvector(v, hf2)); }
DEV float rbf(float a) { return bf2f(pkbf(a, a)); }
DEV void mma_bf(f4& c, v4u a, v4u b) { asm volatile("v_mfma_f32_16x16x32_bf16 %0, %1, %2, %0" : "+v"(c) : "v"(a), "v"(b)); }
DEV void mma_hf(f4& c, v4u a, v4u b) { asm volatile("v_mfma_f32_16x16x32_f16 %0, %1, %2, %0" : "+v"(c) : "v"(a), "v"(b)); }
DEV void mma_hf0(f4& c, v4u a, v4u b) { asm volatile("v_mfma_f32_16x16x32_f16 %0, %1, %2, 0" : "=&v"(c) : "v"(a), "v"(b)); }
DEV void mma_bf0(f4& c, v4u a, v4u b) { asm volatile("v_mfma_f32_16x16x32_bf16 %0, %1, %2, 0" : "=&v"(c) : "v"(a), "v"(b)); }
DEV v2u trd(u32 a) { return __builtin_bit_cast(v2u, __builtin_amdgcn_ds_read_tr16_b64_v4i16((LAS s4*)a)); }
DEV v4u trd2(u32 a, u32 b) { v2u x = trd(a), y = trd(b); return v4u{x.x, x.y, y.x, y.y}; }
DEV v4u lds128(u32 a) { return *(LAS v4u*)a; }
DEV v2u lds64(u32 a) { return *(LAS v2u*)a; }
DEV u32 rfl(u32 v) { return __builtin_amdgcn_readfirstlane(v); }
DEV v4u mkrs(const void* p, u32 bytes) { u64 a = (u64)p; return v4u{(u32)a, (u32)(a >> 32) & 0xffffu, bytes, 0x00020000u}; }
DEV rsrc_t mkr(const void* p, u32 bytes) { return __builtin_amdgcn_make_buffer_rsrc((void*)p, 0, bytes, 0x00020000); }
DEV v4u ldc128(rsrc_t r, u32 v, u32 s) { return __builtin_amdgcn_raw_buffer_load_b128(r, v, s, AUX_SC1); }
DEV u32 ldc32(rsrc_t r, u32 v) { return __builtin_amdgcn_raw_buffer_load_b32(r, v, 0, AUX_SC1); }
DEV void stc128(v4u d, rsrc_t r, u32 v, u32 s) {
  __builtin_amdgcn_raw_buffer_store_b128(d, r, v, s, AUX_SC1); asm volatile("s_nop 1" ::"v"(d) : "memory"); }
DEV void stc32(u32 d, rsrc_t r, u32 v) { __builtin_amdgcn_raw_buffer_store_b32(d, r, v, 0, AUX_SC1); }
#define CBAR() asm volatile("" ::: "memory")
#define SCHED() __builtin_amdgcn_sched_barrier(0)
#define ORDER() __builtin_amdgcn_fence(__ATOMIC_ACQUIRE, "wavefront")
#define BARRIER() do { asm volatile("s_waitcnt lgkmcnt(0)" ::: "memory"); __builtin_amdgcn_s_barrier(); CBAR(); } while (0)
template <int N> DEV void waitv() { asm volatile("s_waitcnt vmcnt(%0)" ::"i"(N) : "memory"); }
#define DMA_(V, RS, SO, OFF, SC) "buffer_load_dwordx4 " V ", " RS ", " SO " offen" OFF SC " lds\n\t"
#define DMA4_(NAME, SC) DEV void NAME(v4u rs, u32 v0, u32 v1, u32 v2, u32 v3, u32 so, u32 lds) { \
  asm volatile("s_mov_b32 m0, %0\n\ts_nop 0\n\t" DMA_("%1", "%5", "%6", "", SC) DMA_("%2", "%5", "%6", " offset:1024", SC) DMA_("%3", "%5", "%6", " offset:2048", SC) DMA_("%4", "%5", "%6", " offset:3072", SC) \
               ::"s"(lds), "v"(v0), "v"(v1), "v"(v2), "v"(v3), "s"(rs), "s"(so) : "memory"); }
#define DMA2_(NAME, SC) DEV void NAME(v4u rs, u32 v0, u32 v1, u32 so, u32 lds) { \
  asm volatile("s_mov_b32 m0, %0\n\ts_nop 0\n\t" DMA_("%1", "%3", "%4", "", SC) DMA_("%2", "%3", "%4", " offset:1024", SC) ::"s"(lds), "v"(v0), "v"(v1), "s"(rs), "s"(so) : "memory"); }
#define DMA1_(NAME, SC) DEV void NAME(v4u rs, u32 v0, u32 so, u32 lds) { \
  asm volatile("s_mov_b32 m0, %0\n\ts_nop 0\n\t" DMA_("%1", "%2", "%3", "", SC) ::"s"(lds), "v"(v0), "s"(rs), "s"(so) : "memory"); }
DMA4_(dma4, "") DMA2_(dma2, "") DMA1_(dma1, "") DMA4_(dma4c, " sc1") DMA2_(dma2c, " sc1") DMA1_(dma1c, " sc1")
DEV void sig0(v4u rz, u32 lds) {
  asm volatile("s_mov_b32 m0, %0\n\ts_mov_b64 exec, 1\n\tbuffer_load_dword off, %1, 0 lds\n\ts_mov_b64 exec, -1" ::"s"(lds), "s"(rz) : "memory");
}
DEV u32 mktag(u32 gen) { return gen * 0x9E3779B1u + 0x7F4A7C15u; }
DEV u32 ldsv(u32 a) { return *(volatile LAS u32*)a; }
DEV void ldsw(u32 a, u32 v) { asm volatile("s_mov_b64 exec, 1\n\tds_write_b32 %0, %1\n\ts_mov_b64 exec, -1" ::"v"(a), "v"(v) : "memory"); }
DEV void spin0(u32 a, u32 cur) {
  u32 tv, cnt;
  asm volatile("s_cmp_eq_u32 %3, 0\n\ts_cbranch_scc0 3f\n2:\n\t.subsection 1\n3:\n\ts_mov_b32 %1, 0x10000\n"
               "1:\n\tds_read_b32 %0, %2\n\ts_sub_u32 %1, %1, 1\n\ts_waitcnt lgkmcnt(0)\n\tv_cmp_ne_u32 vcc, 0, %0\n\ts_nop 1\n\ts_min_u32 vcc_lo, vcc_lo, %1\n\ts_cmp_eq_u32 vcc_lo, 0\n\ts_cbranch_scc1 2b\n\t"
               "s_sleep 1\n\ts_branch 1b\n\t.subsection 0"
               : "=&v"(tv), "=&s"(cnt) : "v"(a), "s"(cur) : "memory", "scc", "vcc");
}
DEV void wait0(u32 a) {
  u32 tv, ts, cnt;
  asm volatile("s_mov_b32 %2, 0x10000\n1:\n\tds_read_b32 %0, %3\n\ts_sub_u32 %2, %2, 1\n\ts_waitcnt lgkmcnt(0)\n\tv_readfirstlane_b32 %1, %0\n\ts_nop 0\n\t"
               "s_min_u32 %1, %1, %2\n\ts_cmp_eq_u32 %1, 0\n\ts_cbranch_scc1 2f\n\ts_sleep 1\n\ts_branch 1b\n2:"
               : "=&v"(tv), "=&s"(ts), "=&s"(cnt) : "v"(a) : "memory", "scc");
}
DEV void spin_zero4(u32 a) {
  u32 tv, cnt;
  const u32 al = a + (__builtin_amdgcn_mbcnt_lo(~0u, 0) & 3) * 4;
  asm volatile("s_mov_b32 %1, 0x10000\n1:\n\tds_read_b32 %0, %2\n\ts_sub_u32 %1, %1, 1\n\ts_waitcnt lgkmcnt(0)\n\tv_cmp_ne_u32 vcc, 0, %0\n\ts_nop 1\n\t"
               "s_min_u32 vcc_lo, vcc_lo, %1\n\ts_cmp_eq_u32 vcc_lo, 0\n\ts_cbranch_scc1 2f\n\ts_sleep 1\n\ts_branch 1b\n2:"
               : "=&v"(tv), "=&s"(cnt) : "v"(al) : "memory", "scc", "vcc");
}
template <int SL> DEV void poll_flags(rsrc_t rw, u32 off, u32 off2, u32 tag) {
#pragma unroll 1
  for (u32 i = 0; i < SPIN_CAP; i++) {
    const u32 a = ldc32(rw, off), b = ldc32(rw, off2);
    if (__all(a == tag && b == tag)) break;
    __builtin_amdgcn_s_sleep(SL);
  }
  ORDER();
}
DEV float fmul_(float a, float b) { float r; asm("v_mul_f32 %0, %1, %2" : "=v"(r) : "v"(a), "v"(b)); return r; }
DEV float fadd_(float a, float b) { float r; asm("v_add_f32 %0, %1, %2" : "=v"(r) : "v"(a), "v"(b)); return r; }
DEV float ffma_(float a, float b, float c) { float r; asm("v_fma_f32 %0, %1, %2, %3" : "=v"(r) : "v"(a), "v"(b), "v"(c)); return r; }
DEV float fnma_(float a, float b, float c) { float r; asm("v_fma_f32 %0, -%1, %2, %3" : "=v"(r) : "v"(a), "v"(b), "v"(c)); return r; }
struct cf { float x, y; };
DEV cf csq(cf a) { const float t = fmul_(a.x, a.y); return cf{fnma_(a.y, a.y, fmul_(a.x, a.x)), fadd_(t, t)}; }
DEV u32 pkc(cf a) { return pkhf(a.x, a.y); }
DEV cf cexp2(float lr, float rev) { float m = __builtin_amdgcn_exp2f(lr); const float f = __builtin_amdgcn_fractf(rev); float c = __builtin_amdgcn_cosf(f), s = __builtin_amdgcn_sinf(f);
  asm volatile("s_nop 0" : "+v"(c), "+v"(s), "+v"(m)); return cf{fmul_(m, c), fmul_(m, s)}; }
DEV u32 hmul(u32 a, u32 m) { u32 t, r;
  asm("v_pk_mul_f16 %0, %1, %2 op_sel_hi:[0,1]" : "=v"(t) : "v"(a), "v"(m));
  asm("v_pk_fma_f16 %0, %1, %2, %3 op_sel:[1,1,0] op_sel_hi:[1,0,1] neg_lo:[0,1,0]" : "=v"(r) : "v"(a), "v"(m), "v"(t)); return r; }
DEV u32 hmulc(u32 a, u32 m) { u32 t, r;
  asm("v_pk_mul_f16 %0, %1, %2 op_sel_hi:[0,1] neg_hi:[0,1]" : "=v"(t) : "v"(a), "v"(m));
  asm("v_pk_fma_f16 %0, %1, %2, %3 op_sel:[1,1,0] op_sel_hi:[1,0,1]" : "=v"(r) : "v"(a), "v"(m), "v"(t)); return r; }
DEV f2 pfma_x(f2 s, f2 b, f2 c) { f2 r; asm("v_pk_fma_f32 %0, %1, %2, %3 op_sel_hi:[0,1,1]" : "=v"(r) : "v"(s), "v"(b), "v"(c)); return r; }
DEV f2 pmul_y(f2 s, f2 b) { f2 r; asm("v_pk_mul_f32 %0, %1, %2 op_sel:[1,0]" : "=v"(r) : "v"(s), "v"(b)); return r; }
DEV f2 pfma_xx(f2 s, f2 b) { f2 r; asm("s_nop 0\n\tv_pk_fma_f32 %0, %1, %2, %1 op_sel_hi:[0,1,0]" : "=v"(r) : "v"(s), "v"(b)); return r; }
DEV f2 pmul(f2 a, f2 b) { f2 r; asm("s_nop 0\n\tv_pk_mul_f32 %0, %1, %2" : "=v"(r) : "v"(a), "v"(b)); return r; }
DEV void swap16(float& a, float& b) { auto r = __builtin_amdgcn_permlane16_swap(__float_as_uint(a), __float_as_uint(b), false, false); a = __uint_as_float(r[0]); b = __uint_as_float(r[1]); }
DEV void swap32(float& a, float& b) { auto r = __builtin_amdgcn_permlane32_swap(__float_as_uint(a), __float_as_uint(b), false, false); a = __uint_as_float(r[0]); b = __uint_as_float(r[1]); }

DEV void ln_unit(const Params& p, u32 sb, int pn, int lane) {
  const rsrc_t rx = mkr(p.x, L * D * 2), rw = mkr(p.ws, WS_BYTES);
  const int h = lane & 1, m = lane >> 1, row0 = 64 * (pn >> 2) + (pn & 3);
  __builtin_amdgcn_s_setprio(2);
  v2u xv[8][5]; float xf[8][20], s[16];
#pragma unroll
  for (int e = 0; e < 8; e++)
#pragma unroll
    for (int i = 0; i < 5; i++) xv[e][i] = __builtin_amdgcn_raw_buffer_load_b64(rx, (row0 + 32 * h) * 1280 + m * 8 + i * 256, e * 5120, 0);
  SCHED();
  __builtin_amdgcn_s_setprio(3);
#pragma unroll
  for (int e = 0; e < 8; e++) {
#pragma unroll
    for (int i = 0; i < 10; i++) { const u32 w = xv[e][i >> 1][i & 1]; xf[e][2 * i] = bf2f(w); xf[e][2 * i + 1] = bfhi(w); }
    float a = fadd_(xf[e][0], xf[e][1]), q = fmul_(xf[e][0], xf[e][0]);
#pragma unroll
    for (int i = 1; i < 20; i++) { if (i > 1) a = fadd_(a, xf[e][i]); asm("v_fmac_f32 %0, %1, %1" : "+v"(q) : "v"(xf[e][i])); }
    s[e] = a; s[8 + e] = q;
  }
#pragma unroll
  for (int e = 0; e < 16; e++) asm volatile("" : "+v"(s[e]));
  asm volatile("s_nop 1");
  float z[4];
#pragma unroll
  for (int k = 0; k < 4; k++) {
    swap16(s[4 * k], s[4 * k + 1]); swap16(s[4 * k + 2], s[4 * k + 3]);
    float a = fadd_(s[4 * k], s[4 * k + 1]), b = fadd_(s[4 * k + 2], s[4 * k + 3]);
    asm volatile("s_nop 1" : "+v"(a), "+v"(b));
    swap32(a, b); z[k] = fadd_(a, b);
  }
#define DPPADD(C) asm volatile("s_nop 1\n\tv_add_f32_dpp %0, %0, %0 " C " row_mask:0xf bank_mask:0xf\n\tv_add_f32_dpp %1, %1, %1 " C " row_mask:0xf bank_mask:0xf\n\t" \
    "v_add_f32_dpp %2, %2, %2 " C " row_mask:0xf bank_mask:0xf\n\tv_add_f32_dpp %3, %3, %3 " C " row_mask:0xf bank_mask:0xf" : "+v"(z[0]), "+v"(z[1]), "+v"(z[2]), "+v"(z[3]))
  DPPADD("quad_perm:[2,3,0,1]"); DPPADD("row_ror:4"); DPPADD("row_ror:8");
  float rs[8], nb[8];
#pragma unroll
  for (int k = 0; k < 2; k++) {
    const float mean = fmul_(z[k], 1.f / D), var = fmaxf(fnma_(mean, mean, fmul_(z[2 + k], 1.f / D)), 0.f);
    const float mb = rbf(mean), sd = rbf(__builtin_amdgcn_sqrtf(rbf(rbf(var) + 1e-5f)));
    float r = __builtin_amdgcn_rcpf(sd);
    asm volatile("s_nop 0" : "+v"(r));
    r = ffma_(fnma_(sd, r, 1.f), r, r);
    if ((lane & 15) < 2) __builtin_amdgcn_raw_buffer_store_b64(v2u{__float_as_uint(mb), __float_as_uint(r)}, rw, WS_STAT + (row0 + 32 * h + 4 * (4 * k + (lane >> 4))) * 8, 0, AUX_SC1);
    float t[2] = {r, fmul_(-mb, r)};
#pragma unroll
    for (int w = 0; w < 2; w++) {
      float a = t[w], c = t[w];
      asm volatile("s_nop 1" : "+v"(a), "+v"(c));
      swap32(a, c); float b = a, d = c; swap16(a, b); swap16(c, d);
      float* o = w ? nb : rs; o[4 * k] = a; o[4 * k + 1] = b; o[4 * k + 2] = c; o[4 * k + 3] = d;
    }
  }
#pragma unroll
  for (int i = 0; i < 5; i++)
#pragma unroll
    for (int c = 0; c < 4; c++) {
      float y[8];
#pragma unroll
      for (int e = 0; e < 8; e++) y[e] = ffma_(xf[e][4 * i + c], rs[e], nb[e]);
      stc128(v4u{pkhf(y[0], y[1]), pkhf(y[2], y[3]), pkhf(y[4], y[5]), pkhf(y[6], y[7])}, rw, WS_XT + lane * 16, pn * 20480 + (i * 4 + c) * 1024);
    }
  wait0(sb + LD_CTL + C_FULL + 8);
  const u32 tag = mktag(rfl(ldsv(sb + LD_CTL + C_TAG)));
  waitv<0>();
  if (lane < 8) stc32(tag, rw, WS_LFLAG + lane * 2048 + pn * 4);
}

template <bool CJ> DEV void grp8(u32* v, u32 a, u32 b, u32 c) {
#define HM(x, y) (CJ ? hmulc(x, y) : hmul(x, y))
  v[1] = HM(v[0], a); v[2] = HM(v[0], b); v[3] = HM(v[1], b); v[4] = HM(v[0], c); v[5] = HM(v[1], c); v[6] = HM(v[2], c); v[7] = HM(v[3], c);
#undef HM
}
DEV void wr4(u32 a, const u32* v) {
  asm volatile("ds_write2_b32 %0, %1, %2 offset1:68\n\tds_write2_b32 %0, %3, %4 offset0:136 offset1:204" ::"v"(a), "v"(v[0]), "v"(v[1]), "v"(v[2]), "v"(v[3]) : "memory");
}
DEV void rd4(v4u (&f)[4], u32 a) {
  asm volatile("ds_read_b128 %0, %4\n\tds_read_b128 %1, %4 offset:64\n\tds_read_b128 %2, %4 offset:128\n\tds_read_b128 %3, %4 offset:192"
               : "=&v"(f[0]), "=&v"(f[1]), "=&v"(f[2]), "=&v"(f[3]) : "v"(a) : "memory");
}
template <int N> DEV void wait4(v4u (&f)[4], v4u (&h)[4]) {
  asm volatile("s_waitcnt lgkmcnt(%8)" : "+v"(f[0]), "+v"(f[1]), "+v"(f[2]), "+v"(f[3]), "+v"(h[0]), "+v"(h[1]), "+v"(h[2]), "+v"(h[3]) : "i"(N));
}
DEV void conv_wave(const Params& p, u32 sb, int slot, int ch, int lane, int wv, bool g1, int ts, int cj ST_ARG) {
  const u32 UB = sb + LD_K + slot * K_BYTES, KB = sb + LD_R + 2 * WSLOT + slot * 4608, CTL = sb + LD_CTL;
  const int g = lane >> 4, q = (lane >> 2) & 3, pp = lane & 3, j = lane & 15, b = blockIdx.x;
  const rsrc_t rw = mkr(p.ws, WS_BYTES);
  if (AUX_STAGE >= 0) wait0(CTL + C_FULL + AUX_STAGE * 32);
  __builtin_amdgcn_s_setprio(3);
  const bool retile = !((b >> 3) & 1);
  const int wu = ((b >> 4) * 8 + (b & 7)) * 3 + slot;
  if (retile) {
    u32 vo[5];
#pragma unroll
    for (int k = 0; k < 5; k++) { const int n = k * 64 + lane; vo[k] = (n / 40) * 1280 + (n % 40) * 16 - (k & 3) * 1024; }
    const v4u rd = mkrs(p.wd, F * D * 2);
    const u32 so = (wu >> 1) * 10240 + (wu & 1) * 640;
    dma4(rd, vo[0], vo[1], vo[2], vo[3], so, UB); dma1(rd, vo[4], so, UB + 4096);
  }
  u32 la[3], ai[3], cr[3], ci[3];
#pragma unroll
  for (int c = 0; c < 3; c++) { const int o = ch * NS + 64 * c + lane; la[c] = p.lar[o]; ai[c] = p.aim[o]; cr[c] = p.cre[o]; ci[c] = p.cim[o]; }
  const u32 ldt = p.ldt[ch];
#pragma unroll
  for (int c = 0; c < 3; c++) asm volatile("" : "+v"(la[c]), "+v"(ai[c]), "+v"(cr[c]), "+v"(ci[c]));
  if (retile) {
    waitv<0>();
    __builtin_amdgcn_s_setprio(3);
    const u32 ra = UB + q * 640 + (80 * g + 20 * pp) * 2;
    const u32 wo = WS_WDF + ((4 * (wu & 1) + g) * 48 * 5) * 1024 + j * 16, s2 = (wu >> 3) * 5120 + ((wu >> 1) & 3) * 256;
#pragma unroll
    for (int k = 0; k < 5; k++) stc128(trd2(ra + k * 8, ra + k * 8 + 2560), rw, wo + k * 1024, s2);
  }
  __builtin_amdgcn_s_setprio(0);
  u32 tag = 0;
  const float dt = rbf(__expf(bf2f(ldt)));
  u32 mx = 0;
#pragma unroll
  for (int c = 0; c < 3; c++) mx = max(mx, max(cr[c] & 0x7fffu, ci[c] & 0x7fffu));
  mx = max(mx, (u32)__builtin_amdgcn_update_dpp(0, (int)mx, 0x111, 0xf, 0xf, false));
  mx = max(mx, (u32)__builtin_amdgcn_update_dpp(0, (int)mx, 0x112, 0xf, 0xf, false));
  mx = max(mx, (u32)__builtin_amdgcn_update_dpp(0, (int)mx, 0x114, 0xf, 0xf, false));
  mx = max(mx, (u32)__builtin_amdgcn_update_dpp(0, (int)mx, 0x118, 0xf, 0xf, false));
  mx = max(max((u32)__builtin_amdgcn_readlane(mx, 15), (u32)__builtin_amdgcn_readlane(mx, 31)), max((u32)__builtin_amdgcn_readlane(mx, 47), (u32)__builtin_amdgcn_readlane(mx, 63)));
  STAMP(16);
  const u32 ex = min(max(mx >> 7, 1u), 252u);
  const float cscale = __uint_as_float((253u - ex) << 23);
  const float oscale = 2.f * dt * __uint_as_float((ex + 1u) << 23);
  u32 G[3][57];
#pragma unroll
  for (int c = 0; c < 3; c++) {
    const float zr = fmul_(fmul_(rbf(__expf(bf2f(la[c]))), dt), -1.44269504089f), zi = fmul_(fmul_(bf2f(ai[c]), dt), 0.15915494309f);
    const cf P0 = cexp2(zr, zi), P4 = cexp2(fmul_(zr, 16.f), fmul_(zi, 16.f)), P8 = cexp2(fmul_(zr, 256.f), fmul_(zi, 256.f));
    const cf P1 = csq(P0), P2 = csq(P1), P3 = csq(P2), P5 = csq(P4), P6 = csq(P5), P7 = csq(P6), P9 = csq(P8), P10 = csq(P9), P11 = csq(P10);
    const u32 M0 = pkc(P0), M1 = pkc(P1), M2 = pkc(P2), M3 = pkc(P3), M4 = pkc(P4), M5 = pkc(P5), M7 = pkc(P7), M8 = pkc(P8), M9 = pkc(P9);
    u32* v = G[c];
    const float cx = fmul_(bf2f(cr[c]), cscale), cy = fmul_(bf2f(ci[c]), -cscale);
    v[0] = pkhf(cx, cy); grp8<true>(v, M3, M4, M5);
    v[8] = pkhf(ffma_(cy, P6.y, fmul_(cx, P6.x)), fnma_(cx, P6.y, fmul_(cy, P6.x))); grp8<true>(v + 8, M3, M4, M5);
    v[16] = 0x00003c00u; v[17] = M0; v[18] = M1; v[19] = hmul(M0, M1); v[20] = M2; v[21] = hmul(M0, M2); v[22] = hmul(M1, M2); v[23] = hmul(v[19], M2);
    v[24] = M7; grp8<false>(v + 24, M0, M1, M2);
    v[32] = M8; grp8<false>(v + 32, M1, M2, M7);
    v[40] = M9; grp8<false>(v + 40, M2, M7, M8);
    v[48] = pkc(P10); grp8<false>(v + 48, M7, M8, M9);
    v[56] = pkc(P11);
#pragma unroll
    for (int k = 0; k < 57; k++) asm volatile("" : "+v"(v[k]));
    if (c == 0) {
      wait0(CTL + C_FULL + 8);
      tag = mktag(rfl(ldsv(CTL + C_TAG)));
      if (retile) { waitv<0>(); if (lane < 8) stc32(tag, rw, WS_WFLAG + lane * 2048 + wu * 4); }
    }
  }
  STAMP(22);
  __builtin_amdgcn_s_setprio(KL_PRIO);
  wait0(CTL + C_FULL + 9 * 32);
  STAMP(17);
  f4 kd[3] = {f4{0, 0, 0, 0}, f4{0, 0, 0, 0}, f4{0, 0, 0, 0}};
  {
    const u32 rb = KB + j * 272 + g * 16;
    u32 wa[4] = {KB + lane * 4, KB + lane * 4 + 1088, KB + lane * 4 + 2176, KB + lane * 4 + 3264};
    asm volatile("" : "+v"(kd[0]), "+v"(kd[1]), "+v"(kd[2]), "+v"(wa[0]), "+v"(wa[1]), "+v"(wa[2]), "+v"(wa[3]));
    v4u Fr[12][4];
#pragma unroll
    for (int ps = 0; ps <= 12; ps++) {
      if (ps < 12) {
        const int c = ps >> 2, t = ps & 3;
        wr4(wa[0], G[c] + 16 * t); wr4(wa[1], G[c] + 16 * t + 4);
        if (t < 3) { wr4(wa[2], G[c] + 16 * t + 8); wr4(wa[3], G[c] + 16 * t + 12); }
        else asm volatile("ds_write_b32 %0, %1" ::"v"(wa[2]), "v"(G[c][56]) : "memory");
        asm volatile("ds_read_b128 %0, %4\n\tds_read_b128 %1, %4 offset:64\n\tds_read_b128 %2, %4 offset:128\n\tds_read_b128 %3, %4 offset:192"
                     : "=&v"(Fr[ps][0]), "=&v"(Fr[ps][1]), "=&v"(Fr[ps][2]), "=&v"(Fr[ps][3]) : "v"(rb) : "memory");
      }
      if (ps > 0) {
        const int pv = ps - 1;
        if (ps < 12) asm volatile("s_waitcnt lgkmcnt(9)" : "+v"(Fr[pv][0]), "+v"(Fr[pv][1]), "+v"(Fr[pv][2]), "+v"(Fr[pv][3]));
        else asm volatile("s_waitcnt lgkmcnt(0)" : "+v"(Fr[pv][0]), "+v"(Fr[pv][1]), "+v"(Fr[pv][2]), "+v"(Fr[pv][3]));
        if (pv & 3) {
#pragma unroll
          for (int ks = 0; ks < 4; ks++) mma_hf(kd[(pv & 3) - 1], Fr[pv][ks], Fr[pv & ~3][ks]);
        }
      }
    }
    asm volatile("s_nop 7" : "+v"(kd[0]), "+v"(kd[1]), "+v"(kd[2]));
  }
  STAMP(9);
  {
    const u32 K0 = KB + 32;
    const u32 a00 = pkhf(kd[1][0], kd[1][0]), a11 = pkhf(kd[1][1], kd[1][1]), a22 = pkhf(kd[1][2], kd[1][2]), a33 = pkhf(kd[1][3], kd[1][3]);
    const u32 a12 = pkhf(kd[1][1], kd[1][2]), a01 = pkhf(kd[1][0], kd[1][1]), a23 = pkhf(kd[1][2], kd[1][3]);
    v2u bb[4];
#pragma unroll
    for (int r = 0; r < 4; r++) { bb[r].x = pkhf(kd[2][r], kd[2][r]); bb[r].y = bb[r].x; }
    const v2u r0 = {pkhf(kd[0][0], kd[0][1]), pkhf(kd[0][2], kd[0][3])}, w1 = {a00, a12}, w2a = {a00, a01}, w2b = {a22, a23};
    const v4u z4 = {0, 0, 0, 0};
    const u32 p3 = K0 + 2 * (1024 + 512 * g + 8 * j) - 8, p2 = K0 + 2 * (256 * g + 8 * j) - 4, p1 = K0 + 2 * (256 + 128 * g + 8 * j) - 2;
    const u32 p0 = K0 + 2 * (128 * (g >> 1) + 8 * j + 4 * (g & 1)), pz = KB + lane * 16;
    asm volatile(
      "s_mov_b32 exec_hi, 1\n\t"
      "ds_write2_b64 %0, %5, %5 offset1:1\n\t"
      "s_mov_b32 exec_hi, 0\n\t"
      "ds_write2_b64 %0, %6, %6 offset0:32 offset1:33\n\t"
      "ds_write2_b64 %0, %7, %7 offset0:64 offset1:65\n\t"
      "ds_write2_b64 %0, %8, %8 offset0:96 offset1:97\n\t"
      "s_mov_b32 exec_lo, 0x55555555\n\t"
      "ds_write_b16 %0, %9 offset:16\n\tds_write_b16 %0, %10 offset:272\n\tds_write_b16 %0, %11 offset:528\n\tds_write_b16 %0, %12 offset:784\n\t"
      "s_mov_b32 exec_lo, 0\n\ts_mov_b32 exec_hi, -1\n\t"
      "ds_write_b32 %1, %13\n\tds_write_b64 %1, %17 offset:4\n\tds_write_b32 %1, %14 offset:12\n\t"
      "ds_write_b32 %1, %15 offset:256\n\tds_write_b64 %1, %18 offset:260\n\tds_write_b32 %1, %16 offset:268\n\t"
      "s_mov_b32 exec_lo, -1\n\ts_mov_b32 exec_hi, 0\n\t"
      "ds_write_b16 %2, %13\n\tds_write_b64 %2, %19 offset:2\n\tds_write_b32 %2, %15 offset:10\n\tds_write_b16 %2, %16 offset:14\n\t"
      "s_mov_b32 exec_lo, 3\n\t"
      "ds_write_b128 %4, %21\n\t"
      "s_mov_b64 exec, -1\n\t"
      "ds_write_b64 %3, %20"
      ::"v"(p3), "v"(p2), "v"(p1), "v"(p0), "v"(pz), "v"(bb[0]), "v"(bb[1]), "v"(bb[2]), "v"(bb[3]), "v"(bb[0].x), "v"(bb[1].x), "v"(bb[2].x), "v"(bb[3].x),
        "v"(a00), "v"(a11), "v"(a22), "v"(a33), "v"(w2a), "v"(w2b), "v"(w1), "v"(r0), "v"(z4) : "memory");
  }
  v4u A[16];
  {
    const u32 ab = KB + 32 + 2 * (-12 + 4 * (8 * g + 7 - q) + 4 * pp);
#pragma unroll
    for (int s = 0; s < 16; s++) A[s] = trd2(ab + 256 * s, ab + 256 * s - 32);
  }
  ldsw(CTL + C_KDONE + slot * 4, 0);
  STAMP(13);
  wait0(CTL + C_UFULL);
  wait0(CTL + C_DONE + 160 + slot * 4);
  __builtin_amdgcn_s_setprio(CONV_PRIO);
  STAMP(18);
  f4 acc[4][8]; v4u B[9]; float y[4], c[2], cv[3], pc[3] = {0.f, 0.f, 0.f};
  const u32 ub = UB + 128 + 2 * (4 * j - 4 - 8 * g) - 64, srcl = (lane == 0 ? 63 : (g > 0 ? lane - 16 : lane + 47)) * 4;
  const u32 zo = WS_ZT + (g & 1) * 512 + j * 32 + (g >> 1) * 16;
  const u64 m63 = 1ull << 63;
  u32 ut[4] = {ub, ub + 1152, ub + 2304, ub + 3456};
  asm volatile("" : "+v"(ut[0]), "+v"(ut[1]), "+v"(ut[2]), "+v"(ut[3]));
#define LDB(k, tau, q) do { const u32 a_ = ut[tau] + 64 * (q); const v2u x_ = lds64(a_), y_ = lds64(a_ + 8); B[k] = v4u{x_.x, x_.y, y_.x, y_.y}; SCHED(); } while (0)
#define VADD(d, a_, b_) do { float t_; asm volatile("v_add_f32 %0, %1, %2" : "=v"(t_) : "v"(a_), "v"(b_)); d = t_; } while (0)
#define VMULS(d) asm volatile("v_mul_f32 %0, %0, %1" : "+v"(d) : "v"(oscale))
#define VSEL(d, a_, b_) asm volatile("v_cndmask_b32 %0, %1, %2, %3" : "=v"(d) : "v"(a_), "v"(b_), "s"(m63))
#define VPK(d, a_, b_) do { u32 t_; asm volatile("v_cvt_pk_bf16_f32 %0, %1, %2" : "=v"(t_) : "v"(a_), "v"(b_)); d = t_; } while (0)
#define NOPS() asm volatile("s_nop 3")
#define BPERM() asm volatile("ds_bpermute_b32 %0, %3, %0\n\tds_bpermute_b32 %1, %3, %1\n\tds_bpermute_b32 %2, %3, %2" : "+v"(cv[0]), "+v"(cv[1]), "+v"(cv[2]) : "v"(srcl) : "memory")
#define WAITB(n) asm volatile("s_waitcnt lgkmcnt(" #n ")" : "+v"(cv[0]), "+v"(cv[1]), "+v"(cv[2]) :: "memory")
#define ZSTORE(T) do { asm volatile("s_nop 1\n\tv_permlane16_swap_b32 %0, %2\n\tv_permlane16_swap_b32 %1, %3" : "+v"(zx0), "+v"(zy0), "+v"(zx1), "+v"(zy1)); \
    if (!ABL_Z) stc128(v4u{zx0, zy0, zx1, zy1}, rw, zo, ch * 4096 + (T) * 512); } while (0)
  u32 zx0, zy0, zx1, zy1;
  LDB(0, 0, 0);
  LDB(1, 1, 0);
  LDB(2, 2, 0);
  LDB(3, 3, 0);
  LDB(4, 0, 1);
  LDB(5, 1, 1);
  LDB(6, 2, 1);
  LDB(7, 3, 1);
  LDB(8, 0, 2);
  mma_hf0(acc[0][7], A[15], B[0]);
  mma_hf0(acc[0][6], A[13], B[0]);
  mma_hf0(acc[0][5], A[11], B[0]);
  mma_hf0(acc[0][4], A[9], B[0]);
  mma_hf0(acc[0][3], A[7], B[0]);
  mma_hf0(acc[0][2], A[5], B[0]);
  mma_hf0(acc[0][1], A[3], B[0]);
  mma_hf0(acc[0][0], A[1], B[0]);
  LDB(0, 1, 2);
  mma_hf0(acc[1][7], A[15], B[1]);
  mma_hf0(acc[1][6], A[13], B[1]);
  mma_hf0(acc[1][5], A[11], B[1]);
  mma_hf0(acc[1][4], A[9], B[1]);
  mma_hf0(acc[1][3], A[7], B[1]);
  mma_hf0(acc[1][2], A[5], B[1]);
  mma_hf0(acc[1][1], A[3], B[1]);
  mma_hf0(acc[1][0], A[1], B[1]);
  LDB(1, 2, 2);
  mma_hf0(acc[2][7], A[15], B[2]);
  mma_hf0(acc[2][6], A[13], B[2]);
  mma_hf0(acc[2][5], A[11], B[2]);
  mma_hf0(acc[2][4], A[9], B[2]);
  mma_hf0(acc[2][3], A[7], B[2]);
  mma_hf0(acc[2][2], A[5], B[2]);
  mma_hf0(acc[2][1], A[3], B[2]);
  mma_hf0(acc[2][0], A[1], B[2]);
  LDB(2, 3, 2);
  mma_hf0(acc[3][7], A[15], B[3]);
  mma_hf0(acc[3][6], A[13], B[3]);
  mma_hf0(acc[3][5], A[11], B[3]);
  mma_hf0(acc[3][4], A[9], B[3]);
  mma_hf0(acc[3][3], A[7], B[3]);
  mma_hf0(acc[3][2], A[5], B[3]);
  mma_hf0(acc[3][1], A[3], B[3]);
  mma_hf0(acc[3][0], A[1], B[3]);
  LDB(3, 0, 3);
  mma_hf(acc[0][7], A[14], B[4]);
  mma_hf(acc[0][6], A[12], B[4]);
  mma_hf(acc[0][5], A[10], B[4]);
  mma_hf(acc[0][4], A[8], B[4]);
  mma_hf(acc[0][3], A[6], B[4]);
  mma_hf(acc[0][2], A[4], B[4]);
  mma_hf(acc[0][1], A[2], B[4]);
  mma_hf(acc[0][0], A[0], B[4]);
  LDB(4, 1, 3);
  mma_hf(acc[1][7], A[14], B[5]);
  mma_hf(acc[1][6], A[12], B[5]);
  mma_hf(acc[1][5], A[10], B[5]);
  mma_hf(acc[1][4], A[8], B[5]);
  mma_hf(acc[1][3], A[6], B[5]);
  mma_hf(acc[1][2], A[4], B[5]);
  mma_hf(acc[1][1], A[2], B[5]);
  mma_hf(acc[1][0], A[0], B[5]);
  LDB(5, 2, 3);
  mma_hf(acc[2][7], A[14], B[6]);
  mma_hf(acc[2][6], A[12], B[6]);
  mma_hf(acc[2][5], A[10], B[6]);
  mma_hf(acc[2][4], A[8], B[6]);
  mma_hf(acc[2][3], A[6], B[6]);
  mma_hf(acc[2][2], A[4], B[6]);
  mma_hf(acc[2][1], A[2], B[6]);
  mma_hf(acc[2][0], A[0], B[6]);
  LDB(6, 3, 3);
  mma_hf(acc[3][7], A[14], B[7]);
  mma_hf(acc[3][6], A[12], B[7]);
  mma_hf(acc[3][5], A[10], B[7]);
  mma_hf(acc[3][4], A[8], B[7]);
  mma_hf(acc[3][3], A[6], B[7]);
  mma_hf(acc[3][2], A[4], B[7]);
  mma_hf(acc[3][1], A[2], B[7]);
  mma_hf(acc[3][0], A[0], B[7]);
  LDB(7, 0, 4);
  mma_hf(acc[0][7], A[13], B[8]);
  VADD(y[1], acc[0][0][1], acc[1][0][0]);
  VADD(y[2], acc[0][0][2], acc[1][0][1]);
  mma_hf(acc[0][6], A[11], B[8]);
  VADD(y[3], acc[0][0][3], acc[1][0][2]);
  VADD(c[0], acc[1][0][3], acc[2][0][2]);
  mma_hf(acc[0][5], A[9], B[8]);
  VADD(y[2], y[2], acc[2][0][0]);
  VADD(y[3], y[3], acc[2][0][1]);
  mma_hf(acc[0][4], A[7], B[8]);
  NOPS();
  VADD(c[0], c[0], acc[3][0][1]);
  mma_hf(acc[0][3], A[5], B[8]);
  VADD(c[1], acc[2][0][3], acc[3][0][2]);
  VADD(y[3], y[3], acc[3][0][0]);
  mma_hf(acc[0][2], A[3], B[8]);
  VSEL(cv[0], c[0], pc[0]);
  mma_hf(acc[0][1], A[1], B[8]);
  VSEL(cv[1], c[1], pc[1]);
  LDB(8, 1, 4);
  mma_hf(acc[1][7], A[13], B[0]);
  VSEL(cv[2], acc[3][0][3], pc[2]);
  mma_hf(acc[1][6], A[11], B[0]);
  BPERM(); pc[0] = c[0]; pc[1] = c[1]; pc[2] = acc[3][0][3];
  mma_hf(acc[1][5], A[9], B[0]);
  VMULS(y[3]);
  mma_hf(acc[1][4], A[7], B[0]);
  mma_hf(acc[1][3], A[5], B[0]);
  mma_hf(acc[1][2], A[3], B[0]);
  mma_hf(acc[1][1], A[1], B[0]);
  LDB(0, 2, 4);
  mma_hf(acc[2][7], A[13], B[1]);
  mma_hf(acc[2][6], A[11], B[1]);
  mma_hf(acc[2][5], A[9], B[1]);
  WAITB(1);
  VADD(y[0], acc[0][0][0], cv[0]);
  mma_hf(acc[2][4], A[7], B[1]);
  VADD(y[1], y[1], cv[1]);
  mma_hf(acc[2][3], A[5], B[1]);
  VADD(y[2], y[2], cv[2]);
  mma_hf(acc[2][2], A[3], B[1]);
  VMULS(y[0]);
  mma_hf(acc[2][1], A[1], B[1]);
  VMULS(y[1]);
  LDB(1, 3, 4);
  mma_hf(acc[3][7], A[13], B[2]);
  VMULS(y[2]);
  mma_hf(acc[3][6], A[11], B[2]);
  VPK(zx0, y[0], y[1]);
  mma_hf(acc[3][5], A[9], B[2]);
  VPK(zy0, y[2], y[3]);
  mma_hf(acc[3][4], A[7], B[2]);
  mma_hf(acc[3][3], A[5], B[2]);
  mma_hf(acc[3][2], A[3], B[2]);
  mma_hf(acc[3][1], A[1], B[2]);
  LDB(2, 0, 5);
  mma_hf(acc[0][7], A[12], B[3]);
  mma_hf(acc[0][6], A[10], B[3]);
  mma_hf(acc[0][5], A[8], B[3]);
  mma_hf(acc[0][4], A[6], B[3]);
  mma_hf(acc[0][3], A[4], B[3]);
  mma_hf(acc[0][2], A[2], B[3]);
  mma_hf(acc[0][1], A[0], B[3]);
  LDB(3, 1, 5);
  mma_hf(acc[1][7], A[12], B[4]);
  mma_hf(acc[1][6], A[10], B[4]);
  mma_hf(acc[1][5], A[8], B[4]);
  mma_hf(acc[1][4], A[6], B[4]);
  mma_hf(acc[1][3], A[4], B[4]);
  mma_hf(acc[1][2], A[2], B[4]);
  mma_hf(acc[1][1], A[0], B[4]);
  LDB(4, 2, 5);
  mma_hf(acc[2][7], A[12], B[5]);
  mma_hf(acc[2][6], A[10], B[5]);
  mma_hf(acc[2][5], A[8], B[5]);
  mma_hf(acc[2][4], A[6], B[5]);
  mma_hf(acc[2][3], A[4], B[5]);
  mma_hf(acc[2][2], A[2], B[5]);
  mma_hf(acc[2][1], A[0], B[5]);
  LDB(5, 3, 5);
  mma_hf(acc[3][7], A[12], B[6]);
  mma_hf(acc[3][6], A[10], B[6]);
  mma_hf(acc[3][5], A[8], B[6]);
  mma_hf(acc[3][4], A[6], B[6]);
  mma_hf(acc[3][3], A[4], B[6]);
  mma_hf(acc[3][2], A[2], B[6]);
  mma_hf(acc[3][1], A[0], B[6]);
  LDB(6, 0, 6);
  mma_hf(acc[0][7], A[11], B[7]);
  VADD(y[1], acc[0][1][1], acc[1][1][0]);
  VADD(y[2], acc[0][1][2], acc[1][1][1]);
  mma_hf(acc[0][6], A[9], B[7]);
  VADD(y[3], acc[0][1][3], acc[1][1][2]);
  VADD(c[0], acc[1][1][3], acc[2][1][2]);
  mma_hf(acc[0][5], A[7], B[7]);
  VADD(y[2], y[2], acc[2][1][0]);
  VADD(y[3], y[3], acc[2][1][1]);
  mma_hf(acc[0][4], A[5], B[7]);
  NOPS();
  VADD(c[0], c[0], acc[3][1][1]);
  mma_hf(acc[0][3], A[3], B[7]);
  VADD(c[1], acc[2][1][3], acc[3][1][2]);
  VADD(y[3], y[3], acc[3][1][0]);
  mma_hf(acc[0][2], A[1], B[7]);
  VSEL(cv[0], c[0], pc[0]);
  VSEL(cv[1], c[1], pc[1]);
  LDB(7, 1, 6);
  mma_hf(acc[1][7], A[11], B[8]);
  VSEL(cv[2], acc[3][1][3], pc[2]);
  mma_hf(acc[1][6], A[9], B[8]);
  BPERM(); pc[0] = c[0]; pc[1] = c[1]; pc[2] = acc[3][1][3];
  mma_hf(acc[1][5], A[7], B[8]);
  VMULS(y[3]);
  mma_hf(acc[1][4], A[5], B[8]);
  mma_hf(acc[1][3], A[3], B[8]);
  mma_hf(acc[1][2], A[1], B[8]);
  LDB(8, 2, 6);
  mma_hf(acc[2][7], A[11], B[0]);
  mma_hf(acc[2][6], A[9], B[0]);
  mma_hf(acc[2][5], A[7], B[0]);
  mma_hf(acc[2][4], A[5], B[0]);
  WAITB(1);
  VADD(y[0], acc[0][1][0], cv[0]);
  mma_hf(acc[2][3], A[3], B[0]);
  VADD(y[1], y[1], cv[1]);
  mma_hf(acc[2][2], A[1], B[0]);
  VADD(y[2], y[2], cv[2]);
  LDB(0, 3, 6);
  mma_hf(acc[3][7], A[11], B[1]);
  VMULS(y[0]);
  mma_hf(acc[3][6], A[9], B[1]);
  VMULS(y[1]);
  mma_hf(acc[3][5], A[7], B[1]);
  VMULS(y[2]);
  mma_hf(acc[3][4], A[5], B[1]);
  VPK(zx1, y[0], y[1]);
  mma_hf(acc[3][3], A[3], B[1]);
  VPK(zy1, y[2], y[3]);
  mma_hf(acc[3][2], A[1], B[1]);
  ZSTORE(0);
  LDB(1, 0, 7);
  mma_hf(acc[0][7], A[10], B[2]);
  mma_hf(acc[0][6], A[8], B[2]);
  mma_hf(acc[0][5], A[6], B[2]);
  mma_hf(acc[0][4], A[4], B[2]);
  mma_hf(acc[0][3], A[2], B[2]);
  mma_hf(acc[0][2], A[0], B[2]);
  LDB(2, 1, 7);
  mma_hf(acc[1][7], A[10], B[3]);
  mma_hf(acc[1][6], A[8], B[3]);
  mma_hf(acc[1][5], A[6], B[3]);
  mma_hf(acc[1][4], A[4], B[3]);
  mma_hf(acc[1][3], A[2], B[3]);
  mma_hf(acc[1][2], A[0], B[3]);
  LDB(3, 2, 7);
  mma_hf(acc[2][7], A[10], B[4]);
  mma_hf(acc[2][6], A[8], B[4]);
  mma_hf(acc[2][5], A[6], B[4]);
  mma_hf(acc[2][4], A[4], B[4]);
  mma_hf(acc[2][3], A[2], B[4]);
  mma_hf(acc[2][2], A[0], B[4]);
  LDB(4, 3, 7);
  mma_hf(acc[3][7], A[10], B[5]);
  mma_hf(acc[3][6], A[8], B[5]);
  mma_hf(acc[3][5], A[6], B[5]);
  mma_hf(acc[3][4], A[4], B[5]);
  mma_hf(acc[3][3], A[2], B[5]);
  mma_hf(acc[3][2], A[0], B[5]);
  LDB(5, 0, 8);
  mma_hf(acc[0][7], A[9], B[6]);
  VADD(y[1], acc[0][2][1], acc[1][2][0]);
  VADD(y[2], acc[0][2][2], acc[1][2][1]);
  mma_hf(acc[0][6], A[7], B[6]);
  VADD(y[3], acc[0][2][3], acc[1][2][2]);
  VADD(c[0], acc[1][2][3], acc[2][2][2]);
  mma_hf(acc[0][5], A[5], B[6]);
  VADD(y[2], y[2], acc[2][2][0]);
  VADD(y[3], y[3], acc[2][2][1]);
  mma_hf(acc[0][4], A[3], B[6]);
  NOPS();
  VADD(c[0], c[0], acc[3][2][1]);
  mma_hf(acc[0][3], A[1], B[6]);
  VADD(c[1], acc[2][2][3], acc[3][2][2]);
  VADD(y[3], y[3], acc[3][2][0]);
  LDB(6, 1, 8);
  mma_hf(acc[1][7], A[9], B[7]);
  VSEL(cv[0], c[0], pc[0]);
  mma_hf(acc[1][6], A[7], B[7]);
  VSEL(cv[1], c[1], pc[1]);
  mma_hf(acc[1][5], A[5], B[7]);
  VSEL(cv[2], acc[3][2][3], pc[2]);
  mma_hf(acc[1][4], A[3], B[7]);
  BPERM(); pc[0] = c[0]; pc[1] = c[1]; pc[2] = acc[3][2][3];
  mma_hf(acc[1][3], A[1], B[7]);
  VMULS(y[3]);
  LDB(7, 2, 8);
  mma_hf(acc[2][7], A[9], B[8]);
  mma_hf(acc[2][6], A[7], B[8]);
  mma_hf(acc[2][5], A[5], B[8]);
  mma_hf(acc[2][4], A[3], B[8]);
  mma_hf(acc[2][3], A[1], B[8]);
  LDB(8, 3, 8);
  mma_hf(acc[3][7], A[9], B[0]);
  mma_hf(acc[3][6], A[7], B[0]);
  WAITB(2);
  VADD(y[0], acc[0][2][0], cv[0]);
  mma_hf(acc[3][5], A[5], B[0]);
  VADD(y[1], y[1], cv[1]);
  mma_hf(acc[3][4], A[3], B[0]);
  VADD(y[2], y[2], cv[2]);
  mma_hf(acc[3][3], A[1], B[0]);
  VMULS(y[0]);
  LDB(0, 0, 9);
  mma_hf(acc[0][7], A[8], B[1]);
  VMULS(y[1]);
  mma_hf(acc[0][6], A[6], B[1]);
  VMULS(y[2]);
  mma_hf(acc[0][5], A[4], B[1]);
  VPK(zx0, y[0], y[1]);
  mma_hf(acc[0][4], A[2], B[1]);
  VPK(zy0, y[2], y[3]);
  mma_hf(acc[0][3], A[0], B[1]);
  LDB(1, 1, 9);
  mma_hf(acc[1][7], A[8], B[2]);
  mma_hf(acc[1][6], A[6], B[2]);
  mma_hf(acc[1][5], A[4], B[2]);
  mma_hf(acc[1][4], A[2], B[2]);
  mma_hf(acc[1][3], A[0], B[2]);
  LDB(2, 2, 9);
  mma_hf(acc[2][7], A[8], B[3]);
  mma_hf(acc[2][6], A[6], B[3]);
  mma_hf(acc[2][5], A[4], B[3]);
  mma_hf(acc[2][4], A[2], B[3]);
  mma_hf(acc[2][3], A[0], B[3]);
  LDB(3, 3, 9);
  mma_hf(acc[3][7], A[8], B[4]);
  mma_hf(acc[3][6], A[6], B[4]);
  mma_hf(acc[3][5], A[4], B[4]);
  mma_hf(acc[3][4], A[2], B[4]);
  mma_hf(acc[3][3], A[0], B[4]);
  LDB(4, 0, 10);
  mma_hf(acc[0][7], A[7], B[5]);
  VADD(y[1], acc[0][3][1], acc[1][3][0]);
  VADD(y[2], acc[0][3][2], acc[1][3][1]);
  mma_hf(acc[0][6], A[5], B[5]);
  VADD(y[3], acc[0][3][3], acc[1][3][2]);
  VADD(c[0], acc[1][3][3], acc[2][3][2]);
  mma_hf(acc[0][5], A[3], B[5]);
  VADD(y[2], y[2], acc[2][3][0]);
  VADD(y[3], y[3], acc[2][3][1]);
  mma_hf(acc[0][4], A[1], B[5]);
  NOPS();
  VADD(c[0], c[0], acc[3][3][1]);
  LDB(5, 1, 10);
  mma_hf(acc[1][7], A[7], B[6]);
  VADD(c[1], acc[2][3][3], acc[3][3][2]);
  VADD(y[3], y[3], acc[3][3][0]);
  mma_hf(acc[1][6], A[5], B[6]);
  VSEL(cv[0], c[0], pc[0]);
  VSEL(cv[1], c[1], pc[1]);
  mma_hf(acc[1][5], A[3], B[6]);
  VSEL(cv[2], acc[3][3][3], pc[2]);
  mma_hf(acc[1][4], A[1], B[6]);
  BPERM(); pc[0] = c[0]; pc[1] = c[1]; pc[2] = acc[3][3][3];
  LDB(6, 2, 10);
  mma_hf(acc[2][7], A[7], B[7]);
  VMULS(y[3]);
  mma_hf(acc[2][6], A[5], B[7]);
  mma_hf(acc[2][5], A[3], B[7]);
  mma_hf(acc[2][4], A[1], B[7]);
  LDB(7, 3, 10);
  mma_hf(acc[3][7], A[7], B[8]);
  mma_hf(acc[3][6], A[5], B[8]);
  mma_hf(acc[3][5], A[3], B[8]);
  mma_hf(acc[3][4], A[1], B[8]);
  WAITB(2);
  VADD(y[0], acc[0][3][0], cv[0]);
  LDB(8, 0, 11);
  mma_hf(acc[0][7], A[6], B[0]);
  VADD(y[1], y[1], cv[1]);
  mma_hf(acc[0][6], A[4], B[0]);
  VADD(y[2], y[2], cv[2]);
  mma_hf(acc[0][5], A[2], B[0]);
  VMULS(y[0]);
  mma_hf(acc[0][4], A[0], B[0]);
  VMULS(y[1]);
  LDB(0, 1, 11);
  mma_hf(acc[1][7], A[6], B[1]);
  VMULS(y[2]);
  mma_hf(acc[1][6], A[4], B[1]);
  VPK(zx1, y[0], y[1]);
  mma_hf(acc[1][5], A[2], B[1]);
  VPK(zy1, y[2], y[3]);
  mma_hf(acc[1][4], A[0], B[1]);
  ZSTORE(2);
  LDB(1, 2, 11);
  mma_hf(acc[2][7], A[6], B[2]);
  mma_hf(acc[2][6], A[4], B[2]);
  mma_hf(acc[2][5], A[2], B[2]);
  mma_hf(acc[2][4], A[0], B[2]);
  LDB(2, 3, 11);
  mma_hf(acc[3][7], A[6], B[3]);
  mma_hf(acc[3][6], A[4], B[3]);
  mma_hf(acc[3][5], A[2], B[3]);
  mma_hf(acc[3][4], A[0], B[3]);
  LDB(3, 0, 12);
  mma_hf(acc[0][7], A[5], B[4]);
  VADD(y[1], acc[0][4][1], acc[1][4][0]);
  VADD(y[2], acc[0][4][2], acc[1][4][1]);
  mma_hf(acc[0][6], A[3], B[4]);
  VADD(y[3], acc[0][4][3], acc[1][4][2]);
  VADD(c[0], acc[1][4][3], acc[2][4][2]);
  mma_hf(acc[0][5], A[1], B[4]);
  VADD(y[2], y[2], acc[2][4][0]);
  VADD(y[3], y[3], acc[2][4][1]);
  LDB(4, 1, 12);
  mma_hf(acc[1][7], A[5], B[5]);
  NOPS();
  VADD(c[0], c[0], acc[3][4][1]);
  mma_hf(acc[1][6], A[3], B[5]);
  VADD(c[1], acc[2][4][3], acc[3][4][2]);
  VADD(y[3], y[3], acc[3][4][0]);
  mma_hf(acc[1][5], A[1], B[5]);
  VSEL(cv[0], c[0], pc[0]);
  VSEL(cv[1], c[1], pc[1]);
  LDB(5, 2, 12);
  mma_hf(acc[2][7], A[5], B[6]);
  VSEL(cv[2], acc[3][4][3], pc[2]);
  BPERM(); pc[0] = c[0]; pc[1] = c[1]; pc[2] = acc[3][4][3];
  mma_hf(acc[2][6], A[3], B[6]);
  VMULS(y[3]);
  mma_hf(acc[2][5], A[1], B[6]);
  LDB(6, 3, 12);
  mma_hf(acc[3][7], A[5], B[7]);
  mma_hf(acc[3][6], A[3], B[7]);
  WAITB(1);
  VADD(y[0], acc[0][4][0], cv[0]);
  mma_hf(acc[3][5], A[1], B[7]);
  VADD(y[1], y[1], cv[1]);
  VADD(y[2], y[2], cv[2]);
  LDB(7, 0, 13);
  mma_hf(acc[0][7], A[4], B[8]);
  VMULS(y[0]);
  VMULS(y[1]);
  mma_hf(acc[0][6], A[2], B[8]);
  VMULS(y[2]);
  VPK(zx0, y[0], y[1]);
  mma_hf(acc[0][5], A[0], B[8]);
  VPK(zy0, y[2], y[3]);
  LDB(8, 1, 13);
  mma_hf(acc[1][7], A[4], B[0]);
  mma_hf(acc[1][6], A[2], B[0]);
  mma_hf(acc[1][5], A[0], B[0]);
  LDB(0, 2, 13);
  mma_hf(acc[2][7], A[4], B[1]);
  mma_hf(acc[2][6], A[2], B[1]);
  mma_hf(acc[2][5], A[0], B[1]);
  LDB(1, 3, 13);
  mma_hf(acc[3][7], A[4], B[2]);
  mma_hf(acc[3][6], A[2], B[2]);
  mma_hf(acc[3][5], A[0], B[2]);
  LDB(2, 0, 14);
  mma_hf(acc[0][7], A[3], B[3]);
  VADD(y[1], acc[0][5][1], acc[1][5][0]);
  VADD(y[2], acc[0][5][2], acc[1][5][1]);
  mma_hf(acc[0][6], A[1], B[3]);
  VADD(y[3], acc[0][5][3], acc[1][5][2]);
  VADD(c[0], acc[1][5][3], acc[2][5][2]);
  LDB(3, 1, 14);
  mma_hf(acc[1][7], A[3], B[4]);
  VADD(y[2], y[2], acc[2][5][0]);
  VADD(y[3], y[3], acc[2][5][1]);
  mma_hf(acc[1][6], A[1], B[4]);
  NOPS();
  VADD(c[0], c[0], acc[3][5][1]);
  LDB(4, 2, 14);
  mma_hf(acc[2][7], A[3], B[5]);
  VADD(c[1], acc[2][5][3], acc[3][5][2]);
  VADD(y[3], y[3], acc[3][5][0]);
  mma_hf(acc[2][6], A[1], B[5]);
  VSEL(cv[0], c[0], pc[0]);
  VSEL(cv[1], c[1], pc[1]);
  LDB(5, 3, 14);
  mma_hf(acc[3][7], A[3], B[6]);
  VSEL(cv[2], acc[3][5][3], pc[2]);
  BPERM(); pc[0] = c[0]; pc[1] = c[1]; pc[2] = acc[3][5][3];
  mma_hf(acc[3][6], A[1], B[6]);
  VMULS(y[3]);
  LDB(6, 0, 15);
  mma_hf(acc[0][7], A[2], B[7]);
  mma_hf(acc[0][6], A[0], B[7]);
  LDB(7, 1, 15);
  mma_hf(acc[1][7], A[2], B[8]);
  WAITB(2);
  VADD(y[0], acc[0][5][0], cv[0]);
  mma_hf(acc[1][6], A[0], B[8]);
  VADD(y[1], y[1], cv[1]);
  VADD(y[2], y[2], cv[2]);
  LDB(8, 2, 15);
  mma_hf(acc[2][7], A[2], B[0]);
  VMULS(y[0]);
  VMULS(y[1]);
  mma_hf(acc[2][6], A[0], B[0]);
  VMULS(y[2]);
  VPK(zx1, y[0], y[1]);
  LDB(0, 3, 15);
  mma_hf(acc[3][7], A[2], B[1]);
  VPK(zy1, y[2], y[3]);
  ZSTORE(4);
  mma_hf(acc[3][6], A[0], B[1]);
  mma_hf(acc[0][7], A[1], B[2]);
  VADD(y[1], acc[0][6][1], acc[1][6][0]);
  VADD(y[2], acc[0][6][2], acc[1][6][1]);
  mma_hf(acc[1][7], A[1], B[3]);
  VADD(y[3], acc[0][6][3], acc[1][6][2]);
  VADD(c[0], acc[1][6][3], acc[2][6][2]);
  mma_hf(acc[2][7], A[1], B[4]);
  VADD(y[2], y[2], acc[2][6][0]);
  VADD(y[3], y[3], acc[2][6][1]);
  mma_hf(acc[3][7], A[1], B[5]);
  NOPS();
  VADD(c[0], c[0], acc[3][6][1]);
  mma_hf(acc[0][7], A[0], B[6]);
  VADD(c[1], acc[2][6][3], acc[3][6][2]);
  VADD(y[3], y[3], acc[3][6][0]);
  mma_hf(acc[1][7], A[0], B[7]);
  VSEL(cv[0], c[0], pc[0]);
  VSEL(cv[1], c[1], pc[1]);
  mma_hf(acc[2][7], A[0], B[8]);
  VSEL(cv[2], acc[3][6][3], pc[2]);
  BPERM(); pc[0] = c[0]; pc[1] = c[1]; pc[2] = acc[3][6][3];
  mma_hf(acc[3][7], A[0], B[0]);
  VMULS(y[3]);
  STAMP(19);
  WAITB(0);
  VADD(y[0], acc[0][6][0], cv[0]);
  VADD(y[1], y[1], cv[1]);
  VADD(y[2], y[2], cv[2]);
  VMULS(y[0]);
  VMULS(y[1]);
  VMULS(y[2]);
  VPK(zx0, y[0], y[1]);
  VPK(zy0, y[2], y[3]);
  VADD(y[1], acc[0][7][1], acc[1][7][0]);
  VADD(y[2], acc[0][7][2], acc[1][7][1]);
  VADD(y[3], acc[0][7][3], acc[1][7][2]);
  VADD(c[0], acc[1][7][3], acc[2][7][2]);
  VADD(y[2], y[2], acc[2][7][0]);
  VADD(y[3], y[3], acc[2][7][1]);
  NOPS();
  VADD(c[0], c[0], acc[3][7][1]);
  VADD(c[1], acc[2][7][3], acc[3][7][2]);
  VADD(y[3], y[3], acc[3][7][0]);
  VSEL(cv[0], c[0], pc[0]);
  VSEL(cv[1], c[1], pc[1]);
  VSEL(cv[2], acc[3][7][3], pc[2]);
  BPERM(); pc[0] = c[0]; pc[1] = c[1]; pc[2] = acc[3][7][3];
  VMULS(y[3]);
  WAITB(0);
  VADD(y[0], acc[0][7][0], cv[0]);
  VADD(y[1], y[1], cv[1]);
  VADD(y[2], y[2], cv[2]);
  VMULS(y[0]);
  VMULS(y[1]);
  VMULS(y[2]);
  VPK(zx1, y[0], y[1]);
  VPK(zy1, y[2], y[3]);
  ZSTORE(6);
  STAMP(20);
  waitv<0>();
  ldsw(CTL + C_CDONE + slot * 4, 0);
  STAMP(11);
}

__global__ __launch_bounds__(512, 2) void k_main(Params p) {
  extern __shared__ __attribute__((aligned(16))) char smem[];
  const u32 sb = (u32)(size_t)(LAS char*)smem;
  const int lane = threadIdx.x & 63, wv = rfl(threadIdx.x >> 6);
  const int b = blockIdx.x, xc = b & 7, r = b >> 3;
  const bool g1 = r < 30;
  const int r5 = r / 6, ri = (xc >> 2) * 5 + r5, cj = (xc & 3) * 6 + r % 6, ts = (ri * 64) / 5;
  const int i2 = xc * 4 + (r >> 3), j2 = r & 7;
#define KS2(k) (k)
  const u32 CTL = sb + LD_CTL;
  u64 pc0; u32 codeb;
  asm volatile("s_getpc_b64 %0\n.Lk_start:\n\ts_mov_b32 %1, .Lk_end-.Lk_start\n\t"
               ".subsection 2\n.Lk_end:\n\t.fill 128, 4, 0xBF800000\n\t.subsection 0" : "=s"(pc0), "=s"(codeb));
#define CSTAMP(k)
  STAMP(0);
  const int c6 = r % 6, rot = (OFFS >> (4 * ((r5 + c6) % NGRP))) & 15;
  const v4u rz = mkrs(p.ws, 0);
  {
    if (wv == 6)
      asm volatile("s_mov_b32 m0, %2\n\ts_nop 0\n\tbuffer_load_dword %0, %1, 0 offen lds" ::"v"(lane * 64 + (r & 15) * 4096), "s"(mkrs((const void*)pc0, codeb)), "s"(sb + LD_ST + 512) : "memory");
    const bool isw = (wv & 6) == 4;
    const int xi = wv < 4 ? wv : wv - 2;
    const u32 pitch = isw ? 3072 : 1280;
    const int rr = lane >> 3;
    const u32 sw0 = isw ? (rr & 2) : (rr >> 1);
    const u32 a0 = rr * pitch + (((lane & 7) ^ sw0) << 4), a1 = (a0 ^ 64) + 8 * pitch - 1024, a2 = a0 + 16 * pitch - 2048, a3 = a1 + 16 * pitch - 2048;
    const v4u rs = mkrs(wv == 4 ? p.wg : (wv == 5 ? p.wu : p.x), isw ? D * F * 2 : L * D * 2);
    u32 so = isw ? rot * 64 * 3072 + cj * 128 : (ts + 2 * xi) * 20480 + rot * 128, ld = isw ? sb + LD_R + (wv & 1) * 8192 : sb + LD_X + xi * 4096;
    const u32 dso = isw ? 32 * 3072 : 20480, dld = isw ? 4096 : 2048;
    const bool two = isw || wv == 7;
#pragma unroll 1
    for (int st = 0;; st++) {
      if (g1) { dma4(rs, a0, a1, a2, a3, so, ld); if (two) dma4(rs, a0, a1, a2, a3, so + dso, ld + dld); }
      if (st == 0) {
        if (wv == 2) asm volatile("s_mov_b32 m0, %0\n\ts_mov_b64 exec, 1\n\tbuffer_load_dword off, %1, 0 sc1 lds\n\ts_mov_b64 exec, -1" ::"s"(CTL + C_TAG), "s"(mkrs(p.ws, 64)) : "memory");
        if (wv < 4) {
          const u64 m = wv == 0 ? ((r & 1) ? 0x1f3f37800ull : 0x1f7f77800ull) : (wv == 1 ? (g1 ? ~0ull : 0ull) : (wv == 2 ? (g1 ? 0x01010101010131ffull : 0xffull) : (g1 ? 0x0101ull : 0ull)));
          if (!(wv == 0 && lane == 15) && !(wv == 3 && lane >= 32)) *(volatile LAS u32*)(CTL + wv * 256 + lane * 4) = (u32)(m >> lane) & 1u;
        }
        BARRIER();
      }
      sig0(rz, CTL + C_FULL + st * 32 + wv * 4);
      if (st == 1 || !isw) break;
      so += 64 * 3072; ld += WSLOT;
    }
    STAMP(30);
  }
  __builtin_amdgcn_wave_barrier();
  const int g = lane >> 4, i = lane & 15;
  const rsrc_t rw = mkr(p.ws, WS_BYTES);
  const int cstep = ((c6 == 0 ? CT0 : c6 == 1 ? CT1 : c6 == 2 ? CT2 : c6 == 3 ? CT3 : c6 == 4 ? CT4 : CT5) >> (3 * ri)) & 7;
  const int ch0 = 80 * xc + 5 * (r >> 1) + (r & 1) * 3;
  __builtin_amdgcn_wave_barrier();
  if (wv < 4) {
    __builtin_amdgcn_s_setprio(2);
    const rsrc_t rx = mkr(p.x, L * D * 2);
    constexpr int NWR = 5;
    v4u Wr[NWR][5];
    const u32 wdo = WS_WDF + (j2 * 48 + 12 * wv) * 5120;
    u32 tag;
    if (g1) {
      f4 acc[3][8], a13[2];
#pragma unroll
      for (int t = 0; t < 3; t++)
#pragma unroll
        for (int c = 0; c < 8; c++) { acc[t][c] = f4{0, 0, 0, 0}; asm volatile("" : "+v"(acc[t][c])); }
      a13[0] = a13[1] = f4{0, 0, 0, 0};
      const int q = (lane >> 2) & 3, pp = lane & 3, fp = ((q >> 1) & 1) | ((g & 1) << 1);
      u32 wb[4];
#pragma unroll
      for (int c = 0; c < 4; c++) wb[c] = sb + LD_R + (8 * g + q) * 128 + ((c ^ wv ^ fp) * 32) + pp * 8;
      u32 xb0 = sb + LD_X + wv * 6144 + i * 128 + ((g ^ (i >> 1)) << 4), xb1 = xb0 ^ 64;
      u32 yb0 = xb0 + (12 - 3 * wv) * 2048, yb1 = yb0 ^ 64, fa = CTL + C_FULL + (lane & 7) * 4;
      v4u Wf[2][8], Xf[2][4];
      asm volatile("" : "+v"(a13[0]), "+v"(a13[1]), "+v"(wb[0]), "+v"(wb[1]), "+v"(wb[2]), "+v"(wb[3]), "+v"(xb0), "+v"(xb1), "+v"(yb0), "+v"(yb1), "+v"(fa));
#define RDP(B, n, XA, YA, OFF) do { if ((n) == 0) { Xf[B][0] = lds128(XA); Xf[B][1] = lds128(XA + 2048); } \
        else if ((n) == 2) { Xf[B][2] = lds128(XA + 4096); Xf[B][3] = lds128(YA); } \
        else { constexpr int c = (n) == 1 ? 0 : (n) - 2; const u32 a = wb[c & 3] + (OFF) + (c >> 2) * 8192; Wf[B][c] = trd2(a, a + 512); } SCHED(); } while (0)
#define MM(KS, m) do { constexpr int c = (m) < 4 ? 0 : (m) < 13 ? 1 + ((m) - 4) / 3 : (m) < 17 ? 4 : 5 + ((m) - 17) / 3; \
        constexpr int t = (m) < 4 ? (m) : (m) < 13 ? ((m) - 4) % 3 : (m) < 17 ? (m) - 13 : ((m) - 17) % 3; \
        if (t == 3) mma_bf(a13[c >> 2], Wf[KS][c], Xf[KS][3]); else mma_bf(acc[t < 3 ? t : 0][c], Wf[KS][c], Xf[KS][t]); SCHED(); } while (0)
#define R0(m) MM(0, m); RDP(1, m, xb1, yb1, 4096);
#define R1(m) MM(1, m); RDP(0, (m) - 12, xb0, yb0, 0);
#pragma unroll
      for (int c = 0; c < 8; c++) { Wf[1][c] = v4u{0, 0, 0, 0}; asm volatile("" : "+v"(Wf[1][c])); }
#pragma unroll
      for (int c = 0; c < 4; c++) { Xf[1][c] = v4u{0, 0, 0, 0}; asm volatile("" : "+v"(Xf[1][c])); }
      u32 sw = 0, sx = 0, fo = 1;
#pragma unroll 1
      for (int s = 0; s < 10; s++) {
        u32 fw;
        spin0(fa, fo);
        if (s == 0) STAMP(1);
        R1(12) R1(13) R1(14) R1(15) R1(16) R1(17) R1(18) R1(19) R1(20) R1(21)
        MM(1, 22); fa += 32; asm volatile("" : "+v"(fa)); SCHED(); MM(1, 23); MM(1, 24); MM(1, 25);
        R0(0) R0(1) R0(2) R0(3) R0(4) R0(5) R0(6) R0(7) R0(8) R0(9)
        sx += XSLOT; u32 dx = XSLOT; if (sx == NXS * XSLOT) { sx = 0; dx = XSLOT - NXS * XSLOT; }
        sw += WSLOT; u32 dl = WSLOT; if (sw == NWS * WSLOT) { sw = 0; dl = WSLOT - NWS * WSLOT; }
        MM(0, 10); xb0 += dx; xb1 += dx; asm volatile("" : "+v"(xb0), "+v"(xb1)); SCHED();
        MM(0, 11); yb0 += dx; yb1 += dx; asm volatile("" : "+v"(yb0), "+v"(yb1)); SCHED();
        MM(0, 12); wb[0] += dl; wb[1] += dl; asm volatile("" : "+v"(wb[0]), "+v"(wb[1])); SCHED();
        MM(0, 13); wb[2] += dl; wb[3] += dl; asm volatile("" : "+v"(wb[2]), "+v"(wb[3])); SCHED();
        MM(0, 14); MM(0, 15);
        asm volatile("s_waitcnt lgkmcnt(0)" ::: "memory");
        if (G1ONLY) STAMPD(3 + s - (s == 9 ? -1 : 0));
        sig0(rz, CTL + C_DONE + s * 16 + wv * 4); SCHED();
        if (s == cstep) {
          const u32 tg = mktag(ldsv(CTL + C_TAG));
          f4 c0 = {0, 0, 0, 0}, c1 = {0, 0, 0, 0}; v4u on = {0x3f803f80u, 0x3f803f80u, 0x3f803f80u, 0x3f803f80u};
          asm volatile("s_nop 1" : "+v"(c0), "+v"(c1), "+v"(on));
          mma_bf(c0, Wf[0][0], on); mma_bf(c1, Wf[0][4], on); mma_bf(c0, Wf[1][0], on); mma_bf(c1, Wf[1][4], on);
          asm volatile("s_nop 7" : "+v"(c0), "+v"(c1));
          const f4 cc = (i & 1) ? c1 : c0;
          if (i < 2) {
            const u32 o = WS_CSP + (i * 64 + 16 * wv + 4 * g) * 8, so = (cj * 10 + ri) * 1024;
            stc128(v4u{__float_as_uint(cc[0]), tg, __float_as_uint(cc[1]), tg}, rw, o, so); stc128(v4u{__float_as_uint(cc[2]), tg, __float_as_uint(cc[3]), tg}, rw, o + 16, so);
          }
          asm volatile("s_nop 3"); SCHED();
        }
        MM(0, 16); MM(0, 17); MM(0, 18); MM(0, 19); MM(0, 20); MM(0, 21); MM(0, 22); MM(0, 23); MM(0, 24); MM(0, 25);
        MM(1, 0); MM(1, 1); MM(1, 2); MM(1, 3); fw = ldsv(fa); SCHED();
        MM(1, 4); MM(1, 5); MM(1, 6); MM(1, 7); MM(1, 8); MM(1, 9); fo = (u32)__builtin_amdgcn_ballot_w64(fw != 0); SCHED(); MM(1, 10); MM(1, 11);
      }
      MM(1, 12); MM(1, 13); MM(1, 14); MM(1, 15); MM(1, 16); MM(1, 17); MM(1, 18); MM(1, 19); MM(1, 20); MM(1, 21); MM(1, 22); MM(1, 23); MM(1, 24); MM(1, 25);
      CSTAMP(14);
      if (G1ONLY) { STAMP(2); float sm = a13[0][0] + a13[1][0]; for (int t = 0; t < 3; t++) for (int c = 0; c < 8; c++) sm += acc[t][c][0]; if (sm == 1.2345f) p.out[0] = 1; ST_FLUSH(); __builtin_amdgcn_endpgm(); }
      { const v4u cw = *(volatile LAS v4u*)(CTL + C_RDY); tag = mktag(rfl(cw.w)); spin0(CTL + C_RDY, rfl(cw.x)); }
      STAMP(2);
      f4 cg[4], cu[4]; f2 st[4];
#pragma unroll
      for (int t = 0; t < 4; t++) st[t] = *(LAS f2*)(sb + LD_ST + ((t < 3 ? 3 * wv + t : 12) * 16 + i) * 8);
#pragma unroll
      for (int c = 0; c < 4; c++) { cg[c] = *(LAS f4*)(sb + LD_CS + (16 * (c ^ wv) + 4 * g) * 4); cu[c] = *(LAS f4*)(sb + LD_CS + 256 + (16 * (c ^ wv) + 4 * g) * 4); }
      const u32 hb = WS_HF + (2 * cj) * 1024 + (((((g ^ wv) & 1) << 1) | (g >> 1)) * 16 + i) * 16;
#pragma unroll
      for (int t = 0; t < 4; t++) {
        f2 k1 = {-st[t].x, -1.44269504089f * st[t].y}, k2 = {__builtin_amdgcn_rcpf(st[t].y), 0.f};
        asm volatile("s_nop 0" : "+v"(k1), "+v"(k2));
        v2u P[4];
#pragma unroll
        for (int c = 0; c < 4; c++) {
          if (t == 3 && c > 0) break;
          const f4 ag = t < 3 ? acc[t < 3 ? t : 0][c] : a13[0], au = t < 3 ? acc[t < 3 ? t : 0][4 + c] : a13[1];
#pragma unroll
          for (int e = 0; e < 2; e++) {
            const f2 a2 = e ? ag.zw : ag.xy, u2 = e ? au.zw : au.xy, cg2 = e ? cg[c].zw : cg[c].xy, cu2 = e ? cu[c].zw : cu[c].xy;
            const f2 gg = pmul_y(k1, pfma_x(k1, cg2, a2)), uu = pfma_x(k1, cu2, u2);
            const f2 den = pfma_xx(k2, f2{__builtin_amdgcn_exp2f(gg.x), __builtin_amdgcn_exp2f(gg.y)});
            const f2 h = pmul(uu, f2{__builtin_amdgcn_rcpf(den.x), __builtin_amdgcn_rcpf(den.y)});
            P[c][e] = pkbf(h.x, h.y);
          }
          if (c & 1) SCHED();
        }
        if (t < 3) {
#pragma unroll
          for (int k = 0; k < 2; k++) {
            auto sx2 = __builtin_amdgcn_permlane16_swap(P[2 * k].x, P[2 * k + 1].x, false, false);
            auto sy2 = __builtin_amdgcn_permlane16_swap(P[2 * k].y, P[2 * k + 1].y, false, false);
            stc128(v4u{sx2[0], sy2[0], sx2[1], sy2[1]}, rw, hb + (k ^ (wv >> 1)) * 1024, (ts + 3 * wv + t) * 49152);
          }
        } else if (r5 != 0) {
          __builtin_amdgcn_raw_buffer_store_b64(P[0], rw, WS_HF + (2 * cj + (wv >> 1)) * 1024 + ((2 * (wv & 1) + (g >> 1)) * 16 + i) * 16 + (g & 1) * 8, (ts + 12) * 49152, AUX_SC1);
        }
        SCHED();
      }
      STAMP(3);
      __builtin_amdgcn_s_setprio(3);
      ldsw(CTL + C_DONE + 160 + wv * 4, 0);
      waitv<0>();
      if (lane == 0 && __hip_atomic_fetch_add((LAS u32*)(CTL + C_HCNT), 1u, __ATOMIC_RELAXED, __HIP_MEMORY_SCOPE_WORKGROUP) == 3) stc32(tag, rw, WS_HFLAG + ri * 128 + cj * 4);
      STAMP(4);
    } else {
      if (G1ONLY) __builtin_amdgcn_endpgm();
      wait0(CTL + C_FULL + 8);
      tag = mktag(rfl(ldsv(CTL + C_TAG)));
      wait0(CTL + C_RDY);
      wait0(CTL + C_WDFULL);
    }
    const int tt = 4 * i2 + wv;
    f4 o[4][5];
#pragma unroll
    for (int n = 0; n < 25; n++) Wr[n / 5][n % 5] = ldc128(rw, lane * 16, wdo + n * 1024);
    SCHED();
    u32 rc[3];
#pragma unroll
    for (int m = 0; m < 3; m++) { const int n = min(m * 64 + lane, 159), row = n / 10; rc[m] = row | (n - row * 10) << 8; asm volatile("" : "+v"(rc[m])); }
#define OO(m) ((rc[m] & 255) * (D * 2) + (rc[m] >> 8) * 16)
    if (HPOLL4) {
      const int l12 = lane < 12 ? lane : 0;
      const u32 fo = WS_HFLAG + (l12 < 6 ? ((20 * i2 + 4) >> 6) * 128 + l12 * 4 : ((20 * i2 + 19) >> 6) * 128 + (l12 - 6) * 4) + wv * 24;
#pragma unroll 1
      for (u32 it = 0; it < SPIN_CAP; it++) { if (__all(ldc32(rw, fo) == tag)) break; __builtin_amdgcn_s_sleep(HP_SLEEP); }
      ORDER();
    } else if (wv == 3) {
      const int l48 = lane < 48 ? lane : 0;
      const u32 fo = WS_HFLAG + (l48 < 24 ? ((20 * i2 + 4) >> 6) * 128 + l48 * 4 : ((20 * i2 + 19) >> 6) * 128 + (l48 - 24) * 4);
      u32 left = 15;
#pragma unroll 1
      for (u32 it = 0; it < SPIN_CAP; it++) {
        const u64 bad = __builtin_amdgcn_ballot_w64(ldc32(rw, fo) != tag);
        const u32 bb = ((u32)bad | (u32)(bad >> 24)) & 0xffffffu;
        ORDER();
#pragma unroll
        for (int k = 0; k < 4; k++) if (((left >> k) & 1) && !((bb >> (6 * k)) & 63)) { if (k < 3) sig0(rz, CTL + C_HRDY + k * 4); left &= ~(1u << k); }
        if (!left) break;
        __builtin_amdgcn_s_sleep(HP_SLEEP);
      }
    } else wait0(CTL + C_HRDY + wv * 4);
    STAMP(5);
    constexpr int HD = 3;
    v4u hr[12][4], Wl[2][5], Wz[5], xq[3], dq[3]; v2u sq[3];
    u32 ho[4];
#pragma unroll
    for (int t = 0; t < 4; t++) ho[t] = ABL_H ? WS_WDF + t * 12288 : WS_HF + (4 * i2 + (t ^ wv)) * 49152 + 12 * wv * 1024;
#define LOAD_H(k) do { _Pragma("unroll") for (int t = 0; t < 4; t++) hr[k][t] = ldc128(rw, lane * 16, ho[t] + KS2(k) * 1024); } while (0)
#pragma unroll
    for (int k = 0; k < HD; k++) LOAD_H(k);
    SCHED();
    __builtin_amdgcn_s_setprio(G2_PRIO);
    const u32 wl = sb + wd_off(wv) + lane * 16;
    float base[3][8];
#define VFMA(d, a_, b_, c_) do { float t_; asm volatile("v_fma_f32 %0, %1, %2, %3" : "=v"(t_) : "v"(a_), "v"(b_), "v"(c_)); d = t_; } while (0)
#define VLO(d, w_) asm volatile("v_lshlrev_b32 %0, 16, %1" : "=v"(d) : "v"(w_))
#define VHI(d, w_) asm volatile("v_and_b32 %0, 0xffff0000, %1" : "=v"(d) : "v"(w_))
    float nbv[3];
#define BASE0(n) VFMA(nbv[n], -__uint_as_float(sq[n].x), __uint_as_float(sq[n].y), 0.f)
    float bxv, bdv;
#define BASE1(m, e) do { if ((e) & 1) { VHI(bxv, xq[m][(e) >> 1]); VHI(bdv, dq[m][(e) >> 1]); } else { VLO(bxv, xq[m][(e) >> 1]); VLO(bdv, dq[m][(e) >> 1]); } } while (0)
#define BASE2(m, e) do { float xn; VFMA(xn, bxv, __uint_as_float(sq[m].y), nbv[m]); VFMA(base[m][e], xn, bdv, bxv); } while (0)
#define LOAD_X(m) do { asm volatile("" : "+v"(rc[m])); xq[m] = __builtin_amdgcn_raw_buffer_load_b128(rx, OO(m), (16 * tt * D + 80 * j2) * 2, 0); \
      dq[m] = __builtin_amdgcn_raw_buffer_load_b128(mkr(p.dsk, D * 2), (rc[m] >> 8) * 16, 160 * j2, 0); \
      sq[m] = __builtin_amdgcn_raw_buffer_load_b64(rw, (rc[m] & 255) * 8, WS_STAT + 128 * tt, AUX_SC1); } while (0)
    mma_bf0(o[3][0], Wr[0][0], hr[0][3]); SCHED();
    mma_bf0(o[2][0], Wr[0][0], hr[0][2]); SCHED();
    mma_bf0(o[1][0], Wr[0][0], hr[0][1]); SCHED();
    mma_bf0(o[0][0], Wr[0][0], hr[0][0]); SCHED();
    mma_bf0(o[3][1], Wr[0][1], hr[0][3]); SCHED();
    mma_bf0(o[2][1], Wr[0][1], hr[0][2]); SCHED();
    mma_bf0(o[1][1], Wr[0][1], hr[0][1]); SCHED();
    mma_bf0(o[0][1], Wr[0][1], hr[0][0]); SCHED();
    mma_bf0(o[3][2], Wr[0][2], hr[0][3]); SCHED();
    mma_bf0(o[2][2], Wr[0][2], hr[0][2]); SCHED();
    mma_bf0(o[1][2], Wr[0][2], hr[0][1]); SCHED();
    mma_bf0(o[0][2], Wr[0][2], hr[0][0]); SCHED();
    mma_bf0(o[3][3], Wr[0][3], hr[0][3]); SCHED();
    mma_bf0(o[2][3], Wr[0][3], hr[0][2]); SCHED();
    mma_bf0(o[1][3], Wr[0][3], hr[0][1]); SCHED();
    mma_bf0(o[0][3], Wr[0][3], hr[0][0]); SCHED();
    mma_bf0(o[3][4], Wr[0][4], hr[0][3]); SCHED();
    hr[3][3] = ldc128(rw, lane * 16, ho[3] + 3072); SCHED();
    mma_bf0(o[2][4], Wr[0][4], hr[0][2]); SCHED();
    hr[3][2] = ldc128(rw, lane * 16, ho[2] + 3072); SCHED();
    mma_bf0(o[1][4], Wr[0][4], hr[0][1]); SCHED();
    hr[3][1] = ldc128(rw, lane * 16, ho[1] + 3072); SCHED();
    mma_bf0(o[0][4], Wr[0][4], hr[0][0]); SCHED();
    hr[3][0] = ldc128(rw, lane * 16, ho[0] + 3072); SCHED();
    STAMP(26);
    mma_bf(o[3][0], Wr[1][0], hr[1][3]); SCHED();
    hr[4][3] = ldc128(rw, lane * 16, ho[3] + 4096); SCHED();
    mma_bf(o[2][0], Wr[1][0], hr[1][2]); SCHED();
    mma_bf(o[1][0], Wr[1][0], hr[1][1]); SCHED();
    mma_bf(o[0][0], Wr[1][0], hr[1][0]); SCHED();
    mma_bf(o[3][1], Wr[1][1], hr[1][3]); SCHED();
    mma_bf(o[2][1], Wr[1][1], hr[1][2]); SCHED();
    hr[4][2] = ldc128(rw, lane * 16, ho[2] + 4096); SCHED();
    mma_bf(o[1][1], Wr[1][1], hr[1][1]); SCHED();
    mma_bf(o[0][1], Wr[1][1], hr[1][0]); SCHED();
    mma_bf(o[3][2], Wr[1][2], hr[1][3]); SCHED();
    mma_bf(o[2][2], Wr[1][2], hr[1][2]); SCHED();
    mma_bf(o[1][2], Wr[1][2], hr[1][1]); SCHED();
    hr[4][1] = ldc128(rw, lane * 16, ho[1] + 4096); SCHED();
    mma_bf(o[0][2], Wr[1][2], hr[1][0]); SCHED();
    mma_bf(o[3][3], Wr[1][3], hr[1][3]); SCHED();
    mma_bf(o[2][3], Wr[1][3], hr[1][2]); SCHED();
    mma_bf(o[1][3], Wr[1][3], hr[1][1]); SCHED();
    mma_bf(o[0][3], Wr[1][3], hr[1][0]); SCHED();
    hr[4][0] = ldc128(rw, lane * 16, ho[0] + 4096); SCHED();
    mma_bf(o[3][4], Wr[1][4], hr[1][3]); SCHED();
    mma_bf(o[2][4], Wr[1][4], hr[1][2]); SCHED();
    mma_bf(o[1][4], Wr[1][4], hr[1][1]); SCHED();
    mma_bf(o[0][4], Wr[1][4], hr[1][0]); SCHED();
    mma_bf(o[3][0], Wr[2][0], hr[2][3]); SCHED();
    hr[5][3] = ldc128(rw, lane * 16, ho[3] + 5120); SCHED();
    mma_bf(o[2][0], Wr[2][0], hr[2][2]); SCHED();
    mma_bf(o[1][0], Wr[2][0], hr[2][1]); SCHED();
    mma_bf(o[0][0], Wr[2][0], hr[2][0]); SCHED();
    mma_bf(o[3][1], Wr[2][1], hr[2][3]); SCHED();
    mma_bf(o[2][1], Wr[2][1], hr[2][2]); SCHED();
    hr[5][2] = ldc128(rw, lane * 16, ho[2] + 5120); SCHED();
    mma_bf(o[1][1], Wr[2][1], hr[2][1]); SCHED();
    mma_bf(o[0][1], Wr[2][1], hr[2][0]); SCHED();
    mma_bf(o[3][2], Wr[2][2], hr[2][3]); SCHED();
    mma_bf(o[2][2], Wr[2][2], hr[2][2]); SCHED();
    mma_bf(o[1][2], Wr[2][2], hr[2][1]); SCHED();
    hr[5][1] = ldc128(rw, lane * 16, ho[1] + 5120); SCHED();
    mma_bf(o[0][2], Wr[2][2], hr[2][0]); SCHED();
    mma_bf(o[3][3], Wr[2][3], hr[2][3]); SCHED();
    mma_bf(o[2][3], Wr[2][3], hr[2][2]); SCHED();
    mma_bf(o[1][3], Wr[2][3], hr[2][1]); SCHED();
    mma_bf(o[0][3], Wr[2][3], hr[2][0]); SCHED();
    hr[5][0] = ldc128(rw, lane * 16, ho[0] + 5120); SCHED();
    mma_bf(o[3][4], Wr[2][4], hr[2][3]); SCHED();
    mma_bf(o[2][4], Wr[2][4], hr[2][2]); SCHED();
    mma_bf(o[1][4], Wr[2][4], hr[2][1]); SCHED();
    mma_bf(o[0][4], Wr[2][4], hr[2][0]); SCHED();
    mma_bf(o[3][0], Wr[3][0], hr[3][3]); SCHED();
    hr[6][3] = ldc128(rw, lane * 16, ho[3] + 6144); SCHED();
    mma_bf(o[2][0], Wr[3][0], hr[3][2]); SCHED();
    mma_bf(o[1][0], Wr[3][0], hr[3][1]); SCHED();
    mma_bf(o[0][0], Wr[3][0], hr[3][0]); SCHED();
    mma_bf(o[3][1], Wr[3][1], hr[3][3]); SCHED();
    mma_bf(o[2][1], Wr[3][1], hr[3][2]); SCHED();
    hr[6][2] = ldc128(rw, lane * 16, ho[2] + 6144); SCHED();
    mma_bf(o[1][1], Wr[3][1], hr[3][1]); SCHED();
    mma_bf(o[0][1], Wr[3][1], hr[3][0]); SCHED();
    mma_bf(o[3][2], Wr[3][2], hr[3][3]); SCHED();
    mma_bf(o[2][2], Wr[3][2], hr[3][2]); SCHED();
    mma_bf(o[1][2], Wr[3][2], hr[3][1]); SCHED();
    hr[6][1] = ldc128(rw, lane * 16, ho[1] + 6144); SCHED();
    mma_bf(o[0][2], Wr[3][2], hr[3][0]); SCHED();
    mma_bf(o[3][3], Wr[3][3], hr[3][3]); SCHED();
    mma_bf(o[2][3], Wr[3][3], hr[3][2]); SCHED();
    mma_bf(o[1][3], Wr[3][3], hr[3][1]); SCHED();
    mma_bf(o[0][3], Wr[3][3], hr[3][0]); SCHED();
    hr[6][0] = ldc128(rw, lane * 16, ho[0] + 6144); SCHED();
    mma_bf(o[3][4], Wr[3][4], hr[3][3]); SCHED();
    mma_bf(o[2][4], Wr[3][4], hr[3][2]); SCHED();
    mma_bf(o[1][4], Wr[3][4], hr[3][1]); SCHED();
    mma_bf(o[0][4], Wr[3][4], hr[3][0]); SCHED();
    wait0(CTL + C_WDFULL); STAMP(28);
    mma_bf(o[3][0], Wr[4][0], hr[4][3]); SCHED();
    hr[7][3] = ldc128(rw, lane * 16, ho[3] + 7168); SCHED();
    mma_bf(o[2][0], Wr[4][0], hr[4][2]); SCHED();
    Wl[1][0] = lds128(wl + 0); SCHED();
    mma_bf(o[1][0], Wr[4][0], hr[4][1]); SCHED();
    mma_bf(o[0][0], Wr[4][0], hr[4][0]); SCHED();
    mma_bf(o[3][1], Wr[4][1], hr[4][3]); SCHED();
    mma_bf(o[2][1], Wr[4][1], hr[4][2]); SCHED();
    Wl[1][1] = lds128(wl + 1024); SCHED();
    hr[7][2] = ldc128(rw, lane * 16, ho[2] + 7168); SCHED();
    mma_bf(o[1][1], Wr[4][1], hr[4][1]); SCHED();
    mma_bf(o[0][1], Wr[4][1], hr[4][0]); SCHED();
    mma_bf(o[3][2], Wr[4][2], hr[4][3]); SCHED();
    mma_bf(o[2][2], Wr[4][2], hr[4][2]); SCHED();
    Wl[1][2] = lds128(wl + 2048); SCHED();
    mma_bf(o[1][2], Wr[4][2], hr[4][1]); SCHED();
    hr[7][1] = ldc128(rw, lane * 16, ho[1] + 7168); SCHED();
    mma_bf(o[0][2], Wr[4][2], hr[4][0]); SCHED();
    mma_bf(o[3][3], Wr[4][3], hr[4][3]); SCHED();
    mma_bf(o[2][3], Wr[4][3], hr[4][2]); SCHED();
    Wl[1][3] = lds128(wl + 3072); SCHED();
    mma_bf(o[1][3], Wr[4][3], hr[4][1]); SCHED();
    mma_bf(o[0][3], Wr[4][3], hr[4][0]); SCHED();
    hr[7][0] = ldc128(rw, lane * 16, ho[0] + 7168); SCHED();
    mma_bf(o[3][4], Wr[4][4], hr[4][3]); SCHED();
    mma_bf(o[2][4], Wr[4][4], hr[4][2]); SCHED();
    Wl[1][4] = lds128(wl + 4096); SCHED();
    mma_bf(o[1][4], Wr[4][4], hr[4][1]); SCHED();
    mma_bf(o[0][4], Wr[4][4], hr[4][0]); SCHED();
    mma_bf(o[3][0], Wl[1][0], hr[5][3]); SCHED();
    hr[8][3] = ldc128(rw, lane * 16, ho[3] + 8192); SCHED();
    mma_bf(o[2][0], Wl[1][0], hr[5][2]); SCHED();
    Wl[0][0] = lds128(wl + 5120); SCHED();
    mma_bf(o[1][0], Wl[1][0], hr[5][1]); SCHED();
    LOAD_X(0); SCHED();
    mma_bf(o[0][0], Wl[1][0], hr[5][0]); SCHED();
    mma_bf(o[3][1], Wl[1][1], hr[5][3]); SCHED();
    mma_bf(o[2][1], Wl[1][1], hr[5][2]); SCHED();
    Wl[0][1] = lds128(wl + 6144); SCHED();
    hr[8][2] = ldc128(rw, lane * 16, ho[2] + 8192); SCHED();
    mma_bf(o[1][1], Wl[1][1], hr[5][1]); SCHED();
    LOAD_X(1); SCHED();
    mma_bf(o[0][1], Wl[1][1], hr[5][0]); SCHED();
    mma_bf(o[3][2], Wl[1][2], hr[5][3]); SCHED();
    mma_bf(o[2][2], Wl[1][2], hr[5][2]); SCHED();
    Wl[0][2] = lds128(wl + 7168); SCHED();
    mma_bf(o[1][2], Wl[1][2], hr[5][1]); SCHED();
    LOAD_X(2); SCHED();
    hr[8][1] = ldc128(rw, lane * 16, ho[1] + 8192); SCHED();
    mma_bf(o[0][2], Wl[1][2], hr[5][0]); SCHED();
    mma_bf(o[3][3], Wl[1][3], hr[5][3]); SCHED();
    mma_bf(o[2][3], Wl[1][3], hr[5][2]); SCHED();
    Wl[0][3] = lds128(wl + 8192); SCHED();
    mma_bf(o[1][3], Wl[1][3], hr[5][1]); SCHED();
    mma_bf(o[0][3], Wl[1][3], hr[5][0]); SCHED();
    hr[8][0] = ldc128(rw, lane * 16, ho[0] + 8192); SCHED();
    mma_bf(o[3][4], Wl[1][4], hr[5][3]); SCHED();
    mma_bf(o[2][4], Wl[1][4], hr[5][2]); SCHED();
    Wl[0][4] = lds128(wl + 9216); SCHED();
    mma_bf(o[1][4], Wl[1][4], hr[5][1]); SCHED();
    mma_bf(o[0][4], Wl[1][4], hr[5][0]); SCHED();
    mma_bf(o[3][0], Wl[0][0], hr[6][3]); SCHED();
    hr[9][3] = ldc128(rw, lane * 16, ho[3] + 9216); SCHED();
    mma_bf(o[2][0], Wl[0][0], hr[6][2]); SCHED();
    Wl[1][0] = lds128(wl + 10240); SCHED();
    mma_bf(o[1][0], Wl[0][0], hr[6][1]); SCHED();
    mma_bf(o[0][0], Wl[0][0], hr[6][0]); SCHED();
    mma_bf(o[3][1], Wl[0][1], hr[6][3]); SCHED();
    mma_bf(o[2][1], Wl[0][1], hr[6][2]); SCHED();
    Wl[1][1] = lds128(wl + 11264); SCHED();
    hr[9][2] = ldc128(rw, lane * 16, ho[2] + 9216); SCHED();
    mma_bf(o[1][1], Wl[0][1], hr[6][1]); SCHED();
    mma_bf(o[0][1], Wl[0][1], hr[6][0]); SCHED();
    mma_bf(o[3][2], Wl[0][2], hr[6][3]); SCHED();
    mma_bf(o[2][2], Wl[0][2], hr[6][2]); SCHED();
    Wl[1][2] = lds128(wl + 12288); SCHED();
    mma_bf(o[1][2], Wl[0][2], hr[6][1]); SCHED();
    hr[9][1] = ldc128(rw, lane * 16, ho[1] + 9216); SCHED();
    mma_bf(o[0][2], Wl[0][2], hr[6][0]); SCHED();
    mma_bf(o[3][3], Wl[0][3], hr[6][3]); SCHED();
    mma_bf(o[2][3], Wl[0][3], hr[6][2]); SCHED();
    Wl[1][3] = lds128(wl + 13312); SCHED();
    mma_bf(o[1][3], Wl[0][3], hr[6][1]); SCHED();
    mma_bf(o[0][3], Wl[0][3], hr[6][0]); SCHED();
    hr[9][0] = ldc128(rw, lane * 16, ho[0] + 9216); SCHED();
    mma_bf(o[3][4], Wl[0][4], hr[6][3]); SCHED();
    mma_bf(o[2][4], Wl[0][4], hr[6][2]); SCHED();
    Wl[1][4] = lds128(wl + 14336); SCHED();
    mma_bf(o[1][4], Wl[0][4], hr[6][1]); SCHED();
    mma_bf(o[0][4], Wl[0][4], hr[6][0]); SCHED();
    mma_bf(o[3][0], Wl[1][0], hr[7][3]); SCHED();
    hr[10][3] = ldc128(rw, lane * 16, ho[3] + 10240); SCHED();
    mma_bf(o[2][0], Wl[1][0], hr[7][2]); SCHED();
    Wl[0][0] = lds128(wl + 15360); SCHED();
    mma_bf(o[1][0], Wl[1][0], hr[7][1]); SCHED();
    Wz[0] = ldc128(rw, lane * 16, wdo + 56320); SCHED();
    mma_bf(o[0][0], Wl[1][0], hr[7][0]); SCHED();
    mma_bf(o[3][1], Wl[1][1], hr[7][3]); SCHED();
    mma_bf(o[2][1], Wl[1][1], hr[7][2]); SCHED();
    Wl[0][1] = lds128(wl + 16384); SCHED();
    hr[10][2] = ldc128(rw, lane * 16, ho[2] + 10240); SCHED();
    mma_bf(o[1][1], Wl[1][1], hr[7][1]); SCHED();
    Wz[1] = ldc128(rw, lane * 16, wdo + 57344); SCHED();
    mma_bf(o[0][1], Wl[1][1], hr[7][0]); SCHED();
    mma_bf(o[3][2], Wl[1][2], hr[7][3]); SCHED();
    mma_bf(o[2][2], Wl[1][2], hr[7][2]); SCHED();
    Wl[0][2] = lds128(wl + 17408); SCHED();
    mma_bf(o[1][2], Wl[1][2], hr[7][1]); SCHED();
    Wz[2] = ldc128(rw, lane * 16, wdo + 58368); SCHED();
    hr[10][1] = ldc128(rw, lane * 16, ho[1] + 10240); SCHED();
    mma_bf(o[0][2], Wl[1][2], hr[7][0]); SCHED();
    mma_bf(o[3][3], Wl[1][3], hr[7][3]); SCHED();
    mma_bf(o[2][3], Wl[1][3], hr[7][2]); SCHED();
    Wl[0][3] = lds128(wl + 18432); SCHED();
    mma_bf(o[1][3], Wl[1][3], hr[7][1]); SCHED();
    Wz[3] = ldc128(rw, lane * 16, wdo + 59392); SCHED();
    mma_bf(o[0][3], Wl[1][3], hr[7][0]); SCHED();
    hr[10][0] = ldc128(rw, lane * 16, ho[0] + 10240); SCHED();
    mma_bf(o[3][4], Wl[1][4], hr[7][3]); SCHED();
    mma_bf(o[2][4], Wl[1][4], hr[7][2]); SCHED();
    Wl[0][4] = lds128(wl + 19456); SCHED();
    mma_bf(o[1][4], Wl[1][4], hr[7][1]); SCHED();
    Wz[4] = ldc128(rw, lane * 16, wdo + 60416); SCHED();
    mma_bf(o[0][4], Wl[1][4], hr[7][0]); SCHED();
    STAMP(31);
    mma_bf(o[3][0], Wl[0][0], hr[8][3]); SCHED();
    hr[11][3] = ldc128(rw, lane * 16, ho[3] + 11264); SCHED();
    mma_bf(o[2][0], Wl[0][0], hr[8][2]); SCHED();
    Wl[1][0] = lds128(wl + 20480); SCHED();
    mma_bf(o[1][0], Wl[0][0], hr[8][1]); SCHED();
    mma_bf(o[0][0], Wl[0][0], hr[8][0]); SCHED();
    mma_bf(o[3][1], Wl[0][1], hr[8][3]); SCHED();
    mma_bf(o[2][1], Wl[0][1], hr[8][2]); SCHED();
    Wl[1][1] = lds128(wl + 21504); SCHED();
    hr[11][2] = ldc128(rw, lane * 16, ho[2] + 11264); SCHED();
    mma_bf(o[1][1], Wl[0][1], hr[8][1]); SCHED();
    mma_bf(o[0][1], Wl[0][1], hr[8][0]); SCHED();
    mma_bf(o[3][2], Wl[0][2], hr[8][3]); SCHED();
    mma_bf(o[2][2], Wl[0][2], hr[8][2]); SCHED();
    Wl[1][2] = lds128(wl + 22528); SCHED();
    BASE0(0); SCHED();
    mma_bf(o[1][2], Wl[0][2], hr[8][1]); SCHED();
    hr[11][1] = ldc128(rw, lane * 16, ho[1] + 11264); SCHED();
    BASE0(1); SCHED();
    mma_bf(o[0][2], Wl[0][2], hr[8][0]); SCHED();
    BASE0(2); SCHED();
    mma_bf(o[3][3], Wl[0][3], hr[8][3]); SCHED();
    BASE1(0, 0); SCHED();
    mma_bf(o[2][3], Wl[0][3], hr[8][2]); SCHED();
    Wl[1][3] = lds128(wl + 23552); SCHED();
    BASE2(0, 0); SCHED();
    mma_bf(o[1][3], Wl[0][3], hr[8][1]); SCHED();
    BASE1(0, 1); SCHED();
    mma_bf(o[0][3], Wl[0][3], hr[8][0]); SCHED();
    hr[11][0] = ldc128(rw, lane * 16, ho[0] + 11264); SCHED();
    BASE2(0, 1); SCHED();
    mma_bf(o[3][4], Wl[0][4], hr[8][3]); SCHED();
    BASE1(0, 2); SCHED();
    mma_bf(o[2][4], Wl[0][4], hr[8][2]); SCHED();
    Wl[1][4] = lds128(wl + 24576); SCHED();
    BASE2(0, 2); SCHED();
    mma_bf(o[1][4], Wl[0][4], hr[8][1]); SCHED();
    BASE1(0, 3); SCHED();
    mma_bf(o[0][4], Wl[0][4], hr[8][0]); SCHED();
    BASE2(0, 3); SCHED();
    mma_bf(o[3][0], Wl[1][0], hr[9][3]); SCHED();
    BASE1(0, 4); SCHED();
    mma_bf(o[2][0], Wl[1][0], hr[9][2]); SCHED();
    Wl[0][0] = lds128(wl + 25600); SCHED();
    BASE2(0, 4); SCHED();
    mma_bf(o[1][0], Wl[1][0], hr[9][1]); SCHED();
    BASE1(0, 5); SCHED();
    mma_bf(o[0][0], Wl[1][0], hr[9][0]); SCHED();
    BASE2(0, 5); SCHED();
    mma_bf(o[3][1], Wl[1][1], hr[9][3]); SCHED();
    BASE1(0, 6); SCHED();
    mma_bf(o[2][1], Wl[1][1], hr[9][2]); SCHED();
    Wl[0][1] = lds128(wl + 26624); SCHED();
    BASE2(0, 6); SCHED();
    mma_bf(o[1][1], Wl[1][1], hr[9][1]); SCHED();
    BASE1(0, 7); SCHED();
    mma_bf(o[0][1], Wl[1][1], hr[9][0]); SCHED();
    BASE2(0, 7); SCHED();
    mma_bf(o[3][2], Wl[1][2], hr[9][3]); SCHED();
    BASE1(1, 0); SCHED();
    mma_bf(o[2][2], Wl[1][2], hr[9][2]); SCHED();
    Wl[0][2] = lds128(wl + 27648); SCHED();
    BASE2(1, 0); SCHED();
    mma_bf(o[1][2], Wl[1][2], hr[9][1]); SCHED();
    BASE1(1, 1); SCHED();
    mma_bf(o[0][2], Wl[1][2], hr[9][0]); SCHED();
    BASE2(1, 1); SCHED();
    mma_bf(o[3][3], Wl[1][3], hr[9][3]); SCHED();
    BASE1(1, 2); SCHED();
    mma_bf(o[2][3], Wl[1][3], hr[9][2]); SCHED();
    Wl[0][3] = lds128(wl + 28672); SCHED();
    BASE2(1, 2); SCHED();
    mma_bf(o[1][3], Wl[1][3], hr[9][1]); SCHED();
    BASE1(1, 3); SCHED();
    mma_bf(o[0][3], Wl[1][3], hr[9][0]); SCHED();
    BASE2(1, 3); SCHED();
    mma_bf(o[3][4], Wl[1][4], hr[9][3]); SCHED();
    BASE1(1, 4); SCHED();
    mma_bf(o[2][4], Wl[1][4], hr[9][2]); SCHED();
    Wl[0][4] = lds128(wl + 29696); SCHED();
    BASE2(1, 4); SCHED();
    mma_bf(o[1][4], Wl[1][4], hr[9][1]); SCHED();
    BASE1(1, 5); SCHED();
    mma_bf(o[0][4], Wl[1][4], hr[9][0]); SCHED();
    BASE2(1, 5); SCHED();
    mma_bf(o[3][0], Wl[0][0], hr[10][3]); SCHED();
    BASE1(1, 6); SCHED();
    mma_bf(o[2][0], Wl[0][0], hr[10][2]); SCHED();
    BASE2(1, 6); SCHED();
    mma_bf(o[1][0], Wl[0][0], hr[10][1]); SCHED();
    BASE1(1, 7); SCHED();
    mma_bf(o[0][0], Wl[0][0], hr[10][0]); SCHED();
    BASE2(1, 7); SCHED();
    mma_bf(o[3][1], Wl[0][1], hr[10][3]); SCHED();
    BASE1(2, 0); SCHED();
    mma_bf(o[2][1], Wl[0][1], hr[10][2]); SCHED();
    BASE2(2, 0); SCHED();
    mma_bf(o[1][1], Wl[0][1], hr[10][1]); SCHED();
    BASE1(2, 1); SCHED();
    mma_bf(o[0][1], Wl[0][1], hr[10][0]); SCHED();
    BASE2(2, 1); SCHED();
    mma_bf(o[3][2], Wl[0][2], hr[10][3]); SCHED();
    BASE1(2, 2); SCHED();
    mma_bf(o[2][2], Wl[0][2], hr[10][2]); SCHED();
    BASE2(2, 2); SCHED();
    mma_bf(o[1][2], Wl[0][2], hr[10][1]); SCHED();
    BASE1(2, 3); SCHED();
    mma_bf(o[0][2], Wl[0][2], hr[10][0]); SCHED();
    BASE2(2, 3); SCHED();
    mma_bf(o[3][3], Wl[0][3], hr[10][3]); SCHED();
    BASE1(2, 4); SCHED();
    mma_bf(o[2][3], Wl[0][3], hr[10][2]); SCHED();
    BASE2(2, 4); SCHED();
    mma_bf(o[1][3], Wl[0][3], hr[10][1]); SCHED();
    BASE1(2, 5); SCHED();
    mma_bf(o[0][3], Wl[0][3], hr[10][0]); SCHED();
    BASE2(2, 5); SCHED();
    mma_bf(o[3][4], Wl[0][4], hr[10][3]); SCHED();
    BASE1(2, 6); SCHED();
    mma_bf(o[2][4], Wl[0][4], hr[10][2]); SCHED();
    BASE2(2, 6); SCHED();
    mma_bf(o[1][4], Wl[0][4], hr[10][1]); SCHED();
    BASE1(2, 7); SCHED();
    mma_bf(o[0][4], Wl[0][4], hr[10][0]); SCHED();
    BASE2(2, 7); SCHED();
    STAMP(20);
    {
#pragma unroll
      for (int n = 0; n < 25; n++) {
        if (n < 20) { mma_bf(o[3 - n / 5][n % 5], Wz[n % 5], hr[11][3 - n / 5]); SCHED(); }
        if (n >= 5 && n < 20) { const int m = n - 5; asm volatile("s_nop 2" : "+v"(o[3 - m / 5][m % 5])); *(LAS f4*)(wl + ((2 - m / 5) * 5 + m % 5) * 1024) = o[3 - m / 5][m % 5]; SCHED(); }
      }
      ldsw(CTL + C_RED + wv * 4, 0);
      STAMP(6);
      asm volatile("s_nop 7" : "+v"(o[0][0]), "+v"(o[0][1]), "+v"(o[0][2]), "+v"(o[0][3]), "+v"(o[0][4]));
      u32 oo[3], lo3[3];
#pragma unroll
      for (int m = 0; m < 3; m++) { asm volatile("" : "+v"(rc[m])); oo[m] = OO(m); lo3[m] = (rc[m] & 255) * 336 + (rc[m] >> 8) * 32; asm volatile("" : "+v"(oo[m]), "+v"(lo3[m])); }
      spin_zero4(CTL + C_RED);
#pragma unroll
      for (int c = 0; c < 5; c++) {
        f4 s[3];
#pragma unroll
        for (int t = 1; t < 4; t++) s[t - 1] = *(LAS f4*)(sb + wd_off(wv ^ t) + lane * 16 + ((t - 1) * 5 + c) * 1024);
#pragma unroll
        for (int e = 0; e < 4; e++) o[0][c][e] = fadd_(fadd_(o[0][c][e], s[0][e]), fadd_(s[1][e], s[2][e]));
        asm volatile("" : "+v"(o[0][c]));
      }
      wait0(CTL + C_ZFULL);
      STAMP(7);
      const int q = (lane >> 2) & 3, pp = lane & 3;
      const u32 tb = sb + wd_off(wv) + 15360;
      v2u z[5];
#pragma unroll
      for (int c = 0; c < 5; c++) z[c] = trd(sb + LD_Z + (20 * g + 4 * c + q) * 128 + wv * 32 + pp * 8);
#pragma unroll
      for (int c = 0; c < 5; c++)
        *(LAS f4*)(tb + i * 336 + g * 80 + c * 16) = f4{fadd_(o[0][c][0], bf2f(z[c].x)), fadd_(o[0][c][1], bfhi(z[c].x)), fadd_(o[0][c][2], bf2f(z[c].y)), fadd_(o[0][c][3], bfhi(z[c].y))};
      CBAR();
      const rsrc_t ro = mkr(p.out, L * D * 2);
#pragma unroll
      for (int m = 0; m < 3; m++) {
        const f4 u0 = *(LAS f4*)(tb + lo3[m]), u1 = *(LAS f4*)(tb + lo3[m] + 16);
        const v4u w = {pkbf(fadd_(u0[0], base[m][0]), fadd_(u0[1], base[m][1])), pkbf(fadd_(u0[2], base[m][2]), fadd_(u0[3], base[m][3])),
                       pkbf(fadd_(u1[0], base[m][4]), fadd_(u1[1], base[m][5])), pkbf(fadd_(u1[2], base[m][6]), fadd_(u1[3], base[m][7]))};
        if (m < 2 || lane < 32) stc128(w, ro, oo[m], (16 * tt * D + 80 * j2) * 2);
      }
    }
    STAMP(8); CSTAMP(15);
    ST_FLUSH();
    __builtin_amdgcn_endpgm();
  } else if (wv < 7) {
    const int slot = wv - 4;
    if (G1ONLY == 1) { __builtin_amdgcn_endpgm(); }
    if (slot == 2 && (r & 1)) { ln_unit(p, sb, (16 * (xc >> 2) + (r >> 1)) * 4 + (xc & 3), lane); if (G1ONLY) __builtin_amdgcn_endpgm(); STAMP(21); }
    else { conv_wave(p, sb, slot, ch0 + slot, lane, wv, g1, ts, cj ST_PASS); }
    ST_FLUSH();
    __builtin_amdgcn_endpgm();
  } else {
    __builtin_amdgcn_s_setprio(3);
    v4u fa, fb, cp[10];
    const v4u rws = mkrs(p.ws, WS_BYTES);
#define LDH(d, vo_, so_) asm volatile("buffer_load_dwordx4 %0, %1, %2, %3 offen sc1" : "=v"(d) : "v"(vo_), "s"(rws), "s"(so_) : "memory")
#define LOOK() do { LDH(fa, lane * 16, WS_LFLAG + xc * 2048); LDH(fb, lane * 16, WS_LFLAG + xc * 2048 + 1024); } while (0)
#define LOOKC() do { _Pragma("unroll") for (int k = 0; k < 10; k++) LDH(cp[k], lane * 16, WS_CSP + (cj * 10 + k) * 1024); } while (0)
#define PINF(N) asm volatile("s_waitcnt vmcnt(" #N ")" : "+v"(fa), "+v"(fb), "+v"(cp[0]), "+v"(cp[1]), "+v"(cp[2]), "+v"(cp[3]), "+v"(cp[4]), "+v"(cp[5]), "+v"(cp[6]), "+v"(cp[7]), "+v"(cp[8]), "+v"(cp[9]) :: "memory")
    if (g1) {
      const v4u rg = mkrs(p.wg, D * F * 2), ru = mkrs(p.wu, D * F * 2), rxs = mkrs(p.x, L * D * 2);
      u32 v[4], vx[4];
#pragma unroll
      for (int k = 0; k < 4; k++) {
        const int row = 8 * k + (lane >> 3), fp = ((row >> 1) & 1) | (((row >> 3) & 1) << 1);
        v[k] = row * 3072 + cj * 128 + (((lane & 7) ^ (fp << 1)) << 4) - 1024 * k;
        vx[k] = (ts * 16 + row) * 1280 + (((lane & 7) ^ ((row >> 1) & 7)) << 4) - 1024 * k;
      }
      int kx = rot + 1, kw = rot == 8 ? 0 : rot + 2; u32 lsw = 2 * WSLOT, lsx = XSLOT;
      v4u dn = {0, 0, 0, 0};
#define FETCH_X(n) do { if (rfl(dn.x | dn.y | dn.z | dn.w) != 0) spin_zero4(CTL + C_DONE + ((n) - 3) * 16); \
        _Pragma("unroll") for (int k = 0; k < 6; k++) { if (k == 0) waitv<50>(); if (k == 3) waitv<47>(); dma4(rxs, vx[0], vx[1], vx[2], vx[3], kx * 128 + k * 40960, sb + LD_X + lsx + k * 4096); } \
        dma2(rxs, vx[0], vx[1], kx * 128 + 12 * 20480, sb + LD_X + lsx + 12 * 2048); \
        sig0(rz, CTL + C_FULL + (n) * 32); } while (0)
#pragma unroll 1
      for (int n = 1; n < 9; n++) {
        FETCH_X(n);
        if (n > 1) dn = *(volatile LAS v4u*)(CTL + C_DONE + (n - 2) * 16);
        kx = kx == 9 ? 0 : kx + 1; lsx += XSLOT; if (lsx == NXS * XSLOT) lsx = 0;
        waitv<44>();
#pragma unroll
        for (int k = 0; k < 4; k++) dma4((k & 1) ? ru : rg, v[0], v[1], v[2], v[3], (kw * 64 + (k >> 1) * 32) * 3072, sb + LD_R + lsw + (k & 1) * 8192 + (k >> 1) * 4096);
        kw = kw == 9 ? 0 : kw + 1; lsw += WSLOT; if (lsw == NWS * WSLOT) lsw = 0;
      }
      waitv<48>(); LOOKC();
      FETCH_X(9);
      STAMP(12);
    } else { wait0(CTL + C_FULL + 8); __builtin_amdgcn_s_sleep(127); __builtin_amdgcn_s_sleep(60); LOOKC(); }
    waitv<60>(); LOOK();
    const u32 tag = mktag(rfl(ldsv(CTL + C_TAG)));
    u32 vo[5];
#pragma unroll
    for (int k = 0; k < 5; k++) { const int n = k * 64 + lane, tau = n / 72, a = n % 72 - 8; vo[k] = a >= 0 && tau < 4 ? ((a >> 1) * 4 + tau) * 20480 + (a & 1) * 16 - (k & 3) * 1024 : 0x80000000u; }
    {
      float cs0, cs1;
#pragma unroll 1
      for (u32 it = 0; it < SPIN_CAP; it++) {
        PINF(0);
        u32 bad = 0; cs0 = cs1 = 0.f;
#pragma unroll
        for (int k = 0; k < 10; k++) { bad |= (cp[k].y ^ tag) | (cp[k].w ^ tag); cs0 = fadd_(cs0, __uint_as_float(cp[k].x)); cs1 = fadd_(cs1, __uint_as_float(cp[k].z)); }
        const bool cok = !g1 || __all(bad == 0);
        if (cok && __all(((fa.x ^ tag) | (fa.y ^ tag) | (fa.z ^ tag) | (fa.w ^ tag) | (fb.x ^ tag) | (fb.y ^ tag) | (fb.z ^ tag) | (fb.w ^ tag)) == 0)) break;
        __builtin_amdgcn_s_sleep(8);
        LOOK(); if (!cok) LOOKC();
      }
      if (g1) asm volatile("s_mov_b32 m0, %0\n\ts_nop 0\n\tbuffer_load_dwordx4 %1, %2, %3 offen sc1 lds\n\ts_mov_b32 exec_hi, 0xff\n\t"
                           "buffer_load_dwordx4 %1, %2, %3 offen offset:1024 sc1 lds\n\ts_mov_b32 exec_hi, -1"
                           ::"s"(sb + LD_ST), "v"(lane * 16), "s"(rws), "s"(WS_STAT + ts * 128) : "memory");
      *(LAS f2*)(sb + LD_CS + lane * 8) = f2{cs0, cs1};
      asm volatile("s_waitcnt lgkmcnt(0)" ::: "memory");
      sig0(rz, CTL + C_RDY);
    }
    STAMP(23);
#pragma unroll 1
    for (int sl = 0; sl < 3 - (r & 1); sl++) {
      const int ch = ch0 + sl; const u32 co = WS_XT + ((ch >> 7) * 4 + (ch & 3)) * 1024 + ((ch & 127) >> 2) * 32, UB = sb + LD_K + sl * K_BYTES;
      dma4c(rws, vo[0], vo[1], vo[2], vo[3], co, UB); dma1c(rws, vo[4], co, UB + 4096);
    }
    sig0(rz, CTL + C_UFULL);
    {
      const u32 vl = lane * 16, gb = WS_WDF + (j2 * 48 + 5) * 5120;
      v4u stg[4][NST];
      __builtin_amdgcn_s_setprio(1);
#pragma unroll
      for (int w = 0; w < 4; w++) {
        if (w == 3) waitv<44>();
#pragma unroll
        for (int k = 0; k < NST; k++) stg[w][k] = ldc128(rw, vl, gb + w * 61440 + k * 1024);
      }
      SCHED();
      spin_zero4(CTL + C_KDONE); spin_zero4(CTL + C_DONE + 9 * 16);
      STAMP(27);
#pragma unroll
      for (int w = 0; w < 4; w++)
#pragma unroll
        for (int k = 0; k < NST; k++) *(LAS v4u*)(sb + wd_off(w) + vl + k * 1024) = stg[w][k];
      asm volatile("s_waitcnt lgkmcnt(0)" ::: "memory");
      STAMP(29);
      __builtin_amdgcn_s_setprio(WD_PRIO);
      u32 tleft = HTOUCH ? 7 : 0;
      const u32 tfo = WS_HFLAG + (((lane & 4) ? 20 * i2 + 19 : 20 * i2 + 4) >> 6) * 128 + (3 * j2 + min(lane & 3, 2)) * 4;
      const u32 tvo = WS_HF + (4 * i2 + (lane >> 4)) * 49152 + 6 * j2 * 1024 + (lane & 15) * 128;
#define HT() do { if (tleft) { const u32 bad = (u32)__builtin_amdgcn_ballot_w64(ldc32(rw, tfo) != tag); const u32 rdy = tleft & ~(bad | (bad >> 4)); ORDER(); \
        _Pragma("unroll") for (int k = 0; k < 3; k++) if ((rdy >> k) & 1) \
          asm volatile("s_mov_b32 m0, %3\n\ts_nop 0\n\tbuffer_load_dword %0, %1, %2 offen sc1 lds" ::"v"(tvo), "s"(rws), "s"(k * 2048), "s"(CTL + C_FULL) : "memory"); \
        tleft &= ~rdy; } } while (0)
      HT();
      u32 go = gb;
#pragma unroll 1
      for (int w = 0; w < 4; w++, go += 61440) {
        const u32 lo = sb + wd_off(w);
#pragma unroll
        for (int k = NST; k < 30; k += 4) { waitv<WD_DEPTH>(); if (30 - k >= 4) dma4c(rws, vl, vl, vl, vl, go + k * 1024, lo + k * 1024); else dma2c(rws, vl, vl, go + k * 1024, lo + k * 1024); }
      }
      sig0(rz, CTL + C_WDFULL);
      HT();
    }
    STAMP(24);
    __builtin_amdgcn_s_setprio(3);
    spin_zero4(CTL + C_CDONE);
    if (b == 0 && lane == 0) stc32(tag, rw, WS_GEN);
    if (lane < 8) stc32(tag, rw, WS_ZFLAG + (lane * 8 + xc) * 128 + r * 4);
    __builtin_amdgcn_s_sleep(ZP_FIRST);
#pragma unroll 1
    for (u32 it = 0; it < SPIN_CAP; it++) {
      if (__all(ldc32(rw, WS_ZFLAG + (xc * 8 + j2) * 128 + (lane & 31) * 4) == tag)) break;
      __builtin_amdgcn_s_sleep(ZP_SLEEP);
    }
    ORDER();
    {
      u32 vz[4];
#pragma unroll
      for (int k = 0; k < 4; k++) vz[k] = (8 * k + (lane >> 3)) * 4096 + (lane & 7) * 16 - 1024 * k;
      const u32 go = WS_ZT + 80 * j2 * 4096 + i2 * 128;
      dma4c(rws, vz[0], vz[1], vz[2], vz[3], go, sb + LD_Z); dma4c(rws, vz[0], vz[1], vz[2], vz[3], go + 32 * 4096, sb + LD_Z + 4096);
      dma2c(rws, vz[0], vz[1], go + 64 * 4096, sb + LD_Z + 8192);
      sig0(rz, CTL + C_ZFULL);
      waitv<0>();
    }
    STAMP(25);
  }
  ST_FLUSH();
}

extern "C" void kernel_launch(void* const* d_in, const int* in_sizes, int n_in, void* d_out, int out_size, void* d_ws, size_t ws_size, hipStream_t stream) {
  Params p;
  p.x = (const u16*)d_in[0]; p.lar = (const u16*)d_in[1]; p.aim = (const u16*)d_in[2]; p.cre = (const u16*)d_in[3]; p.cim = (const u16*)d_in[4];
  p.ldt = (const u16*)d_in[5]; p.dsk = (const u16*)d_in[6]; p.wg = (const u16*)d_in[8]; p.wu = (const u16*)d_in[9]; p.wd = (const u16*)d_in[10];
  p.out = (u16*)d_out; p.ws = (char*)d_ws;
  (void)hipFuncSetAttribute((const void*)k_main, hipFuncAttributeMaxDynamicSharedMemorySize, LDS_BYTES);
  k_main<<<256, 512, LDS_BYTES, stream>>>(p);
}
```

```cpp
#include <hip/hip_runtime.h>
#pragma clang diagnostic ignored "-Wint-to-pointer-cast"
typedef unsigned short u16; typedef unsigned u32; typedef unsigned long long u64;
typedef u32 v4u __attribute__((ext_vector_type(4)));
typedef u32 v2u __attribute__((ext_vector_type(2)));
typedef u32 v3u __attribute__((ext_vector_type(3)));
typedef float f4 __attribute__((ext_vector_type(4)));
typedef float f2 __attribute__((ext_vector_type(2)));
typedef short s4 __attribute__((ext_vector_type(4)));
typedef __bf16 bf2 __attribute__((ext_vector_type(2)));
typedef _Float16 hf2 __attribute__((ext_vector_type(2)));
#define LAS __attribute__((address_space(3)))
#define GAS __attribute__((address_space(1)))
#define DEV __device__ __forceinline__
typedef __amdgpu_buffer_rsrc_t rsrc_t;

constexpr int L = 2048, D = 640, NS = 192, F = 1536;
constexpr u32 WS_GEN = 0, WS_HFLAG = 1024, WS_ZFLAG = 40960, WS_LFLAG = 49152, WS_WFLAG = WS_LFLAG + 512, WS_STAT = 327680, WS_CSP = 65536,
              WS_XT = 1u << 20, WS_WDF = 4u << 20, WS_HF = 6u << 20, WS_ZT = 13u << 20, WS_BYTES = 16u << 20;
constexpr u32 LD_K = 0, K_BYTES = 5120;
constexpr u32 LD_CS = LD_K + 3 * K_BYTES, LD_ST = LD_CS + 512, LD_CTL = LD_ST + 208 * 8, LD_R = LD_CTL + 896;
constexpr u32 WSLOT = 16384, NWS = 4, XSLOT = 26624, NXS = 3, LD_X = LD_R + NWS * WSLOT, LDS_BYTES = LD_X + NXS * XSLOT;
constexpr u32 LD_H = LD_X + 2 * XSLOT;
constexpr u32 WD_BYTES = 30720, LD_Z = LD_X;
DEV u32 wd_off(int w) { return LD_R + ((0x70520020u >> (8 * w)) & 0xffu) * 1024; }
static_assert(LD_R % 128 == 0 && 18432 + 2 * WD_BYTES == NXS * XSLOT && LDS_BYTES <= 163840, "lds");
constexpr u32 C_FULL = 512, C_WDFULL = 44, C_RDY = 48, C_ZFULL = 52, C_UFULL = 56, C_TAG = 60, C_KDONE = 64, C_RED = 80, C_CDONE = 96, C_HRDY = 112, C_QUIET = 128, C_HCNT = 132, C_DONE = 256;
constexpr int AUX_SC1 = 16;
constexpr u32 SPIN_CAP = 1u << 15;
#define WD_DEPTH 40
#define WD_PRIO 3
#define POLL_STAGE 8
#define WPOLL_STAGE 7
#define WD_P1 12
#define HP_SLEEP 4
#define G2_PRIO 1
#define ABL_H 0
#define ABL_Z 0
#define ZP_FIRST 16
#define ZP_SLEEP 8
#define GWAIT 0
#define P1MODE 1
#define AUX_STAGE 1
#define NST 14
#define NGRP 2
#define OFFS 0x50
#define CT0 0x1293725u
#define CT1 0x1293725u
#define CT2 0x1293725u
#define CT3 0x1293725u
#define CT4 0x1293725u
#define CT5 0x1293725u
#define HTOUCH 0
#define HPOLL4 1
#define KL_PRIO 1
#define G1ONLY 0
#define CONV_PRIO 2

#define STAMP(k) do { } while (0)
#define ST_ARG
#define STAMPD(k) do { } while (0)
#define ST_PASS
#define ST_FLUSH() do { } while (0)

struct Params {
  const u16 *x, *lar, *aim, *cre, *cim, *ldt, *dsk, *wg, *wu, *wd;
  u16* out; char* ws;
};

DEV float bf2f(u32 v) { return __uint_as_float(v << 16); }
DEV float bfhi(u32 v) { return __uint_as_float(v & 0xffff0000u); }
DEV u32 pkbf(float a, float b) { f2 v = {a, b}; return __builtin_bit_cast(u32, __builtin_convertvector(v, bf2)); }
DEV u32 pkhf(float a, float b) { f2 v = {a, b}; return __builtin_bit_cast(u32, __builtin_convertvector(v, hf2)); }
DEV float rbf(float a) { return bf2f(pkbf(a, a)); }
DEV void mma_bf(f4& c, v4u a, v4u b) { asm volatile("v_mfma_f32_16x16x32_bf16 %0, %1, %2, %0" : "+v"(c) : "v"(a), "v"(b)); }
DEV void mma_hf(f4& c, v4u a, v4u b) { asm volatile("v_mfma_f32_16x16x32_f16 %0, %1, %2, %0" : "+v"(c) : "v"(a), "v"(b)); }
DEV void mma_hf0(f4& c, v4u a, v4u b) { asm volatile("v_mfma_f32_16x16x32_f16 %0, %1, %2, 0" : "=&v"(c) : "v"(a), "v"(b)); }
DEV void mma_bf0(f4& c, v4u a, v4u b) { asm volatile("v_mfma_f32_16x16x32_bf16 %0, %1, %2, 0" : "=&v"(c) : "v"(a), "v"(b)); }
DEV v2u trd(u32 a) { return __builtin_bit_cast(v2u, __builtin_amdgcn_ds_read_tr16_b64_v4i16((LAS s4*)a)); }
DEV v4u trd2(u32 a, u32 b) { v2u x = trd(a), y = trd(b); return v4u{x.x, x.y, y.x, y.y}; }
DEV v4u lds128(u32 a) { return *(LAS v4u*)a; }
DEV v2u lds64(u32 a) { return *(LAS v2u*)a; }
DEV u32 rfl(u32 v) { return __builtin_amdgcn_readfirstlane(v); }
DEV v4u mkrs(const void* p, u32 bytes) { u64 a = (u64)p; return v4u{(u32)a, (u32)(a >> 32) & 0xffffu, bytes, 0x00020000u}; }
DEV rsrc_t mkr(const void* p, u32 bytes) { return __builtin_amdgcn_make_buffer_rsrc((void*)p, 0, bytes, 0x00020000); }
DEV v4u ldc128(rsrc_t r, u32 v, u32 s) { return __builtin_amdgcn_raw_buffer_load_b128(r, v, s, AUX_SC1); }
DEV u32 ldc32(rsrc_t r, u32 v) { return __builtin_amdgcn_raw_buffer_load_b32(r, v, 0, AUX_SC1); }
DEV void stc128(v4u d, rsrc_t r, u32 v, u32 s) {
  __builtin_amdgcn_raw_buffer_store_b128(d, r, v, s, AUX_SC1); asm volatile("s_nop 1" ::"v"(d) : "memory"); }
DEV void stc32(u32 d, rsrc_t r, u32 v) { __builtin_amdgcn_raw_buffer_store_b32(d, r, v, 0, AUX_SC1); }
#define CBAR() asm volatile("" ::: "memory")
#define SCHED() __builtin_amdgcn_sched_barrier(0)
#define ORDER() __builtin_amdgcn_fence(__ATOMIC_ACQUIRE, "wavefront")
#define BARRIER() do { asm volatile("s_waitcnt lgkmcnt(0)" ::: "memory"); __builtin_amdgcn_s_barrier(); CBAR(); } while (0)
template <int N> DEV void waitv() { asm volatile("s_waitcnt vmcnt(%0)" ::"i"(N) : "memory"); }
#define DMA_(V, RS, SO, OFF, SC) "buffer_load_dwordx4 " V ", " RS ", " SO " offen" OFF SC " lds\n\t"
#define DMA4_(NAME, SC) DEV void NAME(v4u rs, u32 v0, u32 v1, u32 v2, u32 v3, u32 so, u32 lds) { \
  asm volatile("s_mov_b32 m0, %0\n\ts_nop 0\n\t" DMA_("%1", "%5", "%6", "", SC) DMA_("%2", "%5", "%6", " offset:1024", SC) DMA_("%3", "%5", "%6", " offset:2048", SC) DMA_("%4", "%5", "%6", " offset:3072", SC) \
               ::"s"(lds), "v"(v0), "v"(v1), "v"(v2), "v"(v3), "s"(rs), "s"(so) : "memory"); }
#define DMA2_(NAME, SC) DEV void NAME(v4u rs, u32 v0, u32 v1, u32 so, u32 lds) { \
  asm volatile("s_mov_b32 m0, %0\n\ts_nop 0\n\t" DMA_("%1", "%3", "%4", "", SC) DMA_("%2", "%3", "%4", " offset:1024", SC) ::"s"(lds), "v"(v0), "v"(v1), "s"(rs), "s"(so) : "memory"); }
#define DMA1_(NAME, SC) DEV void NAME(v4u rs, u32 v0, u32 so, u32 lds) { \
  asm volatile("s_mov_b32 m0, %0\n\ts_nop 0\n\t" DMA_("%1", "%2", "%3", "", SC) ::"s"(lds), "v"(v0), "s"(rs), "s"(so) : "memory"); }
DMA4_(dma4, "") DMA2_(dma2, "") DMA1_(dma1, "") DMA4_(dma4c, " sc1") DMA2_(dma2c, " sc1") DMA1_(dma1c, " sc1")
DEV void sig0(v4u rz, u32 lds) {
  asm volatile("s_mov_b32 m0, %0\n\ts_mov_b64 exec, 1\n\tbuffer_load_dword off, %1, 0 lds\n\ts_mov_b64 exec, -1" ::"s"(lds), "s"(rz) : "memory");
}
DEV u32 mktag(u32 gen) { return gen * 0x9E3779B1u + 0x7F4A7C15u; }
DEV u32 ldsv(u32 a) { return *(volatile LAS u32*)a; }
DEV void ldsw(u32 a, u32 v) { asm volatile("s_mov_b64 exec, 1\n\tds_write_b32 %0, %1\n\ts_mov_b64 exec, -1" ::"v"(a), "v"(v) : "memory"); }
DEV void spin0(u32 a, u32 cur) {
  u32 tv, cnt;
  asm volatile("s_cmp_eq_u32 %3, 0\n\ts_cbranch_scc0 3f\n2:\n\t.subsection 1\n3:\n\ts_mov_b32 %1, 0x10000\n"
               "1:\n\tds_read_b32 %0, %2\n\ts_sub_u32 %1, %1, 1\n\ts_waitcnt lgkmcnt(0)\n\tv_cmp_ne_u32 vcc, 0, %0\n\ts_nop 1\n\ts_min_u32 vcc_lo, vcc_lo, %1\n\ts_cmp_eq_u32 vcc_lo, 0\n\ts_cbranch_scc1 2b\n\t"
               "s_sleep 1\n\ts_branch 1b\n\t.subsection 0"
               : "=&v"(tv), "=&s"(cnt) : "v"(a), "s"(cur) : "memory", "scc", "vcc");
}
DEV void wait0(u32 a) {
  u32 tv, ts, cnt;
  asm volatile("s_mov_b32 %2, 0x10000\n1:\n\tds_read_b32 %0, %3\n\ts_sub_u32 %2, %2, 1\n\ts_waitcnt lgkmcnt(0)\n\tv_readfirstlane_b32 %1, %0\n\ts_nop 0\n\t"
               "s_min_u32 %1, %1, %2\n\ts_cmp_eq_u32 %1, 0\n\ts_cbranch_scc1 2f\n\ts_sleep 1\n\ts_branch 1b\n2:"
               : "=&v"(tv), "=&s"(ts), "=&s"(cnt) : "v"(a) : "memory", "scc");
}
DEV void spin_zero4(u32 a) {
  u32 tv, cnt;
  const u32 al = a + (__builtin_amdgcn_mbcnt_lo(~0u, 0) & 3) * 4;
  asm volatile("s_mov_b32 %1, 0x10000\n1:\n\tds_read_b32 %0, %2\n\ts_sub_u32 %1, %1, 1\n\ts_waitcnt lgkmcnt(0)\n\tv_cmp_ne_u32 vcc, 0, %0\n\ts_nop 1\n\t"
               "s_min_u32 vcc_lo, vcc_lo, %1\n\ts_cmp_eq_u32 vcc_lo, 0\n\ts_cbranch_scc1 2f\n\ts_sleep 1\n\ts_branch 1b\n2:"
               : "=&v"(tv), "=&s"(cnt) : "v"(al) : "memory", "scc", "vcc");
}
template <int SL> DEV void poll_flags(rsrc_t rw, u32 off, u32 off2, u32 tag) {
#pragma unroll 1
  for (u32 i = 0; i < SPIN_CAP; i++) {
    const u32 a = ldc32(rw, off), b = ldc32(rw, off2);
    if (__all(a == tag && b == tag)) break;
    __builtin_amdgcn_s_sleep(SL);
  }
  ORDER();
}
DEV float fmul_(float a, float b) { float r; asm("v_mul_f32 %0, %1, %2" : "=v"(r) : "v"(a), "v"(b)); return r; }
DEV float fadd_(float a, float b) { float r; asm("v_add_f32 %0, %1, %2" : "=v"(r) : "v"(a), "v"(b)); return r; }
DEV float ffma_(float a, float b, float c) { float r; asm("v_fma_f32 %0, %1, %2, %3" : "=v"(r) : "v"(a), "v"(b), "v"(c)); return r; }
DEV float fnma_(float a, float b, float c) { float r; asm("v_fma_f32 %0, -%1, %2, %3" : "=v"(r) : "v"(a), "v"(b), "v"(c)); return r; }
struct cf { float x, y; };
DEV cf csq(cf a) { const float t = fmul_(a.x, a.y); return cf{fnma_(a.y, a.y, fmul_(a.x, a.x)), fadd_(t, t)}; }
DEV u32 pkc(cf a) { return pkhf(a.x, a.y); }
DEV cf cexp2(float lr, float rev) { float m = __builtin_amdgcn_exp2f(lr); const float f = __builtin_amdgcn_fractf(rev); float c = __builtin_amdgcn_cosf(f), s = __builtin_amdgcn_sinf(f);
  asm volatile("s_nop 0" : "+v"(c), "+v"(s), "+v"(m)); return cf{fmul_(m, c), fmul_(m, s)}; }
DEV u32 hmul(u32 a, u32 m) { u32 t, r;
  asm("v_pk_mul_f16 %0, %1, %2 op_sel_hi:[0,1]" : "=v"(t) : "v"(a), "v"(m));
  asm("v_pk_fma_f16 %0, %1, %2, %3 op_sel:[1,1,0] op_sel_hi:[1,0,1] neg_lo:[0,1,0]" : "=v"(r) : "v"(a), "v"(m), "v"(t)); return r; }
DEV u32 hmulc(u32 a, u32 m) { u32 t, r;
  asm("v_pk_mul_f16 %0, %1, %2 op_sel_hi:[0,1] neg_hi:[0,1]" : "=v"(t) : "v"(a), "v"(m));
  asm("v_pk_fma_f16 %0, %1, %2, %3 op_sel:[1,1,0] op_sel_hi:[1,0,1]" : "=v"(r) : "v"(a), "v"(m), "v"(t)); return r; }
DEV f2 pfma_x(f2 s, f2 b, f2 c) { f2 r; asm("v_pk_fma_f32 %0, %1, %2, %3 op_sel_hi:[0,1,1]" : "=v"(r) : "v"(s), "v"(b), "v"(c)); return r; }
DEV f2 pmul_y(f2 s, f2 b) { f2 r; asm("v_pk_mul_f32 %0, %1, %2 op_sel:[1,0]" : "=v"(r) : "v"(s), "v"(b)); return r; }
DEV f2 pfma_xx(f2 s, f2 b) { f2 r; asm("s_nop 0\n\tv_pk_fma_f32 %0, %1, %2, %1 op_sel_hi:[0,1,0]" : "=v"(r) : "v"(s), "v"(b)); return r; }
DEV f2 pmul(f2 a, f2 b) { f2 r; asm("s_nop 0\n\tv_pk_mul_f32 %0, %1, %2" : "=v"(r) : "v"(a), "v"(b)); return r; }
DEV void swap16(float& a, float& b) { auto r = __builtin_amdgcn_permlane16_swap(__float_as_uint(a), __float_as_uint(b), false, false); a = __uint_as_float(r[0]); b = __uint_as_float(r[1]); }
DEV void swap32(float& a, float& b) { auto r = __builtin_amdgcn_permlane32_swap(__float_as_uint(a), __float_as_uint(b), false, false); a = __uint_as_float(r[0]); b = __uint_as_float(r[1]); }

DEV void ln_unit(const Params& p, u32 sb, int pn, int lane) {
  const rsrc_t rx = mkr(p.x, L * D * 2), rw = mkr(p.ws, WS_BYTES);
  const int h = lane & 1, m = lane >> 1, row0 = 64 * (pn >> 2) + (pn & 3);
  __builtin_amdgcn_s_setprio(2);
  v2u xv[8][5]; float xf[8][20], s[16];
#pragma unroll
  for (int e = 0; e < 8; e++)
#pragma unroll
    for (int i = 0; i < 5; i++) xv[e][i] = __builtin_amdgcn_raw_buffer_load_b64(rx, (row0 + 32 * h) * 1280 + m * 8 + i * 256, e * 5120, 0);
  SCHED();
  __builtin_amdgcn_s_setprio(3);
#pragma unroll
  for (int e = 0; e < 8; e++) {
#pragma unroll
    for (int i = 0; i < 10; i++) { const u32 w = xv[e][i >> 1][i & 1]; xf[e][2 * i] = bf2f(w); xf[e][2 * i + 1] = bfhi(w); }
    float a = fadd_(xf[e][0], xf[e][1]), q = fmul_(xf[e][0], xf[e][0]);
#pragma unroll
    for (int i = 1; i < 20; i++) { if (i > 1) a = fadd_(a, xf[e][i]); asm("v_fmac_f32 %0, %1, %1" : "+v"(q) : "v"(xf[e][i])); }
    s[e] = a; s[8 + e] = q;
  }
#pragma unroll
  for (int e = 0; e < 16; e++) asm volatile("" : "+v"(s[e]));
  asm volatile("s_nop 1");
  wait0(sb + LD_CTL + C_FULL + 8);
  const u32 tag = mktag(rfl(ldsv(sb + LD_CTL + C_TAG)));
  float z[4];
#pragma unroll
  for (int k = 0; k < 4; k++) {
    swap16(s[4 * k], s[4 * k + 1]); swap16(s[4 * k + 2], s[4 * k + 3]);
    float a = fadd_(s[4 * k], s[4 * k + 1]), b = fadd_(s[4 * k + 2], s[4 * k + 3]);
    asm volatile("s_nop 1" : "+v"(a), "+v"(b));
    swap32(a, b); z[k] = fadd_(a, b);
  }
#define DPPADD(C) asm volatile("s_nop 1\n\tv_add_f32_dpp %0, %0, %0 " C " row_mask:0xf bank_mask:0xf\n\tv_add_f32_dpp %1, %1, %1 " C " row_mask:0xf bank_mask:0xf\n\t" \
    "v_add_f32_dpp %2, %2, %2 " C " row_mask:0xf bank_mask:0xf\n\tv_add_f32_dpp %3, %3, %3 " C " row_mask:0xf bank_mask:0xf" : "+v"(z[0]), "+v"(z[1]), "+v"(z[2]), "+v"(z[3]))
  DPPADD("quad_perm:[2,3,0,1]"); DPPADD("row_ror:4"); DPPADD("row_ror:8");
  float rs[8], nb[8];
#pragma unroll
  for (int k = 0; k < 2; k++) {
    const float mean = fmul_(z[k], 1.f / D), var = fmaxf(fnma_(mean, mean, fmul_(z[2 + k], 1.f / D)), 0.f);
    const float mb = rbf(mean), sd = rbf(__builtin_amdgcn_sqrtf(rbf(rbf(var) + 1e-5f)));
    float r = __builtin_amdgcn_rcpf(sd);
    asm volatile("s_nop 0" : "+v"(r));
    r = ffma_(fnma_(sd, r, 1.f), r, r);
    if ((lane & 15) < 2) stc128(v4u{__float_as_uint(mb), tag, __float_as_uint(r), tag}, rw, WS_STAT + (row0 + 32 * h + 4 * (4 * k + (lane >> 4))) * 16, 0);
    float t[2] = {r, fmul_(-mb, r)};
#pragma unroll
    for (int w = 0; w < 2; w++) {
      float a = t[w], c = t[w];
      asm volatile("s_nop 1" : "+v"(a), "+v"(c));
      swap32(a, c); float b = a, d = c; swap16(a, b); swap16(c, d);
      float* o = w ? nb : rs; o[4 * k] = a; o[4 * k + 1] = b; o[4 * k + 2] = c; o[4 * k + 3] = d;
    }
  }
#pragma unroll
  for (int i = 0; i < 5; i++)
#pragma unroll
    for (int c = 0; c < 4; c++) {
      float y[8];
#pragma unroll
      for (int e = 0; e < 8; e++) y[e] = ffma_(xf[e][4 * i + c], rs[e], nb[e]);
      stc128(v4u{pkhf(y[0], y[1]), pkhf(y[2], y[3]), pkhf(y[4], y[5]), pkhf(y[6], y[7])}, rw, WS_XT + lane * 16, pn * 20480 + (i * 4 + c) * 1024);
    }
  waitv<0>();
  if (lane < 8) stc32(tag, rw, WS_LFLAG + lane * 2048 + pn * 4);
}

template <bool CJ> DEV void grp8(u32* v, u32 a, u32 b, u32 c) {
#define HM(x, y) (CJ ? hmulc(x, y) : hmul(x, y))
  v[1] = HM(v[0], a); v[2] = HM(v[0], b); v[3] = HM(v[1], b); v[4] = HM(v[0], c); v[5] = HM(v[1], c); v[6] = HM(v[2], c); v[7] = HM(v[3], c);
#undef HM
}
DEV void wr4(u32 a, const u32* v) {
  asm volatile("ds_write2_b32 %0, %1, %2 offset1:68\n\tds_write2_b32 %0, %3, %4 offset0:136 offset1:204" ::"v"(a), "v"(v[0]), "v"(v[1]), "v"(v[2]), "v"(v[3]) : "memory");
}
DEV void rd4(v4u (&f)[4], u32 a) {
  asm volatile("ds_read_b128 %0, %4\n\tds_read_b128 %1, %4 offset:64\n\tds_read_b128 %2, %4 offset:128\n\tds_read_b128 %3, %4 offset:192"
               : "=&v"(f[0]), "=&v"(f[1]), "=&v"(f[2]), "=&v"(f[3]) : "v"(a) : "memory");
}
template <int N> DEV void wait4(v4u (&f)[4], v4u (&h)[4]) {
  asm volatile("s_waitcnt lgkmcnt(%8)" : "+v"(f[0]), "+v"(f[1]), "+v"(f[2]), "+v"(f[3]), "+v"(h[0]), "+v"(h[1]), "+v"(h[2]), "+v"(h[3]) : "i"(N));
}
DEV void conv_wave(const Params& p, u32 sb, int slot, int ch, int lane, int wv, bool g1, int ts, int cj ST_ARG) {
  const u32 UB = sb + LD_K + slot * K_BYTES, KB = sb + LD_R + 2 * WSLOT + slot * 4608, CTL = sb + LD_CTL;
  const int g = lane >> 4, q = (lane >> 2) & 3, pp = lane & 3, j = lane & 15, b = blockIdx.x;
  const rsrc_t rw = mkr(p.ws, WS_BYTES);
  if (AUX_STAGE >= 0) wait0(CTL + C_FULL + AUX_STAGE * 32);
  __builtin_amdgcn_s_setprio(3);
  const bool retile = !((b >> 3) & 1);
  const int wu = ((b >> 4) * 8 + (b & 7)) * 3 + slot;
  if (retile) {
    u32 vo[5];
#pragma unroll
    for (int k = 0; k < 5; k++) { const int n = k * 64 + lane; vo[k] = (n / 40) * 1280 + (n % 40) * 16 - (k & 3) * 1024; }
    const v4u rd = mkrs(p.wd, F * D * 2);
    const u32 so = (wu >> 1) * 10240 + (wu & 1) * 640;
    dma4(rd, vo[0], vo[1], vo[2], vo[3], so, UB); dma1(rd, vo[4], so, UB + 4096);
  }
  u32 la[3], ai[3], cr[3], ci[3];
#pragma unroll
  for (int c = 0; c < 3; c++) { const int o = ch * NS + 64 * c + lane; la[c] = p.lar[o]; ai[c] = p.aim[o]; cr[c] = p.cre[o]; ci[c] = p.cim[o]; }
  const u32 ldt = p.ldt[ch];
#pragma unroll
  for (int c = 0; c < 3; c++) asm volatile("" : "+v"(la[c]), "+v"(ai[c]), "+v"(cr[c]), "+v"(ci[c]));
  if (retile) {
    waitv<0>();
    __builtin_amdgcn_s_setprio(3);
    const u32 ra = UB + q * 640 + (80 * g + 20 * pp) * 2;
    const u32 wo = WS_WDF + ((4 * (wu & 1) + g) * 48 * 5) * 1024 + j * 16, s2 = (wu >> 3) * 5120 + ((wu >> 1) & 3) * 256;
#pragma unroll
    for (int k = 0; k < 5; k++) stc128(trd2(ra + k * 8, ra + k * 8 + 2560), rw, wo + k * 1024, s2);
  }
  __builtin_amdgcn_s_setprio(0);
  u32 tag = 0;
  const float dt = rbf(__expf(bf2f(ldt)));
  u32 mx = 0;
#pragma unroll
  for (int c = 0; c < 3; c++) mx = max(mx, max(cr[c] & 0x7fffu, ci[c] & 0x7fffu));
  mx = max(mx, (u32)__builtin_amdgcn_update_dpp(0, (int)mx, 0x111, 0xf, 0xf, false));
  mx = max(mx, (u32)__builtin_amdgcn_update_dpp(0, (int)mx, 0x112, 0xf, 0xf, false));
  mx = max(mx, (u32)__builtin_amdgcn_update_dpp(0, (int)mx, 0x114, 0xf, 0xf, false));
  mx = max(mx, (u32)__builtin_amdgcn_update_dpp(0, (int)mx, 0x118, 0xf, 0xf, false));
  mx = max(max((u32)__builtin_amdgcn_readlane(mx, 15), (u32)__builtin_amdgcn_readlane(mx, 31)), max((u32)__builtin_amdgcn_readlane(mx, 47), (u32)__builtin_amdgcn_readlane(mx, 63)));
  STAMP(16);
  const u32 ex = min(max(mx >> 7, 1u), 252u);
  const float cscale = __uint_as_float((253u - ex) << 23);
  const float oscale = 2.f * dt * __uint_as_float((ex + 1u) << 23);
  u32 G[3][57];
#pragma unroll
  for (int c = 0; c < 3; c++) {
    const float zr = fmul_(fmul_(rbf(__expf(bf2f(la[c]))), dt), -1.44269504089f), zi = fmul_(fmul_(bf2f(ai[c]), dt), 0.15915494309f);
    const cf P0 = cexp2(zr, zi), P4 = cexp2(fmul_(zr, 16.f), fmul_(zi, 16.f)), P8 = cexp2(fmul_(zr, 256.f), fmul_(zi, 256.f));
    const cf P1 = csq(P0), P2 = csq(P1), P3 = csq(P2), P5 = csq(P4), P6 = csq(P5), P7 = csq(P6), P9 = csq(P8), P10 = csq(P9), P11 = csq(P10);
    const u32 M0 = pkc(P0), M1 = pkc(P1), M2 = pkc(P2), M3 = pkc(P3), M4 = pkc(P4), M5 = pkc(P5), M7 = pkc(P7), M8 = pkc(P8), M9 = pkc(P9);
    u32* v = G[c];
    const float cx = fmul_(bf2f(cr[c]), cscale), cy = fmul_(bf2f(ci[c]), -cscale);
    v[0] = pkhf(cx, cy); grp8<true>(v, M3, M4, M5);
    v[8] = pkhf(ffma_(cy, P6.y, fmul_(cx, P6.x)), fnma_(cx, P6.y, fmul_(cy, P6.x))); grp8<true>(v + 8, M3, M4, M5);
    v[16] = 0x00003c00u; v[17] = M0; v[18] = M1; v[19] = hmul(M0, M1); v[20] = M2; v[21] = hmul(M0, M2); v[22] = hmul(M1, M2); v[23] = hmul(v[19], M2);
    v[24] = M7; grp8<false>(v + 24, M0, M1, M2);
    v[32] = M8; grp8<false>(v + 32, M1, M2, M7);
    v[40] = M9; grp8<false>(v + 40, M2, M7, M8);
    v[48] = pkc(P10); grp8<false>(v + 48, M7, M8, M9);
    v[56] = pkc(P11);
#pragma unroll
    for (int k = 0; k < 57; k++) asm volatile("" : "+v"(v[k]));
    if (c == 0) {
      wait0(CTL + C_FULL + 8);
      tag = mktag(rfl(ldsv(CTL + C_TAG)));
      if (retile) { waitv<0>(); if (lane < 8) stc32(tag, rw, WS_WFLAG + lane * 2048 + wu * 4); }
    }
  }
  STAMP(22);
  __builtin_amdgcn_s_setprio(KL_PRIO);
  wait0(CTL + C_FULL + 9 * 32);
  STAMP(17);
  f4 kd[3] = {f4{0, 0, 0, 0}, f4{0, 0, 0, 0}, f4{0, 0, 0, 0}};
  {
    const u32 rb = KB + j * 272 + g * 16;
    u32 wa[4] = {KB + lane * 4, KB + lane * 4 + 1088, KB + lane * 4 + 2176, KB + lane * 4 + 3264};
    asm volatile("" : "+v"(kd[0]), "+v"(kd[1]), "+v"(kd[2]), "+v"(wa[0]), "+v"(wa[1]), "+v"(wa[2]), "+v"(wa[3]));
    v4u Fr[12][4];
#pragma unroll
    for (int ps = 0; ps <= 12; ps++) {
      if (ps < 12) {
        const int c = ps >> 2, t = ps & 3;
        wr4(wa[0], G[c] + 16 * t); wr4(wa[1], G[c] + 16 * t + 4);
        if (t < 3) { wr4(wa[2], G[c] + 16 * t + 8); wr4(wa[3], G[c] + 16 * t + 12); }
        else asm volatile("ds_write_b32 %0, %1" ::"v"(wa[2]), "v"(G[c][56]) : "memory");
        asm volatile("ds_read_b128 %0, %4\n\tds_read_b128 %1, %4 offset:64\n\tds_read_b128 %2, %4 offset:128\n\tds_read_b128 %3, %4 offset:192"
                     : "=&v"(Fr[ps][0]), "=&v"(Fr[ps][1]), "=&v"(Fr[ps][2]), "=&v"(Fr[ps][3]) : "v"(rb) : "memory");
      }
      if (ps > 0) {
        const int pv = ps - 1;
        if (ps < 12) asm volatile("s_waitcnt lgkmcnt(9)" : "+v"(Fr[pv][0]), "+v"(Fr[pv][1]), "+v"(Fr[pv][2]), "+v"(Fr[pv][3]));
        else asm volatile("s_waitcnt lgkmcnt(0)" : "+v"(Fr[pv][0]), "+v"(Fr[pv][1]), "+v"(Fr[pv][2]), "+v"(Fr[pv][3]));
        if (pv & 3) {
#pragma unroll
          for (int ks = 0; ks < 4; ks++) mma_hf(kd[(pv & 3) - 1], Fr[pv][ks], Fr[pv & ~3][ks]);
        }
      }
    }
    asm volatile("s_nop 7" : "+v"(kd[0]), "+v"(kd[1]), "+v"(kd[2]));
  }
  STAMP(9);
  {
    const u32 K0 = KB + 32;
    const u32 a00 = pkhf(kd[1][0], kd[1][0]), a11 = pkhf(kd[1][1], kd[1][1]), a22 = pkhf(kd[1][2], kd[1][2]), a33 = pkhf(kd[1][3], kd[1][3]);
    const u32 a12 = pkhf(kd[1][1], kd[1][2]), a01 = pkhf(kd[1][0], kd[1][1]), a23 = pkhf(kd[1][2], kd[1][3]);
    v2u bb[4];
#pragma unroll
    for (int r = 0; r < 4; r++) { bb[r].x = pkhf(kd[2][r], kd[2][r]); bb[r].y = bb[r].x; }
    const v2u r0 = {pkhf(kd[0][0], kd[0][1]), pkhf(kd[0][2], kd[0][3])}, w1 = {a00, a12}, w2a = {a00, a01}, w2b = {a22, a23};
    const v4u z4 = {0, 0, 0, 0};
    const u32 p3 = K0 + 2 * (1024 + 512 * g + 8 * j) - 8, p2 = K0 + 2 * (256 * g + 8 * j) - 4, p1 = K0 + 2 * (256 + 128 * g + 8 * j) - 2;
    const u32 p0 = K0 + 2 * (128 * (g >> 1) + 8 * j + 4 * (g & 1)), pz = KB + lane * 16;
    asm volatile(
      "s_mov_b32 exec_hi, 1\n\t"
      "ds_write2_b64 %0, %5, %5 offset1:1\n\t"
      "s_mov_b32 exec_hi, 0\n\t"
      "ds_write2_b64 %0, %6, %6 offset0:32 offset1:33\n\t"
      "ds_write2_b64 %0, %7, %7 offset0:64 offset1:65\n\t"
      "ds_write2_b64 %0, %8, %8 offset0:96 offset1:97\n\t"
      "s_mov_b32 exec_lo, 0x55555555\n\t"
      "ds_write_b16 %0, %9 offset:16\n\tds_write_b16 %0, %10 offset:272\n\tds_write_b16 %0, %11 offset:528\n\tds_write_b16 %0, %12 offset:784\n\t"
      "s_mov_b32 exec_lo, 0\n\ts_mov_b32 exec_hi, -1\n\t"
      "ds_write_b32 %1, %13\n\tds_write_b64 %1, %17 offset:4\n\tds_write_b32 %1, %14 offset:12\n\t"
      "ds_write_b32 %1, %15 offset:256\n\tds_write_b64 %1, %18 offset:260\n\tds_write_b32 %1, %16 offset:268\n\t"
      "s_mov_b32 exec_lo, -1\n\ts_mov_b32 exec_hi, 0\n\t"
      "ds_write_b16 %2, %13\n\tds_write_b64 %2, %19 offset:2\n\tds_write_b32 %2, %15 offset:10\n\tds_write_b16 %2, %16 offset:14\n\t"
      "s_mov_b32 exec_lo, 3\n\t"
      "ds_write_b128 %4, %21\n\t"
      "s_mov_b64 exec, -1\n\t"
      "ds_write_b64 %3, %20"
      ::"v"(p3), "v"(p2), "v"(p1), "v"(p0), "v"(pz), "v"(bb[0]), "v"(bb[1]), "v"(bb[2]), "v"(bb[3]), "v"(bb[0].x), "v"(bb[1].x), "v"(bb[2].x), "v"(bb[3].x),
        "v"(a00), "v"(a11), "v"(a22), "v"(a33), "v"(w2a), "v"(w2b), "v"(w1), "v"(r0), "v"(z4) : "memory");
  }
  v4u A[16];
  {
    const u32 ab = KB + 32 + 2 * (-12 + 4 * (8 * g + 7 - q) + 4 * pp);
#pragma unroll
    for (int s = 0; s < 16; s++) A[s] = trd2(ab + 256 * s, ab + 256 * s - 32);
  }
  ldsw(CTL + C_KDONE + slot * 4, 0);
  STAMP(13);
  wait0(CTL + C_UFULL);
  wait0(CTL + C_DONE + 160 + slot * 4);
  __builtin_amdgcn_s_setprio(CONV_PRIO);
  STAMP(18);
  f4 acc[4][8]; v4u B[9]; float y[4], c[2], cv[3], pc[3] = {0.f, 0.f, 0.f};
  const u32 ub = UB + 128 + 2 * (4 * j - 4 - 8 * g) - 64, srcl = (lane == 0 ? 63 : (g > 0 ? lane - 16 : lane + 47)) * 4;
  const u32 zo = WS_ZT + (g & 1) * 512 + j * 32 + (g >> 1) * 16;
  const u64 m63 = 1ull << 63;
  u32 ut[4] = {ub, ub + 1152, ub + 2304, ub + 3456};
  asm volatile("" : "+v"(ut[0]), "+v"(ut[1]), "+v"(ut[2]), "+v"(ut[3]));
#define LDB(k, tau, q) do { const u32 a_ = ut[tau] + 64 * (q); const v2u x_ = lds64(a_), y_ = lds64(a_ + 8); B[k] = v4u{x_.x, x_.y, y_.x, y_.y}; SCHED(); } while (0)
#define VADD(d, a_, b_) do { float t_; asm volatile("v_add_f32 %0, %1, %2" : "=v"(t_) : "v"(a_), "v"(b_)); d = t_; } while (0)
#define VMULS(d) asm volatile("v_mul_f32 %0, %0, %1" : "+v"(d) : "v"(oscale))
#define VSEL(d, a_, b_) asm volatile("v_cndmask_b32 %0, %1, %2, %3" : "=v"(d) : "v"(a_), "v"(b_), "s"(m63))
#define VPK(d, a_, b_) do { u32 t_; asm volatile("v_cvt_pk_bf16_f32 %0, %1, %2" : "=v"(t_) : "v"(a_), "v"(b_)); d = t_; } while (0)
#define NOPS() asm volatile("s_nop 3")
#define BPERM() asm volatile("ds_bpermute_b32 %0, %3, %0\n\tds_bpermute_b32 %1, %3, %1\n\tds_bpermute_b32 %2, %3, %2" : "+v"(cv[0]), "+v"(cv[1]), "+v"(cv[2]) : "v"(srcl) : "memory")
#define WAITB(n) asm volatile("s_waitcnt lgkmcnt(" #n ")" : "+v"(cv[0]), "+v"(cv[1]), "+v"(cv[2]) :: "memory")
#define ZSTORE(T) do { asm volatile("s_nop 1\n\tv_permlane16_swap_b32 %0, %2\n\tv_permlane16_swap_b32 %1, %3" : "+v"(zx0), "+v"(zy0), "+v"(zx1), "+v"(zy1)); \
    if (!ABL_Z) stc128(v4u{zx0, zy0, zx1, zy1}, rw, zo, ch * 4096 + (T) * 512); } while (0)
  u32 zx0, zy0, zx1, zy1;
  LDB(0, 0, 0);
  LDB(1, 1, 0);
  LDB(2, 2, 0);
  LDB(3, 3, 0);
  LDB(4, 0, 1);
  LDB(5, 1, 1);
  LDB(6, 2, 1);
  LDB(7, 3, 1);
  LDB(8, 0, 2);
  mma_hf0(acc[0][7], A[15], B[0]);
  mma_hf0(acc[0][6], A[13], B[0]);
  mma_hf0(acc[0][5], A[11], B[0]);
  mma_hf0(acc[0][4], A[9], B[0]);
  mma_hf0(acc[0][3], A[7], B[0]);
  mma_hf0(acc[0][2], A[5], B[0]);
  mma_hf0(acc[0][1], A[3], B[0]);
  mma_hf0(acc[0][0], A[1], B[0]);
  LDB(0, 1, 2);
  mma_hf0(acc[1][7], A[15], B[1]);
  mma_hf0(acc[1][6], A[13], B[1]);
  mma_hf0(acc[1][5], A[11], B[1]);
  mma_hf0(acc[1][4], A[9], B[1]);
  mma_hf0(acc[1][3], A[7], B[1]);
  mma_hf0(acc[1][2], A[5], B[1]);
  mma_hf0(acc[1][1], A[3], B[1]);
  mma_hf0(acc[1][0], A[1], B[1]);
  LDB(1, 2, 2);
  mma_hf0(acc[2][7], A[15], B[2]);
  mma_hf0(acc[2][6], A[13], B[2]);
  mma_hf0(acc[2][5], A[11], B[2]);
  mma_hf0(acc[2][4], A[9], B[2]);
  mma_hf0(acc[2][3], A[7], B[2]);
  mma_hf0(acc[2][2], A[5], B[2]);
  mma_hf0(acc[2][1], A[3], B[2]);
  mma_hf0(acc[2][0], A[1], B[2]);
  LDB(2, 3, 2);
  mma_hf0(acc[3][7], A[15], B[3]);
  mma_hf0(acc[3][6], A[13], B[3]);
  mma_hf0(acc[3][5], A[11], B[3]);
  mma_hf0(acc[3][4], A[9], B[3]);
  mma_hf0(acc[3][3], A[7], B[3]);
  mma_hf0(acc[3][2], A[5], B[3]);
  mma_hf0(acc[3][1], A[3], B[3]);
  mma_hf0(acc[3][0], A[1], B[3]);
  LDB(3, 0, 3);
  mma_hf(acc[0][7], A[14], B[4]);
  mma_hf(acc[0][6], A[12], B[4]);
  mma_hf(acc[0][5], A[10], B[4]);
  mma_hf(acc[0][4], A[8], B[4]);
  mma_hf(acc[0][3], A[6], B[4]);
  mma_hf(acc[0][2], A[4], B[4]);
  mma_hf(acc[0][1], A[2], B[4]);
  mma_hf(acc[0][0], A[0], B[4]);
  LDB(4, 1, 3);
  mma_hf(acc[1][7], A[14], B[5]);
  mma_hf(acc[1][6], A[12], B[5]);
  mma_hf(acc[1][5], A[10], B[5]);
  mma_hf(acc[1][4], A[8], B[5]);
  mma_hf(acc[1][3], A[6], B[5]);
  mma_hf(acc[1][2], A[4], B[5]);
  mma_hf(acc[1][1], A[2], B[5]);
  mma_hf(acc[1][0], A[0], B[5]);
  LDB(5, 2, 3);
  mma_hf(acc[2][7], A[14], B[6]);
  mma_hf(acc[2][6], A[12], B[6]);
  mma_hf(acc[2][5], A[10], B[6]);
  mma_hf(acc[2][4], A[8], B[6]);
  mma_hf(acc[2][3], A[6], B[6]);
  mma_hf(acc[2][2], A[4], B[6]);
  mma_hf(acc[2][1], A[2], B[6]);
  mma_hf(acc[2][0], A[0], B[6]);
  LDB(6, 3, 3);
  mma_hf(acc[3][7], A[14], B[7]);
  mma_hf(acc[3][6], A[12], B[7]);
  mma_hf(acc[3][5], A[10], B[7]);
  mma_hf(acc[3][4], A[8], B[7]);
  mma_hf(acc[3][3], A[6], B[7]);
  mma_hf(acc[3][2], A[4], B[7]);
  mma_hf(acc[3][1], A[2], B[7]);
  mma_hf(acc[3][0], A[0], B[7]);
  LDB(7, 0, 4);
  mma_hf(acc[0][7], A[13], B[8]);
  VADD(y[1], acc[0][0][1], acc[1][0][0]);
  VADD(y[2], acc[0][0][2], acc[1][0][1]);
  mma_hf(acc[0][6], A[11], B[8]);
  VADD(y[3], acc[0][0][3], acc[1][0][2]);
  VADD(c[0], acc[1][0][3], acc[2][0][2]);
  mma_hf(acc[0][5], A[9], B[8]);
  VADD(y[2], y[2], acc[2][0][0]);
  VADD(y[3], y[3], acc[2][0][1]);
  mma_hf(acc[0][4], A[7], B[8]);
  NOPS();
  VADD(c[0], c[0], acc[3][0][1]);
  mma_hf(acc[0][3], A[5], B[8]);
  VADD(c[1], acc[2][0][3], acc[3][0][2]);
  VADD(y[3], y[3], acc[3][0][0]);
  mma_hf(acc[0][2], A[3], B[8]);
  VSEL(cv[0], c[0], pc[0]);
  mma_hf(acc[0][1], A[1], B[8]);
  VSEL(cv[1], c[1], pc[1]);
  LDB(8, 1, 4);
  mma_hf(acc[1][7], A[13], B[0]);
  VSEL(cv[2], acc[3][0][3], pc[2]);
  mma_hf(acc[1][6], A[11], B[0]);
  BPERM(); pc[0] = c[0]; pc[1] = c[1]; pc[2] = acc[3][0][3];
  mma_hf(acc[1][5], A[9], B[0]);
  VMULS(y[3]);
  mma_hf(acc[1][4], A[7], B[0]);
  mma_hf(acc[1][3], A[5], B[0]);
  mma_hf(acc[1][2], A[3], B[0]);
  mma_hf(acc[1][1], A[1], B[0]);
  LDB(0, 2, 4);
  mma_hf(acc[2][7], A[13], B[1]);
  mma_hf(acc[2][6], A[11], B[1]);
  mma_hf(acc[2][5], A[9], B[1]);
  WAITB(1);
  VADD(y[0], acc[0][0][0], cv[0]);
  mma_hf(acc[2][4], A[7], B[1]);
  VADD(y[1], y[1], cv[1]);
  mma_hf(acc[2][3], A[5], B[1]);
  VADD(y[2], y[2], cv[2]);
  mma_hf(acc[2][2], A[3], B[1]);
  VMULS(y[0]);
  mma_hf(acc[2][1], A[1], B[1]);
  VMULS(y[1]);
  LDB(1, 3, 4);
  mma_hf(acc[3][7], A[13], B[2]);
  VMULS(y[2]);
  mma_hf(acc[3][6], A[11], B[2]);
  VPK(zx0, y[0], y[1]);
  mma_hf(acc[3][5], A[9], B[2]);
  VPK(zy0, y[2], y[3]);
  mma_hf(acc[3][4], A[7], B[2]);
  mma_hf(acc[3][3], A[5], B[2]);
  mma_hf(acc[3][2], A[3], B[2]);
  mma_hf(acc[3][1], A[1], B[2]);
  LDB(2, 0, 5);
  mma_hf(acc[0][7], A[12], B[3]);
  mma_hf(acc[0][6], A[10], B[3]);
  mma_hf(acc[0][5], A[8], B[3]);
  mma_hf(acc[0][4], A[6], B[3]);
  mma_hf(acc[0][3], A[4], B[3]);
  mma_hf(acc[0][2], A[2], B[3]);
  mma_hf(acc[0][1], A[0], B[3]);
  LDB(3, 1, 5);
  mma_hf(acc[1][7], A[12], B[4]);
  mma_hf(acc[1][6], A[10], B[4]);
  mma_hf(acc[1][5], A[8], B[4]);
  mma_hf(acc[1][4], A[6], B[4]);
  mma_hf(acc[1][3], A[4], B[4]);
  mma_hf(acc[1][2], A[2], B[4]);
  mma_hf(acc[1][1], A[0], B[4]);
  LDB(4, 2, 5);
  mma_hf(acc[2][7], A[12], B[5]);
  mma_hf(acc[2][6], A[10], B[5]);
  mma_hf(acc[2][5], A[8], B[5]);
  mma_hf(acc[2][4], A[6], B[5]);
  mma_hf(acc[2][3], A[4], B[5]);
  mma_hf(acc[2][2], A[2], B[5]);
  mma_hf(acc[2][1], A[0], B[5]);
  LDB(5, 3, 5);
  mma_hf(acc[3][7], A[12], B[6]);
  mma_hf(acc[3][6], A[10], B[6]);
  mma_hf(acc[3][5], A[8], B[6]);
  mma_hf(acc[3][4], A[6], B[6]);
  mma_hf(acc[3][3], A[4], B[6]);
  mma_hf(acc[3][2], A[2], B[6]);
  mma_hf(acc[3][1], A[0], B[6]);
  LDB(6, 0, 6);
  mma_hf(acc[0][7], A[11], B[7]);
  VADD(y[1], acc[0][1][1], acc[1][1][0]);
  VADD(y[2], acc[0][1][2], acc[1][1][1]);
  mma_hf(acc[0][6], A[9], B[7]);
  VADD(y[3], acc[0][1][3], acc[1][1][2]);
  VADD(c[0], acc[1][1][3], acc[2][1][2]);
  mma_hf(acc[0][5], A[7], B[7]);
  VADD(y[2], y[2], acc[2][1][0]);
  VADD(y[3], y[3], acc[2][1][1]);
  mma_hf(acc[0][4], A[5], B[7]);
  NOPS();
  VADD(c[0], c[0], acc[3][1][1]);
  mma_hf(acc[0][3], A[3], B[7]);
  VADD(c[1], acc[2][1][3], acc[3][1][2]);
  VADD(y[3], y[3], acc[3][1][0]);
  mma_hf(acc[0][2], A[1], B[7]);
  VSEL(cv[0], c[0], pc[0]);
  VSEL(cv[1], c[1], pc[1]);
  LDB(7, 1, 6);
  mma_hf(acc[1][7], A[11], B[8]);
  VSEL(cv[2], acc[3][1][3], pc[2]);
  mma_hf(acc[1][6], A[9], B[8]);
  BPERM(); pc[0] = c[0]; pc[1] = c[1]; pc[2] = acc[3][1][3];
  mma_hf(acc[1][5], A[7], B[8]);
  VMULS(y[3]);
  mma_hf(acc[1][4], A[5], B[8]);
  mma_hf(acc[1][3], A[3], B[8]);
  mma_hf(acc[1][2], A[1], B[8]);
  LDB(8, 2, 6);
  mma_hf(acc[2][7], A[11], B[0]);
  mma_hf(acc[2][6], A[9], B[0]);
  mma_hf(acc[2][5], A[7], B[0]);
  mma_hf(acc[2][4], A[5], B[0]);
  WAITB(1);
  VADD(y[0], acc[0][1][0], cv[0]);
  mma_hf(acc[2][3], A[3], B[0]);
  VADD(y[1], y[1], cv[1]);
  mma_hf(acc[2][2], A[1], B[0]);
  VADD(y[2], y[2], cv[2]);
  LDB(0, 3, 6);
  mma_hf(acc[3][7], A[11], B[1]);
  VMULS(y[0]);
  mma_hf(acc[3][6], A[9], B[1]);
  VMULS(y[1]);
  mma_hf(acc[3][5], A[7], B[1]);
  VMULS(y[2]);
  mma_hf(acc[3][4], A[5], B[1]);
  VPK(zx1, y[0], y[1]);
  mma_hf(acc[3][3], A[3], B[1]);
  VPK(zy1, y[2], y[3]);
  mma_hf(acc[3][2], A[1], B[1]);
  ZSTORE(0);
  LDB(1, 0, 7);
  mma_hf(acc[0][7], A[10], B[2]);
  mma_hf(acc[0][6], A[8], B[2]);
  mma_hf(acc[0][5], A[6], B[2]);
  mma_hf(acc[0][4], A[4], B[2]);
  mma_hf(acc[0][3], A[2], B[2]);
  mma_hf(acc[0][2], A[0], B[2]);
  LDB(2, 1, 7);
  mma_hf(acc[1][7], A[10], B[3]);
  mma_hf(acc[1][6], A[8], B[3]);
  mma_hf(acc[1][5], A[6], B[3]);
  mma_hf(acc[1][4], A[4], B[3]);
  mma_hf(acc[1][3], A[2], B[3]);
  mma_hf(acc[1][2], A[0], B[3]);
  LDB(3, 2, 7);
  mma_hf(acc[2][7], A[10], B[4]);
  mma_hf(acc[2][6], A[8], B[4]);
  mma_hf(acc[2][5], A[6], B[4]);
  mma_hf(acc[2][4], A[4], B[4]);
  mma_hf(acc[2][3], A[2], B[4]);
  mma_hf(acc[2][2], A[0], B[4]);
  LDB(4, 3, 7);
  mma_hf(acc[3][7], A[10], B[5]);
  mma_hf(acc[3][6], A[8], B[5]);
  mma_hf(acc[3][5], A[6], B[5]);
  mma_hf(acc[3][4], A[4], B[5]);
  mma_hf(acc[3][3], A[2], B[5]);
  mma_hf(acc[3][2], A[0], B[5]);
  LDB(5, 0, 8);
  mma_hf(acc[0][7], A[9], B[6]);
  VADD(y[1], acc[0][2][1], acc[1][2][0]);
  VADD(y[2], acc[0][2][2], acc[1][2][1]);
  mma_hf(acc[0][6], A[7], B[6]);
  VADD(y[3], acc[0][2][3], acc[1][2][2]);
  VADD(c[0], acc[1][2][3], acc[2][2][2]);
  mma_hf(acc[0][5], A[5], B[6]);
  VADD(y[2], y[2], acc[2][2][0]);
  VADD(y[3], y[3], acc[2][2][1]);
  mma_hf(acc[0][4], A[3], B[6]);
  NOPS();
  VADD(c[0], c[0], acc[3][2][1]);
  mma_hf(acc[0][3], A[1], B[6]);
  VADD(c[1], acc[2][2][3], acc[3][2][2]);
  VADD(y[3], y[3], acc[3][2][0]);
  LDB(6, 1, 8);
  mma_hf(acc[1][7], A[9], B[7]);
  VSEL(cv[0], c[0], pc[0]);
  mma_hf(acc[1][6], A[7], B[7]);
  VSEL(cv[1], c[1], pc[1]);
  mma_hf(acc[1][5], A[5], B[7]);
  VSEL(cv[2], acc[3][2][3], pc[2]);
  mma_hf(acc[1][4], A[3], B[7]);
  BPERM(); pc[0] = c[0]; pc[1] = c[1]; pc[2] = acc[3][2][3];
  mma_hf(acc[1][3], A[1], B[7]);
  VMULS(y[3]);
  LDB(7, 2, 8);
  mma_hf(acc[2][7], A[9], B[8]);
  mma_hf(acc[2][6], A[7], B[8]);
  mma_hf(acc[2][5], A[5], B[8]);
  mma_hf(acc[2][4], A[3], B[8]);
  mma_hf(acc[2][3], A[1], B[8]);
  LDB(8, 3, 8);
  mma_hf(acc[3][7], A[9], B[0]);
  mma_hf(acc[3][6], A[7], B[0]);
  WAITB(2);
  VADD(y[0], acc[0][2][0], cv[0]);
  mma_hf(acc[3][5], A[5], B[0]);
  VADD(y[1], y[1], cv[1]);
  mma_hf(acc[3][4], A[3], B[0]);
  VADD(y[2], y[2], cv[2]);
  mma_hf(acc[3][3], A[1], B[0]);
  VMULS(y[0]);
  LDB(0, 0, 9);
  mma_hf(acc[0][7], A[8], B[1]);
  VMULS(y[1]);
  mma_hf(acc[0][6], A[6], B[1]);
  VMULS(y[2]);
  mma_hf(acc[0][5], A[4], B[1]);
  VPK(zx0, y[0], y[1]);
  mma_hf(acc[0][4], A[2], B[1]);
  VPK(zy0, y[2], y[3]);
  mma_hf(acc[0][3], A[0], B[1]);
  LDB(1, 1, 9);
  mma_hf(acc[1][7], A[8], B[2]);
  mma_hf(acc[1][6], A[6], B[2]);
  mma_hf(acc[1][5], A[4], B[2]);
  mma_hf(acc[1][4], A[2], B[2]);
  mma_hf(acc[1][3], A[0], B[2]);
  LDB(2, 2, 9);
  mma_hf(acc[2][7], A[8], B[3]);
  mma_hf(acc[2][6], A[6], B[3]);
  mma_hf(acc[2][5], A[4], B[3]);
  mma_hf(acc[2][4], A[2], B[3]);
  mma_hf(acc[2][3], A[0], B[3]);
  LDB(3, 3, 9);
  mma_hf(acc[3][7], A[8], B[4]);
  mma_hf(acc[3][6], A[6], B[4]);
  mma_hf(acc[3][5], A[4], B[4]);
  mma_hf(acc[3][4], A[2], B[4]);
  mma_hf(acc[3][3], A[0], B[4]);
  LDB(4, 0, 10);
  mma_hf(acc[0][7], A[7], B[5]);
  VADD(y[1], acc[0][3][1], acc[1][3][0]);
  VADD(y[2], acc[0][3][2], acc[1][3][1]);
  mma_hf(acc[0][6], A[5], B[5]);
  VADD(y[3], acc[0][3][3], acc[1][3][2]);
  VADD(c[0], acc[1][3][3], acc[2][3][2]);
  mma_hf(acc[0][5], A[3], B[5]);
  VADD(y[2], y[2], acc[2][3][0]);
  VADD(y[3], y[3], acc[2][3][1]);
  mma_hf(acc[0][4], A[1], B[5]);
  NOPS();
  VADD(c[0], c[0], acc[3][3][1]);
  LDB(5, 1, 10);
  mma_hf(acc[1][7], A[7], B[6]);
  VADD(c[1], acc[2][3][3], acc[3][3][2]);
  VADD(y[3], y[3], acc[3][3][0]);
  mma_hf(acc[1][6], A[5], B[6]);
  VSEL(cv[0], c[0], pc[0]);
  VSEL(cv[1], c[1], pc[1]);
  mma_hf(acc[1][5], A[3], B[6]);
  VSEL(cv[2], acc[3][3][3], pc[2]);
  mma_hf(acc[1][4], A[1], B[6]);
  BPERM(); pc[0] = c[0]; pc[1] = c[1]; pc[2] = acc[3][3][3];
  LDB(6, 2, 10);
  mma_hf(acc[2][7], A[7], B[7]);
  VMULS(y[3]);
  mma_hf(acc[2][6], A[5], B[7]);
  mma_hf(acc[2][5], A[3], B[7]);
  mma_hf(acc[2][4], A[1], B[7]);
  LDB(7, 3, 10);
  mma_hf(acc[3][7], A[7], B[8]);
  mma_hf(acc[3][6], A[5], B[8]);
  mma_hf(acc[3][5], A[3], B[8]);
  mma_hf(acc[3][4], A[1], B[8]);
  WAITB(2);
  VADD(y[0], acc[0][3][0], cv[0]);
  LDB(8, 0, 11);
  mma_hf(acc[0][7], A[6], B[0]);
  VADD(y[1], y[1], cv[1]);
  mma_hf(acc[0][6], A[4], B[0]);
  VADD(y[2], y[2], cv[2]);
  mma_hf(acc[0][5], A[2], B[0]);
  VMULS(y[0]);
  mma_hf(acc[0][4], A[0], B[0]);
  VMULS(y[1]);
  LDB(0, 1, 11);
  mma_hf(acc[1][7], A[6], B[1]);
  VMULS(y[2]);
  mma_hf(acc[1][6], A[4], B[1]);
  VPK(zx1, y[0], y[1]);
  mma_hf(acc[1][5], A[2], B[1]);
  VPK(zy1, y[2], y[3]);
  mma_hf(acc[1][4], A[0], B[1]);
  ZSTORE(2);
  LDB(1, 2, 11);
  mma_hf(acc[2][7], A[6], B[2]);
  mma_hf(acc[2][6], A[4], B[2]);
  mma_hf(acc[2][5], A[2], B[2]);
  mma_hf(acc[2][4], A[0], B[2]);
  LDB(2, 3, 11);
  mma_hf(acc[3][7], A[6], B[3]);
  mma_hf(acc[3][6], A[4], B[3]);
  mma_hf(acc[3][5], A[2], B[3]);
  mma_hf(acc[3][4], A[0], B[3]);
  LDB(3, 0, 12);
  mma_hf(acc[0][7], A[5], B[4]);
  VADD(y[1], acc[0][4][1], acc[1][4][0]);
  VADD(y[2], acc[0][4][2], acc[1][4][1]);
  mma_hf(acc[0][6], A[3], B[4]);
  VADD(y[3], acc[0][4][3], acc[1][4][2]);
  VADD(c[0], acc[1][4][3], acc[2][4][2]);
  mma_hf(acc[0][5], A[1], B[4]);
  VADD(y[2], y[2], acc[2][4][0]);
  VADD(y[3], y[3], acc[2][4][1]);
  LDB(4, 1, 12);
  mma_hf(acc[1][7], A[5], B[5]);
  NOPS();
  VADD(c[0], c[0], acc[3][4][1]);
  mma_hf(acc[1][6], A[3], B[5]);
  VADD(c[1], acc[2][4][3], acc[3][4][2]);
  VADD(y[3], y[3], acc[3][4][0]);
  mma_hf(acc[1][5], A[1], B[5]);
  VSEL(cv[0], c[0], pc[0]);
  VSEL(cv[1], c[1], pc[1]);
  LDB(5, 2, 12);
  mma_hf(acc[2][7], A[5], B[6]);
  VSEL(cv[2], acc[3][4][3], pc[2]);
  BPERM(); pc[0] = c[0]; pc[1] = c[1]; pc[2] = acc[3][4][3];
  mma_hf(acc[2][6], A[3], B[6]);
  VMULS(y[3]);
  mma_hf(acc[2][5], A[1], B[6]);
  LDB(6, 3, 12);
  mma_hf(acc[3][7], A[5], B[7]);
  mma_hf(acc[3][6], A[3], B[7]);
  WAITB(1);
  VADD(y[0], acc[0][4][0], cv[0]);
  mma_hf(acc[3][5], A[1], B[7]);
  VADD(y[1], y[1], cv[1]);
  VADD(y[2], y[2], cv[2]);
  LDB(7, 0, 13);
  mma_hf(acc[0][7], A[4], B[8]);
  VMULS(y[0]);
  VMULS(y[1]);
  mma_hf(acc[0][6], A[2], B[8]);
  VMULS(y[2]);
  VPK(zx0, y[0], y[1]);
  mma_hf(acc[0][5], A[0], B[8]);
  VPK(zy0, y[2], y[3]);
  LDB(8, 1, 13);
  mma_hf(acc[1][7], A[4], B[0]);
  mma_hf(acc[1][6], A[2], B[0]);
  mma_hf(acc[1][5], A[0], B[0]);
  LDB(0, 2, 13);
  mma_hf(acc[2][7], A[4], B[1]);
  mma_hf(acc[2][6], A[2], B[1]);
  mma_hf(acc[2][5], A[0], B[1]);
  LDB(1, 3, 13);
  mma_hf(acc[3][7], A[4], B[2]);
  mma_hf(acc[3][6], A[2], B[2]);
  mma_hf(acc[3][5], A[0], B[2]);
  LDB(2, 0, 14);
  mma_hf(acc[0][7], A[3], B[3]);
  VADD(y[1], acc[0][5][1], acc[1][5][0]);
  VADD(y[2], acc[0][5][2], acc[1][5][1]);
  mma_hf(acc[0][6], A[1], B[3]);
  VADD(y[3], acc[0][5][3], acc[1][5][2]);
  VADD(c[0], acc[1][5][3], acc[2][5][2]);
  LDB(3, 1, 14);
  mma_hf(acc[1][7], A[3], B[4]);
  VADD(y[2], y[2], acc[2][5][0]);
  VADD(y[3], y[3], acc[2][5][1]);
  mma_hf(acc[1][6], A[1], B[4]);
  NOPS();
  VADD(c[0], c[0], acc[3][5][1]);
  LDB(4, 2, 14);
  mma_hf(acc[2][7], A[3], B[5]);
  VADD(c[1], acc[2][5][3], acc[3][5][2]);
  VADD(y[3], y[3], acc[3][5][0]);
  mma_hf(acc[2][6], A[1], B[5]);
  VSEL(cv[0], c[0], pc[0]);
  VSEL(cv[1], c[1], pc[1]);
  LDB(5, 3, 14);
  mma_hf(acc[3][7], A[3], B[6]);
  VSEL(cv[2], acc[3][5][3], pc[2]);
  BPERM(); pc[0] = c[0]; pc[1] = c[1]; pc[2] = acc[3][5][3];
  mma_hf(acc[3][6], A[1], B[6]);
  VMULS(y[3]);
  LDB(6, 0, 15);
  mma_hf(acc[0][7], A[2], B[7]);
  mma_hf(acc[0][6], A[0], B[7]);
  LDB(7, 1, 15);
  mma_hf(acc[1][7], A[2], B[8]);
  WAITB(2);
  VADD(y[0], acc[0][5][0], cv[0]);
  mma_hf(acc[1][6], A[0], B[8]);
  VADD(y[1], y[1], cv[1]);
  VADD(y[2], y[2], cv[2]);
  LDB(8, 2, 15);
  mma_hf(acc[2][7], A[2], B[0]);
  VMULS(y[0]);
  VMULS(y[1]);
  mma_hf(acc[2][6], A[0], B[0]);
  VMULS(y[2]);
  VPK(zx1, y[0], y[1]);
  LDB(0, 3, 15);
  mma_hf(acc[3][7], A[2], B[1]);
  VPK(zy1, y[2], y[3]);
  ZSTORE(4);
  mma_hf(acc[3][6], A[0], B[1]);
  mma_hf(acc[0][7], A[1], B[2]);
  VADD(y[1], acc[0][6][1], acc[1][6][0]);
  VADD(y[2], acc[0][6][2], acc[1][6][1]);
  mma_hf(acc[1][7], A[1], B[3]);
  VADD(y[3], acc[0][6][3], acc[1][6][2]);
  VADD(c[0], acc[1][6][3], acc[2][6][2]);
  mma_hf(acc[2][7], A[1], B[4]);
  VADD(y[2], y[2], acc[2][6][0]);
  VADD(y[3], y[3], acc[2][6][1]);
  mma_hf(acc[3][7], A[1], B[5]);
  NOPS();
  VADD(c[0], c[0], acc[3][6][1]);
  mma_hf(acc[0][7], A[0], B[6]);
  VADD(c[1], acc[2][6][3], acc[3][6][2]);
  VADD(y[3], y[3], acc[3][6][0]);
  mma_hf(acc[1][7], A[0], B[7]);
  VSEL(cv[0], c[0], pc[0]);
  VSEL(cv[1], c[1], pc[1]);
  mma_hf(acc[2][7], A[0], B[8]);
  VSEL(cv[2], acc[3][6][3], pc[2]);
  BPERM(); pc[0] = c[0]; pc[1] = c[1]; pc[2] = acc[3][6][3];
  mma_hf(acc[3][7], A[0], B[0]);
  VMULS(y[3]);
  STAMP(19);
  WAITB(0);
  VADD(y[0], acc[0][6][0], cv[0]);
  VADD(y[1], y[1], cv[1]);
  VADD(y[2], y[2], cv[2]);
  VMULS(y[0]);
  VMULS(y[1]);
  VMULS(y[2]);
  VPK(zx0, y[0], y[1]);
  VPK(zy0, y[2], y[3]);
  VADD(y[1], acc[0][7][1], acc[1][7][0]);
  VADD(y[2], acc[0][7][2], acc[1][7][1]);
  VADD(y[3], acc[0][7][3], acc[1][7][2]);
  VADD(c[0], acc[1][7][3], acc[2][7][2]);
  VADD(y[2], y[2], acc[2][7][0]);
  VADD(y[3], y[3], acc[2][7][1]);
  NOPS();
  VADD(c[0], c[0], acc[3][7][1]);
  VADD(c[1], acc[2][7][3], acc[3][7][2]);
  VADD(y[3], y[3], acc[3][7][0]);
  VSEL(cv[0], c[0], pc[0]);
  VSEL(cv[1], c[1], pc[1]);
  VSEL(cv[2], acc[3][7][3], pc[2]);
  BPERM(); pc[0] = c[0]; pc[1] = c[1]; pc[2] = acc[3][7][3];
  VMULS(y[3]);
  WAITB(0);
  VADD(y[0], acc[0][7][0], cv[0]);
  VADD(y[1], y[1], cv[1]);
  VADD(y[2], y[2], cv[2]);
  VMULS(y[0]);
  VMULS(y[1]);
  VMULS(y[2]);
  VPK(zx1, y[0], y[1]);
  VPK(zy1, y[2], y[3]);
  ZSTORE(6);
  STAMP(20);
  waitv<0>();
  ldsw(CTL + C_CDONE + slot * 4, 0);
  STAMP(11);
}

__global__ __launch_bounds__(512, 2) void k_main(Params p) {
  extern __shared__ __attribute__((aligned(16))) char smem[];
  const u32 sb = (u32)(size_t)(LAS char*)smem;
  const int lane = threadIdx.x & 63, wv = rfl(threadIdx.x >> 6);
  const int b = blockIdx.x, xc = b & 7, r = b >> 3;
  const bool g1 = r < 30;
  const int r5 = r / 6, ri = (xc >> 2) * 5 + r5, cj = (xc & 3) * 6 + r % 6, ts = (ri * 64) / 5;
  const int i2 = xc * 4 + (r >> 3), j2 = r & 7;
#define KS2(k) (k)
  const u32 CTL = sb + LD_CTL;
  u64 pc0; u32 codeb;
  asm volatile("s_getpc_b64 %0\n.Lk_start:\n\ts_mov_b32 %1, .Lk_end-.Lk_start\n\t"
               ".subsection 2\n.Lk_end:\n\t.fill 128, 4, 0xBF800000\n\t.subsection 0" : "=s"(pc0), "=s"(codeb));
#define CSTAMP(k)
  STAMP(0);
  const int c6 = r % 6, rot = (OFFS >> (4 * ((r5 + c6) % NGRP))) & 15;
  const v4u rz = mkrs(p.ws, 0);
  {
    if (wv == 6)
      asm volatile("s_mov_b32 m0, %2\n\ts_nop 0\n\tbuffer_load_dword %0, %1, 0 offen lds" ::"v"(lane * 64 + (r & 15) * 4096), "s"(mkrs((const void*)pc0, codeb)), "s"(sb + LD_ST + 512) : "memory");
    const bool isw = (wv & 6) == 4;
    const int xi = wv < 4 ? wv : wv - 2;
    const u32 pitch = isw ? 3072 : 1280;
    const int rr = lane >> 3;
    const u32 sw0 = isw ? (rr & 2) : (rr >> 1);
    const u32 a0 = rr * pitch + (((lane & 7) ^ sw0) << 4), a1 = (a0 ^ 64) + 8 * pitch - 1024, a2 = a0 + 16 * pitch - 2048, a3 = a1 + 16 * pitch - 2048;
    const v4u rs = mkrs(wv == 4 ? p.wg : (wv == 5 ? p.wu : p.x), isw ? D * F * 2 : L * D * 2);
    u32 so = isw ? rot * 64 * 3072 + cj * 128 : (ts + 2 * xi) * 20480 + rot * 128, ld = isw ? sb + LD_R + (wv & 1) * 8192 : sb + LD_X + xi * 4096;
    const u32 dso = isw ? 32 * 3072 : 20480, dld = isw ? 4096 : 2048;
    const bool two = isw || wv == 7;
#pragma unroll 1
    for (int st = 0;; st++) {
      if (g1) { dma4(rs, a0, a1, a2, a3, so, ld); if (two) dma4(rs, a0, a1, a2, a3, so + dso, ld + dld); }
      if (st == 0) {
        if (wv == 2) asm volatile("s_mov_b32 m0, %0\n\ts_mov_b64 exec, 1\n\tbuffer_load_dword off, %1, 0 sc1 lds\n\ts_mov_b64 exec, -1" ::"s"(CTL + C_TAG), "s"(mkrs(p.ws, 64)) : "memory");
        if (wv < 4) {
          const u64 m = wv == 0 ? ((r & 1) ? 0x1f3f37800ull : 0x1f7f77800ull) : (wv == 1 ? (g1 ? ~0ull : 0ull) : (wv == 2 ? (g1 ? 0x01010101010131ffull : 0xffull) : (g1 ? 0x0101ull : 0ull)));
          if (!(wv == 0 && lane == 15) && !(wv == 3 && lane >= 32)) *(volatile LAS u32*)(CTL + wv * 256 + lane * 4) = (u32)(m >> lane) & 1u;
        }
        BARRIER();
      }
      sig0(rz, CTL + C_FULL + st * 32 + wv * 4);
      if (st == 1 || !isw) break;
      so += 64 * 3072; ld += WSLOT;
    }
    STAMP(30);
  }
  __builtin_amdgcn_wave_barrier();
  const int g = lane >> 4, i = lane & 15;
  const rsrc_t rw = mkr(p.ws, WS_BYTES);
  const int cstep = ((c6 == 0 ? CT0 : c6 == 1 ? CT1 : c6 == 2 ? CT2 : c6 == 3 ? CT3 : c6 == 4 ? CT4 : CT5) >> (3 * ri)) & 7;
  const int ch0 = 80 * xc + 5 * (r >> 1) + (r & 1) * 3;
  __builtin_amdgcn_wave_barrier();
  if (wv < 4) {
    __builtin_amdgcn_s_setprio(2);
    const rsrc_t rx = mkr(p.x, L * D * 2);
    constexpr int NWR = 5;
    v4u Wr[NWR][5];
    const u32 wdo = WS_WDF + (j2 * 48 + 12 * wv) * 5120;
    u32 tag;
    if (g1) {
      f4 acc[3][8], a13[2];
#pragma unroll
      for (int t = 0; t < 3; t++)
#pragma unroll
        for (int c = 0; c < 8; c++) { acc[t][c] = f4{0, 0, 0, 0}; asm volatile("" : "+v"(acc[t][c])); }
      a13[0] = a13[1] = f4{0, 0, 0, 0};
      const int q = (lane >> 2) & 3, pp = lane & 3, fp = ((q >> 1) & 1) | ((g & 1) << 1);
      u32 wb[4];
#pragma unroll
      for (int c = 0; c < 4; c++) wb[c] = sb + LD_R + (8 * g + q) * 128 + ((c ^ wv ^ fp) * 32) + pp * 8;
      u32 xb0 = sb + LD_X + wv * 6144 + i * 128 + ((g ^ (i >> 1)) << 4), xb1 = xb0 ^ 64;
      u32 yb0 = xb0 + (12 - 3 * wv) * 2048, yb1 = yb0 ^ 64, fa = CTL + C_FULL + (lane & 7) * 4;
      u32 sta = sb + LD_ST + (3 * wv * 16 + i) * 8, csa = sb + LD_CS + (16 * wv + 4 * g) * 4;
      u32 hb = WS_HF + (2 * cj) * 1024 + (((((g ^ wv) & 1) << 1) | (g >> 1)) * 16 + i) * 16;
      asm volatile("" : "+v"(sta), "+v"(csa), "+v"(hb));
      v4u Wf[2][8], Xf[2][4];
      asm volatile("" : "+v"(a13[0]), "+v"(a13[1]), "+v"(wb[0]), "+v"(wb[1]), "+v"(wb[2]), "+v"(wb[3]), "+v"(xb0), "+v"(xb1), "+v"(yb0), "+v"(yb1), "+v"(fa));
#define RDP(B, n, XA, YA, OFF) do { if ((n) == 0) { Xf[B][0] = lds128(XA); Xf[B][1] = lds128(XA + 2048); } \
        else if ((n) == 2) { Xf[B][2] = lds128(XA + 4096); Xf[B][3] = lds128(YA); } \
        else { constexpr int c = (n) == 1 ? 0 : (n) - 2; const u32 a = wb[c & 3] + (OFF) + (c >> 2) * 8192; Wf[B][c] = trd2(a, a + 512); } SCHED(); } while (0)
#define MM(KS, m) do { constexpr int c = (m) < 4 ? 0 : (m) < 13 ? 1 + ((m) - 4) / 3 : (m) < 17 ? 4 : 5 + ((m) - 17) / 3; \
        constexpr int t = (m) < 4 ? (m) : (m) < 13 ? ((m) - 4) % 3 : (m) < 17 ? (m) - 13 : ((m) - 17) % 3; \
        if (t == 3) mma_bf(a13[c >> 2], Wf[KS][c], Xf[KS][3]); else mma_bf(acc[t < 3 ? t : 0][c], Wf[KS][c], Xf[KS][t]); SCHED(); } while (0)
#define R0(m) MM(0, m); RDP(1, m, xb1, yb1, 4096);
#define R1(m) MM(1, m); RDP(0, (m) - 12, xb0, yb0, 0);
#pragma unroll
      for (int c = 0; c < 8; c++) { Wf[1][c] = v4u{0, 0, 0, 0}; asm volatile("" : "+v"(Wf[1][c])); }
#pragma unroll
      for (int c = 0; c < 4; c++) { Xf[1][c] = v4u{0, 0, 0, 0}; asm volatile("" : "+v"(Xf[1][c])); }
      u32 sw = 0, sx = 0, fo = 1;
#pragma unroll 1
      for (int s = 0; s < 10; s++) {
        u32 fw;
        spin0(fa, fo);
        if (s == 0) STAMP(1);
        R1(12) R1(13) R1(14) R1(15) R1(16) R1(17) R1(18) R1(19) R1(20) R1(21)
        MM(1, 22); fa += 32; asm volatile("" : "+v"(fa)); SCHED(); MM(1, 23); MM(1, 24); MM(1, 25);
        R0(0) R0(1) R0(2) R0(3) R0(4) R0(5) R0(6) R0(7) R0(8) R0(9)
        sx += XSLOT; u32 dx = XSLOT; if (sx == NXS * XSLOT) { sx = 0; dx = XSLOT - NXS * XSLOT; }
        sw += WSLOT; u32 dl = WSLOT; if (sw == NWS * WSLOT) { sw = 0; dl = WSLOT - NWS * WSLOT; }
        MM(0, 10); xb0 += dx; xb1 += dx; asm volatile("" : "+v"(xb0), "+v"(xb1)); SCHED();
        MM(0, 11); yb0 += dx; yb1 += dx; asm volatile("" : "+v"(yb0), "+v"(yb1)); SCHED();
        MM(0, 12); wb[0] += dl; wb[1] += dl; asm volatile("" : "+v"(wb[0]), "+v"(wb[1])); SCHED();
        MM(0, 13); wb[2] += dl; wb[3] += dl; asm volatile("" : "+v"(wb[2]), "+v"(wb[3])); SCHED();
        MM(0, 14); MM(0, 15);
        asm volatile("s_waitcnt lgkmcnt(0)" ::: "memory");
        if (G1ONLY) STAMPD(3 + s - (s == 9 ? -1 : 0));
        sig0(rz, CTL + C_DONE + s * 16 + wv * 4); SCHED();
        if (s == cstep) {
          const u32 tg = mktag(ldsv(CTL + C_TAG));
          f4 c0 = {0, 0, 0, 0}, c1 = {0, 0, 0, 0}; v4u on = {0x3f803f80u, 0x3f803f80u, 0x3f803f80u, 0x3f803f80u};
          asm volatile("s_nop 1" : "+v"(c0), "+v"(c1), "+v"(on));
          mma_bf(c0, Wf[0][0], on); mma_bf(c1, Wf[0][4], on); mma_bf(c0, Wf[1][0], on); mma_bf(c1, Wf[1][4], on);
          asm volatile("s_nop 7" : "+v"(c0), "+v"(c1));
          const f4 cc = (i & 1) ? c1 : c0;
          if (i < 2) {
            const u32 o = WS_CSP + (i * 64 + 16 * wv + 4 * g) * 8, so = (cj * 10 + ri) * 1024;
            stc128(v4u{__float_as_uint(cc[0]), tg, __float_as_uint(cc[1]), tg}, rw, o, so); stc128(v4u{__float_as_uint(cc[2]), tg, __float_as_uint(cc[3]), tg}, rw, o + 16, so);
          }
          asm volatile("s_nop 3"); SCHED();
        }
        MM(0, 16); MM(0, 17); MM(0, 18); MM(0, 19); MM(0, 20); MM(0, 21); MM(0, 22); MM(0, 23); MM(0, 24); MM(0, 25);
        MM(1, 0); MM(1, 1); MM(1, 2); MM(1, 3); fw = ldsv(fa); SCHED();
        MM(1, 4); MM(1, 5); MM(1, 6); MM(1, 7); MM(1, 8); MM(1, 9); fo = (u32)__builtin_amdgcn_ballot_w64(fw != 0); SCHED(); MM(1, 10); MM(1, 11);
      }
      { const v4u cw = *(volatile LAS v4u*)(CTL + C_RDY); MM(1, 12); MM(1, 13); MM(1, 14); MM(1, 15); tag = mktag(rfl(cw.w)); spin0(CTL + C_RDY, rfl(cw.x)); }
      f4 cg[4], cu[4]; f2 st[4], k1[4], k2[4];
#pragma unroll
      for (int t = 0; t < 4; t++) st[t] = *(LAS f2*)(sta + (t < 3 ? t * 128 : 0) + (t < 3 ? 0 : (12 - 3 * wv) * 128));
      SCHED(); MM(1, 16);
      cg[0] = *(LAS f4*)(csa); cu[0] = *(LAS f4*)(csa + 256); SCHED(); MM(1, 17);
      cg[1] = *(LAS f4*)(csa ^ 64); cu[1] = *(LAS f4*)((csa ^ 64) + 256); SCHED(); MM(1, 18);
      cg[2] = *(LAS f4*)(csa ^ 128); cu[2] = *(LAS f4*)((csa ^ 128) + 256); SCHED(); MM(1, 19);
      cg[3] = *(LAS f4*)(csa ^ 192); cu[3] = *(LAS f4*)((csa ^ 192) + 256); SCHED(); MM(1, 20); MM(1, 21);
#define KSET(t) do { k1[t] = f2{__uint_as_float(__float_as_uint(st[t].x) ^ 0x80000000u), fmul_(-1.44269504089f, st[t].y)}; k2[t].x = __builtin_amdgcn_rcpf(st[t].y); asm volatile("" : "+v"(k1[t]), "+v"(k2[t])); SCHED(); } while (0)
      KSET(0); MM(1, 22); KSET(1); MM(1, 23); KSET(2); MM(1, 24); KSET(3); MM(1, 25);
      CSTAMP(14);
      STAMP(2);
#pragma unroll
      for (int t = 0; t < 4; t++) {
        v2u P[4];
#pragma unroll
        for (int c = 0; c < 4; c++) {
          if (t == 3 && c > 0) break;
          const f4 ag = t < 3 ? acc[t < 3 ? t : 0][c] : a13[0], au = t < 3 ? acc[t < 3 ? t : 0][4 + c] : a13[1];
          {
            u32 p0, p1;
            asm volatile(
              "v_pk_fma_f32 v[248:249], %2, %4, %6 op_sel_hi:[0,1,1]\n\tv_pk_fma_f32 v[250:251], %2, %5, %7 op_sel_hi:[0,1,1]\n\t"
              "v_pk_mul_f32 v[248:249], %2, v[248:249] op_sel:[1,0]\n\tv_pk_mul_f32 v[250:251], %2, v[250:251] op_sel:[1,0]\n\t"
              "v_exp_f32 v248, v248\n\tv_exp_f32 v249, v249\n\tv_exp_f32 v250, v250\n\tv_exp_f32 v251, v251\n\t"
              "v_pk_fma_f32 v[252:253], %2, %8, %10 op_sel_hi:[0,1,1]\n\t"
              "v_pk_fma_f32 v[248:249], %3, v[248:249], %3 op_sel_hi:[0,1,0]\n\tv_pk_fma_f32 v[250:251], %3, v[250:251], %3 op_sel_hi:[0,1,0]\n\t"
              "v_rcp_f32 v248, v248\n\tv_rcp_f32 v249, v249\n\tv_rcp_f32 v250, v250\n\tv_rcp_f32 v251, v251\n\t"
              "v_pk_fma_f32 v[254:255], %2, %9, %11 op_sel_hi:[0,1,1]\n\t"
              "v_pk_mul_f32 v[252:253], v[252:253], v[248:249]\n\tv_pk_mul_f32 v[254:255], v[254:255], v[250:251]\n\t"
              "v_cvt_pk_bf16_f32 %0, v252, v253\n\tv_cvt_pk_bf16_f32 %1, v254, v255"
              : "=v"(p0), "=v"(p1) : "v"(k1[t]), "v"(k2[t]), "v"(cg[c].xy), "v"(cg[c].zw), "v"(ag.xy), "v"(ag.zw), "v"(cu[c].xy), "v"(cu[c].zw), "v"(au.xy), "v"(au.zw)
              : "v248", "v249", "v250", "v251", "v252", "v253", "v254", "v255");
            P[c] = v2u{p0, p1};
          }
          if (t < 3 && (c & 1)) {
            u32 x0 = P[c - 1].x, y0 = P[c - 1].y, x1 = P[c].x, y1 = P[c].y;
            asm volatile("s_nop 0\n\tv_permlane16_swap_b32 %0, %2\n\tv_permlane16_swap_b32 %1, %3" : "+v"(x0), "+v"(y0), "+v"(x1), "+v"(y1));
            stc128(v4u{x0, y0, x1, y1}, rw, hb, (ts + 3 * wv + t) * 49152 + ((c >> 1) ^ (wv >> 1)) * 1024);
          }
          if (c & 1) SCHED();
        }
        if (t == 3 && r5 != 0) {
          __builtin_amdgcn_raw_buffer_store_b64(P[0], rw, WS_HF + (2 * cj + (wv >> 1)) * 1024 + ((2 * (wv & 1) + (g >> 1)) * 16 + i) * 16 + (g & 1) * 8, (ts + 12) * 49152, AUX_SC1);
        }
        SCHED();
      }
      STAMP(3);
      __builtin_amdgcn_s_setprio(3);
      ldsw(CTL + C_DONE + 160 + wv * 4, 0);
      waitv<0>();
      if (lane == 0 && __hip_atomic_fetch_add((LAS u32*)(CTL + C_HCNT), 1u, __ATOMIC_RELAXED, __HIP_MEMORY_SCOPE_WORKGROUP) == 3) stc32(tag, rw, WS_HFLAG + ri * 128 + cj * 4);
      STAMP(4);
    } else {
      if (G1ONLY) __builtin_amdgcn_endpgm();
      wait0(CTL + C_FULL + 8);
      tag = mktag(rfl(ldsv(CTL + C_TAG)));
      wait0(CTL + C_WDFULL);
    }
    wait0(CTL + C_UFULL);
    const int tt = 4 * i2 + wv;
    f4 o[4][5];
    u32 rc[3];
#define OO(m) ((rc[m] & 255) * (D * 2) + (rc[m] >> 8) * 16)
    v4u bp[3];
    {
      v4u xq[3], dq[3]; v3u sq[3];
#pragma unroll
      for (int m = 0; m < 3; m++) {
        const int n = min(m * 64 + lane, 159), row = n / 10; rc[m] = row | (n - row * 10) << 8;
        xq[m] = __builtin_amdgcn_raw_buffer_load_b128(rx, OO(m), (16 * tt * D + 80 * j2) * 2, 0);
        dq[m] = __builtin_amdgcn_raw_buffer_load_b128(mkr(p.dsk, D * 2), (rc[m] >> 8) * 16, 160 * j2, 0);
        sq[m] = __builtin_amdgcn_raw_buffer_load_b96(rw, (rc[m] & 255) * 16, WS_STAT + 256 * tt, AUX_SC1);
      }
      SCHED();
#pragma unroll
      for (int n = 0; n < 25; n++) Wr[n / 5][n % 5] = ldc128(rw, lane * 16, wdo + n * 1024);
      SCHED();
#pragma unroll
      for (int m = 0; m < 3; m++) {
        const float rs = __uint_as_float(sq[m].z), nb = fmul_(-__uint_as_float(sq[m].x), rs);
#pragma unroll
        for (int e = 0; e < 4; e++) {
          const u32 xw = xq[m][e], dw = dq[m][e]; const float x0 = bf2f(xw), x1 = bfhi(xw);
          bp[m][e] = pkbf(ffma_(ffma_(x0, rs, nb), bf2f(dw), x0), ffma_(ffma_(x1, rs, nb), bfhi(dw), x1));
        }
        asm volatile("" : "+v"(bp[m]), "+v"(rc[m]));
      }
    }
    if (HPOLL4) {
      const int l12 = lane < 12 ? lane : 0;
      const u32 fo = WS_HFLAG + (l12 < 6 ? ((20 * i2 + 4) >> 6) * 128 + l12 * 4 : ((20 * i2 + 19) >> 6) * 128 + (l12 - 6) * 4) + wv * 24;
#pragma unroll 1
      for (u32 it = 0; it < SPIN_CAP; it++) { if (__all(ldc32(rw, fo) == tag)) break; __builtin_amdgcn_s_sleep(HP_SLEEP); }
      ORDER();
    } else if (wv == 3) {
      const int l48 = lane < 48 ? lane : 0;
      const u32 fo = WS_HFLAG + (l48 < 24 ? ((20 * i2 + 4) >> 6) * 128 + l48 * 4 : ((20 * i2 + 19) >> 6) * 128 + (l48 - 24) * 4);
      u32 left = 15;
#pragma unroll 1
      for (u32 it = 0; it < SPIN_CAP; it++) {
        const u64 bad = __builtin_amdgcn_ballot_w64(ldc32(rw, fo) != tag);
        const u32 bb = ((u32)bad | (u32)(bad >> 24)) & 0xffffffu;
        ORDER();
#pragma unroll
        for (int k = 0; k < 4; k++) if (((left >> k) & 1) && !((bb >> (6 * k)) & 63)) { if (k < 3) sig0(rz, CTL + C_HRDY + k * 4); left &= ~(1u << k); }
        if (!left) break;
        __builtin_amdgcn_s_sleep(HP_SLEEP);
      }
    } else wait0(CTL + C_HRDY + wv * 4);
    STAMP(5);
    constexpr int HD = 3;
    v4u hr[12][4], Wl[2][5], Wz[5];
    u32 ho[4];
#pragma unroll
    for (int t = 0; t < 4; t++) ho[t] = ABL_H ? WS_WDF + t * 12288 : WS_HF + (4 * i2 + (t ^ wv)) * 49152 + 12 * wv * 1024;
#define LOAD_H(k) do { _Pragma("unroll") for (int t = 0; t < 4; t++) hr[k][t] = ldc128(rw, lane * 16, ho[t] + KS2(k) * 1024); } while (0)
#pragma unroll
    for (int k = 0; k < HD; k++) LOAD_H(k);
    SCHED();
    __builtin_amdgcn_s_setprio(G2_PRIO);
    const u32 wl = sb + wd_off(wv) + lane * 16;
    mma_bf0(o[3][0], Wr[0][0], hr[0][3]); SCHED();
    mma_bf0(o[2][0], Wr[0][0], hr[0][2]); SCHED();
    mma_bf0(o[1][0], Wr[0][0], hr[0][1]); SCHED();
    mma_bf0(o[0][0], Wr[0][0], hr[0][0]); SCHED();
    mma_bf0(o[3][1], Wr[0][1], hr[0][3]); SCHED();
    mma_bf0(o[2][1], Wr[0][1], hr[0][2]); SCHED();
    mma_bf0(o[1][1], Wr[0][1], hr[0][1]); SCHED();
    mma_bf0(o[0][1], Wr[0][1], hr[0][0]); SCHED();
    mma_bf0(o[3][2], Wr[0][2], hr[0][3]); SCHED();
    mma_bf0(o[2][2], Wr[0][2], hr[0][2]); SCHED();
    mma_bf0(o[1][2], Wr[0][2], hr[0][1]); SCHED();
    mma_bf0(o[0][2], Wr[0][2], hr[0][0]); SCHED();
    mma_bf0(o[3][3], Wr[0][3], hr[0][3]); SCHED();
    mma_bf0(o[2][3], Wr[0][3], hr[0][2]); SCHED();
    mma_bf0(o[1][3], Wr[0][3], hr[0][1]); SCHED();
    mma_bf0(o[0][3], Wr[0][3], hr[0][0]); SCHED();
    mma_bf0(o[3][4], Wr[0][4], hr[0][3]); SCHED();
    hr[3][3] = ldc128(rw, lane * 16, ho[3] + 3072); SCHED();
    mma_bf0(o[2][4], Wr[0][4], hr[0][2]); SCHED();
    hr[3][2] = ldc128(rw, lane * 16, ho[2] + 3072); SCHED();
    mma_bf0(o[1][4], Wr[0][4], hr[0][1]); SCHED();
    hr[3][1] = ldc128(rw, lane * 16, ho[1] + 3072); SCHED();
    mma_bf0(o[0][4], Wr[0][4], hr[0][0]); SCHED();
    hr[3][0] = ldc128(rw, lane * 16, ho[0] + 3072); SCHED();
    STAMP(26);
    mma_bf(o[3][0], Wr[1][0], hr[1][3]); SCHED();
    hr[4][3] = ldc128(rw, lane * 16, ho[3] + 4096); SCHED();
    mma_bf(o[2][0], Wr[1][0], hr[1][2]); SCHED();
    mma_bf(o[1][0], Wr[1][0], hr[1][1]); SCHED();
    mma_bf(o[0][0], Wr[1][0], hr[1][0]); SCHED();
    mma_bf(o[3][1], Wr[1][1], hr[1][3]); SCHED();
    mma_bf(o[2][1], Wr[1][1], hr[1][2]); SCHED();
    hr[4][2] = ldc128(rw, lane * 16, ho[2] + 4096); SCHED();
    mma_bf(o[1][1], Wr[1][1], hr[1][1]); SCHED();
    mma_bf(o[0][1], Wr[1][1], hr[1][0]); SCHED();
    mma_bf(o[3][2], Wr[1][2], hr[1][3]); SCHED();
    mma_bf(o[2][2], Wr[1][2], hr[1][2]); SCHED();
    mma_bf(o[1][2], Wr[1][2], hr[1][1]); SCHED();
    hr[4][1] = ldc128(rw, lane * 16, ho[1] + 4096); SCHED();
    mma_bf(o[0][2], Wr[1][2], hr[1][0]); SCHED();
    mma_bf(o[3][3], Wr[1][3], hr[1][3]); SCHED();
    mma_bf(o[2][3], Wr[1][3], hr[1][2]); SCHED();
    mma_bf(o[1][3], Wr[1][3], hr[1][1]); SCHED();
    mma_bf(o[0][3], Wr[1][3], hr[1][0]); SCHED();
    hr[4][0] = ldc128(rw, lane * 16, ho[0] + 4096); SCHED();
    mma_bf(o[3][4], Wr[1][4], hr[1][3]); SCHED();
    mma_bf(o[2][4], Wr[1][4], hr[1][2]); SCHED();
    mma_bf(o[1][4], Wr[1][4], hr[1][1]); SCHED();
    mma_bf(o[0][4], Wr[1][4], hr[1][0]); SCHED();
    mma_bf(o[3][0], Wr[2][0], hr[2][3]); SCHED();
    hr[5][3] = ldc128(rw, lane * 16, ho[3] + 5120); SCHED();
    mma_bf(o[2][0], Wr[2][0], hr[2][2]); SCHED();
    mma_bf(o[1][0], Wr[2][0], hr[2][1]); SCHED();
    mma_bf(o[0][0], Wr[2][0], hr[2][0]); SCHED();
    mma_bf(o[3][1], Wr[2][1], hr[2][3]); SCHED();
    mma_bf(o[2][1], Wr[2][1], hr[2][2]); SCHED();
    hr[5][2] = ldc128(rw, lane * 16, ho[2] + 5120); SCHED();
    mma_bf(o[1][1], Wr[2][1], hr[2][1]); SCHED();
    mma_bf(o[0][1], Wr[2][1], hr[2][0]); SCHED();
    mma_bf(o[3][2], Wr[2][2], hr[2][3]); SCHED();
    mma_bf(o[2][2], Wr[2][2], hr[2][2]); SCHED();
    mma_bf(o[1][2], Wr[2][2], hr[2][1]); SCHED();
    hr[5][1] = ldc128(rw, lane * 16, ho[1] + 5120); SCHED();
    mma_bf(o[0][2], Wr[2][2], hr[2][0]); SCHED();
    mma_bf(o[3][3], Wr[2][3], hr[2][3]); SCHED();
    mma_bf(o[2][3], Wr[2][3], hr[2][2]); SCHED();
    mma_bf(o[1][3], Wr[2][3], hr[2][1]); SCHED();
    mma_bf(o[0][3], Wr[2][3], hr[2][0]); SCHED();
    hr[5][0] = ldc128(rw, lane * 16, ho[0] + 5120); SCHED();
    mma_bf(o[3][4], Wr[2][4], hr[2][3]); SCHED();
    mma_bf(o[2][4], Wr[2][4], hr[2][2]); SCHED();
    mma_bf(o[1][4], Wr[2][4], hr[2][1]); SCHED();
    mma_bf(o[0][4], Wr[2][4], hr[2][0]); SCHED();
    mma_bf(o[3][0], Wr[3][0], hr[3][3]); SCHED();
    hr[6][3] = ldc128(rw, lane * 16, ho[3] + 6144); SCHED();
    mma_bf(o[2][0], Wr[3][0], hr[3][2]); SCHED();
    mma_bf(o[1][0], Wr[3][0], hr[3][1]); SCHED();
    mma_bf(o[0][0], Wr[3][0], hr[3][0]); SCHED();
    mma_bf(o[3][1], Wr[3][1], hr[3][3]); SCHED();
    mma_bf(o[2][1], Wr[3][1], hr[3][2]); SCHED();
    hr[6][2] = ldc128(rw, lane * 16, ho[2] + 6144); SCHED();
    mma_bf(o[1][1], Wr[3][1], hr[3][1]); SCHED();
    mma_bf(o[0][1], Wr[3][1], hr[3][0]); SCHED();
    mma_bf(o[3][2], Wr[3][2], hr[3][3]); SCHED();
    mma_bf(o[2][2], Wr[3][2], hr[3][2]); SCHED();
    mma_bf(o[1][2], Wr[3][2], hr[3][1]); SCHED();
    hr[6][1] = ldc128(rw, lane * 16, ho[1] + 6144); SCHED();
    mma_bf(o[0][2], Wr[3][2], hr[3][0]); SCHED();
    mma_bf(o[3][3], Wr[3][3], hr[3][3]); SCHED();
    mma_bf(o[2][3], Wr[3][3], hr[3][2]); SCHED();
    mma_bf(o[1][3], Wr[3][3], hr[3][1]); SCHED();
    mma_bf(o[0][3], Wr[3][3], hr[3][0]); SCHED();
    hr[6][0] = ldc128(rw, lane * 16, ho[0] + 6144); SCHED();
    mma_bf(o[3][4], Wr[3][4], hr[3][3]); SCHED();
    mma_bf(o[2][4], Wr[3][4], hr[3][2]); SCHED();
    mma_bf(o[1][4], Wr[3][4], hr[3][1]); SCHED();
    mma_bf(o[0][4], Wr[3][4], hr[3][0]); SCHED();
    wait0(CTL + C_WDFULL); STAMP(28);
    mma_bf(o[3][0], Wr[4][0], hr[4][3]); SCHED();
    hr[7][3] = ldc128(rw, lane * 16, ho[3] + 7168); SCHED();
    mma_bf(o[2][0], Wr[4][0], hr[4][2]); SCHED();
    Wl[1][0] = lds128(wl + 0); SCHED();
    mma_bf(o[1][0], Wr[4][0], hr[4][1]); SCHED();
    mma_bf(o[0][0], Wr[4][0], hr[4][0]); SCHED();
    mma_bf(o[3][1], Wr[4][1], hr[4][3]); SCHED();
    mma_bf(o[2][1], Wr[4][1], hr[4][2]); SCHED();
    Wl[1][1] = lds128(wl + 1024); SCHED();
    hr[7][2] = ldc128(rw, lane * 16, ho[2] + 7168); SCHED();
    mma_bf(o[1][1], Wr[4][1], hr[4][1]); SCHED();
    mma_bf(o[0][1], Wr[4][1], hr[4][0]); SCHED();
    mma_bf(o[3][2], Wr[4][2], hr[4][3]); SCHED();
    mma_bf(o[2][2], Wr[4][2], hr[4][2]); SCHED();
    Wl[1][2] = lds128(wl + 2048); SCHED();
    mma_bf(o[1][2], Wr[4][2], hr[4][1]); SCHED();
    hr[7][1] = ldc128(rw, lane * 16, ho[1] + 7168); SCHED();
    mma_bf(o[0][2], Wr[4][2], hr[4][0]); SCHED();
    mma_bf(o[3][3], Wr[4][3], hr[4][3]); SCHED();
    mma_bf(o[2][3], Wr[4][3], hr[4][2]); SCHED();
    Wl[1][3] = lds128(wl + 3072); SCHED();
    mma_bf(o[1][3], Wr[4][3], hr[4][1]); SCHED();
    mma_bf(o[0][3], Wr[4][3], hr[4][0]); SCHED();
    hr[7][0] = ldc128(rw, lane * 16, ho[0] + 7168); SCHED();
    mma_bf(o[3][4], Wr[4][4], hr[4][3]); SCHED();
    mma_bf(o[2][4], Wr[4][4], hr[4][2]); SCHED();
    Wl[1][4] = lds128(wl + 4096); SCHED();
    mma_bf(o[1][4], Wr[4][4], hr[4][1]); SCHED();
    mma_bf(o[0][4], Wr[4][4], hr[4][0]); SCHED();
    mma_bf(o[3][0], Wl[1][0], hr[5][3]); SCHED();
    hr[8][3] = ldc128(rw, lane * 16, ho[3] + 8192); SCHED();
    mma_bf(o[2][0], Wl[1][0], hr[5][2]); SCHED();
    Wl[0][0] = lds128(wl + 5120); SCHED();
    mma_bf(o[1][0], Wl[1][0], hr[5][1]); SCHED();
    mma_bf(o[0][0], Wl[1][0], hr[5][0]); SCHED();
    mma_bf(o[3][1], Wl[1][1], hr[5][3]); SCHED();
    mma_bf(o[2][1], Wl[1][1], hr[5][2]); SCHED();
    Wl[0][1] = lds128(wl + 6144); SCHED();
    hr[8][2] = ldc128(rw, lane * 16, ho[2] + 8192); SCHED();
    mma_bf(o[1][1], Wl[1][1], hr[5][1]); SCHED();
    mma_bf(o[0][1], Wl[1][1], hr[5][0]); SCHED();
    mma_bf(o[3][2], Wl[1][2], hr[5][3]); SCHED();
    mma_bf(o[2][2], Wl[1][2], hr[5][2]); SCHED();
    Wl[0][2] = lds128(wl + 7168); SCHED();
    mma_bf(o[1][2], Wl[1][2], hr[5][1]); SCHED();
    hr[8][1] = ldc128(rw, lane * 16, ho[1] + 8192); SCHED();
    mma_bf(o[0][2], Wl[1][2], hr[5][0]); SCHED();
    mma_bf(o[3][3], Wl[1][3], hr[5][3]); SCHED();
    mma_bf(o[2][3], Wl[1][3], hr[5][2]); SCHED();
    Wl[0][3] = lds128(wl + 8192); SCHED();
    mma_bf(o[1][3], Wl[1][3], hr[5][1]); SCHED();
    mma_bf(o[0][3], Wl[1][3], hr[5][0]); SCHED();
    hr[8][0] = ldc128(rw, lane * 16, ho[0] + 8192); SCHED();
    mma_bf(o[3][4], Wl[1][4], hr[5][3]); SCHED();
    mma_bf(o[2][4], Wl[1][4], hr[5][2]); SCHED();
    Wl[0][4] = lds128(wl + 9216); SCHED();
    mma_bf(o[1][4], Wl[1][4], hr[5][1]); SCHED();
    mma_bf(o[0][4], Wl[1][4], hr[5][0]); SCHED();
    mma_bf(o[3][0], Wl[0][0], hr[6][3]); SCHED();
    hr[9][3] = ldc128(rw, lane * 16, ho[3] + 9216); SCHED();
    mma_bf(o[2][0], Wl[0][0], hr[6][2]); SCHED();
    Wl[1][0] = lds128(wl + 10240); SCHED();
    mma_bf(o[1][0], Wl[0][0], hr[6][1]); SCHED();
    mma_bf(o[0][0], Wl[0][0], hr[6][0]); SCHED();
    mma_bf(o[3][1], Wl[0][1], hr[6][3]); SCHED();
    mma_bf(o[2][1], Wl[0][1], hr[6][2]); SCHED();
    Wl[1][1] = lds128(wl + 11264); SCHED();
    hr[9][2] = ldc128(rw, lane * 16, ho[2] + 9216); SCHED();
    mma_bf(o[1][1], Wl[0][1], hr[6][1]); SCHED();
    mma_bf(o[0][1], Wl[0][1], hr[6][0]); SCHED();
    mma_bf(o[3][2], Wl[0][2], hr[6][3]); SCHED();
    mma_bf(o[2][2], Wl[0][2], hr[6][2]); SCHED();
    Wl[1][2] = lds128(wl + 12288); SCHED();
    mma_bf(o[1][2], Wl[0][2], hr[6][1]); SCHED();
    hr[9][1] = ldc128(rw, lane * 16, ho[1] + 9216); SCHED();
    mma_bf(o[0][2], Wl[0][2], hr[6][0]); SCHED();
    mma_bf(o[3][3], Wl[0][3], hr[6][3]); SCHED();
    mma_bf(o[2][3], Wl[0][3], hr[6][2]); SCHED();
    Wl[1][3] = lds128(wl + 13312); SCHED();
    mma_bf(o[1][3], Wl[0][3], hr[6][1]); SCHED();
    mma_bf(o[0][3], Wl[0][3], hr[6][0]); SCHED();
    hr[9][0] = ldc128(rw, lane * 16, ho[0] + 9216); SCHED();
    mma_bf(o[3][4], Wl[0][4], hr[6][3]); SCHED();
    mma_bf(o[2][4], Wl[0][4], hr[6][2]); SCHED();
    Wl[1][4] = lds128(wl + 14336); SCHED();
    mma_bf(o[1][4], Wl[0][4], hr[6][1]); SCHED();
    mma_bf(o[0][4], Wl[0][4], hr[6][0]); SCHED();
    mma_bf(o[3][0], Wl[1][0], hr[7][3]); SCHED();
    hr[10][3] = ldc128(rw, lane * 16, ho[3] + 10240); SCHED();
    mma_bf(o[2][0], Wl[1][0], hr[7][2]); SCHED();
    Wl[0][0] = lds128(wl + 15360); SCHED();
    mma_bf(o[1][0], Wl[1][0], hr[7][1]); SCHED();
    Wz[0] = ldc128(rw, lane * 16, wdo + 56320); SCHED();
    mma_bf(o[0][0], Wl[1][0], hr[7][0]); SCHED();
    mma_bf(o[3][1], Wl[1][1], hr[7][3]); SCHED();
    mma_bf(o[2][1], Wl[1][1], hr[7][2]); SCHED();
    Wl[0][1] = lds128(wl + 16384); SCHED();
    hr[10][2] = ldc128(rw, lane * 16, ho[2] + 10240); SCHED();
    mma_bf(o[1][1], Wl[1][1], hr[7][1]); SCHED();
    Wz[1] = ldc128(rw, lane * 16, wdo + 57344); SCHED();
    mma_bf(o[0][1], Wl[1][1], hr[7][0]); SCHED();
    mma_bf(o[3][2], Wl[1][2], hr[7][3]); SCHED();
    mma_bf(o[2][2], Wl[1][2], hr[7][2]); SCHED();
    Wl[0][2] = lds128(wl + 17408); SCHED();
    mma_bf(o[1][2], Wl[1][2], hr[7][1]); SCHED();
    Wz[2] = ldc128(rw, lane * 16, wdo + 58368); SCHED();
    hr[10][1] = ldc128(rw, lane * 16, ho[1] + 10240); SCHED();
    mma_bf(o[0][2], Wl[1][2], hr[7][0]); SCHED();
    mma_bf(o[3][3], Wl[1][3], hr[7][3]); SCHED();
    mma_bf(o[2][3], Wl[1][3], hr[7][2]); SCHED();
    Wl[0][3] = lds128(wl + 18432); SCHED();
    mma_bf(o[1][3], Wl[1][3], hr[7][1]); SCHED();
    Wz[3] = ldc128(rw, lane * 16, wdo + 59392); SCHED();
    mma_bf(o[0][3], Wl[1][3], hr[7][0]); SCHED();
    hr[10][0] = ldc128(rw, lane * 16, ho[0] + 10240); SCHED();
    mma_bf(o[3][4], Wl[1][4], hr[7][3]); SCHED();
    mma_bf(o[2][4], Wl[1][4], hr[7][2]); SCHED();
    Wl[0][4] = lds128(wl + 19456); SCHED();
    mma_bf(o[1][4], Wl[1][4], hr[7][1]); SCHED();
    Wz[4] = ldc128(rw, lane * 16, wdo + 60416); SCHED();
    mma_bf(o[0][4], Wl[1][4], hr[7][0]); SCHED();
    STAMP(31);
    mma_bf(o[3][0], Wl[0][0], hr[8][3]); SCHED();
    hr[11][3] = ldc128(rw, lane * 16, ho[3] + 11264); SCHED();
    mma_bf(o[2][0], Wl[0][0], hr[8][2]); SCHED();
    Wl[1][0] = lds128(wl + 20480); SCHED();
    mma_bf(o[1][0], Wl[0][0], hr[8][1]); SCHED();
    mma_bf(o[0][0], Wl[0][0], hr[8][0]); SCHED();
    mma_bf(o[3][1], Wl[0][1], hr[8][3]); SCHED();
    mma_bf(o[2][1], Wl[0][1], hr[8][2]); SCHED();
    Wl[1][1] = lds128(wl + 21504); SCHED();
    hr[11][2] = ldc128(rw, lane * 16, ho[2] + 11264); SCHED();
    mma_bf(o[1][1], Wl[0][1], hr[8][1]); SCHED();
    mma_bf(o[0][1], Wl[0][1], hr[8][0]); SCHED();
    mma_bf(o[3][2], Wl[0][2], hr[8][3]); SCHED();
    mma_bf(o[2][2], Wl[0][2], hr[8][2]); SCHED();
    Wl[1][2] = lds128(wl + 22528); SCHED();
    mma_bf(o[1][2], Wl[0][2], hr[8][1]); SCHED();
    hr[11][1] = ldc128(rw, lane * 16, ho[1] + 11264); SCHED();
    mma_bf(o[0][2], Wl[0][2], hr[8][0]); SCHED();
    mma_bf(o[3][3], Wl[0][3], hr[8][3]); SCHED();
    mma_bf(o[2][3], Wl[0][3], hr[8][2]); SCHED();
    Wl[1][3] = lds128(wl + 23552); SCHED();
    mma_bf(o[1][3], Wl[0][3], hr[8][1]); SCHED();
    mma_bf(o[0][3], Wl[0][3], hr[8][0]); SCHED();
    hr[11][0] = ldc128(rw, lane * 16, ho[0] + 11264); SCHED();
    mma_bf(o[3][4], Wl[0][4], hr[8][3]); SCHED();
    mma_bf(o[2][4], Wl[0][4], hr[8][2]); SCHED();
    Wl[1][4] = lds128(wl + 24576); SCHED();
    mma_bf(o[1][4], Wl[0][4], hr[8][1]); SCHED();
    mma_bf(o[0][4], Wl[0][4], hr[8][0]); SCHED();
    mma_bf(o[3][0], Wl[1][0], hr[9][3]); SCHED();
    mma_bf(o[2][0], Wl[1][0], hr[9][2]); SCHED();
    Wl[0][0] = lds128(wl + 25600); SCHED();
    mma_bf(o[1][0], Wl[1][0], hr[9][1]); SCHED();
    mma_bf(o[0][0], Wl[1][0], hr[9][0]); SCHED();
    mma_bf(o[3][1], Wl[1][1], hr[9][3]); SCHED();
    mma_bf(o[2][1], Wl[1][1], hr[9][2]); SCHED();
    Wl[0][1] = lds128(wl + 26624); SCHED();
    mma_bf(o[1][1], Wl[1][1], hr[9][1]); SCHED();
    mma_bf(o[0][1], Wl[1][1], hr[9][0]); SCHED();
    mma_bf(o[3][2], Wl[1][2], hr[9][3]); SCHED();
    mma_bf(o[2][2], Wl[1][2], hr[9][2]); SCHED();
    Wl[0][2] = lds128(wl + 27648); SCHED();
    mma_bf(o[1][2], Wl[1][2], hr[9][1]); SCHED();
    mma_bf(o[0][2], Wl[1][2], hr[9][0]); SCHED();
    mma_bf(o[3][3], Wl[1][3], hr[9][3]); SCHED();
    mma_bf(o[2][3], Wl[1][3], hr[9][2]); SCHED();
    Wl[0][3] = lds128(wl + 28672); SCHED();
    mma_bf(o[1][3], Wl[1][3], hr[9][1]); SCHED();
    mma_bf(o[0][3], Wl[1][3], hr[9][0]); SCHED();
    mma_bf(o[3][4], Wl[1][4], hr[9][3]); SCHED();
    mma_bf(o[2][4], Wl[1][4], hr[9][2]); SCHED();
    Wl[0][4] = lds128(wl + 29696); SCHED();
    mma_bf(o[1][4], Wl[1][4], hr[9][1]); SCHED();
    mma_bf(o[0][4], Wl[1][4], hr[9][0]); SCHED();
    mma_bf(o[3][0], Wl[0][0], hr[10][3]); SCHED();
    mma_bf(o[2][0], Wl[0][0], hr[10][2]); SCHED();
    mma_bf(o[1][0], Wl[0][0], hr[10][1]); SCHED();
    mma_bf(o[0][0], Wl[0][0], hr[10][0]); SCHED();
    mma_bf(o[3][1], Wl[0][1], hr[10][3]); SCHED();
    mma_bf(o[2][1], Wl[0][1], hr[10][2]); SCHED();
    mma_bf(o[1][1], Wl[0][1], hr[10][1]); SCHED();
    mma_bf(o[0][1], Wl[0][1], hr[10][0]); SCHED();
    mma_bf(o[3][2], Wl[0][2], hr[10][3]); SCHED();
    mma_bf(o[2][2], Wl[0][2], hr[10][2]); SCHED();
    mma_bf(o[1][2], Wl[0][2], hr[10][1]); SCHED();
    mma_bf(o[0][2], Wl[0][2], hr[10][0]); SCHED();
    mma_bf(o[3][3], Wl[0][3], hr[10][3]); SCHED();
    mma_bf(o[2][3], Wl[0][3], hr[10][2]); SCHED();
    mma_bf(o[1][3], Wl[0][3], hr[10][1]); SCHED();
    mma_bf(o[0][3], Wl[0][3], hr[10][0]); SCHED();
    mma_bf(o[3][4], Wl[0][4], hr[10][3]); SCHED();
    mma_bf(o[2][4], Wl[0][4], hr[10][2]); SCHED();
    mma_bf(o[1][4], Wl[0][4], hr[10][1]); SCHED();
    mma_bf(o[0][4], Wl[0][4], hr[10][0]); SCHED();
    STAMP(20);
    {
#pragma unroll
      for (int n = 0; n < 25; n++) {
        if (n < 20) { mma_bf(o[3 - n / 5][n % 5], Wz[n % 5], hr[11][3 - n / 5]); SCHED(); }
        if (n >= 5 && n < 20) { const int m = n - 5; asm volatile("s_nop 2" : "+v"(o[3 - m / 5][m % 5])); *(LAS f4*)(wl + ((2 - m / 5) * 5 + m % 5) * 1024) = o[3 - m / 5][m % 5]; SCHED(); }
      }
      ldsw(CTL + C_RED + wv * 4, 0);
      STAMP(6);
      asm volatile("s_nop 7" : "+v"(o[0][0]), "+v"(o[0][1]), "+v"(o[0][2]), "+v"(o[0][3]), "+v"(o[0][4]));
      u32 oo[3], lo3[3];
#pragma unroll
      for (int m = 0; m < 3; m++) { asm volatile("" : "+v"(rc[m])); oo[m] = OO(m); lo3[m] = (rc[m] & 255) * 336 + (rc[m] >> 8) * 32; asm volatile("" : "+v"(oo[m]), "+v"(lo3[m])); }
      float base[3][8];
#pragma unroll
      for (int m = 0; m < 3; m++)
#pragma unroll
        for (int e = 0; e < 4; e++) { const u32 w = bp[m][e]; base[m][2 * e] = bf2f(w); base[m][2 * e + 1] = bfhi(w); asm volatile("" : "+v"(base[m][2 * e]), "+v"(base[m][2 * e + 1])); }
      spin_zero4(CTL + C_RED);
#pragma unroll
      for (int c = 0; c < 5; c++) {
        f4 s[3];
#pragma unroll
        for (int t = 1; t < 4; t++) s[t - 1] = *(LAS f4*)(sb + wd_off(wv ^ t) + lane * 16 + ((t - 1) * 5 + c) * 1024);
#pragma unroll
        for (int e = 0; e < 4; e++) o[0][c][e] = fadd_(fadd_(o[0][c][e], s[0][e]), fadd_(s[1][e], s[2][e]));
        asm volatile("" : "+v"(o[0][c]));
      }
      wait0(CTL + C_ZFULL);
      STAMP(7);
      const int q = (lane >> 2) & 3, pp = lane & 3;
      const u32 tb = sb + wd_off(wv) + 15360;
      v2u z[5];
#pragma unroll
      for (int c = 0; c < 5; c++) z[c] = trd(sb + LD_Z + (20 * g + 4 * c + q) * 128 + wv * 32 + pp * 8);
#pragma unroll
      for (int c = 0; c < 5; c++)
        *(LAS f4*)(tb + i * 336 + g * 80 + c * 16) = f4{fadd_(o[0][c][0], bf2f(z[c].x)), fadd_(o[0][c][1], bfhi(z[c].x)), fadd_(o[0][c][2], bf2f(z[c].y)), fadd_(o[0][c][3], bfhi(z[c].y))};
      CBAR();
      const rsrc_t ro = mkr(p.out, L * D * 2);
#pragma unroll
      for (int m = 0; m < 3; m++) {
        const f4 u0 = *(LAS f4*)(tb + lo3[m]), u1 = *(LAS f4*)(tb + lo3[m] + 16);
        const v4u w = {pkbf(fadd_(u0[0], base[m][0]), fadd_(u0[1], base[m][1])), pkbf(fadd_(u0[2], base[m][2]), fadd_(u0[3], base[m][3])),
                       pkbf(fadd_(u1[0], base[m][4]), fadd_(u1[1], base[m][5])), pkbf(fadd_(u1[2], base[m][6]), fadd_(u1[3], base[m][7]))};
        if (m < 2 || lane < 32) stc128(w, ro, oo[m], (16 * tt * D + 80 * j2) * 2);
      }
    }
    STAMP(8); CSTAMP(15);
    ST_FLUSH();
    __builtin_amdgcn_endpgm();
  } else if (wv < 7) {
    const int slot = wv - 4;
    if (G1ONLY == 1) { __builtin_amdgcn_endpgm(); }
    if (slot == 2 && (r & 1)) { ln_unit(p, sb, (16 * (xc >> 2) + (r >> 1)) * 4 + (xc & 3), lane); if (G1ONLY) __builtin_amdgcn_endpgm(); STAMP(21); }
    else { conv_wave(p, sb, slot, ch0 + slot, lane, wv, g1, ts, cj ST_PASS); }
    ST_FLUSH();
    __builtin_amdgcn_endpgm();
  } else {
    __builtin_amdgcn_s_setprio(3);
    v4u fa, fb, cp[14];
    const v4u rws = mkrs(p.ws, WS_BYTES);
#define LDI(d, vo_, io_) asm volatile("buffer_load_dwordx4 %0, %1, %2, 0 offen offset:%3 sc1" : "=v"(d) : "v"(vo_), "s"(rws), "i"(io_) : "memory")
    const u32 vlf = lane * 16 + WS_LFLAG + xc * 2048, vcp = lane * 16 + WS_CSP + cj * 10240, vst = lane * 16 + WS_STAT + ts * 256, vs3 = (lane & 15) * 16 + WS_STAT + ts * 256;
#define LOOK() do { LDI(fa, vlf, 0); LDI(fb, vlf, 1024); } while (0)
#define LOOKC() do { _Pragma("unroll") for (int k = 0; k < 10; k++) LDI(cp[k], vcp + (k >> 2) * 4096, (k & 3) * 1024); \
      LDI(cp[10], vst, 0); LDI(cp[11], vst, 1024); LDI(cp[12], vst, 2048); LDI(cp[13], vs3, 3072); } while (0)
#define PINC(N) asm volatile("s_waitcnt vmcnt(" #N ")" : "+v"(cp[0]), "+v"(cp[1]), "+v"(cp[2]), "+v"(cp[3]), "+v"(cp[4]), "+v"(cp[5]), "+v"(cp[6]), "+v"(cp[7]), "+v"(cp[8]), "+v"(cp[9]), \
      "+v"(cp[10]), "+v"(cp[11]), "+v"(cp[12]), "+v"(cp[13]) :: "memory")
#define PINF() asm volatile("s_waitcnt vmcnt(0)" : "+v"(fa), "+v"(fb) :: "memory")
    if (g1) {
      const v4u rg = mkrs(p.wg, D * F * 2), ru = mkrs(p.wu, D * F * 2), rxs = mkrs(p.x, L * D * 2);
      u32 v[4], vx[4];
#pragma unroll
      for (int k = 0; k < 4; k++) {
        const int row = 8 * k + (lane >> 3), fp = ((row >> 1) & 1) | (((row >> 3) & 1) << 1);
        v[k] = row * 3072 + cj * 128 + (((lane & 7) ^ (fp << 1)) << 4) - 1024 * k;
        vx[k] = (ts * 16 + row) * 1280 + (((lane & 7) ^ ((row >> 1) & 7)) << 4) - 1024 * k;
      }
      int kx = rot + 1, kw = rot == 8 ? 0 : rot + 2; u32 lsw = 2 * WSLOT, lsx = XSLOT;
      v4u dn = {0, 0, 0, 0};
#define FETCH_X(n) do { if (rfl(dn.x | dn.y | dn.z | dn.w) != 0) spin_zero4(CTL + C_DONE + ((n) - 3) * 16); \
        _Pragma("unroll") for (int k = 0; k < 6; k++) { if (k == 0) waitv<50>(); if (k == 3) waitv<47>(); dma4(rxs, vx[0], vx[1], vx[2], vx[3], kx * 128 + k * 40960, sb + LD_X + lsx + k * 4096); } \
        dma2(rxs, vx[0], vx[1], kx * 128 + 12 * 20480, sb + LD_X + lsx + 12 * 2048); \
        sig0(rz, CTL + C_FULL + (n) * 32); } while (0)
#pragma unroll 1
      for (int n = 1; n < 9; n++) {
        FETCH_X(n);
        if (n > 1) dn = *(volatile LAS v4u*)(CTL + C_DONE + (n - 2) * 16);
        kx = kx == 9 ? 0 : kx + 1; lsx += XSLOT; if (lsx == NXS * XSLOT) lsx = 0;
        waitv<44>();
#pragma unroll
        for (int k = 0; k < 4; k++) dma4((k & 1) ? ru : rg, v[0], v[1], v[2], v[3], (kw * 64 + (k >> 1) * 32) * 3072, sb + LD_R + lsw + (k & 1) * 8192 + (k >> 1) * 4096);
        kw = kw == 9 ? 0 : kw + 1; lsw += WSLOT; if (lsw == NWS * WSLOT) lsw = 0;
      }
      waitv<44>(); LOOKC();
      FETCH_X(9);
      STAMP(12);
      waitv<60>(); LOOK();
      const u32 tg = mktag(rfl(ldsv(CTL + C_TAG)));
      float cs0, cs1;
#pragma unroll 1
      for (u32 it = 0; it < SPIN_CAP; it++) {
        PINC(29);
        u32 bad = 0; cs0 = cs1 = 0.f;
#pragma unroll
        for (int k = 0; k < 14; k++) { bad |= (cp[k].y ^ tg) | (cp[k].w ^ tg); if (k < 10) { cs0 = fadd_(cs0, __uint_as_float(cp[k].x)); cs1 = fadd_(cs1, __uint_as_float(cp[k].z)); } }
        if (__all(bad == 0)) break;
        __builtin_amdgcn_s_sleep(8);
        LOOKC(); waitv<0>();
      }
      waitv<2>();
      *(LAS f2*)(sb + LD_CS + lane * 8) = f2{cs0, cs1};
#pragma unroll
      for (int k = 0; k < 4; k++) *(LAS v2u*)(sb + LD_ST + (k < 3 ? lane : lane & 15) * 8 + k * 512) = v2u{cp[10 + k].x, cp[10 + k].z};
      ldsw(CTL + C_RDY, 0);
      STAMP(23);
    } else { wait0(CTL + C_FULL + 8); __builtin_amdgcn_s_sleep(127); __builtin_amdgcn_s_sleep(60); LOOK(); }
    const u32 tag = mktag(rfl(ldsv(CTL + C_TAG)));
    u32 vo[5];
#pragma unroll
    for (int k = 0; k < 5; k++) { const int n = k * 64 + lane, tau = (n * 911) >> 16, a = n - tau * 72 - 8; vo[k] = (((a >> 1) * 4 + tau) * 20480 + (a & 1) * 16 - (k & 3) * 1024) | ((a | (3 - tau)) & 0x80000000u); asm volatile("" : "+v"(vo[k])); }
#pragma unroll 1
    for (u32 it = 0; it < SPIN_CAP; it++) {
      PINF();
      if (__all(((fa.x ^ tag) | (fa.y ^ tag) | (fa.z ^ tag) | (fa.w ^ tag) | (fb.x ^ tag) | (fb.y ^ tag) | (fb.z ^ tag) | (fb.w ^ tag)) == 0)) break;
      __builtin_amdgcn_s_sleep(8);
      LOOK();
    }
#pragma unroll 1
    for (int sl = 0; sl < 3 - (r & 1); sl++) {
      const int ch = ch0 + sl; const u32 co = WS_XT + ((ch >> 7) * 4 + (ch & 3)) * 1024 + ((ch & 127) >> 2) * 32, UB = sb + LD_K + sl * K_BYTES;
      dma4c(rws, vo[0], vo[1], vo[2], vo[3], co, UB); dma1c(rws, vo[4], co, UB + 4096);
    }
    sig0(rz, CTL + C_UFULL);
    {
      const u32 vl = lane * 16, gb = WS_WDF + (j2 * 48 + 5) * 5120;
      v4u stg[4][NST];
      __builtin_amdgcn_s_setprio(1);
#pragma unroll
      for (int w = 0; w < 4; w++) {
        if (w == 3) waitv<44>();
#pragma unroll
        for (int k = 0; k < NST; k++) stg[w][k] = ldc128(rw, vl, gb + w * 61440 + k * 1024);
      }
      SCHED();
      spin_zero4(CTL + C_KDONE); spin_zero4(CTL + C_DONE + 9 * 16);
      STAMP(27);
#pragma unroll
      for (int w = 0; w < 4; w++)
#pragma unroll
        for (int k = 0; k < NST; k++) *(LAS v4u*)(sb + wd_off(w) + vl + k * 1024) = stg[w][k];
      asm volatile("s_waitcnt lgkmcnt(0)" ::: "memory");
      STAMP(29);
      __builtin_amdgcn_s_setprio(WD_PRIO);
      u32 tleft = HTOUCH ? 7 : 0;
      const u32 tfo = WS_HFLAG + (((lane & 4) ? 20 * i2 + 19 : 20 * i2 + 4) >> 6) * 128 + (3 * j2 + min(lane & 3, 2)) * 4;
      const u32 tvo = WS_HF + (4 * i2 + (lane >> 4)) * 49152 + 6 * j2 * 1024 + (lane & 15) * 128;
#define HT() do { if (tleft) { const u32 bad = (u32)__builtin_amdgcn_ballot_w64(ldc32(rw, tfo) != tag); const u32 rdy = tleft & ~(bad | (bad >> 4)); ORDER(); \
        _Pragma("unroll") for (int k = 0; k < 3; k++) if ((rdy >> k) & 1) \
          asm volatile("s_mov_b32 m0, %3\n\ts_nop 0\n\tbuffer_load_dword %0, %1, %2 offen sc1 lds" ::"v"(tvo), "s"(rws), "s"(k * 2048), "s"(CTL + C_FULL) : "memory"); \
        tleft &= ~rdy; } } while (0)
      HT();
      u32 go = gb;
#pragma unroll 1
      for (int w = 0; w < 4; w++, go += 61440) {
        const u32 lo = sb + wd_off(w);
#pragma unroll
        for (int k = NST; k < 30; k += 4) { waitv<WD_DEPTH>(); if (30 - k >= 4) dma4c(rws, vl, vl, vl, vl, go + k * 1024, lo + k * 1024); else dma2c(rws, vl, vl, go + k * 1024, lo + k * 1024); }
      }
      sig0(rz, CTL + C_WDFULL);
      HT();
    }
    STAMP(24);
    __builtin_amdgcn_s_setprio(3);
    spin_zero4(CTL + C_CDONE);
    if (b == 0 && lane == 0) stc32(tag, rw, WS_GEN);
    if (lane < 8) stc32(tag, rw, WS_ZFLAG + (lane * 8 + xc) * 128 + r * 4);
    __builtin_amdgcn_s_sleep(ZP_FIRST);
#pragma unroll 1
    for (u32 it = 0; it < SPIN_CAP; it++) {
      if (__all(ldc32(rw, WS_ZFLAG + (xc * 8 + j2) * 128 + (lane & 31) * 4) == tag)) break;
      __builtin_amdgcn_s_sleep(ZP_SLEEP);
    }
    ORDER();
    {
      u32 vz[4];
#pragma unroll
      for (int k = 0; k < 4; k++) vz[k] = (8 * k + (lane >> 3)) * 4096 + (lane & 7) * 16 - 1024 * k;
      const u32 go = WS_ZT + 80 * j2 * 4096 + i2 * 128;
      dma4c(rws, vz[0], vz[1], vz[2], vz[3], go, sb + LD_Z); dma4c(rws, vz[0], vz[1], vz[2], vz[3], go + 32 * 4096, sb + LD_Z + 4096);
      dma2c(rws, vz[0], vz[1], go + 64 * 4096, sb + LD_Z + 8192);
      sig0(rz, CTL + C_ZFULL);
      waitv<0>();
    }
    STAMP(25);
  }
  ST_FLUSH();
}

extern "C" void kernel_launch(void* const* d_in, const int* in_sizes, int n_in, void* d_out, int out_size, void* d_ws, size_t ws_size, hipStream_t stream) {
  Params p;
  p.x = (const u16*)d_in[0]; p.lar = (const u16*)d_in[1]; p.aim = (const u16*)d_in[2]; p.cre = (const u16*)d_in[3]; p.cim = (const u16*)d_in[4];
  p.ldt = (const u16*)d_in[5]; p.dsk = (const u16*)d_in[6]; p.wg = (const u16*)d_in[8]; p.wu = (const u16*)d_in[9]; p.wd = (const u16*)d_in[10];
  p.out = (u16*)d_out; p.ws = (char*)d_ws;
  (void)hipFuncSetAttribute((const void*)k_main, hipFuncAttributeMaxDynamicSharedMemorySize, LDS_BYTES);
  k_main<<<256, 512, LDS_BYTES, stream>>>(p);
}
```

```cpp
#define DIAG 30
#include <hip/hip_runtime.h>
#pragma clang diagnostic ignored "-Wint-to-pointer-cast"
typedef unsigned short u16; typedef unsigned u32; typedef unsigned long long u64;
typedef u32 v4u __attribute__((ext_vector_type(4)));
typedef u32 v2u __attribute__((ext_vector_type(2)));
typedef u32 v3u __attribute__((ext_vector_type(3)));
typedef float f4 __attribute__((ext_vector_type(4)));
typedef float f2 __attribute__((ext_vector_type(2)));
typedef short s4 __attribute__((ext_vector_type(4)));
typedef __bf16 bf2 __attribute__((ext_vector_type(2)));
typedef _Float16 hf2 __attribute__((ext_vector_type(2)));
#define LAS __attribute__((address_space(3)))
#define GAS __attribute__((address_space(1)))
#define DEV __device__ __forceinline__
typedef __amdgpu_buffer_rsrc_t rsrc_t;

constexpr int L = 2048, D = 640, NS = 192, F = 1536;
constexpr u32 WS_GEN = 0, WS_HFLAG = 1024, WS_ZFLAG = 40960, WS_LFLAG = 49152, WS_WFLAG = WS_LFLAG + 512, WS_STAT = 327680, WS_CSP = 65536,
              WS_XT = 1u << 20, WS_WDF = 4u << 20, WS_HF = 6u << 20, WS_ZT = 13u << 20, WS_BYTES = 16u << 20;
constexpr u32 LD_K = 0, K_BYTES = 5120;
constexpr u32 LD_CS = LD_K + 3 * K_BYTES, LD_ST = LD_CS + 512, LD_CTL = LD_ST + 208 * 8, LD_R = LD_CTL + 896;
constexpr u32 WSLOT = 16384, NWS = 4, XSLOT = 26624, NXS = 3, LD_X = LD_R + NWS * WSLOT, LDS_BYTES = LD_X + NXS * XSLOT;
constexpr u32 LD_H = LD_X + 2 * XSLOT;
constexpr u32 WD_BYTES = 30720, LD_Z = LD_X;
DEV u32 wd_off(int w) { return LD_R + ((0x70520020u >> (8 * w)) & 0xffu) * 1024; }
static_assert(LD_R % 128 == 0 && 18432 + 2 * WD_BYTES == NXS * XSLOT && LDS_BYTES <= 163840, "lds");
constexpr u32 C_FULL = 512, C_WDFULL = 44, C_RDY = 48, C_ZFULL = 52, C_UFULL = 56, C_TAG = 60, C_KDONE = 64, C_RED = 80, C_CDONE = 96, C_HRDY = 112, C_QUIET = 128, C_HCNT = 132, C_DONE = 256;
constexpr int AUX_SC1 = 16;
constexpr u32 SPIN_CAP = 1u << 15;
#define WD_DEPTH 40
#define WD_PRIO 3
#define POLL_STAGE 8
#define WPOLL_STAGE 7
#define WD_P1 12
#define HP_SLEEP 4
#define G2_PRIO 1
#define ABL_H 0
#define ABL_Z 0
#define ZP_FIRST 16
#define ZP_SLEEP 8
#define GWAIT 0
#define P1MODE 1
#define AUX_STAGE 1
#define NST 14
#define NGRP 2
#define OFFS 0x50
#define CT0 0x1293725u
#define CT1 0x1293725u
#define CT2 0x1293725u
#define CT3 0x1293725u
#define CT4 0x1293725u
#define CT5 0x1293725u
#define HTOUCH 0
#define HPOLL4 1
#define KL_PRIO 1
#define G1ONLY 0
#define CONV_PRIO 2

#define STAMP(k) do { if ((k) == 0) dg0 = __builtin_amdgcn_s_memrealtime(); if ((k) == DIAG) dgk = __builtin_amdgcn_s_memrealtime(); \
    if ((k) == 8 && dgk) { const u64 e_ = __builtin_amdgcn_s_memrealtime() + 10 * (dgk - dg0); while (__builtin_amdgcn_s_memrealtime() < e_) __builtin_amdgcn_s_sleep(4); } } while (0)
#define ST_ARG , u64& dg0, u64& dgk
#define ST_PASS , dg0, dgk
#define STAMPD(k) do { } while (0)
#define ST_FLUSH() do { } while (0)

struct Params {
  const u16 *x, *lar, *aim, *cre, *cim, *ldt, *dsk, *wg, *wu, *wd;
  u16* out; char* ws;
};

DEV float bf2f(u32 v) { return __uint_as_float(v << 16); }
DEV float bfhi(u32 v) { return __uint_as_float(v & 0xffff0000u); }
DEV u32 pkbf(float a, float b) { f2 v = {a, b}; return __builtin_bit_cast(u32, __builtin_convertvector(v, bf2)); }
DEV u32 pkhf(float a, float b) { f2 v = {a, b}; return __builtin_bit_cast(u32, __builtin_convertvector(v, hf2)); }
DEV float rbf(float a) { return bf2f(pkbf(a, a)); }
DEV void mma_bf(f4& c, v4u a, v4u b) { asm volatile("v_mfma_f32_16x16x32_bf16 %0, %1, %2, %0" : "+v"(c) : "v"(a), "v"(b)); }
DEV void mma_hf(f4& c, v4u a, v4u b) { asm volatile("v_mfma_f32_16x16x32_f16 %0, %1, %2, %0" : "+v"(c) : "v"(a), "v"(b)); }
DEV void mma_hf0(f4& c, v4u a, v4u b) { asm volatile("v_mfma_f32_16x16x32_f16 %0, %1, %2, 0" : "=&v"(c) : "v"(a), "v"(b)); }
DEV void mma_bf0(f4& c, v4u a, v4u b) { asm volatile("v_mfma_f32_16x16x32_bf16 %0, %1, %2, 0" : "=&v"(c) : "v"(a), "v"(b)); }
DEV v2u trd(u32 a) { return __builtin_bit_cast(v2u, __builtin_amdgcn_ds_read_tr16_b64_v4i16((LAS s4*)a)); }
DEV v4u trd2(u32 a, u32 b) { v2u x = trd(a), y = trd(b); return v4u{x.x, x.y, y.x, y.y}; }
DEV v4u lds128(u32 a) { return *(LAS v4u*)a; }
DEV v2u lds64(u32 a) { return *(LAS v2u*)a; }
DEV u32 rfl(u32 v) { return __builtin_amdgcn_readfirstlane(v); }
DEV v4u mkrs(const void* p, u32 bytes) { u64 a = (u64)p; return v4u{(u32)a, (u32)(a >> 32) & 0xffffu, bytes, 0x00020000u}; }
DEV rsrc_t mkr(const void* p, u32 bytes) { return __builtin_amdgcn_make_buffer_rsrc((void*)p, 0, bytes, 0x00020000); }
DEV v4u ldc128(rsrc_t r, u32 v, u32 s) { return __builtin_amdgcn_raw_buffer_load_b128(r, v, s, AUX_SC1); }
DEV u32 ldc32(rsrc_t r, u32 v) { return __builtin_amdgcn_raw_buffer_load_b32(r, v, 0, AUX_SC1); }
DEV void stc128(v4u d, rsrc_t r, u32 v, u32 s) {
  __builtin_amdgcn_raw_buffer_store_b128(d, r, v, s, AUX_SC1); asm volatile("s_nop 1" ::"v"(d) : "memory"); }
DEV void stc32(u32 d, rsrc_t r, u32 v) { __builtin_amdgcn_raw_buffer_store_b32(d, r, v, 0, AUX_SC1); }
#define CBAR() asm volatile("" ::: "memory")
#define SCHED() __builtin_amdgcn_sched_barrier(0)
#define ORDER() __builtin_amdgcn_fence(__ATOMIC_ACQUIRE, "wavefront")
#define BARRIER() do { asm volatile("s_waitcnt lgkmcnt(0)" ::: "memory"); __builtin_amdgcn_s_barrier(); CBAR(); } while (0)
template <int N> DEV void waitv() { asm volatile("s_waitcnt vmcnt(%0)" ::"i"(N) : "memory"); }
#define DMA_(V, RS, SO, OFF, SC) "buffer_load_dwordx4 " V ", " RS ", " SO " offen" OFF SC " lds\n\t"
#define DMA4_(NAME, SC) DEV void NAME(v4u rs, u32 v0, u32 v1, u32 v2, u32 v3, u32 so, u32 lds) { \
  asm volatile("s_mov_b32 m0, %0\n\ts_nop 0\n\t" DMA_("%1", "%5", "%6", "", SC) DMA_("%2", "%5", "%6", " offset:1024", SC) DMA_("%3", "%5", "%6", " offset:2048", SC) DMA_("%4", "%5", "%6", " offset:3072", SC) \
               ::"s"(lds), "v"(v0), "v"(v1), "v"(v2), "v"(v3), "s"(rs), "s"(so) : "memory"); }
#define DMA2_(NAME, SC) DEV void NAME(v4u rs, u32 v0, u32 v1, u32 so, u32 lds) { \
  asm volatile("s_mov_b32 m0, %0\n\ts_nop 0\n\t" DMA_("%1", "%3", "%4", "", SC) DMA_("%2", "%3", "%4", " offset:1024", SC) ::"s"(lds), "v"(v0), "v"(v1), "s"(rs), "s"(so) : "memory"); }
#define DMA1_(NAME, SC) DEV void NAME(v4u rs, u32 v0, u32 so, u32 lds) { \
  asm volatile("s_mov_b32 m0, %0\n\ts_nop 0\n\t" DMA_("%1", "%2", "%3", "", SC) ::"s"(lds), "v"(v0), "s"(rs), "s"(so) : "memory"); }
DMA4_(dma4, "") DMA2_(dma2, "") DMA1_(dma1, "") DMA4_(dma4c, " sc1") DMA2_(dma2c, " sc1") DMA1_(dma1c, " sc1")
DEV void sig0(v4u rz, u32 lds) {
  asm volatile("s_mov_b32 m0, %0\n\ts_mov_b64 exec, 1\n\tbuffer_load_dword off, %1, 0 lds\n\ts_mov_b64 exec, -1" ::"s"(lds), "s"(rz) : "memory");
}
DEV u32 mktag(u32 gen) { return gen * 0x9E3779B1u + 0x7F4A7C15u; }
DEV u32 ldsv(u32 a) { return *(volatile LAS u32*)a; }
DEV void ldsw(u32 a, u32 v) { asm volatile("s_mov_b64 exec, 1\n\tds_write_b32 %0, %1\n\ts_mov_b64 exec, -1" ::"v"(a), "v"(v) : "memory"); }
DEV void spin0(u32 a, u32 cur) {
  u32 tv, cnt;
  asm volatile("s_cmp_eq_u32 %3, 0\n\ts_cbranch_scc0 3f\n2:\n\t.subsection 1\n3:\n\ts_mov_b32 %1, 0x10000\n"
               "1:\n\tds_read_b32 %0, %2\n\ts_sub_u32 %1, %1, 1\n\ts_waitcnt lgkmcnt(0)\n\tv_cmp_ne_u32 vcc, 0, %0\n\ts_nop 1\n\ts_min_u32 vcc_lo, vcc_lo, %1\n\ts_cmp_eq_u32 vcc_lo, 0\n\ts_cbranch_scc1 2b\n\t"
               "s_sleep 1\n\ts_branch 1b\n\t.subsection 0"
               : "=&v"(tv), "=&s"(cnt) : "v"(a), "s"(cur) : "memory", "scc", "vcc");
}
DEV void wait0(u32 a) {
  u32 tv, ts, cnt;
  asm volatile("s_mov_b32 %2, 0x10000\n1:\n\tds_read_b32 %0, %3\n\ts_sub_u32 %2, %2, 1\n\ts_waitcnt lgkmcnt(0)\n\tv_readfirstlane_b32 %1, %0\n\ts_nop 0\n\t"
               "s_min_u32 %1, %1, %2\n\ts_cmp_eq_u32 %1, 0\n\ts_cbranch_scc1 2f\n\ts_sleep 1\n\ts_branch 1b\n2:"
               : "=&v"(tv), "=&s"(ts), "=&s"(cnt) : "v"(a) : "memory", "scc");
}
DEV void spin_zero4(u32 a) {
  u32 tv, cnt;
  const u32 al = a + (__builtin_amdgcn_mbcnt_lo(~0u, 0) & 3) * 4;
  asm volatile("s_mov_b32 %1, 0x10000\n1:\n\tds_read_b32 %0, %2\n\ts_sub_u32 %1, %1, 1\n\ts_waitcnt lgkmcnt(0)\n\tv_cmp_ne_u32 vcc, 0, %0\n\ts_nop 1\n\t"
               "s_min_u32 vcc_lo, vcc_lo, %1\n\ts_cmp_eq_u32 vcc_lo, 0\n\ts_cbranch_scc1 2f\n\ts_sleep 1\n\ts_branch 1b\n2:"
               : "=&v"(tv), "=&s"(cnt) : "v"(al) : "memory", "scc", "vcc");
}
template <int SL> DEV void poll_flags(rsrc_t rw, u32 off, u32 off2, u32 tag) {
#pragma unroll 1
  for (u32 i = 0; i < SPIN_CAP; i++) {
    const u32 a = ldc32(rw, off), b = ldc32(rw, off2);
    if (__all(a == tag && b == tag)) break;
    __builtin_amdgcn_s_sleep(SL);
  }
  ORDER();
}
DEV float fmul_(float a, float b) { float r; asm("v_mul_f32 %0, %1, %2" : "=v"(r) : "v"(a), "v"(b)); return r; }
DEV float fadd_(float a, float b) { float r; asm("v_add_f32 %0, %1, %2" : "=v"(r) : "v"(a), "v"(b)); return r; }
DEV float ffma_(float a, float b, float c) { float r; asm("v_fma_f32 %0, %1, %2, %3" : "=v"(r) : "v"(a), "v"(b), "v"(c)); return r; }
DEV float fnma_(float a, float b, float c) { float r; asm("v_fma_f32 %0, -%1, %2, %3" : "=v"(r) : "v"(a), "v"(b), "v"(c)); return r; }
struct cf { float x, y; };
DEV cf csq(cf a) { const float t = fmul_(a.x, a.y); return cf{fnma_(a.y, a.y, fmul_(a.x, a.x)), fadd_(t, t)}; }
DEV u32 pkc(cf a) { return pkhf(a.x, a.y); }
DEV cf cexp2(float lr, float rev) { float m = __builtin_amdgcn_exp2f(lr); const float f = __builtin_amdgcn_fractf(rev); float c = __builtin_amdgcn_cosf(f), s = __builtin_amdgcn_sinf(f);
  asm volatile("s_nop 0" : "+v"(c), "+v"(s), "+v"(m)); return cf{fmul_(m, c), fmul_(m, s)}; }
DEV u32 hmul(u32 a, u32 m) { u32 t, r;
  asm("v_pk_mul_f16 %0, %1, %2 op_sel_hi:[0,1]" : "=v"(t) : "v"(a), "v"(m));
  asm("v_pk_fma_f16 %0, %1, %2, %3 op_sel:[1,1,0] op_sel_hi:[1,0,1] neg_lo:[0,1,0]" : "=v"(r) : "v"(a), "v"(m), "v"(t)); return r; }
DEV u32 hmulc(u32 a, u32 m) { u32 t, r;
  asm("v_pk_mul_f16 %0, %1, %2 op_sel_hi:[0,1] neg_hi:[0,1]" : "=v"(t) : "v"(a), "v"(m));
  asm("v_pk_fma_f16 %0, %1, %2, %3 op_sel:[1,1,0] op_sel_hi:[1,0,1]" : "=v"(r) : "v"(a), "v"(m), "v"(t)); return r; }
DEV f2 pfma_x(f2 s, f2 b, f2 c) { f2 r; asm("v_pk_fma_f32 %0, %1, %2, %3 op_sel_hi:[0,1,1]" : "=v"(r) : "v"(s), "v"(b), "v"(c)); return r; }
DEV f2 pmul_y(f2 s, f2 b) { f2 r; asm("v_pk_mul_f32 %0, %1, %2 op_sel:[1,0]" : "=v"(r) : "v"(s), "v"(b)); return r; }
DEV f2 pfma_xx(f2 s, f2 b) { f2 r; asm("s_nop 0\n\tv_pk_fma_f32 %0, %1, %2, %1 op_sel_hi:[0,1,0]" : "=v"(r) : "v"(s), "v"(b)); return r; }
DEV f2 pmul(f2 a, f2 b) { f2 r; asm("s_nop 0\n\tv_pk_mul_f32 %0, %1, %2" : "=v"(r) : "v"(a), "v"(b)); return r; }
DEV void swap16(float& a, float& b) { auto r = __builtin_amdgcn_permlane16_swap(__float_as_uint(a), __float_as_uint(b), false, false); a = __uint_as_float(r[0]); b = __uint_as_float(r[1]); }
DEV void swap32(float& a, float& b) { auto r = __builtin_amdgcn_permlane32_swap(__float_as_uint(a), __float_as_uint(b), false, false); a = __uint_as_float(r[0]); b = __uint_as_float(r[1]); }

DEV void ln_unit(const Params& p, u32 sb, int pn, int lane) {
  const rsrc_t rx = mkr(p.x, L * D * 2), rw = mkr(p.ws, WS_BYTES);
  const int h = lane & 1, m = lane >> 1, row0 = 64 * (pn >> 2) + (pn & 3);
  __builtin_amdgcn_s_setprio(2);
  v2u xv[8][5]; float xf[8][20], s[16];
#pragma unroll
  for (int e = 0; e < 8; e++)
#pragma unroll
    for (int i = 0; i < 5; i++) xv[e][i] = __builtin_amdgcn_raw_buffer_load_b64(rx, (row0 + 32 * h) * 1280 + m * 8 + i * 256, e * 5120, 0);
  SCHED();
  __builtin_amdgcn_s_setprio(3);
#pragma unroll
  for (int e = 0; e < 8; e++) {
#pragma unroll
    for (int i = 0; i < 10; i++) { const u32 w = xv[e][i >> 1][i & 1]; xf[e][2 * i] = bf2f(w); xf[e][2 * i + 1] = bfhi(w); }
    float a = fadd_(xf[e][0], xf[e][1]), q = fmul_(xf[e][0], xf[e][0]);
#pragma unroll
    for (int i = 1; i < 20; i++) { if (i > 1) a = fadd_(a, xf[e][i]); asm("v_fmac_f32 %0, %1, %1" : "+v"(q) : "v"(xf[e][i])); }
    s[e] = a; s[8 + e] = q;
  }
#pragma unroll
  for (int e = 0; e < 16; e++) asm volatile("" : "+v"(s[e]));
  asm volatile("s_nop 1");
  wait0(sb + LD_CTL + C_FULL + 8);
  const u32 tag = mktag(rfl(ldsv(sb + LD_CTL + C_TAG)));
  float z[4];
#pragma unroll
  for (int k = 0; k < 4; k++) {
    swap16(s[4 * k], s[4 * k + 1]); swap16(s[4 * k + 2], s[4 * k + 3]);
    float a = fadd_(s[4 * k], s[4 * k + 1]), b = fadd_(s[4 * k + 2], s[4 * k + 3]);
    asm volatile("s_nop 1" : "+v"(a), "+v"(b));
    swap32(a, b); z[k] = fadd_(a, b);
  }
#define DPPADD(C) asm volatile("s_nop 1\n\tv_add_f32_dpp %0, %0, %0 " C " row_mask:0xf bank_mask:0xf\n\tv_add_f32_dpp %1, %1, %1 " C " row_mask:0xf bank_mask:0xf\n\t" \
    "v_add_f32_dpp %2, %2, %2 " C " row_mask:0xf bank_mask:0xf\n\tv_add_f32_dpp %3, %3, %3 " C " row_mask:0xf bank_mask:0xf" : "+v"(z[0]), "+v"(z[1]), "+v"(z[2]), "+v"(z[3]))
  DPPADD("quad_perm:[2,3,0,1]"); DPPADD("row_ror:4"); DPPADD("row_ror:8");
  float rs[8], nb[8];
#pragma unroll
  for (int k = 0; k < 2; k++) {
    const float mean = fmul_(z[k], 1.f / D), var = fmaxf(fnma_(mean, mean, fmul_(z[2 + k], 1.f / D)), 0.f);
    const float mb = rbf(mean), sd = rbf(__builtin_amdgcn_sqrtf(rbf(rbf(var) + 1e-5f)));
    float r = __builtin_amdgcn_rcpf(sd);
    asm volatile("s_nop 0" : "+v"(r));
    r = ffma_(fnma_(sd, r, 1.f), r, r);
    if ((lane & 15) < 2) stc128(v4u{__float_as_uint(mb), tag, __float_as_uint(r), tag}, rw, WS_STAT + (row0 + 32 * h + 4 * (4 * k + (lane >> 4))) * 16, 0);
    float t[2] = {r, fmul_(-mb, r)};
#pragma unroll
    for (int w = 0; w < 2; w++) {
      float a = t[w], c = t[w];
      asm volatile("s_nop 1" : "+v"(a), "+v"(c));
      swap32(a, c); float b = a, d = c; swap16(a, b); swap16(c, d);
      float* o = w ? nb : rs; o[4 * k] = a; o[4 * k + 1] = b; o[4 * k + 2] = c; o[4 * k + 3] = d;
    }
  }
#pragma unroll
  for (int i = 0; i < 5; i++)
#pragma unroll
    for (int c = 0; c < 4; c++) {
      float y[8];
#pragma unroll
      for (int e = 0; e < 8; e++) y[e] = ffma_(xf[e][4 * i + c], rs[e], nb[e]);
      stc128(v4u{pkhf(y[0], y[1]), pkhf(y[2], y[3]), pkhf(y[4], y[5]), pkhf(y[6], y[7])}, rw, WS_XT + lane * 16, pn * 20480 + (i * 4 + c) * 1024);
    }
  waitv<0>();
  if (lane < 8) stc32(tag, rw, WS_LFLAG + lane * 2048 + pn * 4);
}

template <bool CJ> DEV void grp8(u32* v, u32 a, u32 b, u32 c) {
#define HM(x, y) (CJ ? hmulc(x, y) : hmul(x, y))
  v[1] = HM(v[0], a); v[2] = HM(v[0], b); v[3] = HM(v[1], b); v[4] = HM(v[0], c); v[5] = HM(v[1], c); v[6] = HM(v[2], c); v[7] = HM(v[3], c);
#undef HM
}
DEV void wr4(u32 a, const u32* v) {
  asm volatile("ds_write2_b32 %0, %1, %2 offset1:68\n\tds_write2_b32 %0, %3, %4 offset0:136 offset1:204" ::"v"(a), "v"(v[0]), "v"(v[1]), "v"(v[2]), "v"(v[3]) : "memory");
}
DEV void rd4(v4u (&f)[4], u32 a) {
  asm volatile("ds_read_b128 %0, %4\n\tds_read_b128 %1, %4 offset:64\n\tds_read_b128 %2, %4 offset:128\n\tds_read_b128 %3, %4 offset:192"
               : "=&v"(f[0]), "=&v"(f[1]), "=&v"(f[2]), "=&v"(f[3]) : "v"(a) : "memory");
}
template <int N> DEV void wait4(v4u (&f)[4], v4u (&h)[4]) {
  asm volatile("s_waitcnt lgkmcnt(%8)" : "+v"(f[0]), "+v"(f[1]), "+v"(f[2]), "+v"(f[3]), "+v"(h[0]), "+v"(h[1]), "+v"(h[2]), "+v"(h[3]) : "i"(N));
}
DEV void conv_wave(const Params& p, u32 sb, int slot, int ch, int lane, int wv, bool g1, int ts, int cj ST_ARG) {
  const u32 UB = sb + LD_K + slot * K_BYTES, KB = sb + LD_R + 2 * WSLOT + slot * 4608, CTL = sb + LD_CTL;
  const int g = lane >> 4, q = (lane >> 2) & 3, pp = lane & 3, j = lane & 15, b = blockIdx.x;
  const rsrc_t rw = mkr(p.ws, WS_BYTES);
  if (AUX_STAGE >= 0) wait0(CTL + C_FULL + AUX_STAGE * 32);
  __builtin_amdgcn_s_setprio(3);
  const bool retile = !((b >> 3) & 1);
  const int wu = ((b >> 4) * 8 + (b & 7)) * 3 + slot;
  if (retile) {
    u32 vo[5];
#pragma unroll
    for (int k = 0; k < 5; k++) { const int n = k * 64 + lane; vo[k] = (n / 40) * 1280 + (n % 40) * 16 - (k & 3) * 1024; }
    const v4u rd = mkrs(p.wd, F * D * 2);
    const u32 so = (wu >> 1) * 10240 + (wu & 1) * 640;
    dma4(rd, vo[0], vo[1], vo[2], vo[3], so, UB); dma1(rd, vo[4], so, UB + 4096);
  }
  u32 la[3], ai[3], cr[3], ci[3];
#pragma unroll
  for (int c = 0; c < 3; c++) { const int o = ch * NS + 64 * c + lane; la[c] = p.lar[o]; ai[c] = p.aim[o]; cr[c] = p.cre[o]; ci[c] = p.cim[o]; }
  const u32 ldt = p.ldt[ch];
#pragma unroll
  for (int c = 0; c < 3; c++) asm volatile("" : "+v"(la[c]), "+v"(ai[c]), "+v"(cr[c]), "+v"(ci[c]));
  if (retile) {
    waitv<0>();
    __builtin_amdgcn_s_setprio(3);
    const u32 ra = UB + q * 640 + (80 * g + 20 * pp) * 2;
    const u32 wo = WS_WDF + ((4 * (wu & 1) + g) * 48 * 5) * 1024 + j * 16, s2 = (wu >> 3) * 5120 + ((wu >> 1) & 3) * 256;
#pragma unroll
    for (int k = 0; k < 5; k++) stc128(trd2(ra + k * 8, ra + k * 8 + 2560), rw, wo + k * 1024, s2);
  }
  __builtin_amdgcn_s_setprio(0);
  u32 tag = 0;
  const float dt = rbf(__expf(bf2f(ldt)));
  u32 mx = 0;
#pragma unroll
  for (int c = 0; c < 3; c++) mx = max(mx, max(cr[c] & 0x7fffu, ci[c] & 0x7fffu));
  mx = max(mx, (u32)__builtin_amdgcn_update_dpp(0, (int)mx, 0x111, 0xf, 0xf, false));
  mx = max(mx, (u32)__builtin_amdgcn_update_dpp(0, (int)mx, 0x112, 0xf, 0xf, false));
  mx = max(mx, (u32)__builtin_amdgcn_update_dpp(0, (int)mx, 0x114, 0xf, 0xf, false));
  mx = max(mx, (u32)__builtin_amdgcn_update_dpp(0, (int)mx, 0x118, 0xf, 0xf, false));
  mx = max(max((u32)__builtin_amdgcn_readlane(mx, 15), (u32)__builtin_amdgcn_readlane(mx, 31)), max((u32)__builtin_amdgcn_readlane(mx, 47), (u32)__builtin_amdgcn_readlane(mx, 63)));
  STAMP(16);
  const u32 ex = min(max(mx >> 7, 1u), 252u);
  const float cscale = __uint_as_float((253u - ex) << 23);
  const float oscale = 2.f * dt * __uint_as_float((ex + 1u) << 23);
  u32 G[3][57];
#pragma unroll
  for (int c = 0; c < 3; c++) {
    const float zr = fmul_(fmul_(rbf(__expf(bf2f(la[c]))), dt), -1.44269504089f), zi = fmul_(fmul_(bf2f(ai[c]), dt), 0.15915494309f);
    const cf P0 = cexp2(zr, zi), P4 = cexp2(fmul_(zr, 16.f), fmul_(zi, 16.f)), P8 = cexp2(fmul_(zr, 256.f), fmul_(zi, 256.f));
    const cf P1 = csq(P0), P2 = csq(P1), P3 = csq(P2), P5 = csq(P4), P6 = csq(P5), P7 = csq(P6), P9 = csq(P8), P10 = csq(P9), P11 = csq(P10);
    const u32 M0 = pkc(P0), M1 = pkc(P1), M2 = pkc(P2), M3 = pkc(P3), M4 = pkc(P4), M5 = pkc(P5), M7 = pkc(P7), M8 = pkc(P8), M9 = pkc(P9);
    u32* v = G[c];
    const float cx = fmul_(bf2f(cr[c]), cscale), cy = fmul_(bf2f(ci[c]), -cscale);
    v[0] = pkhf(cx, cy); grp8<true>(v, M3, M4, M5);
    v[8] = pkhf(ffma_(cy, P6.y, fmul_(cx, P6.x)), fnma_(cx, P6.y, fmul_(cy, P6.x))); grp8<true>(v + 8, M3, M4, M5);
    v[16] = 0x00003c00u; v[17] = M0; v[18] = M1; v[19] = hmul(M0, M1); v[20] = M2; v[21] = hmul(M0, M2); v[22] = hmul(M1, M2); v[23] = hmul(v[19], M2);
    v[24] = M7; grp8<false>(v + 24, M0, M1, M2);
    v[32] = M8; grp8<false>(v + 32, M1, M2, M7);
    v[40] = M9; grp8<false>(v + 40, M2, M7, M8);
    v[48] = pkc(P10); grp8<false>(v + 48, M7, M8, M9);
    v[56] = pkc(P11);
#pragma unroll
    for (int k = 0; k < 57; k++) asm volatile("" : "+v"(v[k]));
    if (c == 0) {
      wait0(CTL + C_FULL + 8);
      tag = mktag(rfl(ldsv(CTL + C_TAG)));
      if (retile) { waitv<0>(); if (lane < 8) stc32(tag, rw, WS_WFLAG + lane * 2048 + wu * 4); }
    }
  }
  STAMP(22);
  __builtin_amdgcn_s_setprio(KL_PRIO);
  wait0(CTL + C_FULL + 9 * 32);
  STAMP(17);
  f4 kd[3] = {f4{0, 0, 0, 0}, f4{0, 0, 0, 0}, f4{0, 0, 0, 0}};
  {
    const u32 rb = KB + j * 272 + g * 16;
    u32 wa[4] = {KB + lane * 4, KB + lane * 4 + 1088, KB + lane * 4 + 2176, KB + lane * 4 + 3264};
    asm volatile("" : "+v"(kd[0]), "+v"(kd[1]), "+v"(kd[2]), "+v"(wa[0]), "+v"(wa[1]), "+v"(wa[2]), "+v"(wa[3]));
    v4u Fr[12][4];
#pragma unroll
    for (int ps = 0; ps <= 12; ps++) {
      if (ps < 12) {
        const int c = ps >> 2, t = ps & 3;
        wr4(wa[0], G[c] + 16 * t); wr4(wa[1], G[c] + 16 * t + 4);
        if (t < 3) { wr4(wa[2], G[c] + 16 * t + 8); wr4(wa[3], G[c] + 16 * t + 12); }
        else asm volatile("ds_write_b32 %0, %1" ::"v"(wa[2]), "v"(G[c][56]) : "memory");
        asm volatile("ds_read_b128 %0, %4\n\tds_read_b128 %1, %4 offset:64\n\tds_read_b128 %2, %4 offset:128\n\tds_read_b128 %3, %4 offset:192"
                     : "=&v"(Fr[ps][0]), "=&v"(Fr[ps][1]), "=&v"(Fr[ps][2]), "=&v"(Fr[ps][3]) : "v"(rb) : "memory");
      }
      if (ps > 0) {
        const int pv = ps - 1;
        if (ps < 12) asm volatile("s_waitcnt lgkmcnt(9)" : "+v"(Fr[pv][0]), "+v"(Fr[pv][1]), "+v"(Fr[pv][2]), "+v"(Fr[pv][3]));
        else asm volatile("s_waitcnt lgkmcnt(0)" : "+v"(Fr[pv][0]), "+v"(Fr[pv][1]), "+v"(Fr[pv][2]), "+v"(Fr[pv][3]));
        if (pv & 3) {
#pragma unroll
          for (int ks = 0; ks < 4; ks++) mma_hf(kd[(pv & 3) - 1], Fr[pv][ks], Fr[pv & ~3][ks]);
        }
      }
    }
    asm volatile("s_nop 7" : "+v"(kd[0]), "+v"(kd[1]), "+v"(kd[2]));
  }
  STAMP(9);
  {
    const u32 K0 = KB + 32;
    const u32 a00 = pkhf(kd[1][0], kd[1][0]), a11 = pkhf(kd[1][1], kd[1][1]), a22 = pkhf(kd[1][2], kd[1][2]), a33 = pkhf(kd[1][3], kd[1][3]);
    const u32 a12 = pkhf(kd[1][1], kd[1][2]), a01 = pkhf(kd[1][0], kd[1][1]), a23 = pkhf(kd[1][2], kd[1][3]);
    v2u bb[4];
#pragma unroll
    for (int r = 0; r < 4; r++) { bb[r].x = pkhf(kd[2][r], kd[2][r]); bb[r].y = bb[r].x; }
    const v2u r0 = {pkhf(kd[0][0], kd[0][1]), pkhf(kd[0][2], kd[0][3])}, w1 = {a00, a12}, w2a = {a00, a01}, w2b = {a22, a23};
    const v4u z4 = {0, 0, 0, 0};
    const u32 p3 = K0 + 2 * (1024 + 512 * g + 8 * j) - 8, p2 = K0 + 2 * (256 * g + 8 * j) - 4, p1 = K0 + 2 * (256 + 128 * g + 8 * j) - 2;
    const u32 p0 = K0 + 2 * (128 * (g >> 1) + 8 * j + 4 * (g & 1)), pz = KB + lane * 16;
    asm volatile(
      "s_mov_b32 exec_hi, 1\n\t"
      "ds_write2_b64 %0, %5, %5 offset1:1\n\t"
      "s_mov_b32 exec_hi, 0\n\t"
      "ds_write2_b64 %0, %6, %6 offset0:32 offset1:33\n\t"
      "ds_write2_b64 %0, %7, %7 offset0:64 offset1:65\n\t"
      "ds_write2_b64 %0, %8, %8 offset0:96 offset1:97\n\t"
      "s_mov_b32 exec_lo, 0x55555555\n\t"
      "ds_write_b16 %0, %9 offset:16\n\tds_write_b16 %0, %10 offset:272\n\tds_write_b16 %0, %11 offset:528\n\tds_write_b16 %0, %12 offset:784\n\t"
      "s_mov_b32 exec_lo, 0\n\ts_mov_b32 exec_hi, -1\n\t"
      "ds_write_b32 %1, %13\n\tds_write_b64 %1, %17 offset:4\n\tds_write_b32 %1, %14 offset:12\n\t"
      "ds_write_b32 %1, %15 offset:256\n\tds_write_b64 %1, %18 offset:260\n\tds_write_b32 %1, %16 offset:268\n\t"
      "s_mov_b32 exec_lo, -1\n\ts_mov_b32 exec_hi, 0\n\t"
      "ds_write_b16 %2, %13\n\tds_write_b64 %2, %19 offset:2\n\tds_write_b32 %2, %15 offset:10\n\tds_write_b16 %2, %16 offset:14\n\t"
      "s_mov_b32 exec_lo, 3\n\t"
      "ds_write_b128 %4, %21\n\t"
      "s_mov_b64 exec, -1\n\t"
      "ds_write_b64 %3, %20"
      ::"v"(p3), "v"(p2), "v"(p1), "v"(p0), "v"(pz), "v"(bb[0]), "v"(bb[1]), "v"(bb[2]), "v"(bb[3]), "v"(bb[0].x), "v"(bb[1].x), "v"(bb[2].x), "v"(bb[3].x),
        "v"(a00), "v"(a11), "v"(a22), "v"(a33), "v"(w2a), "v"(w2b), "v"(w1), "v"(r0), "v"(z4) : "memory");
  }
  v4u A[16];
  {
    const u32 ab = KB + 32 + 2 * (-12 + 4 * (8 * g + 7 - q) + 4 * pp);
#pragma unroll
    for (int s = 0; s < 16; s++) A[s] = trd2(ab + 256 * s, ab + 256 * s - 32);
  }
  ldsw(CTL + C_KDONE + slot * 4, 0);
  STAMP(13);
  wait0(CTL + C_UFULL);
  wait0(CTL + C_DONE + 160 + slot * 4);
  __builtin_amdgcn_s_setprio(CONV_PRIO);
  STAMP(18);
  f4 acc[4][8]; v4u B[9]; float y[4], c[2], cv[3], pc[3] = {0.f, 0.f, 0.f};
  const u32 ub = UB + 128 + 2 * (4 * j - 4 - 8 * g) - 64, srcl = (lane == 0 ? 63 : (g > 0 ? lane - 16 : lane + 47)) * 4;
  const u32 zo = WS_ZT + (g & 1) * 512 + j * 32 + (g >> 1) * 16;
  const u64 m63 = 1ull << 63;
  u32 ut[4] = {ub, ub + 1152, ub + 2304, ub + 3456};
  asm volatile("" : "+v"(ut[0]), "+v"(ut[1]), "+v"(ut[2]), "+v"(ut[3]));
#define LDB(k, tau, q) do { const u32 a_ = ut[tau] + 64 * (q); const v2u x_ = lds64(a_), y_ = lds64(a_ + 8); B[k] = v4u{x_.x, x_.y, y_.x, y_.y}; SCHED(); } while (0)
#define VADD(d, a_, b_) do { float t_; asm volatile("v_add_f32 %0, %1, %2" : "=v"(t_) : "v"(a_), "v"(b_)); d = t_; } while (0)
#define VMULS(d) asm volatile("v_mul_f32 %0, %0, %1" : "+v"(d) : "v"(oscale))
#define VSEL(d, a_, b_) asm volatile("v_cndmask_b32 %0, %1, %2, %3" : "=v"(d) : "v"(a_), "v"(b_), "s"(m63))
#define VPK(d, a_, b_) do { u32 t_; asm volatile("v_cvt_pk_bf16_f32 %0, %1, %2" : "=v"(t_) : "v"(a_), "v"(b_)); d = t_; } while (0)
#define NOPS() asm volatile("s_nop 3")
#define BPERM() asm volatile("ds_bpermute_b32 %0, %3, %0\n\tds_bpermute_b32 %1, %3, %1\n\tds_bpermute_b32 %2, %3, %2" : "+v"(cv[0]), "+v"(cv[1]), "+v"(cv[2]) : "v"(srcl) : "memory")
#define WAITB(n) asm volatile("s_waitcnt lgkmcnt(" #n ")" : "+v"(cv[0]), "+v"(cv[1]), "+v"(cv[2]) :: "memory")
#define ZSTORE(T) do { asm volatile("s_nop 1\n\tv_permlane16_swap_b32 %0, %2\n\tv_permlane16_swap_b32 %1, %3" : "+v"(zx0), "+v"(zy0), "+v"(zx1), "+v"(zy1)); \
    if (!ABL_Z) stc128(v4u{zx0, zy0, zx1, zy1}, rw, zo, ch * 4096 + (T) * 512); } while (0)
  u32 zx0, zy0, zx1, zy1;
  LDB(0, 0, 0);
  LDB(1, 1, 0);
  LDB(2, 2, 0);
  LDB(3, 3, 0);
  LDB(4, 0, 1);
  LDB(5, 1, 1);
  LDB(6, 2, 1);
  LDB(7, 3, 1);
  LDB(8, 0, 2);
  mma_hf0(acc[0][7], A[15], B[0]);
  mma_hf0(acc[0][6], A[13], B[0]);
  mma_hf0(acc[0][5], A[11], B[0]);
  mma_hf0(acc[0][4], A[9], B[0]);
  mma_hf0(acc[0][3], A[7], B[0]);
  mma_hf0(acc[0][2], A[5], B[0]);
  mma_hf0(acc[0][1], A[3], B[0]);
  mma_hf0(acc[0][0], A[1], B[0]);
  LDB(0, 1, 2);
  mma_hf0(acc[1][7], A[15], B[1]);
  mma_hf0(acc[1][6], A[13], B[1]);
  mma_hf0(acc[1][5], A[11], B[1]);
  mma_hf0(acc[1][4], A[9], B[1]);
  mma_hf0(acc[1][3], A[7], B[1]);
  mma_hf0(acc[1][2], A[5], B[1]);
  mma_hf0(acc[1][1], A[3], B[1]);
  mma_hf0(acc[1][0], A[1], B[1]);
  LDB(1, 2, 2);
  mma_hf0(acc[2][7], A[15], B[2]);
  mma_hf0(acc[2][6], A[13], B[2]);
  mma_hf0(acc[2][5], A[11], B[2]);
  mma_hf0(acc[2][4], A[9], B[2]);
  mma_hf0(acc[2][3], A[7], B[2]);
  mma_hf0(acc[2][2], A[5], B[2]);
  mma_hf0(acc[2][1], A[3], B[2]);
  mma_hf0(acc[2][0], A[1], B[2]);
  LDB(2, 3, 2);
  mma_hf0(acc[3][7], A[15], B[3]);
  mma_hf0(acc[3][6], A[13], B[3]);
  mma_hf0(acc[3][5], A[11], B[3]);
  mma_hf0(acc[3][4], A[9], B[3]);
  mma_hf0(acc[3][3], A[7], B[3]);
  mma_hf0(acc[3][2], A[5], B[3]);
  mma_hf0(acc[3][1], A[3], B[3]);
  mma_hf0(acc[3][0], A[1], B[3]);
  LDB(3, 0, 3);
  mma_hf(acc[0][7], A[14], B[4]);
  mma_hf(acc[0][6], A[12], B[4]);
  mma_hf(acc[0][5], A[10], B[4]);
  mma_hf(acc[0][4], A[8], B[4]);
  mma_hf(acc[0][3], A[6], B[4]);
  mma_hf(acc[0][2], A[4], B[4]);
  mma_hf(acc[0][1], A[2], B[4]);
  mma_hf(acc[0][0], A[0], B[4]);
  LDB(4, 1, 3);
  mma_hf(acc[1][7], A[14], B[5]);
  mma_hf(acc[1][6], A[12], B[5]);
  mma_hf(acc[1][5], A[10], B[5]);
  mma_hf(acc[1][4], A[8], B[5]);
  mma_hf(acc[1][3], A[6], B[5]);
  mma_hf(acc[1][2], A[4], B[5]);
  mma_hf(acc[1][1], A[2], B[5]);
  mma_hf(acc[1][0], A[0], B[5]);
  LDB(5, 2, 3);
  mma_hf(acc[2][7], A[14], B[6]);
  mma_hf(acc[2][6], A[12], B[6]);
  mma_hf(acc[2][5], A[10], B[6]);
  mma_hf(acc[2][4], A[8], B[6]);
  mma_hf(acc[2][3], A[6], B[6]);
  mma_hf(acc[2][2], A[4], B[6]);
  mma_hf(acc[2][1], A[2], B[6]);
  mma_hf(acc[2][0], A[0], B[6]);
  LDB(6, 3, 3);
  mma_hf(acc[3][7], A[14], B[7]);
  mma_hf(acc[3][6], A[12], B[7]);
  mma_hf(acc[3][5], A[10], B[7]);
  mma_hf(acc[3][4], A[8], B[7]);
  mma_hf(acc[3][3], A[6], B[7]);
  mma_hf(acc[3][2], A[4], B[7]);
  mma_hf(acc[3][1], A[2], B[7]);
  mma_hf(acc[3][0], A[0], B[7]);
  LDB(7, 0, 4);
  mma_hf(acc[0][7], A[13], B[8]);
  VADD(y[1], acc[0][0][1], acc[1][0][0]);
  VADD(y[2], acc[0][0][2], acc[1][0][1]);
  mma_hf(acc[0][6], A[11], B[8]);
  VADD(y[3], acc[0][0][3], acc[1][0][2]);
  VADD(c[0], acc[1][0][3], acc[2][0][2]);
  mma_hf(acc[0][5], A[9], B[8]);
  VADD(y[2], y[2], acc[2][0][0]);
  VADD(y[3], y[3], acc[2][0][1]);
  mma_hf(acc[0][4], A[7], B[8]);
  NOPS();
  VADD(c[0], c[0], acc[3][0][1]);
  mma_hf(acc[0][3], A[5], B[8]);
  VADD(c[1], acc[2][0][3], acc[3][0][2]);
  VADD(y[3], y[3], acc[3][0][0]);
  mma_hf(acc[0][2], A[3], B[8]);
  VSEL(cv[0], c[0], pc[0]);
  mma_hf(acc[0][1], A[1], B[8]);
  VSEL(cv[1], c[1], pc[1]);
  LDB(8, 1, 4);
  mma_hf(acc[1][7], A[13], B[0]);
  VSEL(cv[2], acc[3][0][3], pc[2]);
  mma_hf(acc[1][6], A[11], B[0]);
  BPERM(); pc[0] = c[0]; pc[1] = c[1]; pc[2] = acc[3][0][3];
  mma_hf(acc[1][5], A[9], B[0]);
  VMULS(y[3]);
  mma_hf(acc[1][4], A[7], B[0]);
  mma_hf(acc[1][3], A[5], B[0]);
  mma_hf(acc[1][2], A[3], B[0]);
  mma_hf(acc[1][1], A[1], B[0]);
  LDB(0, 2, 4);
  mma_hf(acc[2][7], A[13], B[1]);
  mma_hf(acc[2][6], A[11], B[1]);
  mma_hf(acc[2][5], A[9], B[1]);
  WAITB(1);
  VADD(y[0], acc[0][0][0], cv[0]);
  mma_hf(acc[2][4], A[7], B[1]);
  VADD(y[1], y[1], cv[1]);
  mma_hf(acc[2][3], A[5], B[1]);
  VADD(y[2], y[2], cv[2]);
  mma_hf(acc[2][2], A[3], B[1]);
  VMULS(y[0]);
  mma_hf(acc[2][1], A[1], B[1]);
  VMULS(y[1]);
  LDB(1, 3, 4);
  mma_hf(acc[3][7], A[13], B[2]);
  VMULS(y[2]);
  mma_hf(acc[3][6], A[11], B[2]);
  VPK(zx0, y[0], y[1]);
  mma_hf(acc[3][5], A[9], B[2]);
  VPK(zy0, y[2], y[3]);
  mma_hf(acc[3][4], A[7], B[2]);
  mma_hf(acc[3][3], A[5], B[2]);
  mma_hf(acc[3][2], A[3], B[2]);
  mma_hf(acc[3][1], A[1], B[2]);
  LDB(2, 0, 5);
  mma_hf(acc[0][7], A[12], B[3]);
  mma_hf(acc[0][6], A[10], B[3]);
  mma_hf(acc[0][5], A[8], B[3]);
  mma_hf(acc[0][4], A[6], B[3]);
  mma_hf(acc[0][3], A[4], B[3]);
  mma_hf(acc[0][2], A[2], B[3]);
  mma_hf(acc[0][1], A[0], B[3]);
  LDB(3, 1, 5);
  mma_hf(acc[1][7], A[12], B[4]);
  mma_hf(acc[1][6], A[10], B[4]);
  mma_hf(acc[1][5], A[8], B[4]);
  mma_hf(acc[1][4], A[6], B[4]);
  mma_hf(acc[1][3], A[4], B[4]);
  mma_hf(acc[1][2], A[2], B[4]);
  mma_hf(acc[1][1], A[0], B[4]);
  LDB(4, 2, 5);
  mma_hf(acc[2][7], A[12], B[5]);
  mma_hf(acc[2][6], A[10], B[5]);
  mma_hf(acc[2][5], A[8], B[5]);
  mma_hf(acc[2][4], A[6], B[5]);
  mma_hf(acc[2][3], A[4], B[5]);
  mma_hf(acc[2][2], A[2], B[5]);
  mma_hf(acc[2][1], A[0], B[5]);
  LDB(5, 3, 5);
  mma_hf(acc[3][7], A[12], B[6]);
  mma_hf(acc[3][6], A[10], B[6]);
  mma_hf(acc[3][5], A[8], B[6]);
  mma_hf(acc[3][4], A[6], B[6]);
  mma_hf(acc[3][3], A[4], B[6]);
  mma_hf(acc[3][2], A[2], B[6]);
  mma_hf(acc[3][1], A[0], B[6]);
  LDB(6, 0, 6);
  mma_hf(acc[0][7], A[11], B[7]);
  VADD(y[1], acc[0][1][1], acc[1][1][0]);
  VADD(y[2], acc[0][1][2], acc[1][1][1]);
  mma_hf(acc[0][6], A[9], B[7]);
  VADD(y[3], acc[0][1][3], acc[1][1][2]);
  VADD(c[0], acc[1][1][3], acc[2][1][2]);
  mma_hf(acc[0][5], A[7], B[7]);
  VADD(y[2], y[2], acc[2][1][0]);
  VADD(y[3], y[3], acc[2][1][1]);
  mma_hf(acc[0][4], A[5], B[7]);
  NOPS();
  VADD(c[0], c[0], acc[3][1][1]);
  mma_hf(acc[0][3], A[3], B[7]);
  VADD(c[1], acc[2][1][3], acc[3][1][2]);
  VADD(y[3], y[3], acc[3][1][0]);
  mma_hf(acc[0][2], A[1], B[7]);
  VSEL(cv[0], c[0], pc[0]);
  VSEL(cv[1], c[1], pc[1]);
  LDB(7, 1, 6);
  mma_hf(acc[1][7], A[11], B[8]);
  VSEL(cv[2], acc[3][1][3], pc[2]);
  mma_hf(acc[1][6], A[9], B[8]);
  BPERM(); pc[0] = c[0]; pc[1] = c[1]; pc[2] = acc[3][1][3];
  mma_hf(acc[1][5], A[7], B[8]);
  VMULS(y[3]);
  mma_hf(acc[1][4], A[5], B[8]);
  mma_hf(acc[1][3], A[3], B[8]);
  mma_hf(acc[1][2], A[1], B[8]);
  LDB(8, 2, 6);
  mma_hf(acc[2][7], A[11], B[0]);
  mma_hf(acc[2][6], A[9], B[0]);
  mma_hf(acc[2][5], A[7], B[0]);
  mma_hf(acc[2][4], A[5], B[0]);
  WAITB(1);
  VADD(y[0], acc[0][1][0], cv[0]);
  mma_hf(acc[2][3], A[3], B[0]);
  VADD(y[1], y[1], cv[1]);
  mma_hf(acc[2][2], A[1], B[0]);
  VADD(y[2], y[2], cv[2]);
  LDB(0, 3, 6);
  mma_hf(acc[3][7], A[11], B[1]);
  VMULS(y[0]);
  mma_hf(acc[3][6], A[9], B[1]);
  VMULS(y[1]);
  mma_hf(acc[3][5], A[7], B[1]);
  VMULS(y[2]);
  mma_hf(acc[3][4], A[5], B[1]);
  VPK(zx1, y[0], y[1]);
  mma_hf(acc[3][3], A[3], B[1]);
  VPK(zy1, y[2], y[3]);
  mma_hf(acc[3][2], A[1], B[1]);
  ZSTORE(0);
  LDB(1, 0, 7);
  mma_hf(acc[0][7], A[10], B[2]);
  mma_hf(acc[0][6], A[8], B[2]);
  mma_hf(acc[0][5], A[6], B[2]);
  mma_hf(acc[0][4], A[4], B[2]);
  mma_hf(acc[0][3], A[2], B[2]);
  mma_hf(acc[0][2], A[0], B[2]);
  LDB(2, 1, 7);
  mma_hf(acc[1][7], A[10], B[3]);
  mma_hf(acc[1][6], A[8], B[3]);
  mma_hf(acc[1][5], A[6], B[3]);
  mma_hf(acc[1][4], A[4], B[3]);
  mma_hf(acc[1][3], A[2], B[3]);
  mma_hf(acc[1][2], A[0], B[3]);
  LDB(3, 2, 7);
  mma_hf(acc[2][7], A[10], B[4]);
  mma_hf(acc[2][6], A[8], B[4]);
  mma_hf(acc[2][5], A[6], B[4]);
  mma_hf(acc[2][4], A[4], B[4]);
  mma_hf(acc[2][3], A[2], B[4]);
  mma_hf(acc[2][2], A[0], B[4]);
  LDB(4, 3, 7);
  mma_hf(acc[3][7], A[10], B[5]);
  mma_hf(acc[3][6], A[8], B[5]);
  mma_hf(acc[3][5], A[6], B[5]);
  mma_hf(acc[3][4], A[4], B[5]);
  mma_hf(acc[3][3], A[2], B[5]);
  mma_hf(acc[3][2], A[0], B[5]);
  LDB(5, 0, 8);
  mma_hf(acc[0][7], A[9], B[6]);
  VADD(y[1], acc[0][2][1], acc[1][2][0]);
  VADD(y[2], acc[0][2][2], acc[1][2][1]);
  mma_hf(acc[0][6], A[7], B[6]);
  VADD(y[3], acc[0][2][3], acc[1][2][2]);
  VADD(c[0], acc[1][2][3], acc[2][2][2]);
  mma_hf(acc[0][5], A[5], B[6]);
  VADD(y[2], y[2], acc[2][2][0]);
  VADD(y[3], y[3], acc[2][2][1]);
  mma_hf(acc[0][4], A[3], B[6]);
  NOPS();
  VADD(c[0], c[0], acc[3][2][1]);
  mma_hf(acc[0][3], A[1], B[6]);
  VADD(c[1], acc[2][2][3], acc[3][2][2]);
  VADD(y[3], y[3], acc[3][2][0]);
  LDB(6, 1, 8);
  mma_hf(acc[1][7], A[9], B[7]);
  VSEL(cv[0], c[0], pc[0]);
  mma_hf(acc[1][6], A[7], B[7]);
  VSEL(cv[1], c[1], pc[1]);
  mma_hf(acc[1][5], A[5], B[7]);
  VSEL(cv[2], acc[3][2][3], pc[2]);
  mma_hf(acc[1][4], A[3], B[7]);
  BPERM(); pc[0] = c[0]; pc[1] = c[1]; pc[2] = acc[3][2][3];
  mma_hf(acc[1][3], A[1], B[7]);
  VMULS(y[3]);
  LDB(7, 2, 8);
  mma_hf(acc[2][7], A[9], B[8]);
  mma_hf(acc[2][6], A[7], B[8]);
  mma_hf(acc[2][5], A[5], B[8]);
  mma_hf(acc[2][4], A[3], B[8]);
  mma_hf(acc[2][3], A[1], B[8]);
  LDB(8, 3, 8);
  mma_hf(acc[3][7], A[9], B[0]);
  mma_hf(acc[3][6], A[7], B[0]);
  WAITB(2);
  VADD(y[0], acc[0][2][0], cv[0]);
  mma_hf(acc[3][5], A[5], B[0]);
  VADD(y[1], y[1], cv[1]);
  mma_hf(acc[3][4], A[3], B[0]);
  VADD(y[2], y[2], cv[2]);
  mma_hf(acc[3][3], A[1], B[0]);
  VMULS(y[0]);
  LDB(0, 0, 9);
  mma_hf(acc[0][7], A[8], B[1]);
  VMULS(y[1]);
  mma_hf(acc[0][6], A[6], B[1]);
  VMULS(y[2]);
  mma_hf(acc[0][5], A[4], B[1]);
  VPK(zx0, y[0], y[1]);
  mma_hf(acc[0][4], A[2], B[1]);
  VPK(zy0, y[2], y[3]);
  mma_hf(acc[0][3], A[0], B[1]);
  LDB(1, 1, 9);
  mma_hf(acc[1][7], A[8], B[2]);
  mma_hf(acc[1][6], A[6], B[2]);
  mma_hf(acc[1][5], A[4], B[2]);
  mma_hf(acc[1][4], A[2], B[2]);
  mma_hf(acc[1][3], A[0], B[2]);
  LDB(2, 2, 9);
  mma_hf(acc[2][7], A[8], B[3]);
  mma_hf(acc[2][6], A[6], B[3]);
  mma_hf(acc[2][5], A[4], B[3]);
  mma_hf(acc[2][4], A[2], B[3]);
  mma_hf(acc[2][3], A[0], B[3]);
  LDB(3, 3, 9);
  mma_hf(acc[3][7], A[8], B[4]);
  mma_hf(acc[3][6], A[6], B[4]);
  mma_hf(acc[3][5], A[4], B[4]);
  mma_hf(acc[3][4], A[2], B[4]);
  mma_hf(acc[3][3], A[0], B[4]);
  LDB(4, 0, 10);
  mma_hf(acc[0][7], A[7], B[5]);
  VADD(y[1], acc[0][3][1], acc[1][3][0]);
  VADD(y[2], acc[0][3][2], acc[1][3][1]);
  mma_hf(acc[0][6], A[5], B[5]);
  VADD(y[3], acc[0][3][3], acc[1][3][2]);
  VADD(c[0], acc[1][3][3], acc[2][3][2]);
  mma_hf(acc[0][5], A[3], B[5]);
  VADD(y[2], y[2], acc[2][3][0]);
  VADD(y[3], y[3], acc[2][3][1]);
  mma_hf(acc[0][4], A[1], B[5]);
  NOPS();
  VADD(c[0], c[0], acc[3][3][1]);
  LDB(5, 1, 10);
  mma_hf(acc[1][7], A[7], B[6]);
  VADD(c[1], acc[2][3][3], acc[3][3][2]);
  VADD(y[3], y[3], acc[3][3][0]);
  mma_hf(acc[1][6], A[5], B[6]);
  VSEL(cv[0], c[0], pc[0]);
  VSEL(cv[1], c[1], pc[1]);
  mma_hf(acc[1][5], A[3], B[6]);
  VSEL(cv[2], acc[3][3][3], pc[2]);
  mma_hf(acc[1][4], A[1], B[6]);
  BPERM(); pc[0] = c[0]; pc[1] = c[1]; pc[2] = acc[3][3][3];
  LDB(6, 2, 10);
  mma_hf(acc[2][7], A[7], B[7]);
  VMULS(y[3]);
  mma_hf(acc[2][6], A[5], B[7]);
  mma_hf(acc[2][5], A[3], B[7]);
  mma_hf(acc[2][4], A[1], B[7]);
  LDB(7, 3, 10);
  mma_hf(acc[3][7], A[7], B[8]);
  mma_hf(acc[3][6], A[5], B[8]);
  mma_hf(acc[3][5], A[3], B[8]);
  mma_hf(acc[3][4], A[1], B[8]);
  WAITB(2);
  VADD(y[0], acc[0][3][0], cv[0]);
  LDB(8, 0, 11);
  mma_hf(acc[0][7], A[6], B[0]);
  VADD(y[1], y[1], cv[1]);
  mma_hf(acc[0][6], A[4], B[0]);
  VADD(y[2], y[2], cv[2]);
  mma_hf(acc[0][5], A[2], B[0]);
  VMULS(y[0]);
  mma_hf(acc[0][4], A[0], B[0]);
  VMULS(y[1]);
  LDB(0, 1, 11);
  mma_hf(acc[1][7], A[6], B[1]);
  VMULS(y[2]);
  mma_hf(acc[1][6], A[4], B[1]);
  VPK(zx1, y[0], y[1]);
  mma_hf(acc[1][5], A[2], B[1]);
  VPK(zy1, y[2], y[3]);
  mma_hf(acc[1][4], A[0], B[1]);
  ZSTORE(2);
  LDB(1, 2, 11);
  mma_hf(acc[2][7], A[6], B[2]);
  mma_hf(acc[2][6], A[4], B[2]);
  mma_hf(acc[2][5], A[2], B[2]);
  mma_hf(acc[2][4], A[0], B[2]);
  LDB(2, 3, 11);
  mma_hf(acc[3][7], A[6], B[3]);
  mma_hf(acc[3][6], A[4], B[3]);
  mma_hf(acc[3][5], A[2], B[3]);
  mma_hf(acc[3][4], A[0], B[3]);
  LDB(3, 0, 12);
  mma_hf(acc[0][7], A[5], B[4]);
  VADD(y[1], acc[0][4][1], acc[1][4][0]);
  VADD(y[2], acc[0][4][2], acc[1][4][1]);
  mma_hf(acc[0][6], A[3], B[4]);
  VADD(y[3], acc[0][4][3], acc[1][4][2]);
  VADD(c[0], acc[1][4][3], acc[2][4][2]);
  mma_hf(acc[0][5], A[1], B[4]);
  VADD(y[2], y[2], acc[2][4][0]);
  VADD(y[3], y[3], acc[2][4][1]);
  LDB(4, 1, 12);
  mma_hf(acc[1][7], A[5], B[5]);
  NOPS();
  VADD(c[0], c[0], acc[3][4][1]);
  mma_hf(acc[1][6], A[3], B[5]);
  VADD(c[1], acc[2][4][3], acc[3][4][2]);
  VADD(y[3], y[3], acc[3][4][0]);
  mma_hf(acc[1][5], A[1], B[5]);
  VSEL(cv[0], c[0], pc[0]);
  VSEL(cv[1], c[1], pc[1]);
  LDB(5, 2, 12);
  mma_hf(acc[2][7], A[5], B[6]);
  VSEL(cv[2], acc[3][4][3], pc[2]);
  BPERM(); pc[0] = c[0]; pc[1] = c[1]; pc[2] = acc[3][4][3];
  mma_hf(acc[2][6], A[3], B[6]);
  VMULS(y[3]);
  mma_hf(acc[2][5], A[1], B[6]);
  LDB(6, 3, 12);
  mma_hf(acc[3][7], A[5], B[7]);
  mma_hf(acc[3][6], A[3], B[7]);
  WAITB(1);
  VADD(y[0], acc[0][4][0], cv[0]);
  mma_hf(acc[3][5], A[1], B[7]);
  VADD(y[1], y[1], cv[1]);
  VADD(y[2], y[2], cv[2]);
  LDB(7, 0, 13);
  mma_hf(acc[0][7], A[4], B[8]);
  VMULS(y[0]);
  VMULS(y[1]);
  mma_hf(acc[0][6], A[2], B[8]);
  VMULS(y[2]);
  VPK(zx0, y[0], y[1]);
  mma_hf(acc[0][5], A[0], B[8]);
  VPK(zy0, y[2], y[3]);
  LDB(8, 1, 13);
  mma_hf(acc[1][7], A[4], B[0]);
  mma_hf(acc[1][6], A[2], B[0]);
  mma_hf(acc[1][5], A[0], B[0]);
  LDB(0, 2, 13);
  mma_hf(acc[2][7], A[4], B[1]);
  mma_hf(acc[2][6], A[2], B[1]);
  mma_hf(acc[2][5], A[0], B[1]);
  LDB(1, 3, 13);
  mma_hf(acc[3][7], A[4], B[2]);
  mma_hf(acc[3][6], A[2], B[2]);
  mma_hf(acc[3][5], A[0], B[2]);
  LDB(2, 0, 14);
  mma_hf(acc[0][7], A[3], B[3]);
  VADD(y[1], acc[0][5][1], acc[1][5][0]);
  VADD(y[2], acc[0][5][2], acc[1][5][1]);
  mma_hf(acc[0][6], A[1], B[3]);
  VADD(y[3], acc[0][5][3], acc[1][5][2]);
  VADD(c[0], acc[1][5][3], acc[2][5][2]);
  LDB(3, 1, 14);
  mma_hf(acc[1][7], A[3], B[4]);
  VADD(y[2], y[2], acc[2][5][0]);
  VADD(y[3], y[3], acc[2][5][1]);
  mma_hf(acc[1][6], A[1], B[4]);
  NOPS();
  VADD(c[0], c[0], acc[3][5][1]);
  LDB(4, 2, 14);
  mma_hf(acc[2][7], A[3], B[5]);
  VADD(c[1], acc[2][5][3], acc[3][5][2]);
  VADD(y[3], y[3], acc[3][5][0]);
  mma_hf(acc[2][6], A[1], B[5]);
  VSEL(cv[0], c[0], pc[0]);
  VSEL(cv[1], c[1], pc[1]);
  LDB(5, 3, 14);
  mma_hf(acc[3][7], A[3], B[6]);
  VSEL(cv[2], acc[3][5][3], pc[2]);
  BPERM(); pc[0] = c[0]; pc[1] = c[1]; pc[2] = acc[3][5][3];
  mma_hf(acc[3][6], A[1], B[6]);
  VMULS(y[3]);
  LDB(6, 0, 15);
  mma_hf(acc[0][7], A[2], B[7]);
  mma_hf(acc[0][6], A[0], B[7]);
  LDB(7, 1, 15);
  mma_hf(acc[1][7], A[2], B[8]);
  WAITB(2);
  VADD(y[0], acc[0][5][0], cv[0]);
  mma_hf(acc[1][6], A[0], B[8]);
  VADD(y[1], y[1], cv[1]);
  VADD(y[2], y[2], cv[2]);
  LDB(8, 2, 15);
  mma_hf(acc[2][7], A[2], B[0]);
  VMULS(y[0]);
  VMULS(y[1]);
  mma_hf(acc[2][6], A[0], B[0]);
  VMULS(y[2]);
  VPK(zx1, y[0], y[1]);
  LDB(0, 3, 15);
  mma_hf(acc[3][7], A[2], B[1]);
  VPK(zy1, y[2], y[3]);
  ZSTORE(4);
  mma_hf(acc[3][6], A[0], B[1]);
  mma_hf(acc[0][7], A[1], B[2]);
  VADD(y[1], acc[0][6][1], acc[1][6][0]);
  VADD(y[2], acc[0][6][2], acc[1][6][1]);
  mma_hf(acc[1][7], A[1], B[3]);
  VADD(y[3], acc[0][6][3], acc[1][6][2]);
  VADD(c[0], acc[1][6][3], acc[2][6][2]);
  mma_hf(acc[2][7], A[1], B[4]);
  VADD(y[2], y[2], acc[2][6][0]);
  VADD(y[3], y[3], acc[2][6][1]);
  mma_hf(acc[3][7], A[1], B[5]);
  NOPS();
  VADD(c[0], c[0], acc[3][6][1]);
  mma_hf(acc[0][7], A[0], B[6]);
  VADD(c[1], acc[2][6][3], acc[3][6][2]);
  VADD(y[3], y[3], acc[3][6][0]);
  mma_hf(acc[1][7], A[0], B[7]);
  VSEL(cv[0], c[0], pc[0]);
  VSEL(cv[1], c[1], pc[1]);
  mma_hf(acc[2][7], A[0], B[8]);
  VSEL(cv[2], acc[3][6][3], pc[2]);
  BPERM(); pc[0] = c[0]; pc[1] = c[1]; pc[2] = acc[3][6][3];
  mma_hf(acc[3][7], A[0], B[0]);
  VMULS(y[3]);
  STAMP(19);
  WAITB(0);
  VADD(y[0], acc[0][6][0], cv[0]);
  VADD(y[1], y[1], cv[1]);
  VADD(y[2], y[2], cv[2]);
  VMULS(y[0]);
  VMULS(y[1]);
  VMULS(y[2]);
  VPK(zx0, y[0], y[1]);
  VPK(zy0, y[2], y[3]);
  VADD(y[1], acc[0][7][1], acc[1][7][0]);
  VADD(y[2], acc[0][7][2], acc[1][7][1]);
  VADD(y[3], acc[0][7][3], acc[1][7][2]);
  VADD(c[0], acc[1][7][3], acc[2][7][2]);
  VADD(y[2], y[2], acc[2][7][0]);
  VADD(y[3], y[3], acc[2][7][1]);
  NOPS();
  VADD(c[0], c[0], acc[3][7][1]);
  VADD(c[1], acc[2][7][3], acc[3][7][2]);
  VADD(y[3], y[3], acc[3][7][0]);
  VSEL(cv[0], c[0], pc[0]);
  VSEL(cv[1], c[1], pc[1]);
  VSEL(cv[2], acc[3][7][3], pc[2]);
  BPERM(); pc[0] = c[0]; pc[1] = c[1]; pc[2] = acc[3][7][3];
  VMULS(y[3]);
  WAITB(0);
  VADD(y[0], acc[0][7][0], cv[0]);
  VADD(y[1], y[1], cv[1]);
  VADD(y[2], y[2], cv[2]);
  VMULS(y[0]);
  VMULS(y[1]);
  VMULS(y[2]);
  VPK(zx1, y[0], y[1]);
  VPK(zy1, y[2], y[3]);
  ZSTORE(6);
  STAMP(20);
  waitv<0>();
  ldsw(CTL + C_CDONE + slot * 4, 0);
  STAMP(11);
}

__global__ __launch_bounds__(512, 2) void k_main(Params p) {
  extern __shared__ __attribute__((aligned(16))) char smem[];
  const u32 sb = (u32)(size_t)(LAS char*)smem;
  const int lane = threadIdx.x & 63, wv = rfl(threadIdx.x >> 6);
  const int b = blockIdx.x, xc = b & 7, r = b >> 3;
  const bool g1 = r < 30;
  const int r5 = r / 6, ri = (xc >> 2) * 5 + r5, cj = (xc & 3) * 6 + r % 6, ts = (ri * 64) / 5;
  const int i2 = xc * 4 + (r >> 3), j2 = r & 7;
#define KS2(k) (k)
  const u32 CTL = sb + LD_CTL;
  u64 pc0; u32 codeb;
  asm volatile("s_getpc_b64 %0\n.Lk_start:\n\ts_mov_b32 %1, .Lk_end-.Lk_start\n\t"
               ".subsection 2\n.Lk_end:\n\t.fill 128, 4, 0xBF800000\n\t.subsection 0" : "=s"(pc0), "=s"(codeb));
#define CSTAMP(k)
  u64 dg0 = 0, dgk = 0;
  STAMP(0);
  const int c6 = r % 6, rot = (OFFS >> (4 * ((r5 + c6) % NGRP))) & 15;
  const v4u rz = mkrs(p.ws, 0);
  {
    if (wv == 6)
      asm volatile("s_mov_b32 m0, %2\n\ts_nop 0\n\tbuffer_load_dword %0, %1, 0 offen lds" ::"v"(lane * 64 + (r & 15) * 4096), "s"(mkrs((const void*)pc0, codeb)), "s"(sb + LD_ST + 512) : "memory");
    const bool isw = (wv & 6) == 4;
    const int xi = wv < 4 ? wv : wv - 2;
    const u32 pitch = isw ? 3072 : 1280;
    const int rr = lane >> 3;
    const u32 sw0 = isw ? (rr & 2) : (rr >> 1);
    const u32 a0 = rr * pitch + (((lane & 7) ^ sw0) << 4), a1 = (a0 ^ 64) + 8 * pitch - 1024, a2 = a0 + 16 * pitch - 2048, a3 = a1 + 16 * pitch - 2048;
    const v4u rs = mkrs(wv == 4 ? p.wg : (wv == 5 ? p.wu : p.x), isw ? D * F * 2 : L * D * 2);
    u32 so = isw ? rot * 64 * 3072 + cj * 128 : (ts + 2 * xi) * 20480 + rot * 128, ld = isw ? sb + LD_R + (wv & 1) * 8192 : sb + LD_X + xi * 4096;
    const u32 dso = isw ? 32 * 3072 : 20480, dld = isw ? 4096 : 2048;
    const bool two = isw || wv == 7;
#pragma unroll 1
    for (int st = 0;; st++) {
      if (g1) { dma4(rs, a0, a1, a2, a3, so, ld); if (two) dma4(rs, a0, a1, a2, a3, so + dso, ld + dld); }
      if (st == 0) {
        if (wv == 2) asm volatile("s_mov_b32 m0, %0\n\ts_mov_b64 exec, 1\n\tbuffer_load_dword off, %1, 0 sc1 lds\n\ts_mov_b64 exec, -1" ::"s"(CTL + C_TAG), "s"(mkrs(p.ws, 64)) : "memory");
        if (wv < 4) {
          const u64 m = wv == 0 ? ((r & 1) ? 0x1f3f37800ull : 0x1f7f77800ull) : (wv == 1 ? (g1 ? ~0ull : 0ull) : (wv == 2 ? (g1 ? 0x01010101010131ffull : 0xffull) : (g1 ? 0x0101ull : 0ull)));
          if (!(wv == 0 && lane == 15) && !(wv == 3 && lane >= 32)) *(volatile LAS u32*)(CTL + wv * 256 + lane * 4) = (u32)(m >> lane) & 1u;
        }
        BARRIER();
      }
      sig0(rz, CTL + C_FULL + st * 32 + wv * 4);
      if (st == 1 || !isw) break;
      so += 64 * 3072; ld += WSLOT;
    }
    STAMP(30);
  }
  __builtin_amdgcn_wave_barrier();
  const int g = lane >> 4, i = lane & 15;
  const rsrc_t rw = mkr(p.ws, WS_BYTES);
  const int cstep = ((c6 == 0 ? CT0 : c6 == 1 ? CT1 : c6 == 2 ? CT2 : c6 == 3 ? CT3 : c6 == 4 ? CT4 : CT5) >> (3 * ri)) & 7;
  const int ch0 = 80 * xc + 5 * (r >> 1) + (r & 1) * 3;
  __builtin_amdgcn_wave_barrier();
  if (wv < 4) {
    __builtin_amdgcn_s_setprio(2);
    const rsrc_t rx = mkr(p.x, L * D * 2);
    constexpr int NWR = 5;
    v4u Wr[NWR][5];
    const u32 wdo = WS_WDF + (j2 * 48 + 12 * wv) * 5120;
    u32 tag;
    if (g1) {
      f4 acc[3][8], a13[2];
#pragma unroll
      for (int t = 0; t < 3; t++)
#pragma unroll
        for (int c = 0; c < 8; c++) { acc[t][c] = f4{0, 0, 0, 0}; asm volatile("" : "+v"(acc[t][c])); }
      a13[0] = a13[1] = f4{0, 0, 0, 0};
      const int q = (lane >> 2) & 3, pp = lane & 3, fp = ((q >> 1) & 1) | ((g & 1) << 1);
      u32 wb[4];
#pragma unroll
      for (int c = 0; c < 4; c++) wb[c] = sb + LD_R + (8 * g + q) * 128 + ((c ^ wv ^ fp) * 32) + pp * 8;
      u32 xb0 = sb + LD_X + wv * 6144 + i * 128 + ((g ^ (i >> 1)) << 4), xb1 = xb0 ^ 64;
      u32 yb0 = xb0 + (12 - 3 * wv) * 2048, yb1 = yb0 ^ 64, fa = CTL + C_FULL + (lane & 7) * 4;
      u32 sta = sb + LD_ST + (3 * wv * 16 + i) * 8, csa = sb + LD_CS + (16 * wv + 4 * g) * 4;
      u32 hb = WS_HF + (2 * cj) * 1024 + (((((g ^ wv) & 1) << 1) | (g >> 1)) * 16 + i) * 16;
      asm volatile("" : "+v"(sta), "+v"(csa), "+v"(hb));
      v4u Wf[2][8], Xf[2][4];
      asm volatile("" : "+v"(a13[0]), "+v"(a13[1]), "+v"(wb[0]), "+v"(wb[1]), "+v"(wb[2]), "+v"(wb[3]), "+v"(xb0), "+v"(xb1), "+v"(yb0), "+v"(yb1), "+v"(fa));
#define RDP(B, n, XA, YA, OFF) do { if ((n) == 0) { Xf[B][0] = lds128(XA); Xf[B][1] = lds128(XA + 2048); } \
        else if ((n) == 2) { Xf[B][2] = lds128(XA + 4096); Xf[B][3] = lds128(YA); } \
        else { constexpr int c = (n) == 1 ? 0 : (n) - 2; const u32 a = wb[c & 3] + (OFF) + (c >> 2) * 8192; Wf[B][c] = trd2(a, a + 512); } SCHED(); } while (0)
#define MM(KS, m) do { constexpr int c = (m) < 4 ? 0 : (m) < 13 ? 1 + ((m) - 4) / 3 : (m) < 17 ? 4 : 5 + ((m) - 17) / 3; \
        constexpr int t = (m) < 4 ? (m) : (m) < 13 ? ((m) - 4) % 3 : (m) < 17 ? (m) - 13 : ((m) - 17) % 3; \
        if (t == 3) mma_bf(a13[c >> 2], Wf[KS][c], Xf[KS][3]); else mma_bf(acc[t < 3 ? t : 0][c], Wf[KS][c], Xf[KS][t]); SCHED(); } while (0)
#define R0(m) MM(0, m); RDP(1, m, xb1, yb1, 4096);
#define R1(m) MM(1, m); RDP(0, (m) - 12, xb0, yb0, 0);
#pragma unroll
      for (int c = 0; c < 8; c++) { Wf[1][c] = v4u{0, 0, 0, 0}; asm volatile("" : "+v"(Wf[1][c])); }
#pragma unroll
      for (int c = 0; c < 4; c++) { Xf[1][c] = v4u{0, 0, 0, 0}; asm volatile("" : "+v"(Xf[1][c])); }
      u32 sw = 0, sx = 0, fo = 1;
#pragma unroll 1
      for (int s = 0; s < 10; s++) {
        u32 fw;
        spin0(fa, fo);
        if (s == 0) STAMP(1);
        R1(12) R1(13) R1(14) R1(15) R1(16) R1(17) R1(18) R1(19) R1(20) R1(21)
        MM(1, 22); fa += 32; asm volatile("" : "+v"(fa)); SCHED(); MM(1, 23); MM(1, 24); MM(1, 25);
        R0(0) R0(1) R0(2) R0(3) R0(4) R0(5) R0(6) R0(7) R0(8) R0(9)
        sx += XSLOT; u32 dx = XSLOT; if (sx == NXS * XSLOT) { sx = 0; dx = XSLOT - NXS * XSLOT; }
        sw += WSLOT; u32 dl = WSLOT; if (sw == NWS * WSLOT) { sw = 0; dl = WSLOT - NWS * WSLOT; }
        MM(0, 10); xb0 += dx; xb1 += dx; asm volatile("" : "+v"(xb0), "+v"(xb1)); SCHED();
        MM(0, 11); yb0 += dx; yb1 += dx; asm volatile("" : "+v"(yb0), "+v"(yb1)); SCHED();
        MM(0, 12); wb[0] += dl; wb[1] += dl; asm volatile("" : "+v"(wb[0]), "+v"(wb[1])); SCHED();
        MM(0, 13); wb[2] += dl; wb[3] += dl; asm volatile("" : "+v"(wb[2]), "+v"(wb[3])); SCHED();
        MM(0, 14); MM(0, 15);
        asm volatile("s_waitcnt lgkmcnt(0)" ::: "memory");
        if (G1ONLY) STAMPD(3 + s - (s == 9 ? -1 : 0));
        sig0(rz, CTL + C_DONE + s * 16 + wv * 4); SCHED();
        if (s == cstep) {
          const u32 tg = mktag(ldsv(CTL + C_TAG));
          f4 c0 = {0, 0, 0, 0}, c1 = {0, 0, 0, 0}; v4u on = {0x3f803f80u, 0x3f803f80u, 0x3f803f80u, 0x3f803f80u};
          asm volatile("s_nop 1" : "+v"(c0), "+v"(c1), "+v"(on));
          mma_bf(c0, Wf[0][0], on); mma_bf(c1, Wf[0][4], on); mma_bf(c0, Wf[1][0], on); mma_bf(c1, Wf[1][4], on);
          asm volatile("s_nop 7" : "+v"(c0), "+v"(c1));
          const f4 cc = (i & 1) ? c1 : c0;
          if (i < 2) {
            const u32 o = WS_CSP + (i * 64 + 16 * wv + 4 * g) * 8, so = (cj * 10 + ri) * 1024;
            stc128(v4u{__float_as_uint(cc[0]), tg, __float_as_uint(cc[1]), tg}, rw, o, so); stc128(v4u{__float_as_uint(cc[2]), tg, __float_as_uint(cc[3]), tg}, rw, o + 16, so);
          }
          asm volatile("s_nop 3"); SCHED();
        }
        MM(0, 16); MM(0, 17); MM(0, 18); MM(0, 19); MM(0, 20); MM(0, 21); MM(0, 22); MM(0, 23); MM(0, 24); MM(0, 25);
        MM(1, 0); MM(1, 1); MM(1, 2); MM(1, 3); fw = ldsv(fa); SCHED();
        MM(1, 4); MM(1, 5); MM(1, 6); MM(1, 7); MM(1, 8); MM(1, 9); fo = (u32)__builtin_amdgcn_ballot_w64(fw != 0); SCHED(); MM(1, 10); MM(1, 11);
      }
      { const v4u cw = *(volatile LAS v4u*)(CTL + C_RDY); MM(1, 12); MM(1, 13); MM(1, 14); MM(1, 15); tag = mktag(rfl(cw.w)); spin0(CTL + C_RDY, rfl(cw.x)); }
      f4 cg[4], cu[4]; f2 st[4], k1[4], k2[4];
#pragma unroll
      for (int t = 0; t < 4; t++) st[t] = *(LAS f2*)(sta + (t < 3 ? t * 128 : 0) + (t < 3 ? 0 : (12 - 3 * wv) * 128));
      SCHED(); MM(1, 16);
      cg[0] = *(LAS f4*)(csa); cu[0] = *(LAS f4*)(csa + 256); SCHED(); MM(1, 17);
      cg[1] = *(LAS f4*)(csa ^ 64); cu[1] = *(LAS f4*)((csa ^ 64) + 256); SCHED(); MM(1, 18);
      cg[2] = *(LAS f4*)(csa ^ 128); cu[2] = *(LAS f4*)((csa ^ 128) + 256); SCHED(); MM(1, 19);
      cg[3] = *(LAS f4*)(csa ^ 192); cu[3] = *(LAS f4*)((csa ^ 192) + 256); SCHED(); MM(1, 20); MM(1, 21);
#define KSET(t) do { k1[t] = f2{__uint_as_float(__float_as_uint(st[t].x) ^ 0x80000000u), fmul_(-1.44269504089f, st[t].y)}; k2[t].x = __builtin_amdgcn_rcpf(st[t].y); asm volatile("" : "+v"(k1[t]), "+v"(k2[t])); SCHED(); } while (0)
      KSET(0); MM(1, 22); KSET(1); MM(1, 23); KSET(2); MM(1, 24); KSET(3); MM(1, 25);
      CSTAMP(14);
      STAMP(2);
#pragma unroll
      for (int t = 0; t < 4; t++) {
        v2u P[4];
#pragma unroll
        for (int c = 0; c < 4; c++) {
          if (t == 3 && c > 0) break;
          const f4 ag = t < 3 ? acc[t < 3 ? t : 0][c] : a13[0], au = t < 3 ? acc[t < 3 ? t : 0][4 + c] : a13[1];
          {
            u32 p0, p1;
            asm volatile(
              "v_pk_fma_f32 v[248:249], %2, %4, %6 op_sel_hi:[0,1,1]\n\tv_pk_fma_f32 v[250:251], %2, %5, %7 op_sel_hi:[0,1,1]\n\t"
              "v_pk_mul_f32 v[248:249], %2, v[248:249] op_sel:[1,0]\n\tv_pk_mul_f32 v[250:251], %2, v[250:251] op_sel:[1,0]\n\t"
              "v_exp_f32 v248, v248\n\tv_exp_f32 v249, v249\n\tv_exp_f32 v250, v250\n\tv_exp_f32 v251, v251\n\t"
              "v_pk_fma_f32 v[252:253], %2, %8, %10 op_sel_hi:[0,1,1]\n\t"
              "v_pk_fma_f32 v[248:249], %3, v[248:249], %3 op_sel_hi:[0,1,0]\n\tv_pk_fma_f32 v[250:251], %3, v[250:251], %3 op_sel_hi:[0,1,0]\n\t"
              "v_rcp_f32 v248, v248\n\tv_rcp_f32 v249, v249\n\tv_rcp_f32 v250, v250\n\tv_rcp_f32 v251, v251\n\t"
              "v_pk_fma_f32 v[254:255], %2, %9, %11 op_sel_hi:[0,1,1]\n\t"
              "v_pk_mul_f32 v[252:253], v[252:253], v[248:249]\n\tv_pk_mul_f32 v[254:255], v[254:255], v[250:251]\n\t"
              "v_cvt_pk_bf16_f32 %0, v252, v253\n\tv_cvt_pk_bf16_f32 %1, v254, v255"
              : "=v"(p0), "=v"(p1) : "v"(k1[t]), "v"(k2[t]), "v"(cg[c].xy), "v"(cg[c].zw), "v"(ag.xy), "v"(ag.zw), "v"(cu[c].xy), "v"(cu[c].zw), "v"(au.xy), "v"(au.zw)
              : "v248", "v249", "v250", "v251", "v252", "v253", "v254", "v255");
            P[c] = v2u{p0, p1};
          }
          if (t < 3 && (c & 1)) {
            u32 x0 = P[c - 1].x, y0 = P[c - 1].y, x1 = P[c].x, y1 = P[c].y;
            asm volatile("s_nop 0\n\tv_permlane16_swap_b32 %0, %2\n\tv_permlane16_swap_b32 %1, %3" : "+v"(x0), "+v"(y0), "+v"(x1), "+v"(y1));
            stc128(v4u{x0, y0, x1, y1}, rw, hb, (ts + 3 * wv + t) * 49152 + ((c >> 1) ^ (wv >> 1)) * 1024);
          }
          if (c & 1) SCHED();
        }
        if (t == 3 && r5 != 0) {
          __builtin_amdgcn_raw_buffer_store_b64(P[0], rw, WS_HF + (2 * cj + (wv >> 1)) * 1024 + ((2 * (wv & 1) + (g >> 1)) * 16 + i) * 16 + (g & 1) * 8, (ts + 12) * 49152, AUX_SC1);
        }
        SCHED();
      }
      STAMP(3);
      __builtin_amdgcn_s_setprio(3);
      ldsw(CTL + C_DONE + 160 + wv * 4, 0);
      waitv<0>();
      if (lane == 0 && __hip_atomic_fetch_add((LAS u32*)(CTL + C_HCNT), 1u, __ATOMIC_RELAXED, __HIP_MEMORY_SCOPE_WORKGROUP) == 3) stc32(tag, rw, WS_HFLAG + ri * 128 + cj * 4);
      STAMP(4);
    } else {
      if (G1ONLY) __builtin_amdgcn_endpgm();
      wait0(CTL + C_FULL + 8);
      tag = mktag(rfl(ldsv(CTL + C_TAG)));
      wait0(CTL + C_WDFULL);
    }
    wait0(CTL + C_UFULL);
    const int tt = 4 * i2 + wv;
    f4 o[4][5];
    u32 rc[3];
#define OO(m) ((rc[m] & 255) * (D * 2) + (rc[m] >> 8) * 16)
    v4u bp[3];
    {
      v4u xq[3], dq[3]; v3u sq[3];
#pragma unroll
      for (int m = 0; m < 3; m++) {
        const int n = min(m * 64 + lane, 159), row = n / 10; rc[m] = row | (n - row * 10) << 8;
        xq[m] = __builtin_amdgcn_raw_buffer_load_b128(rx, OO(m), (16 * tt * D + 80 * j2) * 2, 0);
        dq[m] = __builtin_amdgcn_raw_buffer_load_b128(mkr(p.dsk, D * 2), (rc[m] >> 8) * 16, 160 * j2, 0);
        sq[m] = __builtin_amdgcn_raw_buffer_load_b96(rw, (rc[m] & 255) * 16, WS_STAT + 256 * tt, AUX_SC1);
      }
      SCHED();
#pragma unroll
      for (int n = 0; n < 25; n++) Wr[n / 5][n % 5] = ldc128(rw, lane * 16, wdo + n * 1024);
      SCHED();
#pragma unroll
      for (int m = 0; m < 3; m++) {
        const float rs = __uint_as_float(sq[m].z), nb = fmul_(-__uint_as_float(sq[m].x), rs);
#pragma unroll
        for (int e = 0; e < 4; e++) {
          const u32 xw = xq[m][e], dw = dq[m][e]; const float x0 = bf2f(xw), x1 = bfhi(xw);
          bp[m][e] = pkbf(ffma_(ffma_(x0, rs, nb), bf2f(dw), x0), ffma_(ffma_(x1, rs, nb), bfhi(dw), x1));
        }
        asm volatile("" : "+v"(bp[m]), "+v"(rc[m]));
      }
    }
    if (HPOLL4) {
      const int l12 = lane < 12 ? lane : 0;
      const u32 fo = WS_HFLAG + (l12 < 6 ? ((20 * i2 + 4) >> 6) * 128 + l12 * 4 : ((20 * i2 + 19) >> 6) * 128 + (l12 - 6) * 4) + wv * 24;
#pragma unroll 1
      for (u32 it = 0; it < SPIN_CAP; it++) { if (__all(ldc32(rw, fo) == tag)) break; __builtin_amdgcn_s_sleep(HP_SLEEP); }
      ORDER();
    } else if (wv == 3) {
      const int l48 = lane < 48 ? lane : 0;
      const u32 fo = WS_HFLAG + (l48 < 24 ? ((20 * i2 + 4) >> 6) * 128 + l48 * 4 : ((20 * i2 + 19) >> 6) * 128 + (l48 - 24) * 4);
      u32 left = 15;
#pragma unroll 1
      for (u32 it = 0; it < SPIN_CAP; it++) {
        const u64 bad = __builtin_amdgcn_ballot_w64(ldc32(rw, fo) != tag);
        const u32 bb = ((u32)bad | (u32)(bad >> 24)) & 0xffffffu;
        ORDER();
#pragma unroll
        for (int k = 0; k < 4; k++) if (((left >> k) & 1) && !((bb >> (6 * k)) & 63)) { if (k < 3) sig0(rz, CTL + C_HRDY + k * 4); left &= ~(1u << k); }
        if (!left) break;
        __builtin_amdgcn_s_sleep(HP_SLEEP);
      }
    } else wait0(CTL + C_HRDY + wv * 4);
    STAMP(5);
    constexpr int HD = 3;
    v4u hr[12][4], Wl[2][5], Wz[5];
    u32 ho[4];
#pragma unroll
    for (int t = 0; t < 4; t++) ho[t] = ABL_H ? WS_WDF + t * 12288 : WS_HF + (4 * i2 + (t ^ wv)) * 49152 + 12 * wv * 1024;
#define LOAD_H(k) do { _Pragma("unroll") for (int t = 0; t < 4; t++) hr[k][t] = ldc128(rw, lane * 16, ho[t] + KS2(k) * 1024); } while (0)
#pragma unroll
    for (int k = 0; k < HD; k++) LOAD_H(k);
    SCHED();
    __builtin_amdgcn_s_setprio(G2_PRIO);
    const u32 wl = sb + wd_off(wv) + lane * 16;
    mma_bf0(o[3][0], Wr[0][0], hr[0][3]); SCHED();
    mma_bf0(o[2][0], Wr[0][0], hr[0][2]); SCHED();
    mma_bf0(o[1][0], Wr[0][0], hr[0][1]); SCHED();
    mma_bf0(o[0][0], Wr[0][0], hr[0][0]); SCHED();
    mma_bf0(o[3][1], Wr[0][1], hr[0][3]); SCHED();
    mma_bf0(o[2][1], Wr[0][1], hr[0][2]); SCHED();
    mma_bf0(o[1][1], Wr[0][1], hr[0][1]); SCHED();
    mma_bf0(o[0][1], Wr[0][1], hr[0][0]); SCHED();
    mma_bf0(o[3][2], Wr[0][2], hr[0][3]); SCHED();
    mma_bf0(o[2][2], Wr[0][2], hr[0][2]); SCHED();
    mma_bf0(o[1][2], Wr[0][2], hr[0][1]); SCHED();
    mma_bf0(o[0][2], Wr[0][2], hr[0][0]); SCHED();
    mma_bf0(o[3][3], Wr[0][3], hr[0][3]); SCHED();
    mma_bf0(o[2][3], Wr[0][3], hr[0][2]); SCHED();
    mma_bf0(o[1][3], Wr[0][3], hr[0][1]); SCHED();
    mma_bf0(o[0][3], Wr[0][3], hr[0][0]); SCHED();
    mma_bf0(o[3][4], Wr[0][4], hr[0][3]); SCHED();
    hr[3][3] = ldc128(rw, lane * 16, ho[3] + 3072); SCHED();
    mma_bf0(o[2][4], Wr[0][4], hr[0][2]); SCHED();
    hr[3][2] = ldc128(rw, lane * 16, ho[2] + 3072); SCHED();
    mma_bf0(o[1][4], Wr[0][4], hr[0][1]); SCHED();
    hr[3][1] = ldc128(rw, lane * 16, ho[1] + 3072); SCHED();
    mma_bf0(o[0][4], Wr[0][4], hr[0][0]); SCHED();
    hr[3][0] = ldc128(rw, lane * 16, ho[0] + 3072); SCHED();
    STAMP(26);
    mma_bf(o[3][0], Wr[1][0], hr[1][3]); SCHED();
    hr[4][3] = ldc128(rw, lane * 16, ho[3] + 4096); SCHED();
    mma_bf(o[2][0], Wr[1][0], hr[1][2]); SCHED();
    mma_bf(o[1][0], Wr[1][0], hr[1][1]); SCHED();
    mma_bf(o[0][0], Wr[1][0], hr[1][0]); SCHED();
    mma_bf(o[3][1], Wr[1][1], hr[1][3]); SCHED();
    mma_bf(o[2][1], Wr[1][1], hr[1][2]); SCHED();
    hr[4][2] = ldc128(rw, lane * 16, ho[2] + 4096); SCHED();
    mma_bf(o[1][1], Wr[1][1], hr[1][1]); SCHED();
    mma_bf(o[0][1], Wr[1][1], hr[1][0]); SCHED();
    mma_bf(o[3][2], Wr[1][2], hr[1][3]); SCHED();
    mma_bf(o[2][2], Wr[1][2], hr[1][2]); SCHED();
    mma_bf(o[1][2], Wr[1][2], hr[1][1]); SCHED();
    hr[4][1] = ldc128(rw, lane * 16, ho[1] + 4096); SCHED();
    mma_bf(o[0][2], Wr[1][2], hr[1][0]); SCHED();
    mma_bf(o[3][3], Wr[1][3], hr[1][3]); SCHED();
    mma_bf(o[2][3], Wr[1][3], hr[1][2]); SCHED();
    mma_bf(o[1][3], Wr[1][3], hr[1][1]); SCHED();
    mma_bf(o[0][3], Wr[1][3], hr[1][0]); SCHED();
    hr[4][0] = ldc128(rw, lane * 16, ho[0] + 4096); SCHED();
    mma_bf(o[3][4], Wr[1][4], hr[1][3]); SCHED();
    mma_bf(o[2][4], Wr[1][4], hr[1][2]); SCHED();
    mma_bf(o[1][4], Wr[1][4], hr[1][1]); SCHED();
    mma_bf(o[0][4], Wr[1][4], hr[1][0]); SCHED();
    mma_bf(o[3][0], Wr[2][0], hr[2][3]); SCHED();
    hr[5][3] = ldc128(rw, lane * 16, ho[3] + 5120); SCHED();
    mma_bf(o[2][0], Wr[2][0], hr[2][2]); SCHED();
    mma_bf(o[1][0], Wr[2][0], hr[2][1]); SCHED();
    mma_bf(o[0][0], Wr[2][0], hr[2][0]); SCHED();
    mma_bf(o[3][1], Wr[2][1], hr[2][3]); SCHED();
    mma_bf(o[2][1], Wr[2][1], hr[2][2]); SCHED();
    hr[5][2] = ldc128(rw, lane * 16, ho[2] + 5120); SCHED();
    mma_bf(o[1][1], Wr[2][1], hr[2][1]); SCHED();
    mma_bf(o[0][1], Wr[2][1], hr[2][0]); SCHED();
    mma_bf(o[3][2], Wr[2][2], hr[2][3]); SCHED();
    mma_bf(o[2][2], Wr[2][2], hr[2][2]); SCHED();
    mma_bf(o[1][2], Wr[2][2], hr[2][1]); SCHED();
    hr[5][1] = ldc128(rw, lane * 16, ho[1] + 5120); SCHED();
    mma_bf(o[0][2], Wr[2][2], hr[2][0]); SCHED();
    mma_bf(o[3][3], Wr[2][3], hr[2][3]); SCHED();
    mma_bf(o[2][3], Wr[2][3], hr[2][2]); SCHED();
    mma_bf(o[1][3], Wr[2][3], hr[2][1]); SCHED();
    mma_bf(o[0][3], Wr[2][3], hr[2][0]); SCHED();
    hr[5][0] = ldc128(rw, lane * 16, ho[0] + 5120); SCHED();
    mma_bf(o[3][4], Wr[2][4], hr[2][3]); SCHED();
    mma_bf(o[2][4], Wr[2][4], hr[2][2]); SCHED();
    mma_bf(o[1][4], Wr[2][4], hr[2][1]); SCHED();
    mma_bf(o[0][4], Wr[2][4], hr[2][0]); SCHED();
    mma_bf(o[3][0], Wr[3][0], hr[3][3]); SCHED();
    hr[6][3] = ldc128(rw, lane * 16, ho[3] + 6144); SCHED();
    mma_bf(o[2][0], Wr[3][0], hr[3][2]); SCHED();
    mma_bf(o[1][0], Wr[3][0], hr[3][1]); SCHED();
    mma_bf(o[0][0], Wr[3][0], hr[3][0]); SCHED();
    mma_bf(o[3][1], Wr[3][1], hr[3][3]); SCHED();
    mma_bf(o[2][1], Wr[3][1], hr[3][2]); SCHED();
    hr[6][2] = ldc128(rw, lane * 16, ho[2] + 6144); SCHED();
    mma_bf(o[1][1], Wr[3][1], hr[3][1]); SCHED();
    mma_bf(o[0][1], Wr[3][1], hr[3][0]); SCHED();
    mma_bf(o[3][2], Wr[3][2], hr[3][3]); SCHED();
    mma_bf(o[2][2], Wr[3][2], hr[3][2]); SCHED();
    mma_bf(o[1][2], Wr[3][2], hr[3][1]); SCHED();
    hr[6][1] = ldc128(rw, lane * 16, ho[1] + 6144); SCHED();
    mma_bf(o[0][2], Wr[3][2], hr[3][0]); SCHED();
    mma_bf(o[3][3], Wr[3][3], hr[3][3]); SCHED();
    mma_bf(o[2][3], Wr[3][3], hr[3][2]); SCHED();
    mma_bf(o[1][3], Wr[3][3], hr[3][1]); SCHED();
    mma_bf(o[0][3], Wr[3][3], hr[3][0]); SCHED();
    hr[6][0] = ldc128(rw, lane * 16, ho[0] + 6144); SCHED();
    mma_bf(o[3][4], Wr[3][4], hr[3][3]); SCHED();
    mma_bf(o[2][4], Wr[3][4], hr[3][2]); SCHED();
    mma_bf(o[1][4], Wr[3][4], hr[3][1]); SCHED();
    mma_bf(o[0][4], Wr[3][4], hr[3][0]); SCHED();
    wait0(CTL + C_WDFULL); STAMP(28);
    mma_bf(o[3][0], Wr[4][0], hr[4][3]); SCHED();
    hr[7][3] = ldc128(rw, lane * 16, ho[3] + 7168); SCHED();
    mma_bf(o[2][0], Wr[4][0], hr[4][2]); SCHED();
    Wl[1][0] = lds128(wl + 0); SCHED();
    mma_bf(o[1][0], Wr[4][0], hr[4][1]); SCHED();
    mma_bf(o[0][0], Wr[4][0], hr[4][0]); SCHED();
    mma_bf(o[3][1], Wr[4][1], hr[4][3]); SCHED();
    mma_bf(o[2][1], Wr[4][1], hr[4][2]); SCHED();
    Wl[1][1] = lds128(wl + 1024); SCHED();
    hr[7][2] = ldc128(rw, lane * 16, ho[2] + 7168); SCHED();
    mma_bf(o[1][1], Wr[4][1], hr[4][1]); SCHED();
    mma_bf(o[0][1], Wr[4][1], hr[4][0]); SCHED();
    mma_bf(o[3][2], Wr[4][2], hr[4][3]); SCHED();
    mma_bf(o[2][2], Wr[4][2], hr[4][2]); SCHED();
    Wl[1][2] = lds128(wl + 2048); SCHED();
    mma_bf(o[1][2], Wr[4][2], hr[4][1]); SCHED();
    hr[7][1] = ldc128(rw, lane * 16, ho[1] + 7168); SCHED();
    mma_bf(o[0][2], Wr[4][2], hr[4][0]); SCHED();
    mma_bf(o[3][3], Wr[4][3], hr[4][3]); SCHED();
    mma_bf(o[2][3], Wr[4][3], hr[4][2]); SCHED();
    Wl[1][3] = lds128(wl + 3072); SCHED();
    mma_bf(o[1][3], Wr[4][3], hr[4][1]); SCHED();
    mma_bf(o[0][3], Wr[4][3], hr[4][0]); SCHED();
    hr[7][0] = ldc128(rw, lane * 16, ho[0] + 7168); SCHED();
    mma_bf(o[3][4], Wr[4][4], hr[4][3]); SCHED();
    mma_bf(o[2][4], Wr[4][4], hr[4][2]); SCHED();
    Wl[1][4] = lds128(wl + 4096); SCHED();
    mma_bf(o[1][4], Wr[4][4], hr[4][1]); SCHED();
    mma_bf(o[0][4], Wr[4][4], hr[4][0]); SCHED();
    mma_bf(o[3][0], Wl[1][0], hr[5][3]); SCHED();
    hr[8][3] = ldc128(rw, lane * 16, ho[3] + 8192); SCHED();
    mma_bf(o[2][0], Wl[1][0], hr[5][2]); SCHED();
    Wl[0][0] = lds128(wl + 5120); SCHED();
    mma_bf(o[1][0], Wl[1][0], hr[5][1]); SCHED();
    mma_bf(o[0][0], Wl[1][0], hr[5][0]); SCHED();
    mma_bf(o[3][1], Wl[1][1], hr[5][3]); SCHED();
    mma_bf(o[2][1], Wl[1][1], hr[5][2]); SCHED();
    Wl[0][1] = lds128(wl + 6144); SCHED();
    hr[8][2] = ldc128(rw, lane * 16, ho[2] + 8192); SCHED();
    mma_bf(o[1][1], Wl[1][1], hr[5][1]); SCHED();
    mma_bf(o[0][1], Wl[1][1], hr[5][0]); SCHED();
    mma_bf(o[3][2], Wl[1][2], hr[5][3]); SCHED();
    mma_bf(o[2][2], Wl[1][2], hr[5][2]); SCHED();
    Wl[0][2] = lds128(wl + 7168); SCHED();
    mma_bf(o[1][2], Wl[1][2], hr[5][1]); SCHED();
    hr[8][1] = ldc128(rw, lane * 16, ho[1] + 8192); SCHED();
    mma_bf(o[0][2], Wl[1][2], hr[5][0]); SCHED();
    mma_bf(o[3][3], Wl[1][3], hr[5][3]); SCHED();
    mma_bf(o[2][3], Wl[1][3], hr[5][2]); SCHED();
    Wl[0][3] = lds128(wl + 8192); SCHED();
    mma_bf(o[1][3], Wl[1][3], hr[5][1]); SCHED();
    mma_bf(o[0][3], Wl[1][3], hr[5][0]); SCHED();
    hr[8][0] = ldc128(rw, lane * 16, ho[0] + 8192); SCHED();
    mma_bf(o[3][4], Wl[1][4], hr[5][3]); SCHED();
    mma_bf(o[2][4], Wl[1][4], hr[5][2]); SCHED();
    Wl[0][4] = lds128(wl + 9216); SCHED();
    mma_bf(o[1][4], Wl[1][4], hr[5][1]); SCHED();
    mma_bf(o[0][4], Wl[1][4], hr[5][0]); SCHED();
    mma_bf(o[3][0], Wl[0][0], hr[6][3]); SCHED();
    hr[9][3] = ldc128(rw, lane * 16, ho[3] + 9216); SCHED();
    mma_bf(o[2][0], Wl[0][0], hr[6][2]); SCHED();
    Wl[1][0] = lds128(wl + 10240); SCHED();
    mma_bf(o[1][0], Wl[0][0], hr[6][1]); SCHED();
    mma_bf(o[0][0], Wl[0][0], hr[6][0]); SCHED();
    mma_bf(o[3][1], Wl[0][1], hr[6][3]); SCHED();
    mma_bf(o[2][1], Wl[0][1], hr[6][2]); SCHED();
    Wl[1][1] = lds128(wl + 11264); SCHED();
    hr[9][2] = ldc128(rw, lane * 16, ho[2] + 9216); SCHED();
    mma_bf(o[1][1], Wl[0][1], hr[6][1]); SCHED();
    mma_bf(o[0][1], Wl[0][1], hr[6][0]); SCHED();
    mma_bf(o[3][2], Wl[0][2], hr[6][3]); SCHED();
    mma_bf(o[2][2], Wl[0][2], hr[6][2]); SCHED();
    Wl[1][2] = lds128(wl + 12288); SCHED();
    mma_bf(o[1][2], Wl[0][2], hr[6][1]); SCHED();
    hr[9][1] = ldc128(rw, lane * 16, ho[1] + 9216); SCHED();
    mma_bf(o[0][2], Wl[0][2], hr[6][0]); SCHED();
    mma_bf(o[3][3], Wl[0][3], hr[6][3]); SCHED();
    mma_bf(o[2][3], Wl[0][3], hr[6][2]); SCHED();
    Wl[1][3] = lds128(wl + 13312); SCHED();
    mma_bf(o[1][3], Wl[0][3], hr[6][1]); SCHED();
    mma_bf(o[0][3], Wl[0][3], hr[6][0]); SCHED();
    hr[9][0] = ldc128(rw, lane * 16, ho[0] + 9216); SCHED();
    mma_bf(o[3][4], Wl[0][4], hr[6][3]); SCHED();
    mma_bf(o[2][4], Wl[0][4], hr[6][2]); SCHED();
    Wl[1][4] = lds128(wl + 14336); SCHED();
    mma_bf(o[1][4], Wl[0][4], hr[6][1]); SCHED();
    mma_bf(o[0][4], Wl[0][4], hr[6][0]); SCHED();
    mma_bf(o[3][0], Wl[1][0], hr[7][3]); SCHED();
    hr[10][3] = ldc128(rw, lane * 16, ho[3] + 10240); SCHED();
    mma_bf(o[2][0], Wl[1][0], hr[7][2]); SCHED();
    Wl[0][0] = lds128(wl + 15360); SCHED();
    mma_bf(o[1][0], Wl[1][0], hr[7][1]); SCHED();
    Wz[0] = ldc128(rw, lane * 16, wdo + 56320); SCHED();
    mma_bf(o[0][0], Wl[1][0], hr[7][0]); SCHED();
    mma_bf(o[3][1], Wl[1][1], hr[7][3]); SCHED();
    mma_bf(o[2][1], Wl[1][1], hr[7][2]); SCHED();
    Wl[0][1] = lds128(wl + 16384); SCHED();
    hr[10][2] = ldc128(rw, lane * 16, ho[2] + 10240); SCHED();
    mma_bf(o[1][1], Wl[1][1], hr[7][1]); SCHED();
    Wz[1] = ldc128(rw, lane * 16, wdo + 57344); SCHED();
    mma_bf(o[0][1], Wl[1][1], hr[7][0]); SCHED();
    mma_bf(o[3][2], Wl[1][2], hr[7][3]); SCHED();
    mma_bf(o[2][2], Wl[1][2], hr[7][2]); SCHED();
    Wl[0][2] = lds128(wl + 17408); SCHED();
    mma_bf(o[1][2], Wl[1][2], hr[7][1]); SCHED();
    Wz[2] = ldc128(rw, lane * 16, wdo + 58368); SCHED();
    hr[10][1] = ldc128(rw, lane * 16, ho[1] + 10240); SCHED();
    mma_bf(o[0][2], Wl[1][2], hr[7][0]); SCHED();
    mma_bf(o[3][3], Wl[1][3], hr[7][3]); SCHED();
    mma_bf(o[2][3], Wl[1][3], hr[7][2]); SCHED();
    Wl[0][3] = lds128(wl + 18432); SCHED();
    mma_bf(o[1][3], Wl[1][3], hr[7][1]); SCHED();
    Wz[3] = ldc128(rw, lane * 16, wdo + 59392); SCHED();
    mma_bf(o[0][3], Wl[1][3], hr[7][0]); SCHED();
    hr[10][0] = ldc128(rw, lane * 16, ho[0] + 10240); SCHED();
    mma_bf(o[3][4], Wl[1][4], hr[7][3]); SCHED();
    mma_bf(o[2][4], Wl[1][4], hr[7][2]); SCHED();
    Wl[0][4] = lds128(wl + 19456); SCHED();
    mma_bf(o[1][4], Wl[1][4], hr[7][1]); SCHED();
    Wz[4] = ldc128(rw, lane * 16, wdo + 60416); SCHED();
    mma_bf(o[0][4], Wl[1][4], hr[7][0]); SCHED();
    STAMP(31);
    mma_bf(o[3][0], Wl[0][0], hr[8][3]); SCHED();
    hr[11][3] = ldc128(rw, lane * 16, ho[3] + 11264); SCHED();
    mma_bf(o[2][0], Wl[0][0], hr[8][2]); SCHED();
    Wl[1][0] = lds128(wl + 20480); SCHED();
    mma_bf(o[1][0], Wl[0][0], hr[8][1]); SCHED();
    mma_bf(o[0][0], Wl[0][0], hr[8][0]); SCHED();
    mma_bf(o[3][1], Wl[0][1], hr[8][3]); SCHED();
    mma_bf(o[2][1], Wl[0][1], hr[8][2]); SCHED();
    Wl[1][1] = lds128(wl + 21504); SCHED();
    hr[11][2] = ldc128(rw, lane * 16, ho[2] + 11264); SCHED();
    mma_bf(o[1][1], Wl[0][1], hr[8][1]); SCHED();
    mma_bf(o[0][1], Wl[0][1], hr[8][0]); SCHED();
    mma_bf(o[3][2], Wl[0][2], hr[8][3]); SCHED();
    mma_bf(o[2][2], Wl[0][2], hr[8][2]); SCHED();
    Wl[1][2] = lds128(wl + 22528); SCHED();
    mma_bf(o[1][2], Wl[0][2], hr[8][1]); SCHED();
    hr[11][1] = ldc128(rw, lane * 16, ho[1] + 11264); SCHED();
    mma_bf(o[0][2], Wl[0][2], hr[8][0]); SCHED();
    mma_bf(o[3][3], Wl[0][3], hr[8][3]); SCHED();
    mma_bf(o[2][3], Wl[0][3], hr[8][2]); SCHED();
    Wl[1][3] = lds128(wl + 23552); SCHED();
    mma_bf(o[1][3], Wl[0][3], hr[8][1]); SCHED();
    mma_bf(o[0][3], Wl[0][3], hr[8][0]); SCHED();
    hr[11][0] = ldc128(rw, lane * 16, ho[0] + 11264); SCHED();
    mma_bf(o[3][4], Wl[0][4], hr[8][3]); SCHED();
    mma_bf(o[2][4], Wl[0][4], hr[8][2]); SCHED();
    Wl[1][4] = lds128(wl + 24576); SCHED();
    mma_bf(o[1][4], Wl[0][4], hr[8][1]); SCHED();
    mma_bf(o[0][4], Wl[0][4], hr[8][0]); SCHED();
    mma_bf(o[3][0], Wl[1][0], hr[9][3]); SCHED();
    mma_bf(o[2][0], Wl[1][0], hr[9][2]); SCHED();
    Wl[0][0] = lds128(wl + 25600); SCHED();
    mma_bf(o[1][0], Wl[1][0], hr[9][1]); SCHED();
    mma_bf(o[0][0], Wl[1][0], hr[9][0]); SCHED();
    mma_bf(o[3][1], Wl[1][1], hr[9][3]); SCHED();
    mma_bf(o[2][1], Wl[1][1], hr[9][2]); SCHED();
    Wl[0][1] = lds128(wl + 26624); SCHED();
    mma_bf(o[1][1], Wl[1][1], hr[9][1]); SCHED();
    mma_bf(o[0][1], Wl[1][1], hr[9][0]); SCHED();
    mma_bf(o[3][2], Wl[1][2], hr[9][3]); SCHED();
    mma_bf(o[2][2], Wl[1][2], hr[9][2]); SCHED();
    Wl[0][2] = lds128(wl + 27648); SCHED();
    mma_bf(o[1][2], Wl[1][2], hr[9][1]); SCHED();
    mma_bf(o[0][2], Wl[1][2], hr[9][0]); SCHED();
    mma_bf(o[3][3], Wl[1][3], hr[9][3]); SCHED();
    mma_bf(o[2][3], Wl[1][3], hr[9][2]); SCHED();
    Wl[0][3] = lds128(wl + 28672); SCHED();
    mma_bf(o[1][3], Wl[1][3], hr[9][1]); SCHED();
    mma_bf(o[0][3], Wl[1][3], hr[9][0]); SCHED();
    mma_bf(o[3][4], Wl[1][4], hr[9][3]); SCHED();
    mma_bf(o[2][4], Wl[1][4], hr[9][2]); SCHED();
    Wl[0][4] = lds128(wl + 29696); SCHED();
    mma_bf(o[1][4], Wl[1][4], hr[9][1]); SCHED();
    mma_bf(o[0][4], Wl[1][4], hr[9][0]); SCHED();
    mma_bf(o[3][0], Wl[0][0], hr[10][3]); SCHED();
    mma_bf(o[2][0], Wl[0][0], hr[10][2]); SCHED();
    mma_bf(o[1][0], Wl[0][0], hr[10][1]); SCHED();
    mma_bf(o[0][0], Wl[0][0], hr[10][0]); SCHED();
    mma_bf(o[3][1], Wl[0][1], hr[10][3]); SCHED();
    mma_bf(o[2][1], Wl[0][1], hr[10][2]); SCHED();
    mma_bf(o[1][1], Wl[0][1], hr[10][1]); SCHED();
    mma_bf(o[0][1], Wl[0][1], hr[10][0]); SCHED();
    mma_bf(o[3][2], Wl[0][2], hr[10][3]); SCHED();
    mma_bf(o[2][2], Wl[0][2], hr[10][2]); SCHED();
    mma_bf(o[1][2], Wl[0][2], hr[10][1]); SCHED();
    mma_bf(o[0][2], Wl[0][2], hr[10][0]); SCHED();
    mma_bf(o[3][3], Wl[0][3], hr[10][3]); SCHED();
    mma_bf(o[2][3], Wl[0][3], hr[10][2]); SCHED();
    mma_bf(o[1][3], Wl[0][3], hr[10][1]); SCHED();
    mma_bf(o[0][3], Wl[0][3], hr[10][0]); SCHED();
    mma_bf(o[3][4], Wl[0][4], hr[10][3]); SCHED();
    mma_bf(o[2][4], Wl[0][4], hr[10][2]); SCHED();
    mma_bf(o[1][4], Wl[0][4], hr[10][1]); SCHED();
    mma_bf(o[0][4], Wl[0][4], hr[10][0]); SCHED();
    STAMP(20);
    {
#pragma unroll
      for (int n = 0; n < 25; n++) {
        if (n < 20) { mma_bf(o[3 - n / 5][n % 5], Wz[n % 5], hr[11][3 - n / 5]); SCHED(); }
        if (n >= 5 && n < 20) { const int m = n - 5; asm volatile("s_nop 2" : "+v"(o[3 - m / 5][m % 5])); *(LAS f4*)(wl + ((2 - m / 5) * 5 + m % 5) * 1024) = o[3 - m / 5][m % 5]; SCHED(); }
      }
      ldsw(CTL + C_RED + wv * 4, 0);
      STAMP(6);
      asm volatile("s_nop 7" : "+v"(o[0][0]), "+v"(o[0][1]), "+v"(o[0][2]), "+v"(o[0][3]), "+v"(o[0][4]));
      u32 oo[3], lo3[3];
#pragma unroll
      for (int m = 0; m < 3; m++) { asm volatile("" : "+v"(rc[m])); oo[m] = OO(m); lo3[m] = (rc[m] & 255) * 336 + (rc[m] >> 8) * 32; asm volatile("" : "+v"(oo[m]), "+v"(lo3[m])); }
      float base[3][8];
#pragma unroll
      for (int m = 0; m < 3; m++)
#pragma unroll
        for (int e = 0; e < 4; e++) { const u32 w = bp[m][e]; base[m][2 * e] = bf2f(w); base[m][2 * e + 1] = bfhi(w); asm volatile("" : "+v"(base[m][2 * e]), "+v"(base[m][2 * e + 1])); }
      spin_zero4(CTL + C_RED);
#pragma unroll
      for (int c = 0; c < 5; c++) {
        f4 s[3];
#pragma unroll
        for (int t = 1; t < 4; t++) s[t - 1] = *(LAS f4*)(sb + wd_off(wv ^ t) + lane * 16 + ((t - 1) * 5 + c) * 1024);
#pragma unroll
        for (int e = 0; e < 4; e++) o[0][c][e] = fadd_(fadd_(o[0][c][e], s[0][e]), fadd_(s[1][e], s[2][e]));
        asm volatile("" : "+v"(o[0][c]));
      }
      wait0(CTL + C_ZFULL);
      STAMP(7);
      const int q = (lane >> 2) & 3, pp = lane & 3;
      const u32 tb = sb + wd_off(wv) + 15360;
      v2u z[5];
#pragma unroll
      for (int c = 0; c < 5; c++) z[c] = trd(sb + LD_Z + (20 * g + 4 * c + q) * 128 + wv * 32 + pp * 8);
#pragma unroll
      for (int c = 0; c < 5; c++)
        *(LAS f4*)(tb + i * 336 + g * 80 + c * 16) = f4{fadd_(o[0][c][0], bf2f(z[c].x)), fadd_(o[0][c][1], bfhi(z[c].x)), fadd_(o[0][c][2], bf2f(z[c].y)), fadd_(o[0][c][3], bfhi(z[c].y))};
      CBAR();
      const rsrc_t ro = mkr(p.out, L * D * 2);
#pragma unroll
      for (int m = 0; m < 3; m++) {
        const f4 u0 = *(LAS f4*)(tb + lo3[m]), u1 = *(LAS f4*)(tb + lo3[m] + 16);
        const v4u w = {pkbf(fadd_(u0[0], base[m][0]), fadd_(u0[1], base[m][1])), pkbf(fadd_(u0[2], base[m][2]), fadd_(u0[3], base[m][3])),
                       pkbf(fadd_(u1[0], base[m][4]), fadd_(u1[1], base[m][5])), pkbf(fadd_(u1[2], base[m][6]), fadd_(u1[3], base[m][7]))};
        if (m < 2 || lane < 32) stc128(w, ro, oo[m], (16 * tt * D + 80 * j2) * 2);
      }
    }
    STAMP(8); CSTAMP(15);
    ST_FLUSH();
    __builtin_amdgcn_endpgm();
  } else if (wv < 7) {
    const int slot = wv - 4;
    if (G1ONLY == 1) { __builtin_amdgcn_endpgm(); }
    if (slot == 2 && (r & 1)) { ln_unit(p, sb, (16 * (xc >> 2) + (r >> 1)) * 4 + (xc & 3), lane); if (G1ONLY) __builtin_amdgcn_endpgm(); STAMP(21); }
    else { conv_wave(p, sb, slot, ch0 + slot, lane, wv, g1, ts, cj ST_PASS); }
    ST_FLUSH();
    __builtin_amdgcn_endpgm();
  } else {
    __builtin_amdgcn_s_setprio(3);
    v4u fa, fb, cp[14];
    const v4u rws = mkrs(p.ws, WS_BYTES);
#define LDI(d, vo_, io_) asm volatile("buffer_load_dwordx4 %0, %1, %2, 0 offen offset:%3 sc1" : "=v"(d) : "v"(vo_), "s"(rws), "i"(io_) : "memory")
    const u32 vlf = lane * 16 + WS_LFLAG + xc * 2048, vcp = lane * 16 + WS_CSP + cj * 10240, vst = lane * 16 + WS_STAT + ts * 256, vs3 = (lane & 15) * 16 + WS_STAT + ts * 256;
#define LOOK() do { LDI(fa, vlf, 0); LDI(fb, vlf, 1024); } while (0)
#define LOOKC() do { _Pragma("unroll") for (int k = 0; k < 10; k++) LDI(cp[k], vcp + (k >> 2) * 4096, (k & 3) * 1024); \
      LDI(cp[10], vst, 0); LDI(cp[11], vst, 1024); LDI(cp[12], vst, 2048); LDI(cp[13], vs3, 3072); } while (0)
#define PINC(N) asm volatile("s_waitcnt vmcnt(" #N ")" : "+v"(cp[0]), "+v"(cp[1]), "+v"(cp[2]), "+v"(cp[3]), "+v"(cp[4]), "+v"(cp[5]), "+v"(cp[6]), "+v"(cp[7]), "+v"(cp[8]), "+v"(cp[9]), \
      "+v"(cp[10]), "+v"(cp[11]), "+v"(cp[12]), "+v"(cp[13]) :: "memory")
#define PINF() asm volatile("s_waitcnt vmcnt(0)" : "+v"(fa), "+v"(fb) :: "memory")
    if (g1) {
      const v4u rg = mkrs(p.wg, D * F * 2), ru = mkrs(p.wu, D * F * 2), rxs = mkrs(p.x, L * D * 2);
      u32 v[4], vx[4];
#pragma unroll
      for (int k = 0; k < 4; k++) {
        const int row = 8 * k + (lane >> 3), fp = ((row >> 1) & 1) | (((row >> 3) & 1) << 1);
        v[k] = row * 3072 + cj * 128 + (((lane & 7) ^ (fp << 1)) << 4) - 1024 * k;
        vx[k] = (ts * 16 + row) * 1280 + (((lane & 7) ^ ((row >> 1) & 7)) << 4) - 1024 * k;
      }
      int kx = rot + 1, kw = rot == 8 ? 0 : rot + 2; u32 lsw = 2 * WSLOT, lsx = XSLOT;
      v4u dn = {0, 0, 0, 0};
#define FETCH_X(n) do { if (rfl(dn.x | dn.y | dn.z | dn.w) != 0) spin_zero4(CTL + C_DONE + ((n) - 3) * 16); \
        _Pragma("unroll") for (int k = 0; k < 6; k++) { if (k == 0) waitv<50>(); if (k == 3) waitv<47>(); dma4(rxs, vx[0], vx[1], vx[2], vx[3], kx * 128 + k * 40960, sb + LD_X + lsx + k * 4096); } \
        dma2(rxs, vx[0], vx[1], kx * 128 + 12 * 20480, sb + LD_X + lsx + 12 * 2048); \
        sig0(rz, CTL + C_FULL + (n) * 32); } while (0)
#pragma unroll 1
      for (int n = 1; n < 9; n++) {
        FETCH_X(n);
        if (n > 1) dn = *(volatile LAS v4u*)(CTL + C_DONE + (n - 2) * 16);
        kx = kx == 9 ? 0 : kx + 1; lsx += XSLOT; if (lsx == NXS * XSLOT) lsx = 0;
        waitv<44>();
#pragma unroll
        for (int k = 0; k < 4; k++) dma4((k & 1) ? ru : rg, v[0], v[1], v[2], v[3], (kw * 64 + (k >> 1) * 32) * 3072, sb + LD_R + lsw + (k & 1) * 8192 + (k >> 1) * 4096);
        kw = kw == 9 ? 0 : kw + 1; lsw += WSLOT; if (lsw == NWS * WSLOT) lsw = 0;
      }
      waitv<44>(); LOOKC();
      FETCH_X(9);
      STAMP(12);
      waitv<60>(); LOOK();
      const u32 tg = mktag(rfl(ldsv(CTL + C_TAG)));
      float cs0, cs1;
#pragma unroll 1
      for (u32 it = 0; it < SPIN_CAP; it++) {
        PINC(29);
        u32 bad = 0; cs0 = cs1 = 0.f;
#pragma unroll
        for (int k = 0; k < 14; k++) { bad |= (cp[k].y ^ tg) | (cp[k].w ^ tg); if (k < 10) { cs0 = fadd_(cs0, __uint_as_float(cp[k].x)); cs1 = fadd_(cs1, __uint_as_float(cp[k].z)); } }
        if (__all(bad == 0)) break;
        __builtin_amdgcn_s_sleep(8);
        LOOKC(); waitv<0>();
      }
      waitv<2>();
      *(LAS f2*)(sb + LD_CS + lane * 8) = f2{cs0, cs1};
#pragma unroll
      for (int k = 0; k < 4; k++) *(LAS v2u*)(sb + LD_ST + (k < 3 ? lane : lane & 15) * 8 + k * 512) = v2u{cp[10 + k].x, cp[10 + k].z};
      ldsw(CTL + C_RDY, 0);
      STAMP(23);
    } else { wait0(CTL + C_FULL + 8); __builtin_amdgcn_s_sleep(127); __builtin_amdgcn_s_sleep(60); LOOK(); }
    const u32 tag = mktag(rfl(ldsv(CTL + C_TAG)));
    u32 vo[5];
#pragma unroll
    for (int k = 0; k < 5; k++) { const int n = k * 64 + lane, tau = (n * 911) >> 16, a = n - tau * 72 - 8; vo[k] = (((a >> 1) * 4 + tau) * 20480 + (a & 1) * 16 - (k & 3) * 1024) | ((a | (3 - tau)) & 0x80000000u); asm volatile("" : "+v"(vo[k])); }
#pragma unroll 1
    for (u32 it = 0; it < SPIN_CAP; it++) {
      PINF();
      if (__all(((fa.x ^ tag) | (fa.y ^ tag) | (fa.z ^ tag) | (fa.w ^ tag) | (fb.x ^ tag) | (fb.y ^ tag) | (fb.z ^ tag) | (fb.w ^ tag)) == 0)) break;
      __builtin_amdgcn_s_sleep(8);
      LOOK();
    }
#pragma unroll 1
    for (int sl = 0; sl < 3 - (r & 1); sl++) {
      const int ch = ch0 + sl; const u32 co = WS_XT + ((ch >> 7) * 4 + (ch & 3)) * 1024 + ((ch & 127) >> 2) * 32, UB = sb + LD_K + sl * K_BYTES;
      dma4c(rws, vo[0], vo[1], vo[2], vo[3], co, UB); dma1c(rws, vo[4], co, UB + 4096);
    }
    sig0(rz, CTL + C_UFULL);
    {
      const u32 vl = lane * 16, gb = WS_WDF + (j2 * 48 + 5) * 5120;
      v4u stg[4][NST];
      __builtin_amdgcn_s_setprio(1);
#pragma unroll
      for (int w = 0; w < 4; w++) {
        if (w == 3) waitv<44>();
#pragma unroll
        for (int k = 0; k < NST; k++) stg[w][k] = ldc128(rw, vl, gb + w * 61440 + k * 1024);
      }
      SCHED();
      spin_zero4(CTL + C_KDONE); spin_zero4(CTL + C_DONE + 9 * 16);
      STAMP(27);
#pragma unroll
      for (int w = 0; w < 4; w++)
#pragma unroll
        for (int k = 0; k < NST; k++) *(LAS v4u*)(sb + wd_off(w) + vl + k * 1024) = stg[w][k];
      asm volatile("s_waitcnt lgkmcnt(0)" ::: "memory");
      STAMP(29);
      __builtin_amdgcn_s_setprio(WD_PRIO);
      u32 tleft = HTOUCH ? 7 : 0;
      const u32 tfo = WS_HFLAG + (((lane & 4) ? 20 * i2 + 19 : 20 * i2 + 4) >> 6) * 128 + (3 * j2 + min(lane & 3, 2)) * 4;
      const u32 tvo = WS_HF + (4 * i2 + (lane >> 4)) * 49152 + 6 * j2 * 1024 + (lane & 15) * 128;
#define HT() do { if (tleft) { const u32 bad = (u32)__builtin_amdgcn_ballot_w64(ldc32(rw, tfo) != tag); const u32 rdy = tleft & ~(bad | (bad >> 4)); ORDER(); \
        _Pragma("unroll") for (int k = 0; k < 3; k++) if ((rdy >> k) & 1) \
          asm volatile("s_mov_b32 m0, %3\n\ts_nop 0\n\tbuffer_load_dword %0, %1, %2 offen sc1 lds" ::"v"(tvo), "s"(rws), "s"(k * 2048), "s"(CTL + C_FULL) : "memory"); \
        tleft &= ~rdy; } } while (0)
      HT();
      u32 go = gb;
#pragma unroll 1
      for (int w = 0; w < 4; w++, go += 61440) {
        const u32 lo = sb + wd_off(w);
#pragma unroll
        for (int k = NST; k < 30; k += 4) { waitv<WD_DEPTH>(); if (30 - k >= 4) dma4c(rws, vl, vl, vl, vl, go + k * 1024, lo + k * 1024); else dma2c(rws, vl, vl, go + k * 1024, lo + k * 1024); }
      }
      sig0(rz, CTL + C_WDFULL);
      HT();
    }
    STAMP(24);
    __builtin_amdgcn_s_setprio(3);
    spin_zero4(CTL + C_CDONE);
    if (b == 0 && lane == 0) stc32(tag, rw, WS_GEN);
    if (lane < 8) stc32(tag, rw, WS_ZFLAG + (lane * 8 + xc) * 128 + r * 4);
    __builtin_amdgcn_s_sleep(ZP_FIRST);
#pragma unroll 1
    for (u32 it = 0; it < SPIN_CAP; it++) {
      if (__all(ldc32(rw, WS_ZFLAG + (xc * 8 + j2) * 128 + (lane & 31) * 4) == tag)) break;
      __builtin_amdgcn_s_sleep(ZP_SLEEP);
    }
    ORDER();
    {
      u32 vz[4];
#pragma unroll
      for (int k = 0; k < 4; k++) vz[k] = (8 * k + (lane >> 3)) * 4096 + (lane & 7) * 16 - 1024 * k;
      const u32 go = WS_ZT + 80 * j2 * 4096 + i2 * 128;
      dma4c(rws, vz[0], vz[1], vz[2], vz[3], go, sb + LD_Z); dma4c(rws, vz[0], vz[1], vz[2], vz[3], go + 32 * 4096, sb + LD_Z + 4096);
      dma2c(rws, vz[0], vz[1], go + 64 * 4096, sb + LD_Z + 8192);
      sig0(rz, CTL + C_ZFULL);
      waitv<0>();
    }
    STAMP(25);
  }
  ST_FLUSH();
}

extern "C" void kernel_launch(void* const* d_in, const int* in_sizes, int n_in, void* d_out, int out_size, void* d_ws, size_t ws_size, hipStream_t stream) {
  Params p;
  p.x = (const u16*)d_in[0]; p.lar = (const u16*)d_in[1]; p.aim = (const u16*)d_in[2]; p.cre = (const u16*)d_in[3]; p.cim = (const u16*)d_in[4];
  p.ldt = (const u16*)d_in[5]; p.dsk = (const u16*)d_in[6]; p.wg = (const u16*)d_in[8]; p.wu = (const u16*)d_in[9]; p.wd = (const u16*)d_in[10];
  p.out = (u16*)d_out; p.ws = (char*)d_ws;
  (void)hipFuncSetAttribute((const void*)k_main, hipFuncAttributeMaxDynamicSharedMemorySize, LDS_BYTES);
  k_main<<<256, 512, LDS_BYTES, stream>>>(p);
}
```

```cpp
#include <hip/hip_runtime.h>
#pragma clang diagnostic ignored "-Wint-to-pointer-cast"
typedef unsigned short u16; typedef unsigned u32; typedef unsigned long long u64;
typedef u32 v4u __attribute__((ext_vector_type(4)));
typedef u32 v2u __attribute__((ext_vector_type(2)));
typedef u32 v3u __attribute__((ext_vector_type(3)));
typedef float f4 __attribute__((ext_vector_type(4)));
typedef float f2 __attribute__((ext_vector_type(2)));
typedef short s4 __attribute__((ext_vector_type(4)));
typedef __bf16 bf2 __attribute__((ext_vector_type(2)));
typedef _Float16 hf2 __attribute__((ext_vector_type(2)));
#define LAS __attribute__((address_space(3)))
#define DEV __device__ __forceinline__
typedef __amdgpu_buffer_rsrc_t rsrc_t;

constexpr int L = 2048, D = 640, NS = 192, F = 1536;
constexpr u32 WS_GEN = 0, WS_HFLAG = 1024, WS_ZFLAG = 40960, WS_LFLAG = 49152, WS_WFLAG = WS_LFLAG + 512, WS_STAT = 327680, WS_CSP = 65536,
              WS_XT = 1u << 20, WS_WDF = 4u << 20, WS_HF = 6u << 20, WS_ZT = 13u << 20, WS_BYTES = 16u << 20;
constexpr u32 LD_K = 0, K_BYTES = 5120;
constexpr u32 LD_CS = LD_K + 3 * K_BYTES, LD_ST = LD_CS + 512, LD_CTL = LD_ST + 208 * 8, LD_R = LD_CTL + 896;
constexpr u32 WSLOT = 16384, NWS = 4, XSLOT = 26624, NXS = 3, LD_X = LD_R + NWS * WSLOT, LDS_BYTES = LD_X + NXS * XSLOT;
constexpr u32 WD_BYTES = 30720, LD_Z = LD_X;
DEV u32 wd_off(int w) { return LD_R + ((0x70520020u >> (8 * w)) & 0xffu) * 1024; }
static_assert(LD_R % 128 == 0 && 18432 + 2 * WD_BYTES == NXS * XSLOT && LDS_BYTES <= 163840, "lds");
constexpr u32 C_TOK = 40, C_WDFULL = 44, C_RDY = 48, C_ZFULL = 52, C_UFULL = 56, C_TAG = 60, C_KDONE = 64, C_RED = 80, C_CDONE = 96, C_HCNT = 132, C_DONE = 256, C_FULL = 512;
constexpr int AUX_SC1 = 16;
constexpr u32 SPIN_CAP = 1u << 18;
constexpr int NST = 14;

struct Params {
  const u16 *x, *lar, *aim, *cre, *cim, *ldt, *dsk, *wg, *wu, *wd;
  u16* out; char* ws;
};

DEV float bf2f(u32 v) { return __uint_as_float(v << 16); }
DEV float bfhi(u32 v) { return __uint_as_float(v & 0xffff0000u); }
DEV u32 pkbf(float a, float b) { f2 v = {a, b}; return __builtin_bit_cast(u32, __builtin_convertvector(v, bf2)); }
DEV u32 pkhf(float a, float b) { f2 v = {a, b}; return __builtin_bit_cast(u32, __builtin_convertvector(v, hf2)); }
DEV float rbf(float a) { return bf2f(pkbf(a, a)); }
DEV void mma_bf(f4& c, v4u a, v4u b) { asm volatile("v_mfma_f32_16x16x32_bf16 %0, %1, %2, %0" : "+v"(c) : "v"(a), "v"(b)); }
DEV void mma_hf(f4& c, v4u a, v4u b) { asm volatile("v_mfma_f32_16x16x32_f16 %0, %1, %2, %0" : "+v"(c) : "v"(a), "v"(b)); }
DEV void mma_hf0(f4& c, v4u a, v4u b) { asm volatile("v_mfma_f32_16x16x32_f16 %0, %1, %2, 0" : "=&v"(c) : "v"(a), "v"(b)); }
DEV void mma_bf0(f4& c, v4u a, v4u b) { asm volatile("v_mfma_f32_16x16x32_bf16 %0, %1, %2, 0" : "=&v"(c) : "v"(a), "v"(b)); }
DEV v2u trd(u32 a) { return __builtin_bit_cast(v2u, __builtin_amdgcn_ds_read_tr16_b64_v4i16((LAS s4*)a)); }
DEV v4u trd2(u32 a, u32 b) { v2u x = trd(a), y = trd(b); return v4u{x.x, x.y, y.x, y.y}; }
DEV v4u lds128(u32 a) { return *(LAS v4u*)a; }
DEV v2u lds64(u32 a) { return *(LAS v2u*)a; }
DEV u32 rfl(u32 v) { return __builtin_amdgcn_readfirstlane(v); }
DEV v4u mkrs(const void* p, u32 bytes) { u64 a = (u64)p; return v4u{(u32)a, (u32)(a >> 32) & 0xffffu, bytes, 0x00020000u}; }
DEV rsrc_t mkr(const void* p, u32 bytes) { return __builtin_amdgcn_make_buffer_rsrc((void*)p, 0, bytes, 0x00020000); }
DEV v4u ldc128(rsrc_t r, u32 v, u32 s) { return __builtin_amdgcn_raw_buffer_load_b128(r, v, s, AUX_SC1); }
DEV u32 ldc32(rsrc_t r, u32 v) { return __builtin_amdgcn_raw_buffer_load_b32(r, v, 0, AUX_SC1); }
DEV void stc128(v4u d, rsrc_t r, u32 v, u32 s) {
  __builtin_amdgcn_raw_buffer_store_b128(d, r, v, s, AUX_SC1); asm volatile("s_nop 1" ::"v"(d) : "memory"); }
DEV void stc32(u32 d, rsrc_t r, u32 v) { __builtin_amdgcn_raw_buffer_store_b32(d, r, v, 0, AUX_SC1); }
#define CBAR() asm volatile("" ::: "memory")
#define SCHED() __builtin_amdgcn_sched_barrier(0)
#define ORDER() __builtin_amdgcn_fence(__ATOMIC_ACQUIRE, "wavefront")
#define BARRIER() do { asm volatile("s_waitcnt lgkmcnt(0)" ::: "memory"); __builtin_amdgcn_s_barrier(); CBAR(); } while (0)
template <int N> DEV void waitv() { asm volatile("s_waitcnt vmcnt(%0)" ::"i"(N) : "memory"); }
#define DMA_(V, RS, SO, OFF, SC) "buffer_load_dwordx4 " V ", " RS ", " SO " offen" OFF SC " lds\n\t"
#define DMA4_(NAME, SC) DEV void NAME(v4u rs, u32 v0, u32 v1, u32 v2, u32 v3, u32 so, u32 lds) { \
  asm volatile("s_mov_b32 m0, %0\n\ts_nop 0\n\t" DMA_("%1", "%5", "%6", "", SC) DMA_("%2", "%5", "%6", " offset:1024", SC) DMA_("%3", "%5", "%6", " offset:2048", SC) DMA_("%4", "%5", "%6", " offset:3072", SC) \
               ::"s"(lds), "v"(v0), "v"(v1), "v"(v2), "v"(v3), "s"(rs), "s"(so) : "memory"); }
#define DMA2_(NAME, SC) DEV void NAME(v4u rs, u32 v0, u32 v1, u32 so, u32 lds) { \
  asm volatile("s_mov_b32 m0, %0\n\ts_nop 0\n\t" DMA_("%1", "%3", "%4", "", SC) DMA_("%2", "%3", "%4", " offset:1024", SC) ::"s"(lds), "v"(v0), "v"(v1), "s"(rs), "s"(so) : "memory"); }
#define DMA1_(NAME, SC) DEV void NAME(v4u rs, u32 v0, u32 so, u32 lds) { \
  asm volatile("s_mov_b32 m0, %0\n\ts_nop 0\n\t" DMA_("%1", "%2", "%3", "", SC) ::"s"(lds), "v"(v0), "s"(rs), "s"(so) : "memory"); }
DMA4_(dma4, "") DMA2_(dma2, "") DMA1_(dma1, "") DMA4_(dma4c, " sc1") DMA2_(dma2c, " sc1") DMA1_(dma1c, " sc1")
DEV void sig0(v4u rz, u32 lds) {
  asm volatile("s_mov_b32 m0, %0\n\ts_mov_b64 exec, 1\n\tbuffer_load_dword off, %1, 0 lds\n\ts_mov_b64 exec, -1" ::"s"(lds), "s"(rz) : "memory");
}
DEV u32 mktag(u32 gen) { return gen * 0x9E3779B1u + 0x7F4A7C15u; }
DEV u32 ldsv(u32 a) { return *(volatile LAS u32*)a; }
DEV void ldsw(u32 a, u32 v) { asm volatile("s_mov_b64 exec, 1\n\tds_write_b32 %0, %1\n\ts_mov_b64 exec, -1" ::"v"(a), "v"(v) : "memory"); }
DEV void spin0(u32 a, u32 cur) {
  u32 tv, cnt;
  asm volatile("s_cmp_eq_u32 %3, 0\n\ts_cbranch_scc0 3f\n2:\n\t.subsection 1\n3:\n\ts_mov_b32 %1, 0x100000\n"
               "1:\n\tds_read_b32 %0, %2\n\ts_sub_u32 %1, %1, 1\n\ts_waitcnt lgkmcnt(0)\n\tv_cmp_ne_u32 vcc, 0, %0\n\ts_nop 1\n\ts_min_u32 vcc_lo, vcc_lo, %1\n\ts_cmp_eq_u32 vcc_lo, 0\n\ts_cbranch_scc1 2b\n\t"
               "s_sleep 1\n\ts_branch 1b\n\t.subsection 0"
               : "=&v"(tv), "=&s"(cnt) : "v"(a), "s"(cur) : "memory", "scc", "vcc");
}
DEV void wait0(u32 a) {
  u32 tv, ts, cnt;
  asm volatile("s_mov_b32 %2, 0x100000\n1:\n\tds_read_b32 %0, %3\n\ts_sub_u32 %2, %2, 1\n\ts_waitcnt lgkmcnt(0)\n\tv_readfirstlane_b32 %1, %0\n\ts_nop 0\n\t"
               "s_min_u32 %1, %1, %2\n\ts_cmp_eq_u32 %1, 0\n\ts_cbranch_scc1 2f\n\ts_sleep 1\n\ts_branch 1b\n2:"
               : "=&v"(tv), "=&s"(ts), "=&s"(cnt) : "v"(a) : "memory", "scc");
}
DEV void spin_zero4(u32 a) {
  u32 tv, cnt;
  const u32 al = a + (__builtin_amdgcn_mbcnt_lo(~0u, 0) & 3) * 4;
  asm volatile("s_mov_b32 %1, 0x100000\n1:\n\tds_read_b32 %0, %2\n\ts_sub_u32 %1, %1, 1\n\ts_waitcnt lgkmcnt(0)\n\tv_cmp_ne_u32 vcc, 0, %0\n\ts_nop 1\n\t"
               "s_min_u32 vcc_lo, vcc_lo, %1\n\ts_cmp_eq_u32 vcc_lo, 0\n\ts_cbranch_scc1 2f\n\ts_sleep 1\n\ts_branch 1b\n2:"
               : "=&v"(tv), "=&s"(cnt) : "v"(al) : "memory", "scc", "vcc");
}
DEV float fmul_(float a, float b) { float r; asm("v_mul_f32 %0, %1, %2" : "=v"(r) : "v"(a), "v"(b)); return r; }
DEV float fadd_(float a, float b) { float r; asm("v_add_f32 %0, %1, %2" : "=v"(r) : "v"(a), "v"(b)); return r; }
DEV float ffma_(float a, float b, float c) { float r; asm("v_fma_f32 %0, %1, %2, %3" : "=v"(r) : "v"(a), "v"(b), "v"(c)); return r; }
DEV float fnma_(float a, float b, float c) { float r; asm("v_fma_f32 %0, -%1, %2, %3" : "=v"(r) : "v"(a), "v"(b), "v"(c)); return r; }
struct cf { float x, y; };
DEV cf csq(cf a) { const float t = fmul_(a.x, a.y); return cf{fnma_(a.y, a.y, fmul_(a.x, a.x)), fadd_(t, t)}; }
DEV u32 pkc(cf a) { return pkhf(a.x, a.y); }
DEV cf cexp2(float lr, float rev) { float m = __builtin_amdgcn_exp2f(lr); const float f = __builtin_amdgcn_fractf(rev); float c = __builtin_amdgcn_cosf(f), s = __builtin_amdgcn_sinf(f);
  asm volatile("s_nop 0" : "+v"(c), "+v"(s), "+v"(m)); return cf{fmul_(m, c), fmul_(m, s)}; }
DEV u32 hmul(u32 a, u32 m) { u32 t, r;
  asm("v_pk_mul_f16 %0, %1, %2 op_sel_hi:[0,1]" : "=v"(t) : "v"(a), "v"(m));
  asm("v_pk_fma_f16 %0, %1, %2, %3 op_sel:[1,1,0] op_sel_hi:[1,0,1] neg_lo:[0,1,0]" : "=v"(r) : "v"(a), "v"(m), "v"(t)); return r; }
DEV u32 hmulc(u32 a, u32 m) { u32 t, r;
  asm("v_pk_mul_f16 %0, %1, %2 op_sel_hi:[0,1] neg_hi:[0,1]" : "=v"(t) : "v"(a), "v"(m));
  asm("v_pk_fma_f16 %0, %1, %2, %3 op_sel:[1,1,0] op_sel_hi:[1,0,1]" : "=v"(r) : "v"(a), "v"(m), "v"(t)); return r; }
DEV void swap16(float& a, float& b) { auto r = __builtin_amdgcn_permlane16_swap(__float_as_uint(a), __float_as_uint(b), false, false); a = __uint_as_float(r[0]); b = __uint_as_float(r[1]); }
DEV void swap32(float& a, float& b) { auto r = __builtin_amdgcn_permlane32_swap(__float_as_uint(a), __float_as_uint(b), false, false); a = __uint_as_float(r[0]); b = __uint_as_float(r[1]); }

DEV void ln_unit(const Params& p, u32 sb, int pn, int lane) {
  const rsrc_t rx = mkr(p.x, L * D * 2), rw = mkr(p.ws, WS_BYTES);
  const int h = lane & 1, m = lane >> 1, row0 = 64 * (pn >> 2) + (pn & 3);
  __builtin_amdgcn_s_setprio(2);
  v2u xv[8][5]; float xf[8][20], s[16];
#pragma unroll
  for (int e = 0; e < 8; e++)
#pragma unroll
    for (int i = 0; i < 5; i++) xv[e][i] = __builtin_amdgcn_raw_buffer_load_b64(rx, (row0 + 32 * h) * 1280 + m * 8 + i * 256, e * 5120, 0);
  SCHED();
  __builtin_amdgcn_s_setprio(3);
#pragma unroll
  for (int e = 0; e < 8; e++) {
#pragma unroll
    for (int i = 0; i < 10; i++) { const u32 w = xv[e][i >> 1][i & 1]; xf[e][2 * i] = bf2f(w); xf[e][2 * i + 1] = bfhi(w); }
    float a = fadd_(xf[e][0], xf[e][1]), q = fmul_(xf[e][0], xf[e][0]);
#pragma unroll
    for (int i = 1; i < 20; i++) { if (i > 1) a = fadd_(a, xf[e][i]); asm("v_fmac_f32 %0, %1, %1" : "+v"(q) : "v"(xf[e][i])); }
    s[e] = a; s[8 + e] = q;
  }
#pragma unroll
  for (int e = 0; e < 16; e++) asm volatile("" : "+v"(s[e]));
  asm volatile("s_nop 1");
  wait0(sb + LD_CTL + C_TOK);
  const u32 tag = mktag(rfl(ldsv(sb + LD_CTL + C_TAG)));
  float z[4];
#pragma unroll
  for (int k = 0; k < 4; k++) {
    swap16(s[4 * k], s[4 * k + 1]); swap16(s[4 * k + 2], s[4 * k + 3]);
    float a = fadd_(s[4 * k], s[4 * k + 1]), b = fadd_(s[4 * k + 2], s[4 * k + 3]);
    asm volatile("s_nop 1" : "+v"(a), "+v"(b));
    swap32(a, b); z[k] = fadd_(a, b);
  }
#define DPPADD(C) asm volatile("s_nop 1\n\tv_add_f32_dpp %0, %0, %0 " C " row_mask:0xf bank_mask:0xf\n\tv_add_f32_dpp %1, %1, %1 " C " row_mask:0xf bank_mask:0xf\n\t" \
    "v_add_f32_dpp %2, %2, %2 " C " row_mask:0xf bank_mask:0xf\n\tv_add_f32_dpp %3, %3, %3 " C " row_mask:0xf bank_mask:0xf" : "+v"(z[0]), "+v"(z[1]), "+v"(z[2]), "+v"(z[3]))
  DPPADD("quad_perm:[2,3,0,1]"); DPPADD("row_ror:4"); DPPADD("row_ror:8");
  float rs[8], nb[8];
#pragma unroll
  for (int k = 0; k < 2; k++) {
    const float mean = fmul_(z[k], 1.f / D), var = fmaxf(fnma_(mean, mean, fmul_(z[2 + k], 1.f / D)), 0.f);
    const float mb = rbf(mean), sd = rbf(__builtin_amdgcn_sqrtf(rbf(rbf(var) + 1e-5f)));
    float r = __builtin_amdgcn_rcpf(sd);
    asm volatile("s_nop 0" : "+v"(r));
    r = ffma_(fnma_(sd, r, 1.f), r, r);
    if ((lane & 15) < 2) stc128(v4u{__float_as_uint(mb), tag, __float_as_uint(r), tag}, rw, WS_STAT + (row0 + 32 * h + 4 * (4 * k + (lane >> 4))) * 16, 0);
    float t[2] = {r, fmul_(-mb, r)};
#pragma unroll
    for (int w = 0; w < 2; w++) {
      float a = t[w], c = t[w];
      asm volatile("s_nop 1" : "+v"(a), "+v"(c));
      swap32(a, c); float b = a, d = c; swap16(a, b); swap16(c, d);
      float* o = w ? nb : rs; o[4 * k] = a; o[4 * k + 1] = b; o[4 * k + 2] = c; o[4 * k + 3] = d;
    }
  }
#pragma unroll
  for (int i = 0; i < 5; i++)
#pragma unroll
    for (int c = 0; c < 4; c++) {
      float y[8];
#pragma unroll
      for (int e = 0; e < 8; e++) y[e] = ffma_(xf[e][4 * i + c], rs[e], nb[e]);
      stc128(v4u{pkhf(y[0], y[1]), pkhf(y[2], y[3]), pkhf(y[4], y[5]), pkhf(y[6], y[7])}, rw, WS_XT + lane * 16, pn * 20480 + (i * 4 + c) * 1024);
    }
  waitv<0>();
  if (lane < 8) stc32(tag, rw, WS_LFLAG + lane * 2048 + pn * 4);
}

template <bool CJ> DEV void grp8(u32* v, u32 a, u32 b, u32 c) {
#define HM(x, y) (CJ ? hmulc(x, y) : hmul(x, y))
  v[1] = HM(v[0], a); v[2] = HM(v[0], b); v[3] = HM(v[1], b); v[4] = HM(v[0], c); v[5] = HM(v[1], c); v[6] = HM(v[2], c); v[7] = HM(v[3], c);
#undef HM
}
DEV void wr4(u32 a, const u32* v) {
  asm volatile("ds_write2_b32 %0, %1, %2 offset1:68\n\tds_write2_b32 %0, %3, %4 offset0:136 offset1:204" ::"v"(a), "v"(v[0]), "v"(v[1]), "v"(v[2]), "v"(v[3]) : "memory");
}
DEV void conv_wave(const Params& p, u32 sb, int slot, int ch, int lane) {
  const u32 UB = sb + LD_K + slot * K_BYTES, KB = sb + LD_R + 2 * WSLOT + slot * 4608, CTL = sb + LD_CTL;
  const int g = lane >> 4, q = (lane >> 2) & 3, pp = lane & 3, j = lane & 15, b = blockIdx.x;
  const rsrc_t rw = mkr(p.ws, WS_BYTES);
  wait0(CTL + C_FULL + 32);
  __builtin_amdgcn_s_setprio(3);
  const bool retile = !((b >> 3) & 1);
  const int wu = ((b >> 4) * 8 + (b & 7)) * 3 + slot;
  if (retile) {
    u32 vo[5];
#pragma unroll
    for (int k = 0; k < 5; k++) { const int n = k * 64 + lane; vo[k] = (n / 40) * 1280 + (n % 40) * 16 - (k & 3) * 1024; }
    const v4u rd = mkrs(p.wd, F * D * 2);
    const u32 so = (wu >> 1) * 10240 + (wu & 1) * 640;
    dma4(rd, vo[0], vo[1], vo[2], vo[3], so, UB); dma1(rd, vo[4], so, UB + 4096);
  }
  u32 la[3], ai[3], cr[3], ci[3];
#pragma unroll
  for (int c = 0; c < 3; c++) { const int o = ch * NS + 64 * c + lane; la[c] = p.lar[o]; ai[c] = p.aim[o]; cr[c] = p.cre[o]; ci[c] = p.cim[o]; }
  const u32 ldt = p.ldt[ch];
#pragma unroll
  for (int c = 0; c < 3; c++) asm volatile("" : "+v"(la[c]), "+v"(ai[c]), "+v"(cr[c]), "+v"(ci[c]));
  if (retile) {
    waitv<0>();
    __builtin_amdgcn_s_setprio(3);
    const u32 ra = UB + q * 640 + (80 * g + 20 * pp) * 2;
    const u32 wo = WS_WDF + ((4 * (wu & 1) + g) * 48 * 5) * 1024 + j * 16, s2 = (wu >> 3) * 5120 + ((wu >> 1) & 3) * 256;
#pragma unroll
    for (int k = 0; k < 5; k++) stc128(trd2(ra + k * 8, ra + k * 8 + 2560), rw, wo + k * 1024, s2);
  }
  __builtin_amdgcn_s_setprio(0);
  u32 tag = 0;
  const float dt = rbf(__expf(bf2f(ldt)));
  u32 mx = 0;
#pragma unroll
  for (int c = 0; c < 3; c++) mx = max(mx, max(cr[c] & 0x7fffu, ci[c] & 0x7fffu));
  mx = max(mx, (u32)__builtin_amdgcn_update_dpp(0, (int)mx, 0x111, 0xf, 0xf, false));
  mx = max(mx, (u32)__builtin_amdgcn_update_dpp(0, (int)mx, 0x112, 0xf, 0xf, false));
  mx = max(mx, (u32)__builtin_amdgcn_update_dpp(0, (int)mx, 0x114, 0xf, 0xf, false));
  mx = max(mx, (u32)__builtin_amdgcn_update_dpp(0, (int)mx, 0x118, 0xf, 0xf, false));
  mx = max(max((u32)__builtin_amdgcn_readlane(mx, 15), (u32)__builtin_amdgcn_readlane(mx, 31)), max((u32)__builtin_amdgcn_readlane(mx, 47), (u32)__builtin_amdgcn_readlane(mx, 63)));
  const u32 ex = min(max(mx >> 7, 1u), 252u);
  const float cscale = __uint_as_float((253u - ex) << 23);
  const float oscale = 2.f * dt * __uint_as_float((ex + 1u) << 23);
  u32 G[3][57];
#pragma unroll
  for (int c = 0; c < 3; c++) {
    const float zr = fmul_(fmul_(rbf(__expf(bf2f(la[c]))), dt), -1.44269504089f), zi = fmul_(fmul_(bf2f(ai[c]), dt), 0.15915494309f);
    const cf P0 = cexp2(zr, zi), P4 = cexp2(fmul_(zr, 16.f), fmul_(zi, 16.f)), P8 = cexp2(fmul_(zr, 256.f), fmul_(zi, 256.f));
    const cf P1 = csq(P0), P2 = csq(P1), P3 = csq(P2), P5 = csq(P4), P6 = csq(P5), P7 = csq(P6), P9 = csq(P8), P10 = csq(P9), P11 = csq(P10);
    const u32 M0 = pkc(P0), M1 = pkc(P1), M2 = pkc(P2), M3 = pkc(P3), M4 = pkc(P4), M5 = pkc(P5), M7 = pkc(P7), M8 = pkc(P8), M9 = pkc(P9);
    u32* v = G[c];
    const float cx = fmul_(bf2f(cr[c]), cscale), cy = fmul_(bf2f(ci[c]), -cscale);
    v[0] = pkhf(cx, cy); grp8<true>(v, M3, M4, M5);
    v[8] = pkhf(ffma_(cy, P6.y, fmul_(cx, P6.x)), fnma_(cx, P6.y, fmul_(cy, P6.x))); grp8<true>(v + 8, M3, M4, M5);
    v[16] = 0x00003c00u; v[17] = M0; v[18] = M1; v[19] = hmul(M0, M1); v[20] = M2; v[21] = hmul(M0, M2); v[22] = hmul(M1, M2); v[23] = hmul(v[19], M2);
    v[24] = M7; grp8<false>(v + 24, M0, M1, M2);
    v[32] = M8; grp8<false>(v + 32, M1, M2, M7);
    v[40] = M9; grp8<false>(v + 40, M2, M7, M8);
    v[48] = pkc(P10); grp8<false>(v + 48, M7, M8, M9);
    v[56] = pkc(P11);
#pragma unroll
    for (int k = 0; k < 57; k++) asm volatile("" : "+v"(v[k]));
    if (c == 0) {
      wait0(CTL + C_TOK);
      tag = mktag(rfl(ldsv(CTL + C_TAG)));
      if (retile) { waitv<0>(); if (lane < 8) stc32(tag, rw, WS_WFLAG + lane * 2048 + wu * 4); }
    }
  }
  __builtin_amdgcn_s_setprio(1);
  wait0(CTL + C_FULL + 9 * 32);
  f4 kd[3] = {f4{0, 0, 0, 0}, f4{0, 0, 0, 0}, f4{0, 0, 0, 0}};
  {
    const u32 rb = KB + j * 272 + g * 16;
    u32 wa[4] = {KB + lane * 4, KB + lane * 4 + 1088, KB + lane * 4 + 2176, KB + lane * 4 + 3264};
    asm volatile("" : "+v"(kd[0]), "+v"(kd[1]), "+v"(kd[2]), "+v"(wa[0]), "+v"(wa[1]), "+v"(wa[2]), "+v"(wa[3]));
    v4u Fr[12][4];
#pragma unroll
    for (int ps = 0; ps <= 12; ps++) {
      if (ps < 12) {
        const int c = ps >> 2, t = ps & 3;
        wr4(wa[0], G[c] + 16 * t); wr4(wa[1], G[c] + 16 * t + 4);
        if (t < 3) { wr4(wa[2], G[c] + 16 * t + 8); wr4(wa[3], G[c] + 16 * t + 12); }
        else asm volatile("ds_write_b32 %0, %1" ::"v"(wa[2]), "v"(G[c][56]) : "memory");
        asm volatile("ds_read_b128 %0, %4\n\tds_read_b128 %1, %4 offset:64\n\tds_read_b128 %2, %4 offset:128\n\tds_read_b128 %3, %4 offset:192"
                     : "=&v"(Fr[ps][0]), "=&v"(Fr[ps][1]), "=&v"(Fr[ps][2]), "=&v"(Fr[ps][3]) : "v"(rb) : "memory");
      }
      if (ps > 0) {
        const int pv = ps - 1;
        if (ps < 12) asm volatile("s_waitcnt lgkmcnt(9)" : "+v"(Fr[pv][0]), "+v"(Fr[pv][1]), "+v"(Fr[pv][2]), "+v"(Fr[pv][3]));
        else asm volatile("s_waitcnt lgkmcnt(0)" : "+v"(Fr[pv][0]), "+v"(Fr[pv][1]), "+v"(Fr[pv][2]), "+v"(Fr[pv][3]));
        if (pv & 3) {
#pragma unroll
          for (int ks = 0; ks < 4; ks++) mma_hf(kd[(pv & 3) - 1], Fr[pv][ks], Fr[pv & ~3][ks]);
        }
      }
    }
    asm volatile("s_nop 7" : "+v"(kd[0]), "+v"(kd[1]), "+v"(kd[2]));
  }
  {
    const u32 K0 = KB + 32;
    const u32 a00 = pkhf(kd[1][0], kd[1][0]), a11 = pkhf(kd[1][1], kd[1][1]), a22 = pkhf(kd[1][2], kd[1][2]), a33 = pkhf(kd[1][3], kd[1][3]);
    const u32 a12 = pkhf(kd[1][1], kd[1][2]), a01 = pkhf(kd[1][0], kd[1][1]), a23 = pkhf(kd[1][2], kd[1][3]);
    v2u bb[4];
#pragma unroll
    for (int r = 0; r < 4; r++) { bb[r].x = pkhf(kd[2][r], kd[2][r]); bb[r].y = bb[r].x; }
    const v2u r0 = {pkhf(kd[0][0], kd[0][1]), pkhf(kd[0][2], kd[0][3])}, w1 = {a00, a12}, w2a = {a00, a01}, w2b = {a22, a23};
    const v4u z4 = {0, 0, 0, 0};
    const u32 p3 = K0 + 2 * (1024 + 512 * g + 8 * j) - 8, p2 = K0 + 2 * (256 * g + 8 * j) - 4, p1 = K0 + 2 * (256 + 128 * g + 8 * j) - 2;
    const u32 p0 = K0 + 2 * (128 * (g >> 1) + 8 * j + 4 * (g & 1)), pz = KB + lane * 16;
    asm volatile(
      "s_mov_b32 exec_hi, 1\n\t"
      "ds_write2_b64 %0, %5, %5 offset1:1\n\t"
      "s_mov_b32 exec_hi, 0\n\t"
      "ds_write2_b64 %0, %6, %6 offset0:32 offset1:33\n\t"
      "ds_write2_b64 %0, %7, %7 offset0:64 offset1:65\n\t"
      "ds_write2_b64 %0, %8, %8 offset0:96 offset1:97\n\t"
      "s_mov_b32 exec_lo, 0x55555555\n\t"
      "ds_write_b16 %0, %9 offset:16\n\tds_write_b16 %0, %10 offset:272\n\tds_write_b16 %0, %11 offset:528\n\tds_write_b16 %0, %12 offset:784\n\t"
      "s_mov_b32 exec_lo, 0\n\ts_mov_b32 exec_hi, -1\n\t"
      "ds_write_b32 %1, %13\n\tds_write_b64 %1, %17 offset:4\n\tds_write_b32 %1, %14 offset:12\n\t"
      "ds_write_b32 %1, %15 offset:256\n\tds_write_b64 %1, %18 offset:260\n\tds_write_b32 %1, %16 offset:268\n\t"
      "s_mov_b32 exec_lo, -1\n\ts_mov_b32 exec_hi, 0\n\t"
      "ds_write_b16 %2, %13\n\tds_write_b64 %2, %19 offset:2\n\tds_write_b32 %2, %15 offset:10\n\tds_write_b16 %2, %16 offset:14\n\t"
      "s_mov_b32 exec_lo, 3\n\t"
      "ds_write_b128 %4, %21\n\t"
      "s_mov_b64 exec, -1\n\t"
      "ds_write_b64 %3, %20"
      ::"v"(p3), "v"(p2), "v"(p1), "v"(p0), "v"(pz), "v"(bb[0]), "v"(bb[1]), "v"(bb[2]), "v"(bb[3]), "v"(bb[0].x), "v"(bb[1].x), "v"(bb[2].x), "v"(bb[3].x),
        "v"(a00), "v"(a11), "v"(a22), "v"(a33), "v"(w2a), "v"(w2b), "v"(w1), "v"(r0), "v"(z4) : "memory");
  }
  v4u A[16];
  {
    const u32 ab = KB + 32 + 2 * (-12 + 4 * (8 * g + 7 - q) + 4 * pp);
#pragma unroll
    for (int s = 0; s < 16; s++) A[s] = trd2(ab + 256 * s, ab + 256 * s - 32);
  }
  ldsw(CTL + C_KDONE + slot * 4, 0);
  wait0(CTL + C_UFULL);
  wait0(CTL + C_DONE + 160 + slot * 4);
  __builtin_amdgcn_s_setprio(2);
  f4 acc[4][8]; v4u B[9]; float y[4], c[2], cv[3], pc[3] = {0.f, 0.f, 0.f};
  const u32 ub = UB + 128 + 2 * (4 * j - 4 - 8 * g) - 64, srcl = (lane == 0 ? 63 : (g > 0 ? lane - 16 : lane + 47)) * 4;
  const u32 zo = WS_ZT + (g & 1) * 512 + j * 32 + (g >> 1) * 16;
  const u64 m63 = 1ull << 63;
  u32 ut[4] = {ub, ub + 1152, ub + 2304, ub + 3456};
  asm volatile("" : "+v"(ut[0]), "+v"(ut[1]), "+v"(ut[2]), "+v"(ut[3]));
#define LDB(k, tau, q) do { const u32 a_ = ut[tau] + 64 * (q); const v2u x_ = lds64(a_), y_ = lds64(a_ + 8); B[k] = v4u{x_.x, x_.y, y_.x, y_.y}; SCHED(); } while (0)
#define VADD(d, a_, b_) do { float t_; asm volatile("v_add_f32 %0, %1, %2" : "=v"(t_) : "v"(a_), "v"(b_)); d = t_; } while (0)
#define VMULS(d) asm volatile("v_mul_f32 %0, %0, %1" : "+v"(d) : "v"(oscale))
#define VSEL(d, a_, b_) asm volatile("v_cndmask_b32 %0, %1, %2, %3" : "=v"(d) : "v"(a_), "v"(b_), "s"(m63))
#define VPK(d, a_, b_) do { u32 t_; asm volatile("v_cvt_pk_bf16_f32 %0, %1, %2" : "=v"(t_) : "v"(a_), "v"(b_)); d = t_; } while (0)
#define NOPS() asm volatile("s_nop 3")
#define BPERM() asm volatile("ds_bpermute_b32 %0, %3, %0\n\tds_bpermute_b32 %1, %3, %1\n\tds_bpermute_b32 %2, %3, %2" : "+v"(cv[0]), "+v"(cv[1]), "+v"(cv[2]) : "v"(srcl) : "memory")
#define WAITB(n) asm volatile("s_waitcnt lgkmcnt(" #n ")" : "+v"(cv[0]), "+v"(cv[1]), "+v"(cv[2]) :: "memory")
#define ZSTORE(T) do { asm volatile("s_nop 1\n\tv_permlane16_swap_b32 %0, %2\n\tv_permlane16_swap_b32 %1, %3" : "+v"(zx0), "+v"(zy0), "+v"(zx1), "+v"(zy1)); \
    stc128(v4u{zx0, zy0, zx1, zy1}, rw, zo, ch * 4096 + (T) * 512); } while (0)
  u32 zx0, zy0, zx1, zy1;
  LDB(0, 0, 0);
  LDB(1, 1, 0);
  LDB(2, 2, 0);
  LDB(3, 3, 0);
  LDB(4, 0, 1);
  LDB(5, 1, 1);
  LDB(6, 2, 1);
  LDB(7, 3, 1);
  LDB(8, 0, 2);
  mma_hf0(acc[0][7], A[15], B[0]);
  mma_hf0(acc[0][6], A[13], B[0]);
  mma_hf0(acc[0][5], A[11], B[0]);
  mma_hf0(acc[0][4], A[9], B[0]);
  mma_hf0(acc[0][3], A[7], B[0]);
  mma_hf0(acc[0][2], A[5], B[0]);
  mma_hf0(acc[0][1], A[3], B[0]);
  mma_hf0(acc[0][0], A[1], B[0]);
  LDB(0, 1, 2);
  mma_hf0(acc[1][7], A[15], B[1]);
  mma_hf0(acc[1][6], A[13], B[1]);
  mma_hf0(acc[1][5], A[11], B[1]);
  mma_hf0(acc[1][4], A[9], B[1]);
  mma_hf0(acc[1][3], A[7], B[1]);
  mma_hf0(acc[1][2], A[5], B[1]);
  mma_hf0(acc[1][1], A[3], B[1]);
  mma_hf0(acc[1][0], A[1], B[1]);
  LDB(1, 2, 2);
  mma_hf0(acc[2][7], A[15], B[2]);
  mma_hf0(acc[2][6], A[13], B[2]);
  mma_hf0(acc[2][5], A[11], B[2]);
  mma_hf0(acc[2][4], A[9], B[2]);
  mma_hf0(acc[2][3], A[7], B[2]);
  mma_hf0(acc[2][2], A[5], B[2]);
  mma_hf0(acc[2][1], A[3], B[2]);
  mma_hf0(acc[2][0], A[1], B[2]);
  LDB(2, 3, 2);
  mma_hf0(acc[3][7], A[15], B[3]);
  mma_hf0(acc[3][6], A[13], B[3]);
  mma_hf0(acc[3][5], A[11], B[3]);
  mma_hf0(acc[3][4], A[9], B[3]);
  mma_hf0(acc[3][3], A[7], B[3]);
  mma_hf0(acc[3][2], A[5], B[3]);
  mma_hf0(acc[3][1], A[3], B[3]);
  mma_hf0(acc[3][0], A[1], B[3]);
  LDB(3, 0, 3);
  mma_hf(acc[0][7], A[14], B[4]);
  mma_hf(acc[0][6], A[12], B[4]);
  mma_hf(acc[0][5], A[10], B[4]);
  mma_hf(acc[0][4], A[8], B[4]);
  mma_hf(acc[0][3], A[6], B[4]);
  mma_hf(acc[0][2], A[4], B[4]);
  mma_hf(acc[0][1], A[2], B[4]);
  mma_hf(acc[0][0], A[0], B[4]);
  LDB(4, 1, 3);
  mma_hf(acc[1][7], A[14], B[5]);
  mma_hf(acc[1][6], A[12], B[5]);
  mma_hf(acc[1][5], A[10], B[5]);
  mma_hf(acc[1][4], A[8], B[5]);
  mma_hf(acc[1][3], A[6], B[5]);
  mma_hf(acc[1][2], A[4], B[5]);
  mma_hf(acc[1][1], A[2], B[5]);
  mma_hf(acc[1][0], A[0], B[5]);
  LDB(5, 2, 3);
  mma_hf(acc[2][7], A[14], B[6]);
  mma_hf(acc[2][6], A[12], B[6]);
  mma_hf(acc[2][5], A[10], B[6]);
  mma_hf(acc[2][4], A[8], B[6]);
  mma_hf(acc[2][3], A[6], B[6]);
  mma_hf(acc[2][2], A[4], B[6]);
  mma_hf(acc[2][1], A[2], B[6]);
  mma_hf(acc[2][0], A[0], B[6]);
  LDB(6, 3, 3);
  mma_hf(acc[3][7], A[14], B[7]);
  mma_hf(acc[3][6], A[12], B[7]);
  mma_hf(acc[3][5], A[10], B[7]);
  mma_hf(acc[3][4], A[8], B[7]);
  mma_hf(acc[3][3], A[6], B[7]);
  mma_hf(acc[3][2], A[4], B[7]);
  mma_hf(acc[3][1], A[2], B[7]);
  mma_hf(acc[3][0], A[0], B[7]);
  LDB(7, 0, 4);
  mma_hf(acc[0][7], A[13], B[8]);
  VADD(y[1], acc[0][0][1], acc[1][0][0]);
  VADD(y[2], acc[0][0][2], acc[1][0][1]);
  mma_hf(acc[0][6], A[11], B[8]);
  VADD(y[3], acc[0][0][3], acc[1][0][2]);
  VADD(c[0], acc[1][0][3], acc[2][0][2]);
  mma_hf(acc[0][5], A[9], B[8]);
  VADD(y[2], y[2], acc[2][0][0]);
  VADD(y[3], y[3], acc[2][0][1]);
  mma_hf(acc[0][4], A[7], B[8]);
  NOPS();
  VADD(c[0], c[0], acc[3][0][1]);
  mma_hf(acc[0][3], A[5], B[8]);
  VADD(c[1], acc[2][0][3], acc[3][0][2]);
  VADD(y[3], y[3], acc[3][0][0]);
  mma_hf(acc[0][2], A[3], B[8]);
  VSEL(cv[0], c[0], pc[0]);
  mma_hf(acc[0][1], A[1], B[8]);
  VSEL(cv[1], c[1], pc[1]);
  LDB(8, 1, 4);
  mma_hf(acc[1][7], A[13], B[0]);
  VSEL(cv[2], acc[3][0][3], pc[2]);
  mma_hf(acc[1][6], A[11], B[0]);
  BPERM(); pc[0] = c[0]; pc[1] = c[1]; pc[2] = acc[3][0][3];
  mma_hf(acc[1][5], A[9], B[0]);
  VMULS(y[3]);
  mma_hf(acc[1][4], A[7], B[0]);
  mma_hf(acc[1][3], A[5], B[0]);
  mma_hf(acc[1][2], A[3], B[0]);
  mma_hf(acc[1][1], A[1], B[0]);
  LDB(0, 2, 4);
  mma_hf(acc[2][7], A[13], B[1]);
  mma_hf(acc[2][6], A[11], B[1]);
  mma_hf(acc[2][5], A[9], B[1]);
  WAITB(1);
  VADD(y[0], acc[0][0][0], cv[0]);
  mma_hf(acc[2][4], A[7], B[1]);
  VADD(y[1], y[1], cv[1]);
  mma_hf(acc[2][3], A[5], B[1]);
  VADD(y[2], y[2], cv[2]);
  mma_hf(acc[2][2], A[3], B[1]);
  VMULS(y[0]);
  mma_hf(acc[2][1], A[1], B[1]);
  VMULS(y[1]);
  LDB(1, 3, 4);
  mma_hf(acc[3][7], A[13], B[2]);
  VMULS(y[2]);
  mma_hf(acc[3][6], A[11], B[2]);
  VPK(zx0, y[0], y[1]);
  mma_hf(acc[3][5], A[9], B[2]);
  VPK(zy0, y[2], y[3]);
  mma_hf(acc[3][4], A[7], B[2]);
  mma_hf(acc[3][3], A[5], B[2]);
  mma_hf(acc[3][2], A[3], B[2]);
  mma_hf(acc[3][1], A[1], B[2]);
  LDB(2, 0, 5);
  mma_hf(acc[0][7], A[12], B[3]);
  mma_hf(acc[0][6], A[10], B[3]);
  mma_hf(acc[0][5], A[8], B[3]);
  mma_hf(acc[0][4], A[6], B[3]);
  mma_hf(acc[0][3], A[4], B[3]);
  mma_hf(acc[0][2], A[2], B[3]);
  mma_hf(acc[0][1], A[0], B[3]);
  LDB(3, 1, 5);
  mma_hf(acc[1][7], A[12], B[4]);
  mma_hf(acc[1][6], A[10], B[4]);
  mma_hf(acc[1][5], A[8], B[4]);
  mma_hf(acc[1][4], A[6], B[4]);
  mma_hf(acc[1][3], A[4], B[4]);
  mma_hf(acc[1][2], A[2], B[4]);
  mma_hf(acc[1][1], A[0], B[4]);
  LDB(4, 2, 5);
  mma_hf(acc[2][7], A[12], B[5]);
  mma_hf(acc[2][6], A[10], B[5]);
  mma_hf(acc[2][5], A[8], B[5]);
  mma_hf(acc[2][4], A[6], B[5]);
  mma_hf(acc[2][3], A[4], B[5]);
  mma_hf(acc[2][2], A[2], B[5]);
  mma_hf(acc[2][1], A[0], B[5]);
  LDB(5, 3, 5);
  mma_hf(acc[3][7], A[12], B[6]);
  mma_hf(acc[3][6], A[10], B[6]);
  mma_hf(acc[3][5], A[8], B[6]);
  mma_hf(acc[3][4], A[6], B[6]);
  mma_hf(acc[3][3], A[4], B[6]);
  mma_hf(acc[3][2], A[2], B[6]);
  mma_hf(acc[3][1], A[0], B[6]);
  LDB(6, 0, 6);
  mma_hf(acc[0][7], A[11], B[7]);
  VADD(y[1], acc[0][1][1], acc[1][1][0]);
  VADD(y[2], acc[0][1][2], acc[1][1][1]);
  mma_hf(acc[0][6], A[9], B[7]);
  VADD(y[3], acc[0][1][3], acc[1][1][2]);
  VADD(c[0], acc[1][1][3], acc[2][1][2]);
  mma_hf(acc[0][5], A[7], B[7]);
  VADD(y[2], y[2], acc[2][1][0]);
  VADD(y[3], y[3], acc[2][1][1]);
  mma_hf(acc[0][4], A[5], B[7]);
  NOPS();
  VADD(c[0], c[0], acc[3][1][1]);
  mma_hf(acc[0][3], A[3], B[7]);
  VADD(c[1], acc[2][1][3], acc[3][1][2]);
  VADD(y[3], y[3], acc[3][1][0]);
  mma_hf(acc[0][2], A[1], B[7]);
  VSEL(cv[0], c[0], pc[0]);
  VSEL(cv[1], c[1], pc[1]);
  LDB(7, 1, 6);
  mma_hf(acc[1][7], A[11], B[8]);
  VSEL(cv[2], acc[3][1][3], pc[2]);
  mma_hf(acc[1][6], A[9], B[8]);
  BPERM(); pc[0] = c[0]; pc[1] = c[1]; pc[2] = acc[3][1][3];
  mma_hf(acc[1][5], A[7], B[8]);
  VMULS(y[3]);
  mma_hf(acc[1][4], A[5], B[8]);
  mma_hf(acc[1][3], A[3], B[8]);
  mma_hf(acc[1][2], A[1], B[8]);
  LDB(8, 2, 6);
  mma_hf(acc[2][7], A[11], B[0]);
  mma_hf(acc[2][6], A[9], B[0]);
  mma_hf(acc[2][5], A[7], B[0]);
  mma_hf(acc[2][4], A[5], B[0]);
  WAITB(1);
  VADD(y[0], acc[0][1][0], cv[0]);
  mma_hf(acc[2][3], A[3], B[0]);
  VADD(y[1], y[1], cv[1]);
  mma_hf(acc[2][2], A[1], B[0]);
  VADD(y[2], y[2], cv[2]);
  LDB(0, 3, 6);
  mma_hf(acc[3][7], A[11], B[1]);
  VMULS(y[0]);
  mma_hf(acc[3][6], A[9], B[1]);
  VMULS(y[1]);
  mma_hf(acc[3][5], A[7], B[1]);
  VMULS(y[2]);
  mma_hf(acc[3][4], A[5], B[1]);
  VPK(zx1, y[0], y[1]);
  mma_hf(acc[3][3], A[3], B[1]);
  VPK(zy1, y[2], y[3]);
  mma_hf(acc[3][2], A[1], B[1]);
  ZSTORE(0);
  LDB(1, 0, 7);
  mma_hf(acc[0][7], A[10], B[2]);
  mma_hf(acc[0][6], A[8], B[2]);
  mma_hf(acc[0][5], A[6], B[2]);
  mma_hf(acc[0][4], A[4], B[2]);
  mma_hf(acc[0][3], A[2], B[2]);
  mma_hf(acc[0][2], A[0], B[2]);
  LDB(2, 1, 7);
  mma_hf(acc[1][7], A[10], B[3]);
  mma_hf(acc[1][6], A[8], B[3]);
  mma_hf(acc[1][5], A[6], B[3]);
  mma_hf(acc[1][4], A[4], B[3]);
  mma_hf(acc[1][3], A[2], B[3]);
  mma_hf(acc[1][2], A[0], B[3]);
  LDB(3, 2, 7);
  mma_hf(acc[2][7], A[10], B[4]);
  mma_hf(acc[2][6], A[8], B[4]);
  mma_hf(acc[2][5], A[6], B[4]);
  mma_hf(acc[2][4], A[4], B[4]);
  mma_hf(acc[2][3], A[2], B[4]);
  mma_hf(acc[2][2], A[0], B[4]);
  LDB(4, 3, 7);
  mma_hf(acc[3][7], A[10], B[5]);
  mma_hf(acc[3][6], A[8], B[5]);
  mma_hf(acc[3][5], A[6], B[5]);
  mma_hf(acc[3][4], A[4], B[5]);
  mma_hf(acc[3][3], A[2], B[5]);
  mma_hf(acc[3][2], A[0], B[5]);
  LDB(5, 0, 8);
  mma_hf(acc[0][7], A[9], B[6]);
  VADD(y[1], acc[0][2][1], acc[1][2][0]);
  VADD(y[2], acc[0][2][2], acc[1][2][1]);
  mma_hf(acc[0][6], A[7], B[6]);
  VADD(y[3], acc[0][2][3], acc[1][2][2]);
  VADD(c[0], acc[1][2][3], acc[2][2][2]);
  mma_hf(acc[0][5], A[5], B[6]);
  VADD(y[2], y[2], acc[2][2][0]);
  VADD(y[3], y[3], acc[2][2][1]);
  mma_hf(acc[0][4], A[3], B[6]);
  NOPS();
  VADD(c[0], c[0], acc[3][2][1]);
  mma_hf(acc[0][3], A[1], B[6]);
  VADD(c[1], acc[2][2][3], acc[3][2][2]);
  VADD(y[3], y[3], acc[3][2][0]);
  LDB(6, 1, 8);
  mma_hf(acc[1][7], A[9], B[7]);
  VSEL(cv[0], c[0], pc[0]);
  mma_hf(acc[1][6], A[7], B[7]);
  VSEL(cv[1], c[1], pc[1]);
  mma_hf(acc[1][5], A[5], B[7]);
  VSEL(cv[2], acc[3][2][3], pc[2]);
  mma_hf(acc[1][4], A[3], B[7]);
  BPERM(); pc[0] = c[0]; pc[1] = c[1]; pc[2] = acc[3][2][3];
  mma_hf(acc[1][3], A[1], B[7]);
  VMULS(y[3]);
  LDB(7, 2, 8);
  mma_hf(acc[2][7], A[9], B[8]);
  mma_hf(acc[2][6], A[7], B[8]);
  mma_hf(acc[2][5], A[5], B[8]);
  mma_hf(acc[2][4], A[3], B[8]);
  mma_hf(acc[2][3], A[1], B[8]);
  LDB(8, 3, 8);
  mma_hf(acc[3][7], A[9], B[0]);
  mma_hf(acc[3][6], A[7], B[0]);
  WAITB(2);
  VADD(y[0], acc[0][2][0], cv[0]);
  mma_hf(acc[3][5], A[5], B[0]);
  VADD(y[1], y[1], cv[1]);
  mma_hf(acc[3][4], A[3], B[0]);
  VADD(y[2], y[2], cv[2]);
  mma_hf(acc[3][3], A[1], B[0]);
  VMULS(y[0]);
  LDB(0, 0, 9);
  mma_hf(acc[0][7], A[8], B[1]);
  VMULS(y[1]);
  mma_hf(acc[0][6], A[6], B[1]);
  VMULS(y[2]);
  mma_hf(acc[0][5], A[4], B[1]);
  VPK(zx0, y[0], y[1]);
  mma_hf(acc[0][4], A[2], B[1]);
  VPK(zy0, y[2], y[3]);
  mma_hf(acc[0][3], A[0], B[1]);
  LDB(1, 1, 9);
  mma_hf(acc[1][7], A[8], B[2]);
  mma_hf(acc[1][6], A[6], B[2]);
  mma_hf(acc[1][5], A[4], B[2]);
  mma_hf(acc[1][4], A[2], B[2]);
  mma_hf(acc[1][3], A[0], B[2]);
  LDB(2, 2, 9);
  mma_hf(acc[2][7], A[8], B[3]);
  mma_hf(acc[2][6], A[6], B[3]);
  mma_hf(acc[2][5], A[4], B[3]);
  mma_hf(acc[2][4], A[2], B[3]);
  mma_hf(acc[2][3], A[0], B[3]);
  LDB(3, 3, 9);
  mma_hf(acc[3][7], A[8], B[4]);
  mma_hf(acc[3][6], A[6], B[4]);
  mma_hf(acc[3][5], A[4], B[4]);
  mma_hf(acc[3][4], A[2], B[4]);
  mma_hf(acc[3][3], A[0], B[4]);
  LDB(4, 0, 10);
  mma_hf(acc[0][7], A[7], B[5]);
  VADD(y[1], acc[0][3][1], acc[1][3][0]);
  VADD(y[2], acc[0][3][2], acc[1][3][1]);
  mma_hf(acc[0][6], A[5], B[5]);
  VADD(y[3], acc[0][3][3], acc[1][3][2]);
  VADD(c[0], acc[1][3][3], acc[2][3][2]);
  mma_hf(acc[0][5], A[3], B[5]);
  VADD(y[2], y[2], acc[2][3][0]);
  VADD(y[3], y[3], acc[2][3][1]);
  mma_hf(acc[0][4], A[1], B[5]);
  NOPS();
  VADD(c[0], c[0], acc[3][3][1]);
  LDB(5, 1, 10);
  mma_hf(acc[1][7], A[7], B[6]);
  VADD(c[1], acc[2][3][3], acc[3][3][2]);
  VADD(y[3], y[3], acc[3][3][0]);
  mma_hf(acc[1][6], A[5], B[6]);
  VSEL(cv[0], c[0], pc[0]);
  VSEL(cv[1], c[1], pc[1]);
  mma_hf(acc[1][5], A[3], B[6]);
  VSEL(cv[2], acc[3][3][3], pc[2]);
  mma_hf(acc[1][4], A[1], B[6]);
  BPERM(); pc[0] = c[0]; pc[1] = c[1]; pc[2] = acc[3][3][3];
  LDB(6, 2, 10);
  mma_hf(acc[2][7], A[7], B[7]);
  VMULS(y[3]);
  mma_hf(acc[2][6], A[5], B[7]);
  mma_hf(acc[2][5], A[3], B[7]);
  mma_hf(acc[2][4], A[1], B[7]);
  LDB(7, 3, 10);
  mma_hf(acc[3][7], A[7], B[8]);
  mma_hf(acc[3][6], A[5], B[8]);
  mma_hf(acc[3][5], A[3], B[8]);
  mma_hf(acc[3][4], A[1], B[8]);
  WAITB(2);
  VADD(y[0], acc[0][3][0], cv[0]);
  LDB(8, 0, 11);
  mma_hf(acc[0][7], A[6], B[0]);
  VADD(y[1], y[1], cv[1]);
  mma_hf(acc[0][6], A[4], B[0]);
  VADD(y[2], y[2], cv[2]);
  mma_hf(acc[0][5], A[2], B[0]);
  VMULS(y[0]);
  mma_hf(acc[0][4], A[0], B[0]);
  VMULS(y[1]);
  LDB(0, 1, 11);
  mma_hf(acc[1][7], A[6], B[1]);
  VMULS(y[2]);
  mma_hf(acc[1][6], A[4], B[1]);
  VPK(zx1, y[0], y[1]);
  mma_hf(acc[1][5], A[2], B[1]);
  VPK(zy1, y[2], y[3]);
  mma_hf(acc[1][4], A[0], B[1]);
  ZSTORE(2);
  LDB(1, 2, 11);
  mma_hf(acc[2][7], A[6], B[2]);
  mma_hf(acc[2][6], A[4], B[2]);
  mma_hf(acc[2][5], A[2], B[2]);
  mma_hf(acc[2][4], A[0], B[2]);
  LDB(2, 3, 11);
  mma_hf(acc[3][7], A[6], B[3]);
  mma_hf(acc[3][6], A[4], B[3]);
  mma_hf(acc[3][5], A[2], B[3]);
  mma_hf(acc[3][4], A[0], B[3]);
  LDB(3, 0, 12);
  mma_hf(acc[0][7], A[5], B[4]);
  VADD(y[1], acc[0][4][1], acc[1][4][0]);
  VADD(y[2], acc[0][4][2], acc[1][4][1]);
  mma_hf(acc[0][6], A[3], B[4]);
  VADD(y[3], acc[0][4][3], acc[1][4][2]);
  VADD(c[0], acc[1][4][3], acc[2][4][2]);
  mma_hf(acc[0][5], A[1], B[4]);
  VADD(y[2], y[2], acc[2][4][0]);
  VADD(y[3], y[3], acc[2][4][1]);
  LDB(4, 1, 12);
  mma_hf(acc[1][7], A[5], B[5]);
  NOPS();
  VADD(c[0], c[0], acc[3][4][1]);
  mma_hf(acc[1][6], A[3], B[5]);
  VADD(c[1], acc[2][4][3], acc[3][4][2]);
  VADD(y[3], y[3], acc[3][4][0]);
  mma_hf(acc[1][5], A[1], B[5]);
  VSEL(cv[0], c[0], pc[0]);
  VSEL(cv[1], c[1], pc[1]);
  LDB(5, 2, 12);
  mma_hf(acc[2][7], A[5], B[6]);
  VSEL(cv[2], acc[3][4][3], pc[2]);
  BPERM(); pc[0] = c[0]; pc[1] = c[1]; pc[2] = acc[3][4][3];
  mma_hf(acc[2][6], A[3], B[6]);
  VMULS(y[3]);
  mma_hf(acc[2][5], A[1], B[6]);
  LDB(6, 3, 12);
  mma_hf(acc[3][7], A[5], B[7]);
  mma_hf(acc[3][6], A[3], B[7]);
  WAITB(1);
  VADD(y[0], acc[0][4][0], cv[0]);
  mma_hf(acc[3][5], A[1], B[7]);
  VADD(y[1], y[1], cv[1]);
  VADD(y[2], y[2], cv[2]);
  LDB(7, 0, 13);
  mma_hf(acc[0][7], A[4], B[8]);
  VMULS(y[0]);
  VMULS(y[1]);
  mma_hf(acc[0][6], A[2], B[8]);
  VMULS(y[2]);
  VPK(zx0, y[0], y[1]);
  mma_hf(acc[0][5], A[0], B[8]);
  VPK(zy0, y[2], y[3]);
  LDB(8, 1, 13);
  mma_hf(acc[1][7], A[4], B[0]);
  mma_hf(acc[1][6], A[2], B[0]);
  mma_hf(acc[1][5], A[0], B[0]);
  LDB(0, 2, 13);
  mma_hf(acc[2][7], A[4], B[1]);
  mma_hf(acc[2][6], A[2], B[1]);
  mma_hf(acc[2][5], A[0], B[1]);
  LDB(1, 3, 13);
  mma_hf(acc[3][7], A[4], B[2]);
  mma_hf(acc[3][6], A[2], B[2]);
  mma_hf(acc[3][5], A[0], B[2]);
  LDB(2, 0, 14);
  mma_hf(acc[0][7], A[3], B[3]);
  VADD(y[1], acc[0][5][1], acc[1][5][0]);
  VADD(y[2], acc[0][5][2], acc[1][5][1]);
  mma_hf(acc[0][6], A[1], B[3]);
  VADD(y[3], acc[0][5][3], acc[1][5][2]);
  VADD(c[0], acc[1][5][3], acc[2][5][2]);
  LDB(3, 1, 14);
  mma_hf(acc[1][7], A[3], B[4]);
  VADD(y[2], y[2], acc[2][5][0]);
  VADD(y[3], y[3], acc[2][5][1]);
  mma_hf(acc[1][6], A[1], B[4]);
  NOPS();
  VADD(c[0], c[0], acc[3][5][1]);
  LDB(4, 2, 14);
  mma_hf(acc[2][7], A[3], B[5]);
  VADD(c[1], acc[2][5][3], acc[3][5][2]);
  VADD(y[3], y[3], acc[3][5][0]);
  mma_hf(acc[2][6], A[1], B[5]);
  VSEL(cv[0], c[0], pc[0]);
  VSEL(cv[1], c[1], pc[1]);
  LDB(5, 3, 14);
  mma_hf(acc[3][7], A[3], B[6]);
  VSEL(cv[2], acc[3][5][3], pc[2]);
  BPERM(); pc[0] = c[0]; pc[1] = c[1]; pc[2] = acc[3][5][3];
  mma_hf(acc[3][6], A[1], B[6]);
  VMULS(y[3]);
  LDB(6, 0, 15);
  mma_hf(acc[0][7], A[2], B[7]);
  mma_hf(acc[0][6], A[0], B[7]);
  LDB(7, 1, 15);
  mma_hf(acc[1][7], A[2], B[8]);
  WAITB(2);
  VADD(y[0], acc[0][5][0], cv[0]);
  mma_hf(acc[1][6], A[0], B[8]);
  VADD(y[1], y[1], cv[1]);
  VADD(y[2], y[2], cv[2]);
  LDB(8, 2, 15);
  mma_hf(acc[2][7], A[2], B[0]);
  VMULS(y[0]);
  VMULS(y[1]);
  mma_hf(acc[2][6], A[0], B[0]);
  VMULS(y[2]);
  VPK(zx1, y[0], y[1]);
  LDB(0, 3, 15);
  mma_hf(acc[3][7], A[2], B[1]);
  VPK(zy1, y[2], y[3]);
  ZSTORE(4);
  mma_hf(acc[3][6], A[0], B[1]);
  mma_hf(acc[0][7], A[1], B[2]);
  VADD(y[1], acc[0][6][1], acc[1][6][0]);
  VADD(y[2], acc[0][6][2], acc[1][6][1]);
  mma_hf(acc[1][7], A[1], B[3]);
  VADD(y[3], acc[0][6][3], acc[1][6][2]);
  VADD(c[0], acc[1][6][3], acc[2][6][2]);
  mma_hf(acc[2][7], A[1], B[4]);
  VADD(y[2], y[2], acc[2][6][0]);
  VADD(y[3], y[3], acc[2][6][1]);
  mma_hf(acc[3][7], A[1], B[5]);
  NOPS();
  VADD(c[0], c[0], acc[3][6][1]);
  mma_hf(acc[0][7], A[0], B[6]);
  VADD(c[1], acc[2][6][3], acc[3][6][2]);
  VADD(y[3], y[3], acc[3][6][0]);
  mma_hf(acc[1][7], A[0], B[7]);
  VSEL(cv[0], c[0], pc[0]);
  VSEL(cv[1], c[1], pc[1]);
  mma_hf(acc[2][7], A[0], B[8]);
  VSEL(cv[2], acc[3][6][3], pc[2]);
  BPERM(); pc[0] = c[0]; pc[1] = c[1]; pc[2] = acc[3][6][3];
  mma_hf(acc[3][7], A[0], B[0]);
  VMULS(y[3]);
  WAITB(0);
  VADD(y[0], acc[0][6][0], cv[0]);
  VADD(y[1], y[1], cv[1]);
  VADD(y[2], y[2], cv[2]);
  VMULS(y[0]);
  VMULS(y[1]);
  VMULS(y[2]);
  VPK(zx0, y[0], y[1]);
  VPK(zy0, y[2], y[3]);
  VADD(y[1], acc[0][7][1], acc[1][7][0]);
  VADD(y[2], acc[0][7][2], acc[1][7][1]);
  VADD(y[3], acc[0][7][3], acc[1][7][2]);
  VADD(c[0], acc[1][7][3], acc[2][7][2]);
  VADD(y[2], y[2], acc[2][7][0]);
  VADD(y[3], y[3], acc[2][7][1]);
  NOPS();
  VADD(c[0], c[0], acc[3][7][1]);
  VADD(c[1], acc[2][7][3], acc[3][7][2]);
  VADD(y[3], y[3], acc[3][7][0]);
  VSEL(cv[0], c[0], pc[0]);
  VSEL(cv[1], c[1], pc[1]);
  VSEL(cv[2], acc[3][7][3], pc[2]);
  BPERM(); pc[0] = c[0]; pc[1] = c[1]; pc[2] = acc[3][7][3];
  VMULS(y[3]);
  WAITB(0);
  VADD(y[0], acc[0][7][0], cv[0]);
  VADD(y[1], y[1], cv[1]);
  VADD(y[2], y[2], cv[2]);
  VMULS(y[0]);
  VMULS(y[1]);
  VMULS(y[2]);
  VPK(zx1, y[0], y[1]);
  VPK(zy1, y[2], y[3]);
  ZSTORE(6);
  waitv<0>();
  ldsw(CTL + C_CDONE + slot * 4, 0);
}

__global__ __launch_bounds__(512, 2) void k_main(Params p) {
  extern __shared__ __attribute__((aligned(16))) char smem[];
  const u32 sb = (u32)(size_t)(LAS char*)smem;
  const int lane = threadIdx.x & 63, wv = rfl(threadIdx.x >> 6);
  u64 p0; u32 pw;
  asm volatile("s_load_dwordx2 %0, %2, %3\n\ts_load_dword %1, %2, %4" : "=&s"(p0), "=&s"(pw) : "s"(__builtin_amdgcn_kernarg_segment_ptr()), "s"((wv & 6) == 4 ? 24u + 8u * wv : 0u), "s"(wv == 5 ? 0u : 64u));
  const int b = blockIdx.x, xc = b & 7, r = b >> 3;
  const bool g1 = r < 30;
  const int r5 = (r * 43) >> 8, c6 = r - 6 * r5, ri = (xc >> 2) * 5 + r5, cj = (xc & 3) * 6 + c6, ts = (ri * 205) >> 4;
  const int i2 = xc * 4 + (r >> 3), j2 = r & 7;
  const u32 CTL = sb + LD_CTL;
  u64 pc0; u32 codeb;
  asm volatile("s_getpc_b64 %0\n.Lk_start:\n\ts_mov_b32 %1, .Lk_end-.Lk_start\n\t"
               ".subsection 2\n.Lk_end:\n\t.fill 128, 4, 0xBF800000\n\t.subsection 0" : "=s"(pc0), "=s"(codeb));
  const int rot = ((r5 + c6) & 1) * 5;
  const v4u rz = mkrs((const void*)pc0, 0);
  {
    if (wv == 6)
      asm volatile("s_mov_b32 m0, %2\n\ts_nop 0\n\tbuffer_load_dword %0, %1, 0 offen lds" ::"v"(lane * 64 + (r & 15) * 4096), "s"(mkrs((const void*)pc0, codeb)), "s"(sb + LD_ST + 512) : "memory");
    const bool isw = (wv & 6) == 4;
    const u32 mw = isw ? ~0u : 0u;
#define SEL(a_, b_) (((u32)(a_) & mw) | ((u32)(b_) & ~mw))
    const int xi = wv - ((wv >> 2) << 1);
    const u32 pitch = SEL(3072, 1280);
    const int rr = lane >> 3;
    const u32 sw0 = isw ? (rr & 2) : (rr >> 1);
    u32 a0 = rr * pitch + (((lane & 7) ^ sw0) << 4), a1 = (a0 ^ 64) + 8 * pitch - 1024, a2 = a0 + 16 * pitch - 2048, a3 = a1 + 16 * pitch - 2048;
    u32 so = SEL(rot * 64 * 3072 + cj * 128, (ts + 2 * xi) * 20480 + rot * 128), ld = SEL(sb + LD_R + (wv & 1) * 8192, sb + LD_X + xi * 4096);
    u32 so2 = so + SEL(32 * 3072, 20480), ld2 = ld + SEL(4096, 2048), nb = g1 ? SEL(D * F * 2, L * D * 2) : 0;
    asm volatile("s_waitcnt lgkmcnt(0)" : "+s"(p0), "+s"(pw), "+v"(a0), "+v"(a1), "+v"(a2), "+v"(a3), "+s"(so), "+s"(ld), "+s"(so2), "+s"(ld2), "+s"(nb) :: "memory");
    const v4u rs = {(u32)p0, (u32)(p0 >> 32) & 0xffffu, nb, 0x00020000u};
    if (g1) { dma4(rs, a0, a1, a2, a3, so, ld); if (isw || wv == 7) dma4(rs, a0, a1, a2, a3, so2, ld2); }
    CBAR();
    if (wv == 1) {
      u32 ln = lane; asm volatile("" : "+v"(ln));
      const u32 mg = g1 ? ~0u : 0u, w8 = ln >> 3;
      u32 m = w8 == 0 ? 0xf3f37c00u | ((~r & 1) * 0x04040000u) : 0x01010101u & mg;
      m = w8 == 1 ? 1u : m; m = (w8 & 6) == 2 ? mg : m; m = w8 == 4 ? (0x01013100u & mg) | 0xffu : m; m = w8 == 6 ? 0x0101u & mg : m;
      m >>= (ln & 7) * 4;
      if (ln < 56) *(volatile LAS v4u*)(CTL + ln * 16) = v4u{m & 1u, (m >> 1) & 1u, (m >> 2) & 1u, (m >> 3) & 1u};
    }
    BARRIER();
    sig0(rz, CTL + C_FULL + wv * 4);
    if (wv == 6) { asm volatile("s_mov_b32 m0, %0\n\ts_mov_b64 exec, 1\n\tbuffer_load_dword off, %1, 0 sc1 lds\n\ts_mov_b64 exec, -1" ::"s"(CTL + C_TAG), "s"(mkrs(p.ws, 64)) : "memory"); sig0(rz, CTL + C_TOK); }
    if (isw) {
      if (g1) { dma4(rs, a0, a1, a2, a3, so + 64 * 3072, ld + WSLOT); dma4(rs, a0, a1, a2, a3, so2 + 64 * 3072, ld2 + WSLOT); }
      sig0(rz, CTL + C_FULL + 32 + wv * 4);
    }
  }
  __builtin_amdgcn_wave_barrier();
  const int g = lane >> 4, i = lane & 15;
  const rsrc_t rw = mkr(p.ws, WS_BYTES);
  const int cstep = (0x1293725u >> (3 * ri)) & 7;
  const int ch0 = 80 * xc + 5 * (r >> 1) + (r & 1) * 3;
  __builtin_amdgcn_wave_barrier();
  if (wv < 4) {
    __builtin_amdgcn_s_setprio(2);
    const rsrc_t rx = mkr(p.x, L * D * 2);
    constexpr int NWR = 5;
    v4u Wr[NWR][5];
    const u32 wdo = WS_WDF + (j2 * 48 + 12 * wv) * 5120;
    u32 tag;
    if (g1) {
      f4 acc[3][8], a13[2];
#pragma unroll
      for (int t = 0; t < 3; t++)
#pragma unroll
        for (int c = 0; c < 8; c++) { acc[t][c] = f4{0, 0, 0, 0}; asm volatile("" : "+v"(acc[t][c])); }
      a13[0] = a13[1] = f4{0, 0, 0, 0};
      const int q = (lane >> 2) & 3, pp = lane & 3, fp = ((q >> 1) & 1) | ((g & 1) << 1);
      u32 wb[4];
#pragma unroll
      for (int c = 0; c < 4; c++) wb[c] = sb + LD_R + (8 * g + q) * 128 + ((c ^ wv ^ fp) * 32) + pp * 8;
      u32 xb0 = sb + LD_X + wv * 6144 + i * 128 + ((g ^ (i >> 1)) << 4), xb1 = xb0 ^ 64;
      u32 yb0 = xb0 + (12 - 3 * wv) * 2048, yb1 = yb0 ^ 64, fa = CTL + C_FULL + (lane & 7) * 4;
      u32 sta = sb + LD_ST + (3 * wv * 16 + i) * 8, csa = sb + LD_CS + (16 * wv + 4 * g) * 4;
      u32 hb = WS_HF + (2 * cj) * 1024 + (((((g ^ wv) & 1) << 1) | (g >> 1)) * 16 + i) * 16;
      asm volatile("" : "+v"(sta), "+v"(csa), "+v"(hb));
      v4u Wf[2][8], Xf[2][4];
      asm volatile("" : "+v"(a13[0]), "+v"(a13[1]), "+v"(wb[0]), "+v"(wb[1]), "+v"(wb[2]), "+v"(wb[3]), "+v"(xb0), "+v"(xb1), "+v"(yb0), "+v"(yb1), "+v"(fa));
#define RDP(B, n, XA, YA, OFF) do { if ((n) == 0) { Xf[B][0] = lds128(XA); Xf[B][1] = lds128(XA + 2048); } \
        else if ((n) == 2) { Xf[B][2] = lds128(XA + 4096); Xf[B][3] = lds128(YA); } \
        else { constexpr int c = (n) == 1 ? 0 : (n) - 2; const u32 a = wb[c & 3] + (OFF) + (c >> 2) * 8192; Wf[B][c] = trd2(a, a + 512); } SCHED(); } while (0)
#define MM(KS, m) do { constexpr int c = (m) < 4 ? 0 : (m) < 13 ? 1 + ((m) - 4) / 3 : (m) < 17 ? 4 : 5 + ((m) - 17) / 3; \
        constexpr int t = (m) < 4 ? (m) : (m) < 13 ? ((m) - 4) % 3 : (m) < 17 ? (m) - 13 : ((m) - 17) % 3; \
        if (t == 3) mma_bf(a13[c >> 2], Wf[KS][c], Xf[KS][3]); else mma_bf(acc[t < 3 ? t : 0][c], Wf[KS][c], Xf[KS][t]); SCHED(); } while (0)
#define R0(m) MM(0, m); RDP(1, m, xb1, yb1, 4096);
#define R1(m) MM(1, m); RDP(0, (m) - 12, xb0, yb0, 0);
#pragma unroll
      for (int c = 0; c < 8; c++) { Wf[1][c] = v4u{0, 0, 0, 0}; asm volatile("" : "+v"(Wf[1][c])); }
#pragma unroll
      for (int c = 0; c < 4; c++) { Xf[1][c] = v4u{0, 0, 0, 0}; asm volatile("" : "+v"(Xf[1][c])); }
      u32 sw = 0, sx = 0, fo = 1;
#pragma unroll 1
      for (int s = 0; s < 10; s++) {
        u32 fw;
        spin0(fa, fo);
        R1(12) R1(13) R1(14) R1(15) R1(16) R1(17) R1(18) R1(19) R1(20) R1(21)
        MM(1, 22); fa += 32; asm volatile("" : "+v"(fa)); SCHED(); MM(1, 23); MM(1, 24); MM(1, 25);
        R0(0) R0(1) R0(2) R0(3) R0(4) R0(5) R0(6) R0(7) R0(8) R0(9)
        sx += XSLOT; u32 dx = XSLOT; if (sx == NXS * XSLOT) { sx = 0; dx = XSLOT - NXS * XSLOT; }
        sw += WSLOT; u32 dl = WSLOT; if (sw == NWS * WSLOT) { sw = 0; dl = WSLOT - NWS * WSLOT; }
        MM(0, 10); xb0 += dx; xb1 += dx; asm volatile("" : "+v"(xb0), "+v"(xb1)); SCHED();
        MM(0, 11); yb0 += dx; yb1 += dx; asm volatile("" : "+v"(yb0), "+v"(yb1)); SCHED();
        MM(0, 12); wb[0] += dl; wb[1] += dl; asm volatile("" : "+v"(wb[0]), "+v"(wb[1])); SCHED();
        MM(0, 13); wb[2] += dl; wb[3] += dl; asm volatile("" : "+v"(wb[2]), "+v"(wb[3])); SCHED();
        MM(0, 14); MM(0, 15);
        asm volatile("s_waitcnt lgkmcnt(0)" ::: "memory");
        sig0(rz, CTL + C_DONE + s * 16 + wv * 4); SCHED();
        if (s == cstep) {
          wait0(CTL + C_TOK);
          const u32 tg = mktag(ldsv(CTL + C_TAG));
          f4 c0 = {0, 0, 0, 0}, c1 = {0, 0, 0, 0}; v4u on = {0x3f803f80u, 0x3f803f80u, 0x3f803f80u, 0x3f803f80u};
          asm volatile("s_nop 1" : "+v"(c0), "+v"(c1), "+v"(on));
          mma_bf(c0, Wf[0][0], on); mma_bf(c1, Wf[0][4], on); mma_bf(c0, Wf[1][0], on); mma_bf(c1, Wf[1][4], on);
          asm volatile("s_nop 7" : "+v"(c0), "+v"(c1));
          const f4 cc = (i & 1) ? c1 : c0;
          if (i < 2) {
            const u32 o = WS_CSP + (i * 64 + 16 * wv + 4 * g) * 8, so = (cj * 10 + ri) * 1024;
            stc128(v4u{__float_as_uint(cc[0]), tg, __float_as_uint(cc[1]), tg}, rw, o, so); stc128(v4u{__float_as_uint(cc[2]), tg, __float_as_uint(cc[3]), tg}, rw, o + 16, so);
          }
          asm volatile("s_nop 3"); SCHED();
        }
        MM(0, 16); MM(0, 17); MM(0, 18); MM(0, 19); MM(0, 20); MM(0, 21); MM(0, 22); MM(0, 23); MM(0, 24); MM(0, 25);
        MM(1, 0); MM(1, 1); MM(1, 2); MM(1, 3); fw = ldsv(fa); SCHED();
        MM(1, 4); MM(1, 5); MM(1, 6); MM(1, 7); MM(1, 8); MM(1, 9); fo = (u32)__builtin_amdgcn_ballot_w64(fw != 0); SCHED(); MM(1, 10); MM(1, 11);
      }
      { const v4u cw = *(volatile LAS v4u*)(CTL + C_RDY); MM(1, 12); MM(1, 13); MM(1, 14); MM(1, 15); tag = mktag(rfl(cw.w)); spin0(CTL + C_RDY, rfl(cw.x)); }
      f4 cg[4], cu[4]; f2 st[4], k1[4], k2[4];
#pragma unroll
      for (int t = 0; t < 4; t++) st[t] = *(LAS f2*)(sta + (t < 3 ? t * 128 : 0) + (t < 3 ? 0 : (12 - 3 * wv) * 128));
      SCHED(); MM(1, 16);
      cg[0] = *(LAS f4*)(csa); cu[0] = *(LAS f4*)(csa + 256); SCHED(); MM(1, 17);
      cg[1] = *(LAS f4*)(csa ^ 64); cu[1] = *(LAS f4*)((csa ^ 64) + 256); SCHED(); MM(1, 18);
      cg[2] = *(LAS f4*)(csa ^ 128); cu[2] = *(LAS f4*)((csa ^ 128) + 256); SCHED(); MM(1, 19);
      cg[3] = *(LAS f4*)(csa ^ 192); cu[3] = *(LAS f4*)((csa ^ 192) + 256); SCHED(); MM(1, 20); MM(1, 21);
#define KSET(t) do { k1[t] = f2{__uint_as_float(__float_as_uint(st[t].x) ^ 0x80000000u), fmul_(-1.44269504089f, st[t].y)}; k2[t].x = __builtin_amdgcn_rcpf(st[t].y); asm volatile("" : "+v"(k1[t]), "+v"(k2[t])); SCHED(); } while (0)
      KSET(0); MM(1, 22); KSET(1); MM(1, 23); KSET(2); MM(1, 24); KSET(3); MM(1, 25);
#pragma unroll
      for (int t = 0; t < 4; t++) {
        v2u P[4];
#pragma unroll
        for (int c = 0; c < 4; c++) {
          if (t == 3 && c > 0) break;
          const f4 ag = t < 3 ? acc[t < 3 ? t : 0][c] : a13[0], au = t < 3 ? acc[t < 3 ? t : 0][4 + c] : a13[1];
          {
            u32 p0, p1;
            asm volatile(
              "v_pk_fma_f32 v[248:249], %2, %4, %6 op_sel_hi:[0,1,1]\n\tv_pk_fma_f32 v[250:251], %2, %5, %7 op_sel_hi:[0,1,1]\n\t"
              "v_pk_mul_f32 v[248:249], %2, v[248:249] op_sel:[1,0]\n\tv_pk_mul_f32 v[250:251], %2, v[250:251] op_sel:[1,0]\n\t"
              "v_exp_f32 v248, v248\n\tv_exp_f32 v249, v249\n\tv_exp_f32 v250, v250\n\tv_exp_f32 v251, v251\n\t"
              "v_pk_fma_f32 v[252:253], %2, %8, %10 op_sel_hi:[0,1,1]\n\t"
              "v_pk_fma_f32 v[248:249], %3, v[248:249], %3 op_sel_hi:[0,1,0]\n\tv_pk_fma_f32 v[250:251], %3, v[250:251], %3 op_sel_hi:[0,1,0]\n\t"
              "v_rcp_f32 v248, v248\n\tv_rcp_f32 v249, v249\n\tv_rcp_f32 v250, v250\n\tv_rcp_f32 v251, v251\n\t"
              "v_pk_fma_f32 v[254:255], %2, %9, %11 op_sel_hi:[0,1,1]\n\t"
              "v_pk_mul_f32 v[252:253], v[252:253], v[248:249]\n\tv_pk_mul_f32 v[254:255], v[254:255], v[250:251]\n\t"
              "v_cvt_pk_bf16_f32 %0, v252, v253\n\tv_cvt_pk_bf16_f32 %1, v254, v255"
              : "=v"(p0), "=v"(p1) : "v"(k1[t]), "v"(k2[t]), "v"(cg[c].xy), "v"(cg[c].zw), "v"(ag.xy), "v"(ag.zw), "v"(cu[c].xy), "v"(cu[c].zw), "v"(au.xy), "v"(au.zw)
              : "v248", "v249", "v250", "v251", "v252", "v253", "v254", "v255");
            P[c] = v2u{p0, p1};
          }
          if (t < 3 && (c & 1)) {
            u32 x0 = P[c - 1].x, y0 = P[c - 1].y, x1 = P[c].x, y1 = P[c].y;
            asm volatile("s_nop 0\n\tv_permlane16_swap_b32 %0, %2\n\tv_permlane16_swap_b32 %1, %3" : "+v"(x0), "+v"(y0), "+v"(x1), "+v"(y1));
            stc128(v4u{x0, y0, x1, y1}, rw, hb, (ts + 3 * wv + t) * 49152 + ((c >> 1) ^ (wv >> 1)) * 1024);
          }
          if (c & 1) SCHED();
        }
        if (t == 3 && r5 != 0) {
          __builtin_amdgcn_raw_buffer_store_b64(P[0], rw, WS_HF + (2 * cj + (wv >> 1)) * 1024 + ((2 * (wv & 1) + (g >> 1)) * 16 + i) * 16 + (g & 1) * 8, (ts + 12) * 49152, AUX_SC1);
        }
        SCHED();
      }
      __builtin_amdgcn_s_setprio(3);
      ldsw(CTL + C_DONE + 160 + wv * 4, 0);
      waitv<0>();
      if (lane == 0 && __hip_atomic_fetch_add((LAS u32*)(CTL + C_HCNT), 1u, __ATOMIC_RELAXED, __HIP_MEMORY_SCOPE_WORKGROUP) == 3) stc32(tag, rw, WS_HFLAG + ri * 128 + cj * 4);
    } else {
      wait0(CTL + C_TOK);
      tag = mktag(rfl(ldsv(CTL + C_TAG)));
      wait0(CTL + C_WDFULL);
    }
    wait0(CTL + C_UFULL);
    const int tt = 4 * i2 + wv;
    f4 o[4][5];
    u32 rc[3];
#define OO(m) ((rc[m] & 255) * (D * 2) + (rc[m] >> 8) * 16)
    v4u bp[3];
    {
      v4u xq[3], dq[3]; v3u sq[3];
#pragma unroll
      for (int m = 0; m < 3; m++) {
        const int n = min(m * 64 + lane, 159), row = n / 10; rc[m] = row | (n - row * 10) << 8;
        xq[m] = __builtin_amdgcn_raw_buffer_load_b128(rx, OO(m), (16 * tt * D + 80 * j2) * 2, 0);
        dq[m] = __builtin_amdgcn_raw_buffer_load_b128(mkr(p.dsk, D * 2), (rc[m] >> 8) * 16, 160 * j2, 0);
        sq[m] = __builtin_amdgcn_raw_buffer_load_b96(rw, (rc[m] & 255) * 16, WS_STAT + 256 * tt, AUX_SC1);
      }
      SCHED();
#pragma unroll
      for (int n = 0; n < 25; n++) Wr[n / 5][n % 5] = ldc128(rw, lane * 16, wdo + n * 1024);
      SCHED();
#pragma unroll
      for (int m = 0; m < 3; m++) {
        const float rs = __uint_as_float(sq[m].z), nb = fmul_(-__uint_as_float(sq[m].x), rs);
#pragma unroll
        for (int e = 0; e < 4; e++) {
          const u32 xw = xq[m][e], dw = dq[m][e]; const float x0 = bf2f(xw), x1 = bfhi(xw);
          bp[m][e] = pkbf(ffma_(ffma_(x0, rs, nb), bf2f(dw), x0), ffma_(ffma_(x1, rs, nb), bfhi(dw), x1));
        }
        asm volatile("" : "+v"(bp[m]), "+v"(rc[m]));
      }
    }
    {
      const int l12 = lane < 12 ? lane : 0;
      const u32 fo = WS_HFLAG + (l12 < 6 ? ((20 * i2 + 4) >> 6) * 128 + l12 * 4 : ((20 * i2 + 19) >> 6) * 128 + (l12 - 6) * 4) + wv * 24;
#pragma unroll 1
      for (u32 it = 0; it < SPIN_CAP; it++) { if (__all(ldc32(rw, fo) == tag)) break; __builtin_amdgcn_s_sleep(4); CBAR(); }
      ORDER();
    }
    constexpr int HD = 3;
    v4u hr[12][4], Wl[2][5], Wz[5];
    u32 ho[4];
#pragma unroll
    for (int t = 0; t < 4; t++) ho[t] = WS_HF + (4 * i2 + (t ^ wv)) * 49152 + 12 * wv * 1024;
#define LOAD_H(k) do { _Pragma("unroll") for (int t = 0; t < 4; t++) hr[k][t] = ldc128(rw, lane * 16, ho[t] + (k) * 1024); } while (0)
#pragma unroll
    for (int k = 0; k < HD; k++) LOAD_H(k);
    SCHED();
    __builtin_amdgcn_s_setprio(1);
    const u32 wl = sb + wd_off(wv) + lane * 16;
    mma_bf0(o[3][0], Wr[0][0], hr[0][3]); SCHED();
    mma_bf0(o[2][0], Wr[0][0], hr[0][2]); SCHED();
    mma_bf0(o[1][0], Wr[0][0], hr[0][1]); SCHED();
    mma_bf0(o[0][0], Wr[0][0], hr[0][0]); SCHED();
    mma_bf0(o[3][1], Wr[0][1], hr[0][3]); SCHED();
    mma_bf0(o[2][1], Wr[0][1], hr[0][2]); SCHED();
    mma_bf0(o[1][1], Wr[0][1], hr[0][1]); SCHED();
    mma_bf0(o[0][1], Wr[0][1], hr[0][0]); SCHED();
    mma_bf0(o[3][2], Wr[0][2], hr[0][3]); SCHED();
    mma_bf0(o[2][2], Wr[0][2], hr[0][2]); SCHED();
    mma_bf0(o[1][2], Wr[0][2], hr[0][1]); SCHED();
    mma_bf0(o[0][2], Wr[0][2], hr[0][0]); SCHED();
    mma_bf0(o[3][3], Wr[0][3], hr[0][3]); SCHED();
    mma_bf0(o[2][3], Wr[0][3], hr[0][2]); SCHED();
    mma_bf0(o[1][3], Wr[0][3], hr[0][1]); SCHED();
    mma_bf0(o[0][3], Wr[0][3], hr[0][0]); SCHED();
    mma_bf0(o[3][4], Wr[0][4], hr[0][3]); SCHED();
    hr[3][3] = ldc128(rw, lane * 16, ho[3] + 3072); SCHED();
    mma_bf0(o[2][4], Wr[0][4], hr[0][2]); SCHED();
    hr[3][2] = ldc128(rw, lane * 16, ho[2] + 3072); SCHED();
    mma_bf0(o[1][4], Wr[0][4], hr[0][1]); SCHED();
    hr[3][1] = ldc128(rw, lane * 16, ho[1] + 3072); SCHED();
    mma_bf0(o[0][4], Wr[0][4], hr[0][0]); SCHED();
    hr[3][0] = ldc128(rw, lane * 16, ho[0] + 3072); SCHED();
    mma_bf(o[3][0], Wr[1][0], hr[1][3]); SCHED();
    hr[4][3] = ldc128(rw, lane * 16, ho[3] + 4096); SCHED();
    mma_bf(o[2][0], Wr[1][0], hr[1][2]); SCHED();
    mma_bf(o[1][0], Wr[1][0], hr[1][1]); SCHED();
    mma_bf(o[0][0], Wr[1][0], hr[1][0]); SCHED();
    mma_bf(o[3][1], Wr[1][1], hr[1][3]); SCHED();
    mma_bf(o[2][1], Wr[1][1], hr[1][2]); SCHED();
    hr[4][2] = ldc128(rw, lane * 16, ho[2] + 4096); SCHED();
    mma_bf(o[1][1], Wr[1][1], hr[1][1]); SCHED();
    mma_bf(o[0][1], Wr[1][1], hr[1][0]); SCHED();
    mma_bf(o[3][2], Wr[1][2], hr[1][3]); SCHED();
    mma_bf(o[2][2], Wr[1][2], hr[1][2]); SCHED();
    mma_bf(o[1][2], Wr[1][2], hr[1][1]); SCHED();
    hr[4][1] = ldc128(rw, lane * 16, ho[1] + 4096); SCHED();
    mma_bf(o[0][2], Wr[1][2], hr[1][0]); SCHED();
    mma_bf(o[3][3], Wr[1][3], hr[1][3]); SCHED();
    mma_bf(o[2][3], Wr[1][3], hr[1][2]); SCHED();
    mma_bf(o[1][3], Wr[1][3], hr[1][1]); SCHED();
    mma_bf(o[0][3], Wr[1][3], hr[1][0]); SCHED();
    hr[4][0] = ldc128(rw, lane * 16, ho[0] + 4096); SCHED();
    mma_bf(o[3][4], Wr[1][4], hr[1][3]); SCHED();
    mma_bf(o[2][4], Wr[1][4], hr[1][2]); SCHED();
    mma_bf(o[1][4], Wr[1][4], hr[1][1]); SCHED();
    mma_bf(o[0][4], Wr[1][4], hr[1][0]); SCHED();
    mma_bf(o[3][0], Wr[2][0], hr[2][3]); SCHED();
    hr[5][3] = ldc128(rw, lane * 16, ho[3] + 5120); SCHED();
    mma_bf(o[2][0], Wr[2][0], hr[2][2]); SCHED();
    mma_bf(o[1][0], Wr[2][0], hr[2][1]); SCHED();
    mma_bf(o[0][0], Wr[2][0], hr[2][0]); SCHED();
    mma_bf(o[3][1], Wr[2][1], hr[2][3]); SCHED();
    mma_bf(o[2][1], Wr[2][1], hr[2][2]); SCHED();
    hr[5][2] = ldc128(rw, lane * 16, ho[2] + 5120); SCHED();
    mma_bf(o[1][1], Wr[2][1], hr[2][1]); SCHED();
    mma_bf(o[0][1], Wr[2][1], hr[2][0]); SCHED();
    mma_bf(o[3][2], Wr[2][2], hr[2][3]); SCHED();
    mma_bf(o[2][2], Wr[2][2], hr[2][2]); SCHED();
    mma_bf(o[1][2], Wr[2][2], hr[2][1]); SCHED();
    hr[5][1] = ldc128(rw, lane * 16, ho[1] + 5120); SCHED();
    mma_bf(o[0][2], Wr[2][2], hr[2][0]); SCHED();
    mma_bf(o[3][3], Wr[2][3], hr[2][3]); SCHED();
    mma_bf(o[2][3], Wr[2][3], hr[2][2]); SCHED();
    mma_bf(o[1][3], Wr[2][3], hr[2][1]); SCHED();
    mma_bf(o[0][3], Wr[2][3], hr[2][0]); SCHED();
    hr[5][0] = ldc128(rw, lane * 16, ho[0] + 5120); SCHED();
    mma_bf(o[3][4], Wr[2][4], hr[2][3]); SCHED();
    mma_bf(o[2][4], Wr[2][4], hr[2][2]); SCHED();
    mma_bf(o[1][4], Wr[2][4], hr[2][1]); SCHED();
    mma_bf(o[0][4], Wr[2][4], hr[2][0]); SCHED();
    mma_bf(o[3][0], Wr[3][0], hr[3][3]); SCHED();
    hr[6][3] = ldc128(rw, lane * 16, ho[3] + 6144); SCHED();
    mma_bf(o[2][0], Wr[3][0], hr[3][2]); SCHED();
    mma_bf(o[1][0], Wr[3][0], hr[3][1]); SCHED();
    mma_bf(o[0][0], Wr[3][0], hr[3][0]); SCHED();
    mma_bf(o[3][1], Wr[3][1], hr[3][3]); SCHED();
    mma_bf(o[2][1], Wr[3][1], hr[3][2]); SCHED();
    hr[6][2] = ldc128(rw, lane * 16, ho[2] + 6144); SCHED();
    mma_bf(o[1][1], Wr[3][1], hr[3][1]); SCHED();
    mma_bf(o[0][1], Wr[3][1], hr[3][0]); SCHED();
    mma_bf(o[3][2], Wr[3][2], hr[3][3]); SCHED();
    mma_bf(o[2][2], Wr[3][2], hr[3][2]); SCHED();
    mma_bf(o[1][2], Wr[3][2], hr[3][1]); SCHED();
    hr[6][1] = ldc128(rw, lane * 16, ho[1] + 6144); SCHED();
    mma_bf(o[0][2], Wr[3][2], hr[3][0]); SCHED();
    mma_bf(o[3][3], Wr[3][3], hr[3][3]); SCHED();
    mma_bf(o[2][3], Wr[3][3], hr[3][2]); SCHED();
    mma_bf(o[1][3], Wr[3][3], hr[3][1]); SCHED();
    mma_bf(o[0][3], Wr[3][3], hr[3][0]); SCHED();
    hr[6][0] = ldc128(rw, lane * 16, ho[0] + 6144); SCHED();
    mma_bf(o[3][4], Wr[3][4], hr[3][3]); SCHED();
    mma_bf(o[2][4], Wr[3][4], hr[3][2]); SCHED();
    mma_bf(o[1][4], Wr[3][4], hr[3][1]); SCHED();
    mma_bf(o[0][4], Wr[3][4], hr[3][0]); SCHED();
    wait0(CTL + C_WDFULL);
    mma_bf(o[3][0], Wr[4][0], hr[4][3]); SCHED();
    hr[7][3] = ldc128(rw, lane * 16, ho[3] + 7168); SCHED();
    mma_bf(o[2][0], Wr[4][0], hr[4][2]); SCHED();
    Wl[1][0] = lds128(wl + 0); SCHED();
    mma_bf(o[1][0], Wr[4][0], hr[4][1]); SCHED();
    mma_bf(o[0][0], Wr[4][0], hr[4][0]); SCHED();
    mma_bf(o[3][1], Wr[4][1], hr[4][3]); SCHED();
    mma_bf(o[2][1], Wr[4][1], hr[4][2]); SCHED();
    Wl[1][1] = lds128(wl + 1024); SCHED();
    hr[7][2] = ldc128(rw, lane * 16, ho[2] + 7168); SCHED();
    mma_bf(o[1][1], Wr[4][1], hr[4][1]); SCHED();
    mma_bf(o[0][1], Wr[4][1], hr[4][0]); SCHED();
    mma_bf(o[3][2], Wr[4][2], hr[4][3]); SCHED();
    mma_bf(o[2][2], Wr[4][2], hr[4][2]); SCHED();
    Wl[1][2] = lds128(wl + 2048); SCHED();
    mma_bf(o[1][2], Wr[4][2], hr[4][1]); SCHED();
    hr[7][1] = ldc128(rw, lane * 16, ho[1] + 7168); SCHED();
    mma_bf(o[0][2], Wr[4][2], hr[4][0]); SCHED();
    mma_bf(o[3][3], Wr[4][3], hr[4][3]); SCHED();
    mma_bf(o[2][3], Wr[4][3], hr[4][2]); SCHED();
    Wl[1][3] = lds128(wl + 3072); SCHED();
    mma_bf(o[1][3], Wr[4][3], hr[4][1]); SCHED();
    mma_bf(o[0][3], Wr[4][3], hr[4][0]); SCHED();
    hr[7][0] = ldc128(rw, lane * 16, ho[0] + 7168); SCHED();
    mma_bf(o[3][4], Wr[4][4], hr[4][3]); SCHED();
    mma_bf(o[2][4], Wr[4][4], hr[4][2]); SCHED();
    Wl[1][4] = lds128(wl + 4096); SCHED();
    mma_bf(o[1][4], Wr[4][4], hr[4][1]); SCHED();
    mma_bf(o[0][4], Wr[4][4], hr[4][0]); SCHED();
    mma_bf(o[3][0], Wl[1][0], hr[5][3]); SCHED();
    hr[8][3] = ldc128(rw, lane * 16, ho[3] + 8192); SCHED();
    mma_bf(o[2][0], Wl[1][0], hr[5][2]); SCHED();
    Wl[0][0] = lds128(wl + 5120); SCHED();
    mma_bf(o[1][0], Wl[1][0], hr[5][1]); SCHED();
    mma_bf(o[0][0], Wl[1][0], hr[5][0]); SCHED();
    mma_bf(o[3][1], Wl[1][1], hr[5][3]); SCHED();
    mma_bf(o[2][1], Wl[1][1], hr[5][2]); SCHED();
    Wl[0][1] = lds128(wl + 6144); SCHED();
    hr[8][2] = ldc128(rw, lane * 16, ho[2] + 8192); SCHED();
    mma_bf(o[1][1], Wl[1][1], hr[5][1]); SCHED();
    mma_bf(o[0][1], Wl[1][1], hr[5][0]); SCHED();
    mma_bf(o[3][2], Wl[1][2], hr[5][3]); SCHED();
    mma_bf(o[2][2], Wl[1][2], hr[5][2]); SCHED();
    Wl[0][2] = lds128(wl + 7168); SCHED();
    mma_bf(o[1][2], Wl[1][2], hr[5][1]); SCHED();
    hr[8][1] = ldc128(rw, lane * 16, ho[1] + 8192); SCHED();
    mma_bf(o[0][2], Wl[1][2], hr[5][0]); SCHED();
    mma_bf(o[3][3], Wl[1][3], hr[5][3]); SCHED();
    mma_bf(o[2][3], Wl[1][3], hr[5][2]); SCHED();
    Wl[0][3] = lds128(wl + 8192); SCHED();
    mma_bf(o[1][3], Wl[1][3], hr[5][1]); SCHED();
    mma_bf(o[0][3], Wl[1][3], hr[5][0]); SCHED();
    hr[8][0] = ldc128(rw, lane * 16, ho[0] + 8192); SCHED();
    mma_bf(o[3][4], Wl[1][4], hr[5][3]); SCHED();
    mma_bf(o[2][4], Wl[1][4], hr[5][2]); SCHED();
    Wl[0][4] = lds128(wl + 9216); SCHED();
    mma_bf(o[1][4], Wl[1][4], hr[5][1]); SCHED();
    mma_bf(o[0][4], Wl[1][4], hr[5][0]); SCHED();
    mma_bf(o[3][0], Wl[0][0], hr[6][3]); SCHED();
    hr[9][3] = ldc128(rw, lane * 16, ho[3] + 9216); SCHED();
    mma_bf(o[2][0], Wl[0][0], hr[6][2]); SCHED();
    Wl[1][0] = lds128(wl + 10240); SCHED();
    mma_bf(o[1][0], Wl[0][0], hr[6][1]); SCHED();
    mma_bf(o[0][0], Wl[0][0], hr[6][0]); SCHED();
    mma_bf(o[3][1], Wl[0][1], hr[6][3]); SCHED();
    mma_bf(o[2][1], Wl[0][1], hr[6][2]); SCHED();
    Wl[1][1] = lds128(wl + 11264); SCHED();
    hr[9][2] = ldc128(rw, lane * 16, ho[2] + 9216); SCHED();
    mma_bf(o[1][1], Wl[0][1], hr[6][1]); SCHED();
    mma_bf(o[0][1], Wl[0][1], hr[6][0]); SCHED();
    mma_bf(o[3][2], Wl[0][2], hr[6][3]); SCHED();
    mma_bf(o[2][2], Wl[0][2], hr[6][2]); SCHED();
    Wl[1][2] = lds128(wl + 12288); SCHED();
    mma_bf(o[1][2], Wl[0][2], hr[6][1]); SCHED();
    hr[9][1] = ldc128(rw, lane * 16, ho[1] + 9216); SCHED();
    mma_bf(o[0][2], Wl[0][2], hr[6][0]); SCHED();
    mma_bf(o[3][3], Wl[0][3], hr[6][3]); SCHED();
    mma_bf(o[2][3], Wl[0][3], hr[6][2]); SCHED();
    Wl[1][3] = lds128(wl + 13312); SCHED();
    mma_bf(o[1][3], Wl[0][3], hr[6][1]); SCHED();
    mma_bf(o[0][3], Wl[0][3], hr[6][0]); SCHED();
    hr[9][0] = ldc128(rw, lane * 16, ho[0] + 9216); SCHED();
    mma_bf(o[3][4], Wl[0][4], hr[6][3]); SCHED();
    mma_bf(o[2][4], Wl[0][4], hr[6][2]); SCHED();
    Wl[1][4] = lds128(wl + 14336); SCHED();
    mma_bf(o[1][4], Wl[0][4], hr[6][1]); SCHED();
    mma_bf(o[0][4], Wl[0][4], hr[6][0]); SCHED();
    mma_bf(o[3][0], Wl[1][0], hr[7][3]); SCHED();
    hr[10][3] = ldc128(rw, lane * 16, ho[3] + 10240); SCHED();
    mma_bf(o[2][0], Wl[1][0], hr[7][2]); SCHED();
    Wl[0][0] = lds128(wl + 15360); SCHED();
    mma_bf(o[1][0], Wl[1][0], hr[7][1]); SCHED();
    Wz[0] = ldc128(rw, lane * 16, wdo + 56320); SCHED();
    mma_bf(o[0][0], Wl[1][0], hr[7][0]); SCHED();
    mma_bf(o[3][1], Wl[1][1], hr[7][3]); SCHED();
    mma_bf(o[2][1], Wl[1][1], hr[7][2]); SCHED();
    Wl[0][1] = lds128(wl + 16384); SCHED();
    hr[10][2] = ldc128(rw, lane * 16, ho[2] + 10240); SCHED();
    mma_bf(o[1][1], Wl[1][1], hr[7][1]); SCHED();
    Wz[1] = ldc128(rw, lane * 16, wdo + 57344); SCHED();
    mma_bf(o[0][1], Wl[1][1], hr[7][0]); SCHED();
    mma_bf(o[3][2], Wl[1][2], hr[7][3]); SCHED();
    mma_bf(o[2][2], Wl[1][2], hr[7][2]); SCHED();
    Wl[0][2] = lds128(wl + 17408); SCHED();
    mma_bf(o[1][2], Wl[1][2], hr[7][1]); SCHED();
    Wz[2] = ldc128(rw, lane * 16, wdo + 58368); SCHED();
    hr[10][1] = ldc128(rw, lane * 16, ho[1] + 10240); SCHED();
    mma_bf(o[0][2], Wl[1][2], hr[7][0]); SCHED();
    mma_bf(o[3][3], Wl[1][3], hr[7][3]); SCHED();
    mma_bf(o[2][3], Wl[1][3], hr[7][2]); SCHED();
    Wl[0][3] = lds128(wl + 18432); SCHED();
    mma_bf(o[1][3], Wl[1][3], hr[7][1]); SCHED();
    Wz[3] = ldc128(rw, lane * 16, wdo + 59392); SCHED();
    mma_bf(o[0][3], Wl[1][3], hr[7][0]); SCHED();
    hr[10][0] = ldc128(rw, lane * 16, ho[0] + 10240); SCHED();
    mma_bf(o[3][4], Wl[1][4], hr[7][3]); SCHED();
    mma_bf(o[2][4], Wl[1][4], hr[7][2]); SCHED();
    Wl[0][4] = lds128(wl + 19456); SCHED();
    mma_bf(o[1][4], Wl[1][4], hr[7][1]); SCHED();
    Wz[4] = ldc128(rw, lane * 16, wdo + 60416); SCHED();
    mma_bf(o[0][4], Wl[1][4], hr[7][0]); SCHED();
    mma_bf(o[3][0], Wl[0][0], hr[8][3]); SCHED();
    hr[11][3] = ldc128(rw, lane * 16, ho[3] + 11264); SCHED();
    mma_bf(o[2][0], Wl[0][0], hr[8][2]); SCHED();
    Wl[1][0] = lds128(wl + 20480); SCHED();
    mma_bf(o[1][0], Wl[0][0], hr[8][1]); SCHED();
    mma_bf(o[0][0], Wl[0][0], hr[8][0]); SCHED();
    mma_bf(o[3][1], Wl[0][1], hr[8][3]); SCHED();
    mma_bf(o[2][1], Wl[0][1], hr[8][2]); SCHED();
    Wl[1][1] = lds128(wl + 21504); SCHED();
    hr[11][2] = ldc128(rw, lane * 16, ho[2] + 11264); SCHED();
    mma_bf(o[1][1], Wl[0][1], hr[8][1]); SCHED();
    mma_bf(o[0][1], Wl[0][1], hr[8][0]); SCHED();
    mma_bf(o[3][2], Wl[0][2], hr[8][3]); SCHED();
    mma_bf(o[2][2], Wl[0][2], hr[8][2]); SCHED();
    Wl[1][2] = lds128(wl + 22528); SCHED();
    mma_bf(o[1][2], Wl[0][2], hr[8][1]); SCHED();
    hr[11][1] = ldc128(rw, lane * 16, ho[1] + 11264); SCHED();
    mma_bf(o[0][2], Wl[0][2], hr[8][0]); SCHED();
    mma_bf(o[3][3], Wl[0][3], hr[8][3]); SCHED();
    mma_bf(o[2][3], Wl[0][3], hr[8][2]); SCHED();
    Wl[1][3] = lds128(wl + 23552); SCHED();
    mma_bf(o[1][3], Wl[0][3], hr[8][1]); SCHED();
    mma_bf(o[0][3], Wl[0][3], hr[8][0]); SCHED();
    hr[11][0] = ldc128(rw, lane * 16, ho[0] + 11264); SCHED();
    mma_bf(o[3][4], Wl[0][4], hr[8][3]); SCHED();
    mma_bf(o[2][4], Wl[0][4], hr[8][2]); SCHED();
    Wl[1][4] = lds128(wl + 24576); SCHED();
    mma_bf(o[1][4], Wl[0][4], hr[8][1]); SCHED();
    mma_bf(o[0][4], Wl[0][4], hr[8][0]); SCHED();
    mma_bf(o[3][0], Wl[1][0], hr[9][3]); SCHED();
    mma_bf(o[2][0], Wl[1][0], hr[9][2]); SCHED();
    Wl[0][0] = lds128(wl + 25600); SCHED();
    mma_bf(o[1][0], Wl[1][0], hr[9][1]); SCHED();
    mma_bf(o[0][0], Wl[1][0], hr[9][0]); SCHED();
    mma_bf(o[3][1], Wl[1][1], hr[9][3]); SCHED();
    mma_bf(o[2][1], Wl[1][1], hr[9][2]); SCHED();
    Wl[0][1] = lds128(wl + 26624); SCHED();
    mma_bf(o[1][1], Wl[1][1], hr[9][1]); SCHED();
    mma_bf(o[0][1], Wl[1][1], hr[9][0]); SCHED();
    mma_bf(o[3][2], Wl[1][2], hr[9][3]); SCHED();
    mma_bf(o[2][2], Wl[1][2], hr[9][2]); SCHED();
    Wl[0][2] = lds128(wl + 27648); SCHED();
    mma_bf(o[1][2], Wl[1][2], hr[9][1]); SCHED();
    mma_bf(o[0][2], Wl[1][2], hr[9][0]); SCHED();
    mma_bf(o[3][3], Wl[1][3], hr[9][3]); SCHED();
    mma_bf(o[2][3], Wl[1][3], hr[9][2]); SCHED();
    Wl[0][3] = lds128(wl + 28672); SCHED();
    mma_bf(o[1][3], Wl[1][3], hr[9][1]); SCHED();
    mma_bf(o[0][3], Wl[1][3], hr[9][0]); SCHED();
    mma_bf(o[3][4], Wl[1][4], hr[9][3]); SCHED();
    mma_bf(o[2][4], Wl[1][4], hr[9][2]); SCHED();
    Wl[0][4] = lds128(wl + 29696); SCHED();
    mma_bf(o[1][4], Wl[1][4], hr[9][1]); SCHED();
    mma_bf(o[0][4], Wl[1][4], hr[9][0]); SCHED();
    mma_bf(o[3][0], Wl[0][0], hr[10][3]); SCHED();
    mma_bf(o[2][0], Wl[0][0], hr[10][2]); SCHED();
    mma_bf(o[1][0], Wl[0][0], hr[10][1]); SCHED();
    mma_bf(o[0][0], Wl[0][0], hr[10][0]); SCHED();
    mma_bf(o[3][1], Wl[0][1], hr[10][3]); SCHED();
    mma_bf(o[2][1], Wl[0][1], hr[10][2]); SCHED();
    mma_bf(o[1][1], Wl[0][1], hr[10][1]); SCHED();
    mma_bf(o[0][1], Wl[0][1], hr[10][0]); SCHED();
    mma_bf(o[3][2], Wl[0][2], hr[10][3]); SCHED();
    mma_bf(o[2][2], Wl[0][2], hr[10][2]); SCHED();
    mma_bf(o[1][2], Wl[0][2], hr[10][1]); SCHED();
    mma_bf(o[0][2], Wl[0][2], hr[10][0]); SCHED();
    mma_bf(o[3][3], Wl[0][3], hr[10][3]); SCHED();
    mma_bf(o[2][3], Wl[0][3], hr[10][2]); SCHED();
    mma_bf(o[1][3], Wl[0][3], hr[10][1]); SCHED();
    mma_bf(o[0][3], Wl[0][3], hr[10][0]); SCHED();
    mma_bf(o[3][4], Wl[0][4], hr[10][3]); SCHED();
    mma_bf(o[2][4], Wl[0][4], hr[10][2]); SCHED();
    mma_bf(o[1][4], Wl[0][4], hr[10][1]); SCHED();
    mma_bf(o[0][4], Wl[0][4], hr[10][0]); SCHED();
    {
#pragma unroll
      for (int n = 0; n < 25; n++) {
        if (n < 20) { mma_bf(o[3 - n / 5][n % 5], Wz[n % 5], hr[11][3 - n / 5]); SCHED(); }
        if (n >= 5 && n < 20) { const int m = n - 5; asm volatile("s_nop 2" : "+v"(o[3 - m / 5][m % 5])); *(LAS f4*)(wl + ((2 - m / 5) * 5 + m % 5) * 1024) = o[3 - m / 5][m % 5]; SCHED(); }
      }
      ldsw(CTL + C_RED + wv * 4, 0);
      asm volatile("s_nop 7" : "+v"(o[0][0]), "+v"(o[0][1]), "+v"(o[0][2]), "+v"(o[0][3]), "+v"(o[0][4]));
      u32 oo[3], lo3[3];
#pragma unroll
      for (int m = 0; m < 3; m++) { asm volatile("" : "+v"(rc[m])); oo[m] = OO(m); lo3[m] = (rc[m] & 255) * 336 + (rc[m] >> 8) * 32; asm volatile("" : "+v"(oo[m]), "+v"(lo3[m])); }
      float base[3][8];
#pragma unroll
      for (int m = 0; m < 3; m++)
#pragma unroll
        for (int e = 0; e < 4; e++) { const u32 w = bp[m][e]; base[m][2 * e] = bf2f(w); base[m][2 * e + 1] = bfhi(w); asm volatile("" : "+v"(base[m][2 * e]), "+v"(base[m][2 * e + 1])); }
      spin_zero4(CTL + C_RED);
#pragma unroll
      for (int c = 0; c < 5; c++) {
        f4 s[3];
#pragma unroll
        for (int t = 1; t < 4; t++) s[t - 1] = *(LAS f4*)(sb + wd_off(wv ^ t) + lane * 16 + ((t - 1) * 5 + c) * 1024);
#pragma unroll
        for (int e = 0; e < 4; e++) o[0][c][e] = fadd_(fadd_(o[0][c][e], s[0][e]), fadd_(s[1][e], s[2][e]));
        asm volatile("" : "+v"(o[0][c]));
      }
      wait0(CTL + C_ZFULL);
      const int q = (lane >> 2) & 3, pp = lane & 3;
      const u32 tb = sb + wd_off(wv) + 15360;
      v2u z[5];
#pragma unroll
      for (int c = 0; c < 5; c++) z[c] = trd(sb + LD_Z + (20 * g + 4 * c + q) * 128 + wv * 32 + pp * 8);
#pragma unroll
      for (int c = 0; c < 5; c++)
        *(LAS f4*)(tb + i * 336 + g * 80 + c * 16) = f4{fadd_(o[0][c][0], bf2f(z[c].x)), fadd_(o[0][c][1], bfhi(z[c].x)), fadd_(o[0][c][2], bf2f(z[c].y)), fadd_(o[0][c][3], bfhi(z[c].y))};
      CBAR();
      const rsrc_t ro = mkr(p.out, L * D * 2);
#pragma unroll
      for (int m = 0; m < 3; m++) {
        const f4 u0 = *(LAS f4*)(tb + lo3[m]), u1 = *(LAS f4*)(tb + lo3[m] + 16);
        const v4u w = {pkbf(fadd_(u0[0], base[m][0]), fadd_(u0[1], base[m][1])), pkbf(fadd_(u0[2], base[m][2]), fadd_(u0[3], base[m][3])),
                       pkbf(fadd_(u1[0], base[m][4]), fadd_(u1[1], base[m][5])), pkbf(fadd_(u1[2], base[m][6]), fadd_(u1[3], base[m][7]))};
        if (m < 2 || lane < 32) stc128(w, ro, oo[m], (16 * tt * D + 80 * j2) * 2);
      }
    }
    __builtin_amdgcn_endpgm();
  } else if (wv < 7) {
    const int slot = wv - 4;
    if (slot == 2 && (r & 1)) { ln_unit(p, sb, (16 * (xc >> 2) + (r >> 1)) * 4 + (xc & 3), lane); }
    else { conv_wave(p, sb, slot, ch0 + slot, lane); }
    __builtin_amdgcn_endpgm();
  } else {
    __builtin_amdgcn_s_setprio(3);
    v4u fa, fb, cp[14];
    const v4u rws = mkrs(p.ws, WS_BYTES);
#define LDI(d, vo_, io_) asm volatile("buffer_load_dwordx4 %0, %1, %2, 0 offen offset:%3 sc1" : "=v"(d) : "v"(vo_), "s"(rws), "i"(io_) : "memory")
    const u32 vlf = lane * 16 + WS_LFLAG + xc * 2048, vcp = lane * 16 + WS_CSP + cj * 10240, vst = lane * 16 + WS_STAT + ts * 256, vs3 = (lane & 15) * 16 + WS_STAT + ts * 256;
#define LOOK() do { LDI(fa, vlf, 0); LDI(fb, vlf, 1024); } while (0)
#define LOOKC() do { _Pragma("unroll") for (int k = 0; k < 10; k++) LDI(cp[k], vcp + (k >> 2) * 4096, (k & 3) * 1024); \
      LDI(cp[10], vst, 0); LDI(cp[11], vst, 1024); LDI(cp[12], vst, 2048); LDI(cp[13], vs3, 3072); } while (0)
#define PINC(N) asm volatile("s_waitcnt vmcnt(" #N ")" : "+v"(cp[0]), "+v"(cp[1]), "+v"(cp[2]), "+v"(cp[3]), "+v"(cp[4]), "+v"(cp[5]), "+v"(cp[6]), "+v"(cp[7]), "+v"(cp[8]), "+v"(cp[9]), \
      "+v"(cp[10]), "+v"(cp[11]), "+v"(cp[12]), "+v"(cp[13]) :: "memory")
#define PINF() asm volatile("s_waitcnt vmcnt(0)" : "+v"(fa), "+v"(fb) :: "memory")
    if (g1) {
      const v4u rg = mkrs(p.wg, D * F * 2), ru = mkrs(p.wu, D * F * 2), rxs = mkrs(p.x, L * D * 2);
      u32 v[4], vx[4];
#pragma unroll
      for (int k = 0; k < 4; k++) {
        const int row = 8 * k + (lane >> 3), fp = ((row >> 1) & 1) | (((row >> 3) & 1) << 1);
        v[k] = row * 3072 + cj * 128 + (((lane & 7) ^ (fp << 1)) << 4) - 1024 * k;
        vx[k] = (ts * 16 + row) * 1280 + (((lane & 7) ^ ((row >> 1) & 7)) << 4) - 1024 * k;
      }
      int kx = rot + 1, kw = rot == 8 ? 0 : rot + 2; u32 lsw = 2 * WSLOT, lsx = XSLOT;
      v4u dn = {0, 0, 0, 0};
#define FETCH_X(n) do { if (rfl(dn.x | dn.y | dn.z | dn.w) != 0) spin_zero4(CTL + C_DONE + ((n) - 3) * 16); \
        _Pragma("unroll") for (int k = 0; k < 6; k++) { if (k == 0) waitv<50>(); if (k == 3) waitv<47>(); dma4(rxs, vx[0], vx[1], vx[2], vx[3], kx * 128 + k * 40960, sb + LD_X + lsx + k * 4096); } \
        dma2(rxs, vx[0], vx[1], kx * 128 + 12 * 20480, sb + LD_X + lsx + 12 * 2048); \
        sig0(rz, CTL + C_FULL + (n) * 32); } while (0)
#pragma unroll 1
      for (int n = 1; n < 9; n++) {
        FETCH_X(n);
        if (n > 1) dn = *(volatile LAS v4u*)(CTL + C_DONE + (n - 2) * 16);
        kx = kx == 9 ? 0 : kx + 1; lsx += XSLOT; if (lsx == NXS * XSLOT) lsx = 0;
        waitv<44>();
#pragma unroll
        for (int k = 0; k < 4; k++) dma4((k & 1) ? ru : rg, v[0], v[1], v[2], v[3], (kw * 64 + (k >> 1) * 32) * 3072, sb + LD_R + lsw + (k & 1) * 8192 + (k >> 1) * 4096);
        kw = kw == 9 ? 0 : kw + 1; lsw += WSLOT; if (lsw == NWS * WSLOT) lsw = 0;
      }
      waitv<44>(); LOOKC();
      FETCH_X(9);
      waitv<60>(); LOOK();
      wait0(CTL + C_TOK);
      const u32 tg = mktag(rfl(ldsv(CTL + C_TAG)));
      float cs0, cs1;
#pragma unroll 1
      for (u32 it = 0; it < SPIN_CAP; it++) {
        PINC(29);
        u32 bad = 0; cs0 = cs1 = 0.f;
#pragma unroll
        for (int k = 0; k < 14; k++) { bad |= (cp[k].y ^ tg) | (cp[k].w ^ tg); if (k < 10) { cs0 = fadd_(cs0, __uint_as_float(cp[k].x)); cs1 = fadd_(cs1, __uint_as_float(cp[k].z)); } }
        if (__all(bad == 0)) break;
        __builtin_amdgcn_s_sleep(8);
        LOOKC(); waitv<0>();
      }
      waitv<2>();
      *(LAS f2*)(sb + LD_CS + lane * 8) = f2{cs0, cs1};
#pragma unroll
      for (int k = 0; k < 4; k++) *(LAS v2u*)(sb + LD_ST + (k < 3 ? lane : lane & 15) * 8 + k * 512) = v2u{cp[10 + k].x, cp[10 + k].z};
      ldsw(CTL + C_RDY, 0);
    } else { wait0(CTL + C_TOK); __builtin_amdgcn_s_sleep(127); __builtin_amdgcn_s_sleep(60); LOOK(); }
    const u32 tag = mktag(rfl(ldsv(CTL + C_TAG)));
    u32 vo[5];
#pragma unroll
    for (int k = 0; k < 5; k++) { const int n = k * 64 + lane, tau = (n * 911) >> 16, a = n - tau * 72 - 8; vo[k] = (((a >> 1) * 4 + tau) * 20480 + (a & 1) * 16 - (k & 3) * 1024) | ((a | (3 - tau)) & 0x80000000u); asm volatile("" : "+v"(vo[k])); }
#pragma unroll 1
    for (u32 it = 0; it < SPIN_CAP; it++) {
      PINF();
      if (__all(((fa.x ^ tag) | (fa.y ^ tag) | (fa.z ^ tag) | (fa.w ^ tag) | (fb.x ^ tag) | (fb.y ^ tag) | (fb.z ^ tag) | (fb.w ^ tag)) == 0)) break;
      __builtin_amdgcn_s_sleep(8);
      LOOK();
    }
#pragma unroll 1
    for (int sl = 0; sl < 3 - (r & 1); sl++) {
      const int ch = ch0 + sl; const u32 co = WS_XT + ((ch >> 7) * 4 + (ch & 3)) * 1024 + ((ch & 127) >> 2) * 32, UB = sb + LD_K + sl * K_BYTES;
      dma4c(rws, vo[0], vo[1], vo[2], vo[3], co, UB); dma1c(rws, vo[4], co, UB + 4096);
    }
    sig0(rz, CTL + C_UFULL);
    {
      const u32 vl = lane * 16, gb = WS_WDF + (j2 * 48 + 5) * 5120;
      v4u stg[4][NST];
      __builtin_amdgcn_s_setprio(1);
#pragma unroll
      for (int w = 0; w < 4; w++) {
        if (w == 3) waitv<44>();
#pragma unroll
        for (int k = 0; k < NST; k++) stg[w][k] = ldc128(rw, vl, gb + w * 61440 + k * 1024);
      }
      SCHED();
      spin_zero4(CTL + C_KDONE); spin_zero4(CTL + C_DONE + 9 * 16);
#pragma unroll
      for (int w = 0; w < 4; w++)
#pragma unroll
        for (int k = 0; k < NST; k++) *(LAS v4u*)(sb + wd_off(w) + vl + k * 1024) = stg[w][k];
      asm volatile("s_waitcnt lgkmcnt(0)" ::: "memory");
      __builtin_amdgcn_s_setprio(3);
      u32 go = gb;
#pragma unroll 1
      for (int w = 0; w < 4; w++, go += 61440) {
        const u32 lo = sb + wd_off(w);
#pragma unroll
        for (int k = NST; k < 30; k += 4) { waitv<40>(); if (30 - k >= 4) dma4c(rws, vl, vl, vl, vl, go + k * 1024, lo + k * 1024); else dma2c(rws, vl, vl, go + k * 1024, lo + k * 1024); }
      }
      sig0(rz, CTL + C_WDFULL);
    }
    __builtin_amdgcn_s_setprio(3);
    spin_zero4(CTL + C_CDONE);
    if (b == 0 && lane == 0) stc32(tag, rw, WS_GEN);
    if (lane < 8) stc32(tag, rw, WS_ZFLAG + (lane * 8 + xc) * 128 + r * 4);
    __builtin_amdgcn_s_sleep(16);
#pragma unroll 1
    for (u32 it = 0; it < SPIN_CAP; it++) {
      if (__all(ldc32(rw, WS_ZFLAG + (xc * 8 + j2) * 128 + (lane & 31) * 4) == tag)) break;
      __builtin_amdgcn_s_sleep(8); CBAR();
    }
    ORDER();
    {
      u32 vz[4];
#pragma unroll
      for (int k = 0; k < 4; k++) vz[k] = (8 * k + (lane >> 3)) * 4096 + (lane & 7) * 16 - 1024 * k;
      const u32 go = WS_ZT + 80 * j2 * 4096 + i2 * 128;
      dma4c(rws, vz[0], vz[1], vz[2], vz[3], go, sb + LD_Z); dma4c(rws, vz[0], vz[1], vz[2], vz[3], go + 32 * 4096, sb + LD_Z + 4096);
      dma2c(rws, vz[0], vz[1], go + 64 * 4096, sb + LD_Z + 8192);
      sig0(rz, CTL + C_ZFULL);
      waitv<0>();
    }
  }
}

extern "C" void kernel_launch(void* const* d_in, const int* in_sizes, int n_in, void* d_out, int out_size, void* d_ws, size_t ws_size, hipStream_t stream) {
  Params p;
  p.x = (const u16*)d_in[0]; p.lar = (const u16*)d_in[1]; p.aim = (const u16*)d_in[2]; p.cre = (const u16*)d_in[3]; p.cim = (const u16*)d_in[4];
  p.ldt = (const u16*)d_in[5]; p.dsk = (const u16*)d_in[6]; p.wg = (const u16*)d_in[8]; p.wu = (const u16*)d_in[9]; p.wd = (const u16*)d_in[10];
  p.out = (u16*)d_out; p.ws = (char*)d_ws;
  (void)hipFuncSetAttribute((const void*)k_main, hipFuncAttributeMaxDynamicSharedMemorySize, LDS_BYTES);
  k_main<<<256, 512, LDS_BYTES, stream>>>(p);
}
```
